```python
import torch

def _softmax(x, axis=-1):
    return torch.softmax(x, dim=axis)

def _gelu(x):
    return torch.nn.functional.gelu(x, approximate='tanh')

def _silu(x):
    return torch.nn.functional.silu(x)

def _sigmoid(x):
    return torch.sigmoid(x)

def _layernorm(x, axis=-1, eps=1e-05):
    m = x.mean(dim=axis, keepdim=True)
    v = x.var(dim=axis, keepdim=True, unbiased=False)
    return (x - m) / torch.sqrt(v + eps)

def _scatter_add(tgt, idx, src, axis=0):
    return tgt.clone().index_add_(axis, idx, src)

def _segment_sum(src, seg, n):
    out = torch.zeros((n,) + src.shape[1:], dtype=src.dtype)
    return out.index_add_(0, seg, src)

def _pad(x, pw):
    flat = tuple((v for pair in reversed(pw) for v in pair))
    return torch.nn.functional.pad(x, flat)
kernel_name = 'gated_mix__ssm_conv_s4__memory_aug_recurrent_mem_tr__prompt'

def setup_inputs():

    _w = lambda a: (lambda t: t.to(torch.bfloat16) if t.is_floating_point() else t)(torch.as_tensor(a))

    def _A_setup():
        B, L, D, N = (1, 2048, 576, 128)
        A_re = -(lambda _x: torch.exp(torch.as_tensor(_x)))((lambda *s: torch.randn(*s, dtype=torch.bfloat16) * 6.4)(D, N))
        A_im = torch.pi * (lambda *s: torch.randn(*s, dtype=torch.bfloat16) * 0.05)(D, N)
        B_re = (lambda *s: torch.randn(*s, dtype=torch.bfloat16) * 0.1)(D, N)
        B_im = (lambda *s: torch.randn(*s, dtype=torch.bfloat16) * 0.1)(D, N)
        C_re = (lambda *s: torch.randn(*s, dtype=torch.bfloat16) * 0.1)(D, N)
        C_im = (lambda *s: torch.randn(*s, dtype=torch.bfloat16) * 0.1)(D, N)
        log_dt = (lambda *s: torch.randn(*s, dtype=torch.bfloat16) * 0.05)(D) * 0.5 - 4.0
        Dskip = (lambda *s: torch.randn(*s, dtype=torch.bfloat16) * 0.05)(D)
        return {'u': _w((lambda *s: torch.randn(*s, dtype=torch.bfloat16) * 0.05)(B, L, D)), 'A_re': _w(A_re), 'A_im': _w(A_im), 'B_re': _w(B_re), 'B_im': _w(B_im), 'C_re': _w(C_re), 'C_im': _w(C_im), 'log_dt': _w(log_dt), 'Dskip': _w(Dskip)}
    _A_in = _A_setup()

    def _B_setup():
        B, S, Dm = (1, 2048, 576)
        H, Dh = (32, 128)
        M, seg = (8, 32)
        seg = min(seg, S)
        S = S // seg * seg
        return {'x': _w((lambda *s: torch.randn(*s, dtype=torch.bfloat16) * 0.05)(B, S, Dm)), 'mem0': _w((lambda *s: torch.randn(*s, dtype=torch.bfloat16) * 0.4)(B, M, Dm)), 'Wq': _w((lambda *s: torch.randn(*s, dtype=torch.bfloat16) * 6.4)(Dm, H, Dh) * Dm ** (-0.5)), 'Wk': _w((lambda *s: torch.randn(*s, dtype=torch.bfloat16) * 6.4)(Dm, H, Dh) * Dm ** (-0.5)), 'Wv': _w((lambda *s: torch.randn(*s, dtype=torch.bfloat16) * 0.4)(Dm, H, Dh) * Dm ** (-0.5)), 'Wo': _w((lambda *s: torch.randn(*s, dtype=torch.bfloat16) * 0.4)(H, Dh, Dm) * (H * Dh) ** (-0.5)), 'seg_len': seg}
    _B_in = _B_setup()
    return {'A_u': _A_in['u'], 'A_A_re': _A_in['A_re'], 'A_A_im': _A_in['A_im'], 'A_B_re': _A_in['B_re'], 'A_B_im': _A_in['B_im'], 'A_C_re': _A_in['C_re'], 'A_C_im': _A_in['C_im'], 'A_log_dt': _A_in['log_dt'], 'A_Dskip': _A_in['Dskip'], 'B_x': _B_in['x'], 'B_mem0': _B_in['mem0'], 'B_Wq': _B_in['Wq'], 'B_Wk': _B_in['Wk'], 'B_Wv': _B_in['Wv'], 'B_Wo': _B_in['Wo'], 'B_seg_len': _B_in['seg_len'], 'gate_w': _w((lambda *s: torch.randn(*s, dtype=torch.bfloat16) * 0.05)(576) * 16.0)}

def reference(A_u, A_A_re, A_A_im, A_B_re, A_B_im, A_C_re, A_C_im, A_log_dt, A_Dskip, B_x, B_mem0, B_Wq, B_Wk, B_Wv, B_Wo, B_seg_len, gate_w):

    def _A_ref(u, A_re, A_im, B_re, B_im, C_re, C_im, log_dt, Dskip):
        Bsz, L, D = u.shape
        dt = torch.exp(log_dt)[:, None]
        A = A_re + 1j * A_im
        Bc = B_re + 1j * B_im
        Cc = C_re + 1j * C_im
        logdA = dt * A
        dB = dt * Bc
        l = (lambda x, dt: x.to(dt))(torch.arange(L), A_re.dtype)
        P = torch.exp(logdA[:, :, None] * l[None, None, :])
        K = torch.einsum('dn,dnl,dn->dl', Cc, P, dB).real
        L2 = 2 * L
        Kp = _pad(K, ((0, 0), (0, L)))
        up = (lambda x, *d: x.permute(*d))(u, 0, 2, 1)
        up = _pad(up, ((0, 0), (0, 0), (0, L)))
        Kf = (lambda x, axes=(-2, -1): torch.fft.rfft2(x.float(), dim=axes))(Kp[:, None, :], axes=(-2, -1))
        Uf = (lambda x, axes=(-2, -1): torch.fft.rfft2(x.float(), dim=axes))(up[:, :, None, :], axes=(-2, -1))
        Yf = Uf * Kf[None, :, :, :]
        y = (lambda x, s, axes=(-2, -1): torch.fft.irfft2(x, s=s, dim=axes).to(torch.bfloat16))(Yf, (1, L2), axes=(-2, -1))[:, :, 0, :L]
        y = (lambda x, *d: x.permute(*d))(y, 0, 2, 1) + u * Dskip[None, None, :]
        return y

    def _B_ref(x, mem0, Wq, Wk, Wv, Wo, seg_len):
        B, S, Dm = x.shape
        H, Dh = (Wq.shape[1], Wq.shape[2])
        M = mem0.shape[1]
        scale = Dh ** (-0.5)
        n_seg = S // seg_len
        L = M + seg_len
        idx = torch.arange(L)
        mask = (idx[:, None] < M) | (idx[:, None] >= idx[None, :])
        mem = mem0
        outs = []
        for s in range(n_seg):
            xs = x[:, s * seg_len:(s + 1) * seg_len, :].clone()
            h = torch.concatenate([mem, xs], axis=1)
            q = torch.einsum('btd,dhe->bhte', h, Wq)
            k = torch.einsum('btd,dhe->bhte', h, Wk)
            v = torch.einsum('btd,dhe->bhte', h, Wv)
            scores = torch.einsum('bhqe,bhke->bhqk', q, k) * scale
            scores = torch.where(mask[None, None], scores, -1000000000.0)
            p = _softmax(scores, axis=-1)
            o = torch.einsum('bhqk,bhke->bhqe', p, v)
            o = torch.einsum('bhte,hed->btd', o, Wo)
            mem = o[:, :M, :].clone()
            outs.append(o[:, M:, :])
        return torch.concatenate(outs, axis=1)
    x = A_u
    g = _sigmoid((x * gate_w).sum(-1, keepdims=True))
    a = _A_ref(x, A_A_re, A_A_im, A_B_re, A_B_im, A_C_re, A_C_im, A_log_dt, A_Dskip)
    b = _B_ref(x, B_mem0, B_Wq, B_Wk, B_Wv, B_Wo, B_seg_len)
    return g * a + (1 - g) * b
```

```cpp
#include <hip/hip_runtime.h>
#include <hip/hip_bf16.h>
#include <stdint.h>

typedef unsigned short u16;
typedef unsigned int u32;
typedef u32 v4u __attribute__((ext_vector_type(4)));
typedef u32 v2u __attribute__((ext_vector_type(2)));
typedef float f32x4 __attribute__((ext_vector_type(4)));
typedef float f32x2 __attribute__((ext_vector_type(2)));
typedef float f32x16 __attribute__((ext_vector_type(16)));
typedef __bf16 bf16x2 __attribute__((ext_vector_type(2)));
typedef __bf16 bf16x8 __attribute__((ext_vector_type(8)));
typedef __bf16 bf16x16 __attribute__((ext_vector_type(16)));
typedef short s16x4 __attribute__((ext_vector_type(4)));
#define LAS __attribute__((address_space(3)))
typedef LAS char* lptr;

constexpr int SEQ = 2048, DM = 576, NH = 32, DH = 128, NSEG = 64, NR = 36, NREP = 4;
constexpr int MEMB = 9216, PB = 20480, PMM_OFF = 16384;
constexpr size_t OFF_MEMB = 0;
constexpr size_t SZ_MEMB = (size_t)65 * NREP * MEMB;
constexpr size_t OFF_PB = OFF_MEMB + SZ_MEMB;
constexpr size_t SZ_PB = (size_t)NSEG * NREP * PB;
constexpr size_t OFF_Z = OFF_PB + SZ_PB;
constexpr size_t SZ_Z = (size_t)NSEG * NR * NH * 256;
constexpr size_t FILL_END = OFF_Z + SZ_Z;
constexpr size_t OFF_QX = FILL_END;
constexpr size_t SZ_QX = (size_t)NH * SEQ * DH * 2;
constexpr size_t OFF_KX = OFF_QX + SZ_QX;
constexpr size_t OFF_VOX = OFF_KX + SZ_QX;
constexpr size_t SZ_VOX = (size_t)NSEG * NR * NH * 1024;
constexpr size_t OFF_KM = OFF_VOX + SZ_VOX;
constexpr size_t SZ_KM = (size_t)NSEG * NH * 2048;
constexpr size_t OFF_PX = OFF_KM + SZ_KM;
constexpr size_t SZ_PX = (size_t)NSEG * NH * 2 * 1024;
constexpr size_t OFF_PXM = OFF_PX + SZ_PX;
constexpr size_t SZ_PXM = (size_t)NSEG * 2 * NH * 256;
constexpr size_t OFF_A = OFF_PXM + SZ_PXM;
constexpr size_t SZ_A = (size_t)SEQ * DM * 2;
constexpr size_t OFF_KF = OFF_A + SZ_A;
constexpr size_t OFF_ERR = OFF_KF + SZ_A;

struct Args {
    const u16* u; const u16* Are; const u16* Aim; const u16* Bre; const u16* Bim; const u16* Cre; const u16* Cim;
    const u16* logdt; const u16* dskip; const u16* mem0; const u16* Wq; const u16* Wk; const u16* Wv; const u16* Wo;
    const u16* gatew; u16* out; char* ws;
};

__device__ __forceinline__ float bf2f(u16 v) { return __uint_as_float((u32)v << 16); }
__device__ __forceinline__ float bflo(u32 v) { return __uint_as_float(v << 16); }
__device__ __forceinline__ float bfhi(u32 v) { return __uint_as_float(v & 0xffff0000u); }
__device__ __forceinline__ u32 pack2(float lo, float hi) { f32x2 f = {lo, hi}; bf16x2 b = __builtin_convertvector(f, bf16x2); return __builtin_bit_cast(u32, b); }
__device__ __forceinline__ float rbf(float x) { return bflo(pack2(x, x)); }
__device__ __forceinline__ u32 nosent(u32 v) { return v == 0xFFFFFFFFu ? 0x7FC07FC0u : v; }
__device__ __forceinline__ v2u trd(lptr p) { return __builtin_bit_cast(v2u, __builtin_amdgcn_ds_read_tr16_b64_v4i16((LAS s16x4*)p)); }
__device__ __forceinline__ bf16x8 as8(v4u v) { return __builtin_bit_cast(bf16x8, v); }
__device__ __forceinline__ bf16x8 as8(v2u a, v2u b) { v4u v = {a.x, a.y, b.x, b.y}; return __builtin_bit_cast(bf16x8, v); }
__device__ __forceinline__ bf16x16 cat16(bf16x8 a, bf16x8 b) { return __builtin_shufflevector(a, b, 0, 1, 2, 3, 4, 5, 6, 7, 8, 9, 10, 11, 12, 13, 14, 15); }
__device__ __forceinline__ bf16x8 lds8(lptr p) { return *(LAS bf16x8*)p; }
__device__ __forceinline__ bool ok4(v4u v) { return (v.x != 0xFFFFFFFFu) & (v.y != 0xFFFFFFFFu) & (v.z != 0xFFFFFFFFu) & (v.w != 0xFFFFFFFFu); }
__device__ __forceinline__ bool ok2(v2u v) { return (v.x != 0xFFFFFFFFu) & (v.y != 0xFFFFFFFFu); }
#define LBAR() do { asm volatile("s_waitcnt lgkmcnt(0)" ::: "memory"); __builtin_amdgcn_s_barrier(); asm volatile("" ::: "memory"); } while (0)
#define MB() asm volatile("" ::: "memory")
#define MFMA16(a, b, c) __builtin_amdgcn_mfma_f32_16x16x32_bf16((a), (b), (c), 0, 0, 0)
#define MFMA32(a, b, c) __builtin_amdgcn_mfma_f32_32x32x16_bf16((a), (b), (c), 0, 0, 0)
#define SMF(a, b, c, idx) __builtin_amdgcn_smfmac_f32_16x16x64_bf16((a), (b), (c), (idx), 0, 0)
__device__ __forceinline__ float swap32_sum(float a, float b) {
    auto r = __builtin_amdgcn_permlane32_swap(__float_as_uint(a), __float_as_uint(b), false, false);
    return __uint_as_float(r[0]) + __uint_as_float(r[1]); }
__device__ __forceinline__ float xmax(float m) {
    auto r = __builtin_amdgcn_permlane16_swap(__float_as_uint(m), __float_as_uint(m), false, false);
    m = fmaxf(__uint_as_float(r[0]), __uint_as_float(r[1]));
    auto q = __builtin_amdgcn_permlane32_swap(__float_as_uint(m), __float_as_uint(m), false, false);
    return fmaxf(__uint_as_float(q[0]), __uint_as_float(q[1])); }
__device__ __forceinline__ float xsum(float m) {
    auto r = __builtin_amdgcn_permlane16_swap(__float_as_uint(m), __float_as_uint(m), false, false);
    m = __uint_as_float(r[0]) + __uint_as_float(r[1]);
    auto q = __builtin_amdgcn_permlane32_swap(__float_as_uint(m), __float_as_uint(m), false, false);
    return __uint_as_float(q[0]) + __uint_as_float(q[1]); }

template <int NRW, int CW>
__device__ __forceinline__ void stage_rows(const u16* g, int gs, lptr lds, int ls, int tid) {
    constexpr int N = NRW * CW / 512;
    static_assert(NRW * CW % 512 == 0, "");
    v4u v[N];
#pragma unroll
    for (int j = 0; j < N; ++j) { const int i = tid + 512 * j, row = i / CW, c = i % CW; v[j] = *(const v4u*)(g + (size_t)row * gs + c * 8); }
#pragma unroll
    for (int j = 0; j < N; ++j) { const int i = tid + 512 * j, row = i / CW, c = i % CW; *(LAS v4u*)(lds + row * ls + c * 16) = v[j]; }
}
__device__ __forceinline__ bf16x8 frag16(lptr base, int ls, int lane) {
    const int i16 = lane & 15, bg = lane >> 4, q = i16 >> 2, p = i16 & 3;
    lptr a = base + (8 * bg + q) * ls + p * 8;
    return as8(trd(a), trd(a + 4 * ls));
}
__device__ __forceinline__ bf16x8 frag32p(lptr base, int ls, int lane) {
    const int i16 = lane & 15, G = lane >> 4, q = i16 >> 2, p = i16 & 3;
    lptr a = base + (8 * (G >> 1) + q) * ls + (16 * (p & 1) + 8 * (G & 1) + 4 * (p >> 1)) * 2;
    return as8(trd(a), trd(a + 4 * ls));
}

constexpr int XS = 1168;
__device__ __forceinline__ void xload(v4u (&st)[9], const u16* x, int tok0, int tid) {
#pragma unroll
    for (int j = 0; j < 9; ++j) st[j] = *(const v4u*)(x + (size_t)tok0 * DM + (tid + 512 * j) * 8);
}
__device__ __forceinline__ void xstore(const v4u (&st)[9], lptr xl, int tid) {
#pragma unroll
    for (int j = 0; j < 9; ++j) { const int i = tid + 512 * j; *(LAS v4u*)(xl + i * 16 + (i / 72) * 16) = st[j]; }
}

__device__ __forceinline__ void role_qkw(const Args& a, lptr lds, int h, int blk0, int nblk, int bstep) {
    const int tid = threadIdx.x, lane = tid & 63, w = __builtin_amdgcn_readfirstlane(tid >> 6);
    bf16x8 A[36];
#pragma unroll
    for (int m = 0; m < 2; ++m) {
#pragma unroll
        for (int half = 0; half < 2; ++half) {
            stage_rows<288, 16>((m ? a.Wk : a.Wq) + (size_t)half * 288 * 4096 + h * DH, 4096, lds, 288, tid);
            __syncthreads();
            if ((w >> 2) == m) {
#pragma unroll
                for (int ks = 0; ks < 18; ++ks) A[half * 18 + ks] = frag32p(lds + ks * 16 * 288 + (w & 3) * 64, 288, lane);
            }
            __syncthreads();
        }
    }
    u16* O = (u16*)(a.ws + ((w >> 2) ? OFF_KX : OFF_QX)) + (size_t)h * SEQ * DH + (w & 3) * 32 + (lane >> 5) * 16;
    v4u st[9];
    xload(st, a.u, blk0 * 64, tid);
    xstore(st, lds, tid);
    __syncthreads();
    for (int it = 0; it < nblk; ++it) {
        const int blk = blk0 + it * bstep;
        lptr xl = lds + (it & 1) * (64 * XS);
        if (it + 1 < nblk) xload(st, a.u, (blk + bstep) * 64, tid);
#pragma unroll
        for (int tt = 0; tt < 2; ++tt) {
            f32x16 acc = {};
            lptr xb = xl + (32 * tt + (lane & 31)) * XS + (lane >> 5) * 16;
#pragma unroll
            for (int ks = 0; ks < 36; ++ks) acc = MFMA32(A[ks], lds8(xb + ks * 32), acc);
            v4u o0 = {pack2(acc[0], acc[1]), pack2(acc[2], acc[3]), pack2(acc[4], acc[5]), pack2(acc[6], acc[7])};
            v4u o1 = {pack2(acc[8], acc[9]), pack2(acc[10], acc[11]), pack2(acc[12], acc[13]), pack2(acc[14], acc[15])};
            u16* op = O + (size_t)(blk * 64 + 32 * tt + (lane & 31)) * DH;
            *(v4u*)op = o0; *(v4u*)(op + 8) = o1;
        }
        if (it + 1 < nblk) xstore(st, lds + ((it + 1) & 1) * (64 * XS), tid);
        __syncthreads();
    }
}

constexpr int VS = 272;
__device__ __forceinline__ void role_vvo(const Args& a, lptr lds, int h, int blk0, int nblk, int bstep) {
    const int tid = threadIdx.x, lane = tid & 63, w = __builtin_amdgcn_readfirstlane(tid >> 6);
    bf16x8 Av[18], Bo[5][4];
#pragma unroll
    for (int half = 0; half < 2; ++half) {
        stage_rows<288, 16>(a.Wv + (size_t)half * 288 * 4096 + h * DH, 4096, lds, 288, tid);
        __syncthreads();
#pragma unroll
        for (int ks = 0; ks < 9; ++ks) Av[half * 9 + ks] = frag16(lds + ks * 32 * 288 + w * 32, 288, lane);
        __syncthreads();
    }
#pragma unroll
    for (int half = 0; half < 2; ++half) {
        stage_rows<64, 72>(a.Wo + ((size_t)h * DH + half * 64) * DM, DM, lds, 1184, tid);
        __syncthreads();
#pragma unroll
        for (int j = 0; j < 5; ++j) {
            const int r = (w + 8 * j < NR) ? w + 8 * j : w;
#pragma unroll
            for (int ks = 0; ks < 2; ++ks) Bo[j][half * 2 + ks] = frag16(lds + ks * 32 * 1184 + r * 32, 1184, lane);
        }
        __syncthreads();
    }
    lptr xl = lds, vl = lds + 64 * XS;
    v4u st[9];
    xload(st, a.u, blk0 * 64, tid);
    xstore(st, xl, tid);
    __syncthreads();
    const int rho = lane & 15, bg = lane >> 4;
    const int tokA = 16 * ((rho >> 2) & 1) + 4 * (rho >> 3) + (rho & 3);
    for (int it = 0; it < nblk; ++it) {
        const int blk = blk0 + it * bstep;
        if (it + 1 < nblk) xload(st, a.u, (blk + bstep) * 64, tid);
#pragma unroll
        for (int tt = 0; tt < 4; ++tt) {
            f32x4 acc = {};
            lptr xb = xl + (16 * tt + rho) * XS + bg * 16;
#pragma unroll
            for (int ks = 0; ks < 18; ++ks) acc = MFMA16(Av[ks], lds8(xb + ks * 64), acc);
            v2u o = {pack2(acc[0], acc[1]), pack2(acc[2], acc[3])};
            *(LAS v2u*)(vl + (16 * tt + rho) * VS + (16 * w + 4 * bg) * 2) = o;
        }
        __syncthreads();
        if (it + 1 < nblk) xstore(st, xl, tid);
#pragma unroll
        for (int sg = 0; sg < 2; ++sg) {
            bf16x8 Af[2][4];
#pragma unroll
            for (int half = 0; half < 2; ++half)
#pragma unroll
                for (int ks = 0; ks < 4; ++ks) Af[half][ks] = lds8(vl + (32 * sg + tokA + 8 * half) * VS + ks * 64 + bg * 16);
            const int s = blk * 2 + sg;
#pragma unroll
            for (int j = 0; j < 5; ++j) {
                if (j < 4 || w < 4) {
                    f32x4 d0 = {}, d1 = {};
#pragma unroll
                    for (int ks = 0; ks < 4; ++ks) { d0 = MFMA16(Af[0][ks], Bo[j][ks], d0); d1 = MFMA16(Af[1][ks], Bo[j][ks], d1); }
                    v4u o = {pack2(d0[0], d0[1]), pack2(d1[0], d1[1]), pack2(d0[2], d0[3]), pack2(d1[2], d1[3])};
                    *(v4u*)(a.ws + OFF_VOX + ((size_t)(s * NR + w + 8 * j) * NH + h) * 1024 + lane * 16) = o;
                }
            }
        }
        __syncthreads();
    }
}

__global__ void __launch_bounds__(512, 2) k_pre(Args a) {
    extern __shared__ __attribute__((aligned(16))) char smem[];
    lptr lds = (lptr)smem;
    const int b = blockIdx.x, tid = threadIdx.x;
    {
        const size_t beg = (size_t)NREP * MEMB;
        const v4u ff = {0xFFFFFFFFu, 0xFFFFFFFFu, 0xFFFFFFFFu, 0xFFFFFFFFu};
        for (size_t o = beg + ((size_t)b * 512 + tid) * 16; o < FILL_END; o += (size_t)256 * 512 * 16) *(v4u*)(a.ws + o) = ff;
        if (b < NREP) {
            for (int i = tid; i < MEMB / 4; i += 512) {
                const int r = i >> 6, half = (i >> 5) & 1, row = (i >> 2) & 7, m = i & 3;
                const u32 v = *(const u32*)(a.mem0 + row * DM + 16 * r + 4 * m + 2 * half);
                *(u32*)(a.ws + OFF_MEMB + (size_t)b * MEMB + i * 4) = nosent(v);
            }
        }
        if (b == 0 && tid == 0) *(u32*)(a.ws + OFF_ERR) = 0;
    }
    const int xg = b & 7, slot = b >> 3;
    const int h = xg * 4 + (slot >> 3), role = (slot >> 2) & 1, quarter = slot & 3;
    if (role == 0) role_qkw(a, lds, h, quarter * 8, 8, 1);
    else role_vvo(a, lds, h, quarter * 8, 8, 1);
}

constexpr int SPIN_MAX = 400000;
typedef __amdgpu_buffer_rsrc_t rsrc_t;
#define LD16(r, vo, so) __builtin_amdgcn_raw_buffer_load_b128((r), (vo), (so), 16)
#define LD8(r, vo, so) __builtin_amdgcn_raw_buffer_load_b64((r), (vo), (so), 16)
#define ST16(v, r, vo, so) __builtin_amdgcn_raw_buffer_store_b128((v), (r), (vo), (so), 16)
#define ST8(v, r, vo, so) __builtin_amdgcn_raw_buffer_store_b64((v), (r), (vo), (so), 16)
#define ST4(v, r, vo, so) __builtin_amdgcn_raw_buffer_store_b32((v), (r), (vo), (so), 16)

__device__ __forceinline__ void gather_mem(rsrc_t rm, int s, int rep, lptr ml, int lane, int w, int& budget) {
    const int o0 = (w * 72 + lane) * 16, o1 = (w * 72 + 64 + (lane & 7)) * 16;
    const int so = (s * NREP + rep) * MEMB;
    v4u v0, v1;
    for (;;) {
        v0 = LD16(rm, o0, so); v1 = LD16(rm, o1, so);
        MB();
        if (__all(ok4(v0) & ok4(v1))) break;
        if (--budget < 0) break;
    }
    *(LAS v4u*)(ml + o0) = v0;
    if (lane < 8) *(LAS v4u*)(ml + o1) = v1;
}

__device__ __forceinline__ void role_qk(const Args& a, lptr lds, int h, int rep) {
    const int tid = threadIdx.x, lane = tid & 63, w = __builtin_amdgcn_readfirstlane(tid >> 6);
    bf16x16 Bq[9], Bk[9];
#pragma unroll
    for (int m = 0; m < 2; ++m) {
#pragma unroll
        for (int c3 = 0; c3 < 3; ++c3) {
            stage_rows<192, 16>((m ? a.Wk : a.Wq) + (size_t)c3 * 192 * 4096 + h * DH, 4096, lds, 288, tid);
            __syncthreads();
#pragma unroll
            for (int t = 0; t < 3; ++t) {
                bf16x16 f = cat16(frag16(lds + (64 * t) * 288 + w * 32, 288, lane), frag16(lds + (64 * t + 32) * 288 + w * 32, 288, lane));
                if (m) Bk[c3 * 3 + t] = f; else Bq[c3 * 3 + t] = f;
            }
            __syncthreads();
        }
    }
    const rsrc_t rm = __builtin_amdgcn_make_buffer_rsrc(a.ws + OFF_MEMB, 0, (int)SZ_MEMB, 0x00020000);
    const rsrc_t rp = __builtin_amdgcn_make_buffer_rsrc(a.ws + OFF_PB, 0, (int)SZ_PB, 0x00020000);
    lptr ml = lds, xl = lds + MEMB;
    const int idx = (lane & 8) ? 0xEEEE : 0x4444;
    const int i16 = lane & 15, g = lane >> 4;
    const u16* kxp = (const u16*)(a.ws + OFF_KX) + ((size_t)h * SEQ + i16) * DH + g * 8;
    int budget = SPIN_MAX;
    bf16x8 kx[2][4];
    const float c2 = 0.08838834764831845f * 1.4426950408889634f;
    for (int s = 0; s < NSEG; ++s) {
        if (w == 0) {
#pragma unroll
            for (int tl = 0; tl < 2; ++tl)
#pragma unroll
                for (int ks = 0; ks < 4; ++ks) kx[tl][ks] = *(const bf16x8*)(kxp + (size_t)(32 * s + 16 * tl) * DH + ks * 32);
        }
        gather_mem(rm, s, rep, ml, lane, w, budget);
        LBAR();
        f32x4 aq = {}, ak = {};
#pragma unroll
        for (int t = 0; t < 9; ++t) {
            const bf16x8 A = lds8(ml + t * 1024 + lane * 16);
            aq = SMF(A, Bq[t], aq, idx); ak = SMF(A, Bk[t], ak, idx);
        }
        {
            const float s0 = swap32_sum(aq[0], ak[0]), s1 = swap32_sum(aq[1], ak[1]), s2 = swap32_sum(aq[2], ak[2]), s3 = swap32_sum(aq[3], ak[3]);
            v2u o = {pack2(s0, s1), pack2(s2, s3)};
            *(LAS v2u*)(xl + (16 * w + i16) * 32 + g * 8) = o;
        }
        LBAR();
        if (w < 2) {
            bf16x8 F[4];
#pragma unroll
            for (int ks = 0; ks < 4; ++ks) { lptr p = xl + (32 * ks + 8 * g + (i16 >> 2)) * 32 + (i16 & 3) * 8; F[ks] = as8(trd(p), trd(p + 128)); }
            if (w == 1) {
                if (i16 >= 8) {
                    u16* km = (u16*)(a.ws + OFF_KM) + ((size_t)(s * NH + h) * 8 + (i16 - 8)) * DH + g * 8;
#pragma unroll
                    for (int ks = 0; ks < 4; ++ks) *(bf16x8*)(km + ks * 32) = F[ks];
                }
            } else if (s < NSEG - 1) {
                f32x4 S0 = {}, S1 = {}, Sm = {};
#pragma unroll
                for (int ks = 0; ks < 4; ++ks) { S0 = MFMA16(kx[0][ks], F[ks], S0); S1 = MFMA16(kx[1][ks], F[ks], S1); Sm = MFMA16(F[ks], F[ks], Sm); }
                if (g < 2) { Sm[0] = Sm[1] = Sm[2] = Sm[3] = -INFINITY; }
                float m = fmaxf(fmaxf(fmaxf(S0[0], S0[1]), fmaxf(S0[2], S0[3])), fmaxf(fmaxf(S1[0], S1[1]), fmaxf(S1[2], S1[3])));
                m = fmaxf(m, fmaxf(fmaxf(Sm[0], Sm[1]), fmaxf(Sm[2], Sm[3])));
                m = xmax(m);
                const float nm = -m * c2;
                float sum = 0.f;
#pragma unroll
                for (int r = 0; r < 4; ++r) {
                    S0[r] = __builtin_amdgcn_exp2f(fmaf(S0[r], c2, nm)); S1[r] = __builtin_amdgcn_exp2f(fmaf(S1[r], c2, nm)); Sm[r] = __builtin_amdgcn_exp2f(fmaf(Sm[r], c2, nm));
                    sum += S0[r] + S1[r] + Sm[r];
                }
                sum = xsum(sum);
                const float inv = __builtin_amdgcn_rcpf(sum);
                v4u px = {nosent(pack2(S0[0] * inv, S0[1] * inv)), nosent(pack2(S0[2] * inv, S0[3] * inv)), nosent(pack2(S1[0] * inv, S1[1] * inv)), nosent(pack2(S1[2] * inv, S1[3] * inv))};
                v2u pm = {nosent(pack2(Sm[0] * inv, Sm[1] * inv)), nosent(pack2(Sm[2] * inv, Sm[3] * inv))};
                if (i16 < 8) {
                    const int ox = h * 512 + ((g & 1) * 16 + (g >> 1) * 8 + i16) * 16;
                    const int om = PMM_OFF + h * 128 + ((g - 2) * 8 + i16) * 8;
#pragma unroll
                    for (int rr = 0; rr < NREP; ++rr) {
                        ST16(px, rp, ox, (s * NREP + rr) * PB);
                        if (g >= 2) ST8(pm, rp, om, (s * NREP + rr) * PB);
                    }
                }
            }
        }
    }
    if (budget < 0 && lane == 0) *(u32*)(a.ws + OFF_ERR) = 1;
}

__device__ __forceinline__ void role_vo(const Args& a, lptr lds, int h, int rep) {
    const int tid = threadIdx.x, lane = tid & 63, w = __builtin_amdgcn_readfirstlane(tid >> 6);
    bf16x16 Bv[9], Bo[5][2];
#pragma unroll
    for (int c3 = 0; c3 < 3; ++c3) {
        stage_rows<192, 16>(a.Wv + (size_t)c3 * 192 * 4096 + h * DH, 4096, lds, 288, tid);
        __syncthreads();
#pragma unroll
        for (int t = 0; t < 3; ++t) Bv[c3 * 3 + t] = cat16(frag16(lds + (64 * t) * 288 + w * 32, 288, lane), frag16(lds + (64 * t + 32) * 288 + w * 32, 288, lane));
        __syncthreads();
    }
#pragma unroll
    for (int half = 0; half < 2; ++half) {
        stage_rows<64, 72>(a.Wo + ((size_t)h * DH + half * 64) * DM, DM, lds, 1184, tid);
        __syncthreads();
#pragma unroll
        for (int j = 0; j < 5; ++j) {
            const int r = (w + 8 * j < NR) ? w + 8 * j : w;
            Bo[j][half] = cat16(frag16(lds + r * 32, 1184, lane), frag16(lds + 32 * 1184 + r * 32, 1184, lane));
        }
        __syncthreads();
    }
    const rsrc_t rm = __builtin_amdgcn_make_buffer_rsrc(a.ws + OFF_MEMB, 0, (int)SZ_MEMB, 0x00020000);
    const rsrc_t rz = __builtin_amdgcn_make_buffer_rsrc(a.ws + OFF_Z, 0, (int)SZ_Z, 0x00020000);
    lptr ml = lds, xl = lds + MEMB;
    const int idx = (lane & 8) ? 0xEEEE : 0x4444;
    const int i16 = lane & 15, g = lane >> 4;
    int budget = SPIN_MAX;
    const int q = i16 >> 2, p = i16 & 3;
    const int aoff = (16 * g + 4 * (q >> 1) + 2 * (p >> 1) + (q & 1)) * 16 + (p & 1) * 8;
    for (int s = 0; s < NSEG; ++s) {
        gather_mem(rm, s, rep, ml, lane, w, budget);
        LBAR();
        f32x4 av = {};
#pragma unroll
        for (int t = 0; t < 9; ++t) av = SMF(lds8(ml + t * 1024 + lane * 16), Bv[t], av, idx);
        {
            const float s0 = swap32_sum(av[0], av[0]), s1 = swap32_sum(av[1], av[1]), s2 = swap32_sum(av[2], av[2]), s3 = swap32_sum(av[3], av[3]);
            v2u o = {pack2(s0, s1), pack2(s2, s3)};
            if (lane < 32) *(LAS v2u*)(xl + (16 * w + i16) * 16 + g * 8) = o;
        }
        LBAR();
        bf16x8 A2[2];
#pragma unroll
        for (int t = 0; t < 2; ++t) A2[t] = as8(trd(xl + t * 1024 + aoff), trd(xl + t * 1024 + aoff + 128));
        f32x4 d[5];
#pragma unroll
        for (int j = 0; j < 5; ++j) {
            d[j] = f32x4{};
            if (j < 4 || w < 4) { d[j] = SMF(A2[0], Bo[j][0], d[j], idx); d[j] = SMF(A2[1], Bo[j][1], d[j], idx); }
        }
        const int zo = h * 256 + i16 * 16 + (g & 1) * 8;
#pragma unroll
        for (int jp = 0; jp < 3; ++jp) {
            const int ja = 2 * jp, jb = (2 * jp + 1 < 5) ? 2 * jp + 1 : 2 * jp;
            if (jp < 2 || w < 4) {
                const float s0 = swap32_sum(d[ja][0], d[jb][0]), s1 = swap32_sum(d[ja][1], d[jb][1]), s2 = swap32_sum(d[ja][2], d[jb][2]), s3 = swap32_sum(d[ja][3], d[jb][3]);
                v2u o = {nosent(pack2(s0, s1)), nosent(pack2(s2, s3))};
                const int r = w + 8 * ((lane < 32) ? ja : jb);
                if (jp < 2 || lane < 32) ST8(o, rz, r * (NH * 256) + zo, s * (NR * NH * 256));
            }
        }
    }
    if (budget < 0 && lane == 0) *(u32*)(a.ws + OFF_ERR) = 2;
}

__device__ __forceinline__ void role_red(const Args& a, lptr lds, int r, int rep) {
    const int tid = threadIdx.x, lane = tid & 63, w = __builtin_amdgcn_readfirstlane(tid >> 6);
    const rsrc_t rm = __builtin_amdgcn_make_buffer_rsrc(a.ws + OFF_MEMB, 0, (int)SZ_MEMB, 0x00020000);
    const rsrc_t rp = __builtin_amdgcn_make_buffer_rsrc(a.ws + OFF_PB, 0, (int)SZ_PB, 0x00020000);
    const rsrc_t rz = __builtin_amdgcn_make_buffer_rsrc(a.ws + OFF_Z, 0, (int)SZ_Z, 0x00020000);
    const int idx = (lane & 8) ? 0xEEEE : 0x4444;
    const int i16 = lane & 15, g = lane >> 4;
    int budget = SPIN_MAX;
    LAS float* R = (LAS float*)lds;
    for (int s = 0; s < NSEG - 1; ++s) {
        const char* vo = a.ws + OFF_VOX + ((size_t)(s * NR + r) * NH + 4 * w) * 1024 + lane * 16;
        const bf16x8 b0 = *(const bf16x8*)(vo), b1 = *(const bf16x8*)(vo + 1024), b2 = *(const bf16x8*)(vo + 2048), b3 = *(const bf16x8*)(vo + 3072);
        v4u z0 = {}, z1 = {};
        if (w < 4) {
            const int zo = (r * NH + 8 * w + g) * 256 + i16 * 16;
            for (;;) {
                z0 = LD16(rz, zo, s * (NR * NH * 256)); z1 = LD16(rz, zo + 1024, s * (NR * NH * 256));
                MB();
                if (__all(ok4(z0) & ok4(z1))) break;
                if (--budget < 0) break;
            }
        }
        const int so = (s * NREP + rep) * PB;
        v4u p0, p1; v2u m0 = {}, m1 = {};
        const int mo = PMM_OFF + (8 * (w & 3) + 2 * g) * 128 + i16 * 8;
        for (;;) {
            p0 = LD16(rp, (2 * w) * 1024 + lane * 16, so); p1 = LD16(rp, (2 * w + 1) * 1024 + lane * 16, so);
            m0 = LD8(rp, mo, so); m1 = LD8(rp, mo + 128, so);
            MB();
            if (__all(ok4(p0) & ok4(p1) & ok2(m0) & ok2(m1))) break;
            if (--budget < 0) break;
        }
        f32x4 d = {};
        d = SMF(as8(p0), cat16(b0, b1), d, idx);
        d = SMF(as8(p1), cat16(b2, b3), d, idx);
        if (w < 4) {
            v4u y0 = {z0.x, z0.z, z0.y, z0.w}, y1 = {z1.x, z1.z, z1.y, z1.w};
            d = SMF(as8(m0, m1), cat16(as8(y0), as8(y1)), d, idx);
        }
        const float s01 = swap32_sum(d[0], d[1]), s23 = swap32_sum(d[2], d[3]);
        {
            const int row = 4 * (g & 1) + (g >> 1);
            R[w * 160 + row * 20 + i16] = s01;
            R[w * 160 + (row + 2) * 20 + i16] = s23;
        }
        LBAR();
        if (w < NREP) {
            const int i = lane >> 3, cp = lane & 7;
            f32x2 acc = {};
#pragma unroll
            for (int ww = 0; ww < 8; ++ww) acc += *(LAS f32x2*)(R + ww * 160 + i * 20 + 2 * cp);
            const u32 o = nosent(pack2(acc[0], acc[1]));
            ST4(o, rm, r * 256 + ((cp & 1) * 8 + i) * 16 + (cp >> 1) * 4, ((s + 1) * NREP + w) * MEMB);
        }
        LBAR();
    }
    if (budget < 0 && lane == 0) *(u32*)(a.ws + OFF_ERR) = 3;
}

__global__ void __launch_bounds__(512, 2) k_chain(Args a) {
    extern __shared__ __attribute__((aligned(16))) char smem[];
    lptr lds = (lptr)smem;
    const int b = blockIdx.x, xg = b & 7, slot = b >> 3;
    if (xg >= 4) return;
    if (slot < 8) role_qk(a, lds, xg * 8 + slot, xg);
    else if (slot < 16) role_vo(a, lds, xg * 8 + slot - 8, xg);
    else if (slot < 25) role_red(a, lds, xg * 9 + slot - 16, xg);
}

__global__ void __launch_bounds__(256) k_post1(Args a) {
    const int lane = threadIdx.x & 63, w = threadIdx.x >> 6;
    const int unit = blockIdx.x * 4 + w, s = unit >> 5, h = unit & 31;
    const int i16 = lane & 15, g = lane >> 4;
    const u16* Q = (const u16*)(a.ws + OFF_QX) + ((size_t)h * SEQ + 32 * s) * DH;
    const u16* K = (const u16*)(a.ws + OFF_KX) + ((size_t)h * SEQ + 32 * s) * DH;
    const u16* KM = (const u16*)(a.ws + OFF_KM) + (size_t)(s * NH + h) * 8 * DH;
    const int tau = 16 * ((i16 >> 2) & 1) + 4 * (i16 >> 3) + (i16 & 3);
    bf16x8 k0[4], k1[4], km[4];
#pragma unroll
    for (int ks = 0; ks < 4; ++ks) {
        k0[ks] = *(const bf16x8*)(K + tau * DH + ks * 32 + g * 8);
        k1[ks] = *(const bf16x8*)(K + (tau + 8) * DH + ks * 32 + g * 8);
        km[ks] = *(const bf16x8*)(KM + (i16 & 7) * DH + ks * 32 + g * 8);
    }
    const float c2 = 0.08838834764831845f * 1.4426950408889634f;
#pragma unroll
    for (int qt = 0; qt < 2; ++qt) {
        f32x4 S0 = {}, S1 = {}, Sm = {};
#pragma unroll
        for (int ks = 0; ks < 4; ++ks) {
            const bf16x8 qf = *(const bf16x8*)(Q + (16 * qt + i16) * DH + ks * 32 + g * 8);
            S0 = MFMA16(k0[ks], qf, S0); S1 = MFMA16(k1[ks], qf, S1); Sm = MFMA16(km[ks], qf, Sm);
        }
        const int qi = 16 * qt + i16, t0 = 16 * (g & 1) + 4 * (g >> 1);
#pragma unroll
        for (int r = 0; r < 4; ++r) {
            if (t0 + r > qi) S0[r] = -INFINITY;
            if (t0 + r + 8 > qi) S1[r] = -INFINITY;
            if (g >= 2) Sm[r] = -INFINITY;
        }
        float m = fmaxf(fmaxf(fmaxf(S0[0], S0[1]), fmaxf(S0[2], S0[3])), fmaxf(fmaxf(S1[0], S1[1]), fmaxf(S1[2], S1[3])));
        m = fmaxf(m, fmaxf(fmaxf(Sm[0], Sm[1]), fmaxf(Sm[2], Sm[3])));
        m = xmax(m);
        const float nm = -m * c2;
        float sum = 0.f;
#pragma unroll
        for (int r = 0; r < 4; ++r) {
            S0[r] = __builtin_amdgcn_exp2f(fmaf(S0[r], c2, nm)); S1[r] = __builtin_amdgcn_exp2f(fmaf(S1[r], c2, nm)); Sm[r] = __builtin_amdgcn_exp2f(fmaf(Sm[r], c2, nm));
            sum += S0[r] + S1[r] + Sm[r];
        }
        sum = xsum(sum);
        const float inv = __builtin_amdgcn_rcpf(sum);
        v4u px = {pack2(S0[0] * inv, S0[1] * inv), pack2(S1[0] * inv, S1[1] * inv), pack2(S0[2] * inv, S0[3] * inv), pack2(S1[2] * inv, S1[3] * inv)};
        *(v4u*)(a.ws + OFF_PX + ((size_t)(s * 2 + qt) * NH + h) * 1024 + lane * 16) = px;
        if (g < 2) {
            v2u pm = {pack2(Sm[0] * inv, Sm[1] * inv), pack2(Sm[2] * inv, Sm[3] * inv)};
            *(v2u*)(a.ws + OFF_PXM + ((size_t)(s * 2 + qt) * NH + h) * 256 + i16 * 16 + g * 8) = pm;
        }
    }
}

__global__ void __launch_bounds__(576) k_post2(Args a) {
    extern __shared__ __attribute__((aligned(16))) char smem[];
    lptr lds = (lptr)smem;
    const int tid = threadIdx.x, lane = tid & 63, w = __builtin_amdgcn_readfirstlane(tid >> 6);
    const int s = blockIdx.x >> 2, cg = blockIdx.x & 3;
    for (int i = tid; i < 2 * 32 * 64; i += 576) *(LAS v4u*)(lds + (i >> 11) * 40960 + (i & 2047) * 16) = *(const v4u*)(a.ws + OFF_PX + (size_t)s * 65536 + (size_t)i * 16);
    for (int i = tid; i < 2 * 8 * 64; i += 576) *(LAS v4u*)(lds + (i >> 9) * 40960 + 32768 + (i & 511) * 16) = *(const v4u*)(a.ws + OFF_PXM + (size_t)s * 16384 + (size_t)i * 16);
    LAS float* G = (LAS float*)(lds + 81920);
    for (int t = w; t < 32; t += 9) {
        const u16* xr = a.u + (size_t)(32 * s + t) * DM;
        float acc = 0.f;
        for (int d = lane; d < DM; d += 64) acc += rbf(bf2f(xr[d]) * bf2f(a.gatew[d]));
#pragma unroll
        for (int o = 32; o > 0; o >>= 1) acc += __shfl_xor(acc, o);
        acc = rbf(acc);
        if (lane == 0) G[t] = rbf(1.0f / (1.0f + __expf(-acc)));
    }
    __syncthreads();
    const int r = cg * 9 + w;
    const char* vo = a.ws + OFF_VOX + (size_t)(s * NR + r) * NH * 1024 + lane * 16;
    const char* zz = a.ws + OFF_Z + (size_t)(s * NR + r) * NH * 256 + lane * 16;
    f32x4 d0 = {}, d1 = {};
#pragma unroll 8
    for (int k = 0; k < 32; ++k) {
        const bf16x8 v = *(const bf16x8*)(vo + k * 1024);
        d0 = MFMA16(v, lds8(lds + k * 1024 + lane * 16), d0);
        d1 = MFMA16(v, lds8(lds + 40960 + k * 1024 + lane * 16), d1);
    }
#pragma unroll
    for (int k = 0; k < 8; ++k) {
        const bf16x8 v = *(const bf16x8*)(zz + k * 1024);
        d0 = MFMA16(v, lds8(lds + 32768 + k * 1024 + lane * 16), d0);
        d1 = MFMA16(v, lds8(lds + 40960 + 32768 + k * 1024 + lane * 16), d1);
    }
    const int i16 = lane & 15, g = lane >> 4;
#pragma unroll
    for (int qt = 0; qt < 2; ++qt) {
        const f32x4 d = qt ? d1 : d0;
        const int t = 16 * qt + i16;
        const size_t o = (size_t)(32 * s + t) * DM + 16 * r + 4 * g;
        const v2u av = *(const v2u*)((const u16*)(a.ws + OFF_A) + o);
        const float gt = G[t], og = rbf(1.0f - gt);
        const float r0 = rbf(gt * bflo(av.x)) + rbf(og * rbf(d[0])), r1 = rbf(gt * bfhi(av.x)) + rbf(og * rbf(d[1]));
        const float r2 = rbf(gt * bflo(av.y)) + rbf(og * rbf(d[2])), r3 = rbf(gt * bfhi(av.y)) + rbf(og * rbf(d[3]));
        v2u ov = {pack2(r0, r1), pack2(r2, r3)};
        *(v2u*)(a.out + o) = ov;
    }
}

__device__ __forceinline__ void cexp_l(float lr, float li, float l, float& er, float& ei) {
    const float mag = __builtin_amdgcn_exp2f(lr * l);
    const float ph = li * l;
    const float pe = fmaf(li, l, -ph);
    const float fr = (ph - floorf(ph)) + pe;
    er = mag * __builtin_amdgcn_cosf(fr);
    ei = mag * __builtin_amdgcn_sinf(fr);
}
__global__ void __launch_bounds__(256) k_s4gen(Args a) {
    __shared__ float T[656];
    const int d = blockIdx.x, lane = threadIdx.x & 63, w = threadIdx.x >> 6;
    const int x = lane & 15, g = lane >> 4;
    const float dt = rbf(expf(bf2f(a.logdt[d])));
    const int sig = 1 << w, base = w ? (128 << w) : 0;
    const float lu = (float)(base + 16 * sig * x), lv = (float)(sig * x);
    f32x4 acc = {};
    for (int jj = 0; jj < 32; ++jj) {
        const int n = d * 128 + 4 * jj + g;
        const float ar = dt * bf2f(a.Are[n]), ai = dt * bf2f(a.Aim[n]);
        const float br = dt * bf2f(a.Bre[n]), bi = dt * bf2f(a.Bim[n]);
        const float cr = bf2f(a.Cre[n]), ci = bf2f(a.Cim[n]);
        const float cbr = cr * br - ci * bi, cbi = cr * bi + ci * br;
        const float lr = ar * 1.4426950408889634f, li = ai * 0.15915494309189535f;
        float ur, ui, vr, vi;
        cexp_l(lr, li, lu, ur, ui);
        cexp_l(lr, li, lv, vr, vi);
        const float Ur = cbr * ur - cbi * ui, Ui = cbr * ui + cbi * ur;
        acc = __builtin_amdgcn_mfma_f32_16x16x4f32(Ur, vr, acc, 0, 0, 0);
        acc = __builtin_amdgcn_mfma_f32_16x16x4f32(-Ui, vi, acc, 0, 0, 0);
    }
    const int tb = w ? (128 + 128 * w) : 0;
#pragma unroll
    for (int r = 0; r < 4; ++r) {
        const int aa = 4 * g + r;
        if (w == 0 || aa < 8 || (w == 3 && aa == 8)) T[tb + 16 * aa + x] = acc[r];
    }
    __syncthreads();
    u16* KF = (u16*)(a.ws + OFF_KF) + (size_t)d * SEQ;
    for (int l2 = threadIdx.x; l2 < SEQ / 2; l2 += 256) {
        float v[2];
#pragma unroll
        for (int e = 0; e < 2; ++e) {
            const int lb = (int)rbf((float)(2 * l2 + e));
            const int ix = lb < 256 ? lb : lb < 512 ? 256 + ((lb - 256) >> 1) : lb < 1024 ? 384 + ((lb - 512) >> 2) : 512 + ((lb - 1024) >> 3);
            v[e] = T[ix];
        }
        *(u32*)(KF + 2 * l2) = pack2(v[0], v[1]);
    }
}

constexpr int CV_W = 8 * 260 * 16;
constexpr int CV_U = 96 * 80;
constexpr int CV_CH = CV_W + CV_U;
__global__ void __launch_bounds__(256) k_s4conv(Args a) {
    extern __shared__ __attribute__((aligned(16))) char smem[];
    lptr lds = (lptr)smem;
    const int tid = threadIdx.x, lane = tid & 63, w = __builtin_amdgcn_readfirstlane(tid >> 6);
    const int d0 = blockIdx.x * 4;
    for (int l = tid; l < SEQ; l += 256) {
        const v2u v = *(const v2u*)(a.u + (size_t)l * DM + d0);
        const int o = CV_W + (32 + (l >> 5)) * 80 + (l & 31) * 2;
        *(LAS u16*)(lds + 0 * CV_CH + o) = (u16)(v.x & 0xffff); *(LAS u16*)(lds + 1 * CV_CH + o) = (u16)(v.x >> 16);
        *(LAS u16*)(lds + 2 * CV_CH + o) = (u16)(v.y & 0xffff); *(LAS u16*)(lds + 3 * CV_CH + o) = (u16)(v.y >> 16);
    }
    lptr ch = lds + w * CV_CH;
    for (int i = lane; i < 32 * 5; i += 64) *(LAS v4u*)(ch + CV_W + i * 16) = v4u{0, 0, 0, 0};
    const u16* KF = (const u16*)(a.ws + OFF_KF) + (size_t)(d0 + w) * SEQ;
    for (int zz = lane; zz < 259; zz += 64) {
        const int z = zz - 3;
        v4u lo = {0, 0, 0, 0}, hi = {0, 0, 0, 0};
        if (z >= 1) lo = *(const v4u*)(KF + 8 * (z - 1));
        if (z >= 0) hi = *(const v4u*)(KF + 8 * z);
        u16 e[16];
#pragma unroll
        for (int k = 0; k < 4; ++k) { e[2 * k] = (u16)(lo[k] & 0xffff); e[2 * k + 1] = (u16)(lo[k] >> 16); e[8 + 2 * k] = (u16)(hi[k] & 0xffff); e[8 + 2 * k + 1] = (u16)(hi[k] >> 16); }
#pragma unroll
        for (int c = 0; c < 8; ++c) {
            v4u o;
#pragma unroll
            for (int k = 0; k < 4; ++k) o[k] = (u32)e[8 + c - 2 * k] | ((u32)e[8 + c - 2 * k - 1] << 16);
            *(LAS v4u*)(ch + (c * 260 + zz) * 16) = o;
        }
    }
    __syncthreads();
    const int b = lane & 31, hh = lane >> 5;
    lptr wa = ch + ((b & 7) * 260 + (b >> 3) - hh + 3) * 16;
    lptr ub = ch + CV_W + (32 + b) * 80 + hh * 16;
    f32x16 y0 = {}, y1 = {};
    for (int dl = 0; dl < 32; ++dl) {
#pragma unroll
        for (int ks = 0; ks < 2; ++ks) {
            const bf16x8 A = lds8(wa + (4 * dl - 2 * ks) * 16);
            y0 = MFMA32(A, lds8(ub - dl * 80 + ks * 32), y0);
            y1 = MFMA32(A, lds8(ub + (32 - dl) * 80 + ks * 32), y1);
        }
    }
    for (int dl = 32; dl < 64; ++dl) {
#pragma unroll
        for (int ks = 0; ks < 2; ++ks) {
            const bf16x8 A = lds8(wa + (4 * dl - 2 * ks) * 16);
            y1 = MFMA32(A, lds8(ub + (32 - dl) * 80 + ks * 32), y1);
        }
    }
    __syncthreads();
    LAS u16* Y = (LAS u16*)lds;
#pragma unroll
    for (int r = 0; r < 16; ++r) {
        const int row = (r & 3) + 8 * (r >> 2) + 4 * hh;
        Y[(32 * b + row) * 4 + w] = (u16)(pack2(y0[r], y0[r]) & 0xffff);
        Y[(32 * (32 + b) + row) * 4 + w] = (u16)(pack2(y1[r], y1[r]) & 0xffff);
    }
    __syncthreads();
    const v2u dk = *(const v2u*)(a.dskip + d0);
    for (int l = tid; l < SEQ; l += 256) {
        const v2u uv = *(const v2u*)(a.u + (size_t)l * DM + d0);
        const v2u yv = *(LAS v2u*)(Y + l * 4);
        v2u o;
        o.x = pack2(bflo(yv.x) + rbf(bflo(uv.x) * bflo(dk.x)), bfhi(yv.x) + rbf(bfhi(uv.x) * bfhi(dk.x)));
        o.y = pack2(bflo(yv.y) + rbf(bflo(uv.y) * bflo(dk.y)), bfhi(yv.y) + rbf(bfhi(uv.y) * bfhi(dk.y)));
        *(v2u*)((u16*)(a.ws + OFF_A) + (size_t)l * DM + d0) = o;
    }
}

extern "C" void kernel_launch(void* const* d_in, const int* in_sizes, int n_in, void* d_out, int out_size, void* d_ws, size_t ws_size, hipStream_t stream) {
    Args a;
    a.u = (const u16*)d_in[0]; a.Are = (const u16*)d_in[1]; a.Aim = (const u16*)d_in[2]; a.Bre = (const u16*)d_in[3]; a.Bim = (const u16*)d_in[4];
    a.Cre = (const u16*)d_in[5]; a.Cim = (const u16*)d_in[6]; a.logdt = (const u16*)d_in[7]; a.dskip = (const u16*)d_in[8];
    a.mem0 = (const u16*)d_in[10]; a.Wq = (const u16*)d_in[11]; a.Wk = (const u16*)d_in[12]; a.Wv = (const u16*)d_in[13]; a.Wo = (const u16*)d_in[14];
    a.gatew = (const u16*)d_in[16]; a.out = (u16*)d_out; a.ws = (char*)d_ws;
    const int LDS_PRE = 2 * 64 * XS, LDS_CH = 96 * 1024, LDS_P2 = 81920 + 256, LDS_CV = 4 * CV_CH;
    hipFuncSetAttribute((const void*)k_pre, hipFuncAttributeMaxDynamicSharedMemorySize, LDS_PRE);
    hipFuncSetAttribute((const void*)k_chain, hipFuncAttributeMaxDynamicSharedMemorySize, LDS_CH);
    hipFuncSetAttribute((const void*)k_post2, hipFuncAttributeMaxDynamicSharedMemorySize, LDS_P2);
    hipFuncSetAttribute((const void*)k_s4conv, hipFuncAttributeMaxDynamicSharedMemorySize, LDS_CV);
    hipLaunchKernelGGL(k_s4gen, dim3(DM), dim3(256), 0, stream, a);
    hipLaunchKernelGGL(k_s4conv, dim3(DM / 4), dim3(256), LDS_CV, stream, a);
    hipLaunchKernelGGL(k_pre, dim3(256), dim3(512), LDS_PRE, stream, a);
    hipLaunchKernelGGL(k_chain, dim3(200), dim3(512), LDS_CH, stream, a);
    hipLaunchKernelGGL(k_post1, dim3(512), dim3(256), 0, stream, a);
    hipLaunchKernelGGL(k_post2, dim3(256), dim3(576), LDS_P2, stream, a);
}
```

```cpp
#include <hip/hip_runtime.h>
#include <hip/hip_bf16.h>
#include <stdint.h>
#include <stdio.h>
#include <stdlib.h>

typedef unsigned short u16;
typedef unsigned int u32;
typedef u32 v4u __attribute__((ext_vector_type(4)));
typedef u32 v2u __attribute__((ext_vector_type(2)));
typedef float f32x4 __attribute__((ext_vector_type(4)));
typedef float f32x2 __attribute__((ext_vector_type(2)));
typedef float f32x16 __attribute__((ext_vector_type(16)));
typedef __bf16 bf16x2 __attribute__((ext_vector_type(2)));
typedef __bf16 bf16x8 __attribute__((ext_vector_type(8)));
typedef __bf16 bf16x16 __attribute__((ext_vector_type(16)));
typedef short s16x4 __attribute__((ext_vector_type(4)));
#define LAS __attribute__((address_space(3)))
typedef LAS char* lptr;
typedef __amdgpu_buffer_rsrc_t rsrc_t;

constexpr int SEQ = 2048, DM = 576, NH = 32, DH = 128, NSEG = 64, NR = 36, NREP = 4;
constexpr int MEMB = 9216, PB = 20480, PMM_OFF = 16384, BC_REP = 72 * 128;
constexpr int FL_K = 0, FL_V = 16, FL_M = 80, FL_P = 112, FL_S = 176, FL_Q = 254, FL_ERR = 255, FL_Z = 256, NS4 = 76;
constexpr int OFF_BLK = 40960;
constexpr int B_MEM = 0, B_P = NREP * MEMB, B_Z = B_P + NREP * PB, B_BC = B_Z + NR * NH * 256, BLK = B_BC + NREP * BC_REP;
constexpr int INIT_BYTES = OFF_BLK + 4 * BLK;
constexpr int OFF_ZH = INIT_BYTES;
constexpr int OFF_KX = OFF_ZH + NSEG * NR * NH * 256;
constexpr int OFF_VOX = OFF_KX + NH * SEQ * DH * 2;
constexpr int OFF_KM = OFF_VOX + NSEG * NR * NH * 1024;
constexpr int OFF_PX = OFF_KM + NSEG * NH * 2048;
constexpr int OFF_A4 = OFF_PX + NSEG * 81920;
constexpr int OFF_G = OFF_A4 + SEQ * DM * 2;
constexpr int OFF_ST = OFF_G + SEQ * 4;
constexpr int WS_BYTES = OFF_ST + 256 * 520 * 8 + 4096;

#define STAMP(k)
#define STAMP_FLUSH
#define STAMP_INIT
#define STAMP_FIN
#define WSTAMP(k)

struct Args {
    const u16* u; const u16* Are; const u16* Aim; const u16* Bre; const u16* Bim; const u16* Cre; const u16* Cim;
    const u16* logdt; const u16* dskip; const u16* mem0; const u16* Wq; const u16* Wk; const u16* Wv; const u16* Wo;
    const u16* gatew; u16* out; char* ws;
    int pd, pha, phz, gap, mask, pad0;
};

__device__ __forceinline__ float bf2f(u16 v) { return __uint_as_float((u32)v << 16); }
__device__ __forceinline__ float bflo(u32 v) { return __uint_as_float(v << 16); }
__device__ __forceinline__ float bfhi(u32 v) { return __uint_as_float(v & 0xffff0000u); }
__device__ __forceinline__ u32 pack2(float lo, float hi) { f32x2 f = {lo, hi}; bf16x2 b = __builtin_convertvector(f, bf16x2); return __builtin_bit_cast(u32, b); }
__device__ __forceinline__ float rbf(float x) { return bflo(pack2(x, x)); }
__device__ __forceinline__ u32 nos(u32 v) { return min(v, 0xFFFFFFFEu); }
__device__ __forceinline__ v2u trd(lptr p) { return __builtin_bit_cast(v2u, __builtin_amdgcn_ds_read_tr16_b64_v4i16((LAS s16x4*)p)); }
__device__ __forceinline__ bf16x8 as8(v4u v) { return __builtin_bit_cast(bf16x8, v); }
__device__ __forceinline__ bf16x8 as8(v2u a, v2u b) { v4u v = {a.x, a.y, b.x, b.y}; return __builtin_bit_cast(bf16x8, v); }
__device__ __forceinline__ v4u asu(bf16x8 v) { return __builtin_bit_cast(v4u, v); }
__device__ __forceinline__ bf16x16 cat16(bf16x8 a, bf16x8 b) { return __builtin_shufflevector(a, b, 0, 1, 2, 3, 4, 5, 6, 7, 8, 9, 10, 11, 12, 13, 14, 15); }
__device__ __forceinline__ bf16x8 lds8(lptr p) { return *(LAS bf16x8*)p; }
__device__ __forceinline__ u32 bad4(v4u v) { return max(max(v.x, v.y), max(v.z, v.w)); }
#define SLOT(s) (((s) & 3) * BLK)
#define LBAR() do { asm volatile("s_waitcnt lgkmcnt(0)" ::: "memory"); __builtin_amdgcn_s_barrier(); asm volatile("" ::: "memory"); } while (0)
#define MB() asm volatile("" ::: "memory")
#define DRAIN() asm volatile("s_waitcnt vmcnt(0)" ::: "memory")
#define PIN4(x) asm volatile("" : "+v"(x))
#define MFMA16(a, b, c) __builtin_amdgcn_mfma_f32_16x16x32_bf16((a), (b), (c), 0, 0, 0)
#define MFMA32(a, b, c) __builtin_amdgcn_mfma_f32_32x32x16_bf16((a), (b), (c), 0, 0, 0)
#define SMF(a, b, c, idx) __builtin_amdgcn_smfmac_f32_16x16x64_bf16((a), (b), (c), (idx), 0, 0)
#define LD16(r, vo, so) __builtin_amdgcn_raw_buffer_load_b128((r), (vo), (so), 16)
#define LD8(r, vo, so) __builtin_amdgcn_raw_buffer_load_b64((r), (vo), (so), 16)
#define LD4(r, vo, so) __builtin_amdgcn_raw_buffer_load_b32((r), (vo), (so), 16)
#define ST16(v, r, vo) __builtin_amdgcn_raw_buffer_store_b128((v), (r), (vo), 0, 16)
#define ST8(v, r, vo) __builtin_amdgcn_raw_buffer_store_b64((v), (r), (vo), 0, 16)
#define ST4(v, r, vo) __builtin_amdgcn_raw_buffer_store_b32((v), (r), (vo), 0, 16)

__device__ __forceinline__ float swap32_sum(float a, float b) {
    auto r = __builtin_amdgcn_permlane32_swap(__float_as_uint(a), __float_as_uint(b), false, false);
    return __uint_as_float(r[0]) + __uint_as_float(r[1]); }
__device__ __forceinline__ float xmax(float m) {
    auto r = __builtin_amdgcn_permlane16_swap(__float_as_uint(m), __float_as_uint(m), false, false);
    m = fmaxf(__uint_as_float(r[0]), __uint_as_float(r[1]));
    auto q = __builtin_amdgcn_permlane32_swap(__float_as_uint(m), __float_as_uint(m), false, false);
    return fmaxf(__uint_as_float(q[0]), __uint_as_float(q[1])); }
__device__ __forceinline__ float xsum(float m) {
    auto r = __builtin_amdgcn_permlane16_swap(__float_as_uint(m), __float_as_uint(m), false, false);
    m = __uint_as_float(r[0]) + __uint_as_float(r[1]);
    auto q = __builtin_amdgcn_permlane32_swap(__float_as_uint(m), __float_as_uint(m), false, false);
    return __uint_as_float(q[0]) + __uint_as_float(q[1]); }
__device__ __forceinline__ float vmax(float a, float b) { float r; asm("v_max_f32 %0, %1, %2" : "=v"(r) : "v"(a), "v"(b)); return r; }
__device__ __forceinline__ float vmax3(float a, float b, float c) { float r; asm("v_max3_f32 %0, %1, %2, %3" : "=v"(r) : "v"(a), "v"(b), "v"(c)); return r; }
__device__ __forceinline__ u32 ror8(u32 v) { return (u32)__builtin_amdgcn_update_dpp(0, (int)v, 0x128, 0xf, 0xf, true); }
__device__ __forceinline__ float ror8f(float v) { return __uint_as_float(ror8(__float_as_uint(v))); }
__device__ __forceinline__ int wave_max(int d) {
    d = max(d, __builtin_amdgcn_update_dpp(d, d, 0x128, 0xf, 0xf, false));
    d = max(d, __builtin_amdgcn_update_dpp(d, d, 0x124, 0xf, 0xf, false));
    d = max(d, __builtin_amdgcn_update_dpp(d, d, 0x122, 0xf, 0xf, false));
    d = max(d, __builtin_amdgcn_update_dpp(d, d, 0x121, 0xf, 0xf, false));
    return max(max(__builtin_amdgcn_readlane(d, 0), __builtin_amdgcn_readlane(d, 16)), max(__builtin_amdgcn_readlane(d, 32), __builtin_amdgcn_readlane(d, 48)));
}

constexpr int SPIN_MAX = 2000000;
__device__ __forceinline__ int wait_flags(rsrc_t rw, int line0, int stride, int n, int need, int lane, int& budget) {
    const int vo = (line0 + (lane < n ? lane : 0) * stride) * 128;
    int mn;
    for (;;) {
        const int v = (int)LD4(rw, vo, 0);
        MB();
        mn = -wave_max(-v);
        if (mn >= need) break;
        if (--budget < 0) break;
        __builtin_amdgcn_s_sleep(8);
    }
    return mn;
}

template <int NRW, int CW>
__device__ __forceinline__ void stage_rows(const u16* g, int gs, lptr lds, int ls, int tid) {
    constexpr int N = NRW * CW / 512;
    static_assert(NRW * CW % 512 == 0, "");
    v4u v[N];
#pragma unroll
    for (int j = 0; j < N; ++j) { const int i = tid + 512 * j, row = i / CW, c = i % CW; v[j] = *(const v4u*)(g + (size_t)row * gs + c * 8); }
#pragma unroll
    for (int j = 0; j < N; ++j) { const int i = tid + 512 * j, row = i / CW, c = i % CW; *(LAS v4u*)(lds + row * ls + c * 16) = v[j]; }
}
template <int NRW, int CW>
__device__ __forceinline__ void rows_ld(v4u (&v)[NRW * CW / 512], const u16* g, int gs, int tid) {
#pragma unroll
    for (int j = 0; j < NRW * CW / 512; ++j) { const int i = tid + 512 * j, row = i / CW, c = i % CW; v[j] = *(const v4u*)(g + (size_t)row * gs + c * 8); }
}
template <int NRW, int CW>
__device__ __forceinline__ void rows_st(const v4u (&v)[NRW * CW / 512], lptr lds, int ls, int tid) {
#pragma unroll
    for (int j = 0; j < NRW * CW / 512; ++j) { const int i = tid + 512 * j, row = i / CW, c = i % CW; *(LAS v4u*)(lds + row * ls + c * 16) = v[j]; }
}
__device__ __forceinline__ bf16x8 frag16(lptr base, int ls, int lane) {
    const int i16 = lane & 15, bg = lane >> 4, q = i16 >> 2, p = i16 & 3;
    lptr a = base + (8 * bg + q) * ls + p * 8;
    return as8(trd(a), trd(a + 4 * ls));
}
__device__ __forceinline__ bf16x8 frag32p(lptr base, int ls, int lane) {
    const int i16 = lane & 15, G = lane >> 4, q = i16 >> 2, p = i16 & 3;
    lptr a = base + (8 * (G >> 1) + q) * ls + (16 * (p & 1) + 8 * (G & 1) + 4 * (p >> 1)) * 2;
    return as8(trd(a), trd(a + 4 * ls));
}

constexpr int XS = 1168;
__device__ __forceinline__ void xload(v4u (&st)[9], const u16* x, int tok0, int tid) {
#pragma unroll
    for (int j = 0; j < 9; ++j) st[j] = *(const v4u*)(x + (size_t)tok0 * DM + (tid + 512 * j) * 8);
}
__device__ __forceinline__ void xstore(const v4u (&st)[9], lptr xl, int tid) {
#pragma unroll
    for (int j = 0; j < 9; ++j) { const int i = tid + 512 * j; *(LAS v4u*)(xl + i * 16 + (i / 72) * 16) = st[j]; }
}
__device__ __forceinline__ void load_w2(bf16x8 (&A)[36], const u16* W, int h0, lptr lds, int tid, int lane, int w) {
    v4u v[6][6];
#pragma unroll
    for (int c = 0; c < 6; ++c) rows_ld<96, 32>(v[c], W + (size_t)c * 96 * 4096 + h0 * DH, 4096, tid);
    __builtin_amdgcn_sched_barrier(0);
#pragma unroll
    for (int c = 0; c < 6; ++c) {
        lptr l = lds + (c & 1) * (96 * 544);
        rows_st<96, 32>(v[c], l, 544, tid);
        LBAR();
#pragma unroll
        for (int ks = 0; ks < 6; ++ks) A[c * 6 + ks] = frag32p(l + ks * 16 * 544 + w * 64, 544, lane);
        __builtin_amdgcn_sched_barrier(0);
    }
    LBAR();
}

__device__ __forceinline__ f32x16 proj_tile(const bf16x8 (&A)[36], lptr xb) {
    f32x16 acc = {};
    bf16x8 f[6];
#pragma unroll
    for (int i = 0; i < 6; ++i) f[i] = lds8(xb + i * 32);
    __builtin_amdgcn_sched_barrier(0);
#pragma unroll
    for (int ks = 0; ks < 36; ++ks) {
        acc = MFMA32(A[ks], f[ks % 6], acc);
        if (ks + 6 < 36) f[ks % 6] = lds8(xb + (ks + 6) * 32);
        __builtin_amdgcn_sched_barrier(0);
    }
    return acc;
}
__device__ __forceinline__ void role_k2(const Args& a, lptr lds, rsrc_t rw, int cu) {
    const int tid = threadIdx.x, lane = tid & 63, w = __builtin_amdgcn_readfirstlane(tid >> 6);
    v4u st[9];
    bf16x8 A[36];
    load_w2(A, a.Wk, 2 * cu, lds, tid, lane, w);
    xload(st, a.u, 0, tid);
    xstore(st, lds, tid);
    __syncthreads();
    const int o0 = OFF_KX + (((2 * cu + (w >> 2)) * SEQ + (lane & 31)) * DH + (w & 3) * 32 + (lane >> 5) * 16) * 2;
    for (int blk = 0; blk < 32; ++blk) {
        lptr xl = lds + (blk & 1) * (64 * XS);
        if (blk + 1 < 32) xload(st, a.u, (blk + 1) * 64, tid);
#pragma unroll
        for (int tt = 0; tt < 2; ++tt) {
            const f32x16 acc = proj_tile(A, xl + (32 * tt + (lane & 31)) * XS + (lane >> 5) * 16);
            v4u c0 = {pack2(acc[0], acc[1]), pack2(acc[2], acc[3]), pack2(acc[4], acc[5]), pack2(acc[6], acc[7])};
            v4u c1 = {pack2(acc[8], acc[9]), pack2(acc[10], acc[11]), pack2(acc[12], acc[13]), pack2(acc[14], acc[15])};
            const int o = o0 + (blk * 64 + 32 * tt) * DH * 2;
            ST16(c0, rw, o); ST16(c1, rw, o + 16);
        }
        if (blk + 1 < 32) xstore(st, lds + ((blk + 1) & 1) * (64 * XS), tid);
        const bool full = blk < 2 || blk == 31;
        if (full) DRAIN(); else asm volatile("s_waitcnt vmcnt(4)" ::: "memory");
        LBAR();
        if (tid == 0) ST4((u32)(full ? blk : blk - 1), rw, (FL_K + cu) * 128);
    }
}

constexpr int VS = 272;
__device__ __forceinline__ void role_q2p(const Args& a, lptr lds, rsrc_t rw, int cu, int par) {
    const int tid = threadIdx.x, lane = tid & 63, w = __builtin_amdgcn_readfirstlane(tid >> 6);
    v4u st[9];
    bf16x8 A[36];
    load_w2(A, a.Wq, 2 * cu, lds, tid, lane, w);
    lptr xl = lds, ql = lds + 64 * XS;
    LAS int* cnt = (LAS int*)(lds + 64 * XS + 4 * 64 * VS);
    if (tid < 2) cnt[tid] = 0;
    xload(st, a.u, par * 64, tid);
    xstore(st, xl, tid);
    __syncthreads();
    int budget = SPIN_MAX, kdone = -1;
    const int i16 = lane & 15, g = lane >> 4;
    const int tau = 16 * ((i16 >> 2) & 1) + 4 * (i16 >> 3) + (i16 & 3);
    const int sg = w >> 2, hh = (w >> 1) & 1, qt = w & 1, h = 2 * cu + hh;
    const float c2 = 0.08838834764831845f * 1.4426950408889634f;
    for (int it = -1; it < 16; ++it) {
        const int blk = 2 * it + par;
        if (it + 1 < 16) {
            if (it + 2 < 16) xload(st, a.u, (blk + 4) * 64, tid);
            lptr qb = ql + (((it + 1) & 1) * 2 + (w >> 2)) * (64 * VS) + ((w & 3) * 32 + (lane >> 5) * 16) * 2;
#pragma unroll
            for (int tt = 0; tt < 2; ++tt) {
                const f32x16 acc = proj_tile(A, xl + (32 * tt + (lane & 31)) * XS + (lane >> 5) * 16);
                v4u c0 = {pack2(acc[0], acc[1]), pack2(acc[2], acc[3]), pack2(acc[4], acc[5]), pack2(acc[6], acc[7])};
                v4u c1 = {pack2(acc[8], acc[9]), pack2(acc[10], acc[11]), pack2(acc[12], acc[13]), pack2(acc[14], acc[15])};
                lptr qp = qb + (32 * tt + (lane & 31)) * VS;
                *(LAS v4u*)qp = c0; *(LAS v4u*)(qp + 16) = c1;
            }
            __syncthreads();
            if (it + 2 < 16) xstore(st, xl, tid);
        }
        if (it >= 0) {
            const int s = 2 * blk + sg;
            if (kdone < blk) kdone = wait_flags(rw, FL_K + cu, 1, 1, blk, lane, budget);
            bf16x8 k0[4], k1[4], km[4], qf[4];
            const int ko = OFF_KX + ((h * SEQ + 32 * s + tau) * DH + g * 8) * 2;
#pragma unroll
            for (int ks = 0; ks < 4; ++ks) { k0[ks] = as8(LD16(rw, ko + ks * 64, 0)); k1[ks] = as8(LD16(rw, ko + 8 * DH * 2 + ks * 64, 0)); }
            lptr qp = ql + ((it & 1) * 2 + hh) * (64 * VS) + (32 * sg + 16 * qt + i16) * VS + g * 16;
#pragma unroll
            for (int ks = 0; ks < 4; ++ks) qf[ks] = lds8(qp + ks * 64);
            f32x4 S0 = {}, S1 = {}, Sm = {};
#pragma unroll
            for (int ks = 0; ks < 4; ++ks) { S0 = MFMA16(k0[ks], qf[ks], S0); S1 = MFMA16(k1[ks], qf[ks], S1); }
            wait_flags(rw, FL_M + h, 1, 1, s, lane, budget);
            const int mo = OFF_KM + (((s * NH + h) * 8 + (i16 & 7)) * DH + g * 8) * 2;
#pragma unroll
            for (int ks = 0; ks < 4; ++ks) km[ks] = as8(LD16(rw, mo + ks * 64, 0));
#pragma unroll
            for (int ks = 0; ks < 4; ++ks) Sm = MFMA16(km[ks], qf[ks], Sm);
            const int qi = 16 * qt + i16, t0 = 16 * (g & 1) + 4 * (g >> 1);
#pragma unroll
            for (int r = 0; r < 4; ++r) {
                if (t0 + r > qi) S0[r] = -INFINITY;
                if (t0 + r + 8 > qi) S1[r] = -INFINITY;
                if (g >= 2) Sm[r] = -INFINITY;
            }
            float m = fmaxf(fmaxf(fmaxf(S0[0], S0[1]), fmaxf(S0[2], S0[3])), fmaxf(fmaxf(S1[0], S1[1]), fmaxf(S1[2], S1[3])));
            m = fmaxf(m, fmaxf(fmaxf(Sm[0], Sm[1]), fmaxf(Sm[2], Sm[3])));
            m = xmax(m);
            const float nm = -m * c2;
            float sum = 0.f;
#pragma unroll
            for (int r = 0; r < 4; ++r) {
                S0[r] = __builtin_amdgcn_exp2f(fmaf(S0[r], c2, nm)); S1[r] = __builtin_amdgcn_exp2f(fmaf(S1[r], c2, nm)); Sm[r] = __builtin_amdgcn_exp2f(fmaf(Sm[r], c2, nm));
                sum += S0[r] + S1[r] + Sm[r];
            }
            sum = xsum(sum);
            const float inv = __builtin_amdgcn_rcpf(sum);
            v4u px = {pack2(S0[0] * inv, S0[1] * inv), pack2(S1[0] * inv, S1[1] * inv), pack2(S0[2] * inv, S0[3] * inv), pack2(S1[2] * inv, S1[3] * inv)};
            const int po = OFF_PX + (s * 2 + qt) * 40960;
            ST16(px, rw, po + h * 1024 + lane * 16);
            const u32 ma = pack2(Sm[0] * inv, Sm[1] * inv), mb = pack2(Sm[2] * inv, Sm[3] * inv);
            auto ra = __builtin_amdgcn_permlane16_swap(ma, ma, false, false);
            auto rb = __builtin_amdgcn_permlane16_swap(mb, mb, false, false);
            v4u pm = {ra[0], rb[0], ra[1], rb[1]};
            if (lane < 16) ST16(pm, rw, po + 32768 + h * 256 + lane * 16);
            DRAIN();
            if (lane == 0) {
                const int old = __hip_atomic_fetch_add(cnt + sg, 1, __ATOMIC_RELAXED, __HIP_MEMORY_SCOPE_WORKGROUP);
                if ((old & 3) == 3) ST4((u32)s, rw, (FL_P + 4 * cu + 2 * par + sg) * 128);
            }
        }
        __syncthreads();
    }
    if (budget < 0 && lane == 0) ST4(5u, rw, FL_ERR * 128);
}

__device__ __forceinline__ void role_vvo(const Args& a, lptr lds, rsrc_t rw, int h, int par) {
    const int tid = threadIdx.x, lane = tid & 63, w = __builtin_amdgcn_readfirstlane(tid >> 6);
    bf16x8 Av[18], Bo[5][4];
    {
        v4u v[3][6], o[2][9];
#pragma unroll
        for (int c = 0; c < 3; ++c) rows_ld<192, 16>(v[c], a.Wv + (size_t)c * 192 * 4096 + h * DH, 4096, tid);
#pragma unroll
        for (int c = 0; c < 2; ++c) rows_ld<64, 72>(o[c], a.Wo + ((size_t)h * DH + c * 64) * DM, DM, tid);
        __builtin_amdgcn_sched_barrier(0);
#pragma unroll
        for (int c = 0; c < 3; ++c) {
            lptr l = lds + (c & 1) * 77824;
            rows_st<192, 16>(v[c], l, 288, tid);
            LBAR();
#pragma unroll
            for (int ks = 0; ks < 6; ++ks) Av[c * 6 + ks] = frag16(l + ks * 32 * 288 + w * 32, 288, lane);
            __builtin_amdgcn_sched_barrier(0);
        }
#pragma unroll
        for (int c = 0; c < 2; ++c) {
            lptr l = lds + ((c + 1) & 1) * 77824;
            rows_st<64, 72>(o[c], l, 1184, tid);
            LBAR();
#pragma unroll
            for (int j = 0; j < 5; ++j) {
                const int r = (w + 8 * j < NR) ? w + 8 * j : w;
#pragma unroll
                for (int ks = 0; ks < 2; ++ks) Bo[j][c * 2 + ks] = frag16(l + ks * 32 * 1184 + r * 32, 1184, lane);
            }
            __builtin_amdgcn_sched_barrier(0);
        }
        LBAR();
    }
    lptr xl = lds, vl = lds + 64 * XS;
    v4u st[9];
    xload(st, a.u, par * 64, tid);
    xstore(st, xl, tid);
    __syncthreads();
    const int rho = lane & 15, bg = lane >> 4;
    const int tokA = 16 * ((rho >> 2) & 1) + 4 * (rho >> 3) + (rho & 3);
    const int vo0 = OFF_VOX + (w * NH + h) * 1024 + lane * 16;
    for (int blk = par; blk < 32; blk += 2) {
        if (blk + 2 < 32) xload(st, a.u, (blk + 2) * 64, tid);
#pragma unroll
        for (int tt = 0; tt < 4; ++tt) {
            f32x4 acc = {};
            lptr xb = xl + (16 * tt + rho) * XS + bg * 16;
            bf16x8 f[6];
#pragma unroll
            for (int i = 0; i < 6; ++i) f[i] = lds8(xb + i * 64);
            __builtin_amdgcn_sched_barrier(0);
#pragma unroll
            for (int ks = 0; ks < 18; ++ks) {
                acc = MFMA16(Av[ks], f[ks % 6], acc);
                if (ks + 6 < 18) f[ks % 6] = lds8(xb + (ks + 6) * 64);
                __builtin_amdgcn_sched_barrier(0);
            }
            v2u o = {pack2(acc[0], acc[1]), pack2(acc[2], acc[3])};
            *(LAS v2u*)(vl + (16 * tt + rho) * VS + (16 * w + 4 * bg) * 2) = o;
        }
        DRAIN();
        LBAR();
        if (tid == 0 && blk >= 4) ST4((u32)(blk - 2), rw, (FL_V + 2 * h + par) * 128);
        if (blk + 2 < 32) xstore(st, xl, tid);
#pragma unroll
        for (int sg = 0; sg < 2; ++sg) {
            bf16x8 Af[2][4];
#pragma unroll
            for (int half = 0; half < 2; ++half)
#pragma unroll
                for (int ks = 0; ks < 4; ++ks) Af[half][ks] = lds8(vl + (32 * sg + tokA + 8 * half) * VS + ks * 64 + bg * 16);
            const int s = blk * 2 + sg;
#pragma unroll
            for (int j = 0; j < 5; ++j) {
                if (j < 4 || w < 4) {
                    f32x4 d0 = {}, d1 = {};
#pragma unroll
                    for (int ks = 0; ks < 4; ++ks) { d0 = MFMA16(Af[0][ks], Bo[j][ks], d0); d1 = MFMA16(Af[1][ks], Bo[j][ks], d1); }
                    v4u o = {pack2(d0[0], d0[1]), pack2(d1[0], d1[1]), pack2(d0[2], d0[3]), pack2(d1[2], d1[3])};
                    if (!(a.mask & 1)) ST16(o, rw, vo0 + (s * NR + 8 * j) * (NH * 1024)); else asm volatile("" :: "v"(o));
                }
            }
        }
        const bool full = blk < 2 || blk >= 30;
        if (full) DRAIN();
        LBAR();
        if (tid == 0 && full) ST4((u32)blk, rw, (FL_V + 2 * h + par) * 128);
    }
}

__device__ __forceinline__ u32 rt32() { return (u32)__builtin_amdgcn_s_memrealtime(); }
__device__ __forceinline__ void wait_until(u32 t) {
    if ((int)(t - rt32()) > 1000) return;
    while ((int)(rt32() - t) < -24) __builtin_amdgcn_s_sleep(4);
    while ((int)(rt32() - t) < 0) {}
}
struct TK { u32 XA, XR, EA, ER; int dA0, dA1, dR0, dR1; int okA, okR; };
__device__ __forceinline__ u32 beacon_max(rsrc_t rw, int s, int rep, int slot0, int n, int lane, int& budget) {
    const int vo = OFF_BLK + B_BC + rep * BC_REP + (slot0 + (lane < n ? lane : 0)) * 128;
    u32 v;
    for (;;) {
        v = LD4(rw, vo, SLOT(s));
        MB();
        if (__all(v != 0xFFFFFFFFu)) break;
        if (--budget < 0) break;
        __builtin_amdgcn_s_sleep(1);
    }
    const u32 base = __builtin_amdgcn_readfirstlane(v);
    return base + (u32)wave_max((int)(v - base));
}
__device__ __forceinline__ void tk_T0(TK& k, u32 T0, const Args& a) {
    if (k.okR) { k.dR1 = k.dR0; k.dR0 = (int)(T0 - k.ER); }
    k.okA = (int)(k.XA - T0) >= a.pha;
    k.EA = k.okA ? k.XA : T0 + a.gap;
    k.XR = k.EA + max(k.dA0, k.dA1) + a.pd;
}
__device__ __forceinline__ void tk_Tp(TK& k, u32 Tp, const Args& a) {
    if (k.okA) { k.dA1 = k.dA0; k.dA0 = (int)(Tp - k.EA); }
    k.okR = (int)(k.XR - Tp) >= a.pha;
    k.ER = k.okR ? k.XR : Tp + a.gap;
    k.XA = k.ER + max(k.dR0, k.dR1) + a.pd;
}
__device__ __forceinline__ void tk_init(TK& k) { const u32 n = rt32(); k.XA = k.XR = k.EA = k.ER = n - 100000u; k.dA0 = k.dA1 = 120; k.dR0 = k.dR1 = 80; k.okA = k.okR = 0; }

__device__ __forceinline__ void tk_post(lptr p, int tag, u32 t, int lane) { if (lane == 0) { v2u v = {(u32)tag, t}; *(volatile LAS v2u*)p = v; } }
__device__ __forceinline__ u32 tk_get(lptr p, int tag, int& budget) {
    for (;;) {
        const v2u v = *(volatile LAS v2u*)p;
        if ((int)__builtin_amdgcn_readfirstlane(v.x) >= tag || --budget < 0) return __builtin_amdgcn_readfirstlane(v.y);
        __builtin_amdgcn_s_sleep(2);
    }
}
__device__ __forceinline__ void gather_mem(const Args& a, rsrc_t rw, int s, int rep, lptr ml, int tid, int lane, int w, int& budget) {
    const int o0 = (w * 64 + lane) * 16;
    if (s == 0) {
        for (int c = tid; c < 576; c += 512) {
            const u32* p = (const u32*)(a.mem0 + (c & 7) * DM + 16 * (c >> 4) + 2 * ((c >> 3) & 1));
            v4u v = {p[0], p[2], p[4], p[6]};
            *(LAS v4u*)(ml + c * 16) = v;
        }
        return;
    }
    const int so = OFF_BLK + SLOT(s) + B_MEM + rep * MEMB;
    if (w >= 6) {
        const int o1 = ((2 * w - 7) * 64 + lane) * 16;
        v4u v0, v1;
        for (;;) {
            v0 = LD16(rw, o1, so); v1 = LD16(rw, o1 + 1024, so);
            MB();
            if (__all(max(bad4(v0), bad4(v1)) != 0xFFFFFFFFu)) break;
            if (--budget < 0) break;
        }
        *(LAS v4u*)(ml + o1) = v0; *(LAS v4u*)(ml + o1 + 1024) = v1;
    } else if (w < 5) {
        v4u v0;
        for (;;) {
            v0 = LD16(rw, o0, so);
            MB();
            if (__all(bad4(v0) != 0xFFFFFFFFu)) break;
            if (--budget < 0) break;
        }
        *(LAS v4u*)(ml + o0) = v0;
    }
}

__device__ __forceinline__ void role_qk(const Args& a, lptr lds, rsrc_t rw, int h, int rep) {
    const int tid = threadIdx.x, lane = tid & 63, w = __builtin_amdgcn_readfirstlane(tid >> 6);
    bf16x16 Bq[9], Bk[9];
    {
        v4u v[6][6];
#pragma unroll
        for (int c = 0; c < 6; ++c) rows_ld<192, 16>(v[c], (c >= 3 ? a.Wk : a.Wq) + (size_t)(c % 3) * 192 * 4096 + h * DH, 4096, tid);
        __builtin_amdgcn_sched_barrier(0);
#pragma unroll
        for (int c = 0; c < 6; ++c) {
            lptr l = lds + (c & 1) * 77824;
            rows_st<192, 16>(v[c], l, 288, tid);
            LBAR();
#pragma unroll
            for (int t = 0; t < 3; ++t) {
                bf16x16 f = cat16(frag16(l + (64 * t) * 288 + w * 32, 288, lane), frag16(l + (64 * t + 32) * 288 + w * 32, 288, lane));
                if (c >= 3) Bk[(c % 3) * 3 + t] = f; else Bq[c * 3 + t] = f;
            }
            __builtin_amdgcn_sched_barrier(0);
        }
        LBAR();
    }
    lptr ml = lds, xl = lds + MEMB;
    const int idx = (lane & 8) ? 0xEEEE : 0x4444;
    const int i16 = lane & 15, g = lane >> 4;
    const bool hi8 = (lane & 8) != 0;
    const int kxo = OFF_KX + ((h * SEQ + i16) * DH + g * 8) * 2;
    int budget = SPIN_MAX, kdone = -1;
    bf16x8 kx[2][4];
    const float c2 = 0.08838834764831845f * 1.4426950408889634f;
    lptr pq = xl + (8 * g + (i16 >> 2)) * 32 + (i16 & 1) * 8;
    lptr kl = lds + 16384 + lane * 16;
    const int ox = OFF_BLK + B_P + h * 512 + ((g & 1) * 16 + (g >> 1) * 8 + (i16 & 7)) * 16 + (i16 >> 3) * PB;
    const int om = OFF_BLK + B_P + PMM_OFF + h * 128 + (i16 & 7) * 16 + g * PB;
    const int ob = OFF_BLK + B_BC + (lane & 3) * BC_REP + (40 + h) * 128;
    TK tk; tk_init(tk);
    lptr tkp = lds + MEMB + 4096;
    if (tid == 0) tk_post(tkp, 0, tk.XA, 0);
    __syncthreads();
    STAMP_INIT
    for (int s = 0; s < NSEG; ++s) {
        if (w == 5 && s < NSEG - 1) {
            if (kdone < (s >> 1)) kdone = wait_flags(rw, FL_K + (h >> 1), 1, 1, s >> 1, lane, budget);
#pragma unroll
            for (int tl = 0; tl < 2; ++tl)
#pragma unroll
                for (int ks = 0; ks < 4; ++ks) kx[tl][ks] = as8(LD16(rw, kxo + tl * 16 * DH * 2 + ks * 64, s * 32 * DH * 2));
#pragma unroll
            for (int tl = 0; tl < 2; ++tl)
#pragma unroll
                for (int ks = 0; ks < 4; ++ks) *(LAS bf16x8*)(kl + (tl * 4 + ks) * 1024) = kx[tl][ks];
        }
        const int oxs = ox + SLOT(s), oms = om + SLOT(s);
        wait_until(tk_get(tkp, s, budget));
        STAMP(0);
        gather_mem(a, rw, s, rep, ml, tid, lane, w, budget);
        STAMP(1);
        LBAR();
        STAMP(2);
        f32x4 aq = {}, ak = {};
        bf16x8 A[9];
#pragma unroll
        for (int t = 0; t < 9; ++t) A[t] = lds8(ml + t * 1024 + lane * 16);
        if (w == 0) {
#pragma unroll
            for (int tl = 0; tl < 2; ++tl)
#pragma unroll
                for (int ks = 0; ks < 4; ++ks) kx[tl][ks] = lds8(kl + (tl * 4 + ks) * 1024);
        }
        if (w == 2) {
            const v4u ff = {0xFFFFFFFFu, 0xFFFFFFFFu, 0xFFFFFFFFu, 0xFFFFFFFFu};
            ST16(ff, rw, ox + SLOT(s + 2)); ST16(ff, rw, ox + SLOT(s + 2) + 2 * PB);
            if (!hi8) ST16(ff, rw, om + SLOT(s + 2));
            if (lane < NREP) ST4(0xFFFFFFFFu, rw, ob + SLOT(s + 2));
        }
        __builtin_amdgcn_sched_barrier(0);
#pragma unroll
        for (int t = 0; t < 9; ++t) { aq = SMF(A[t], Bq[t], aq, idx); ak = SMF(A[t], Bk[t], ak, idx); }
        {
            const float s0 = swap32_sum(aq[0], ak[0]), s1 = swap32_sum(aq[1], ak[1]), s2 = swap32_sum(aq[2], ak[2]), s3 = swap32_sum(aq[3], ak[3]);
            v2u o = {pack2(s0, s1), pack2(s2, s3)};
            *(LAS v2u*)(xl + (16 * w + i16) * 32 + g * 8) = o;
        }
        STAMP(3);
        LBAR();
        STAMP(4);
        if (w == 0) {
            if (s < NSEG - 1) {
            __builtin_amdgcn_s_setprio(3);
            bf16x8 Fq[4], Fk[4];
#pragma unroll
            for (int ks = 0; ks < 4; ++ks) Fq[ks] = as8(trd(pq + ks * 1024), trd(pq + ks * 1024 + 128));
#pragma unroll
            for (int ks = 0; ks < 4; ++ks) Fk[ks] = as8(trd(pq + ks * 1024 + 16), trd(pq + ks * 1024 + 16 + 128));
            __builtin_amdgcn_sched_barrier(0);
            f32x4 S0 = {}, S1 = {}, Sm = {};
#pragma unroll
            for (int ks = 0; ks < 4; ++ks) { S0 = MFMA16(kx[0][ks], Fq[ks], S0); S1 = MFMA16(kx[1][ks], Fq[ks], S1); }
#pragma unroll
            for (int ks = 0; ks < 4; ++ks) Sm = MFMA16(Fk[ks], Fq[ks], Sm);
            float x0 = hi8 ? S1[0] : S0[0], x1 = hi8 ? S1[1] : S0[1], x2 = hi8 ? S1[2] : S0[2], x3 = hi8 ? S1[3] : S0[3];
            float y0 = hi8 ? Sm[2] : Sm[0], y1 = hi8 ? Sm[3] : Sm[1];
            float m = vmax3(vmax3(x0, x1, x2), x3, vmax(y0, y1));
            m = vmax(m, ror8f(m));
            { auto r = __builtin_amdgcn_permlane16_swap(__float_as_uint(m), __float_as_uint(m), false, false); m = vmax(__uint_as_float(r[0]), __uint_as_float(r[1])); }
            { auto r = __builtin_amdgcn_permlane32_swap(__float_as_uint(m), __float_as_uint(m), false, false); m = vmax(__uint_as_float(r[0]), __uint_as_float(r[1])); }
            STAMP(5);
            const float nm = -m * c2;
            x0 = __builtin_amdgcn_exp2f(fmaf(x0, c2, nm)); x1 = __builtin_amdgcn_exp2f(fmaf(x1, c2, nm)); x2 = __builtin_amdgcn_exp2f(fmaf(x2, c2, nm)); x3 = __builtin_amdgcn_exp2f(fmaf(x3, c2, nm));
            y0 = __builtin_amdgcn_exp2f(fmaf(y0, c2, nm)); y1 = __builtin_amdgcn_exp2f(fmaf(y1, c2, nm));
            float sum = fmaf(0.5f, y0 + y1, (x0 + x1) + (x2 + x3));
            sum += ror8f(sum);
            sum = xsum(sum);
            const float inv = __builtin_amdgcn_rcpf(sum);
            const u32 pa = pack2(x0 * inv, x1 * inv) & 0x7fff7fffu, pb = pack2(x2 * inv, x3 * inv) & 0x7fff7fffu, pc = pack2(y0 * inv, y1 * inv) & 0x7fff7fffu;
            const u32 qa = ror8(pa), qb = ror8(pb), qc = ror8(pc);
            v4u px = {hi8 ? qa : pa, hi8 ? qb : pb, hi8 ? pa : qa, hi8 ? pb : qb};
            ST16(px, rw, oxs);
            ST16(px, rw, oxs + 2 * PB);
            auto ra = __builtin_amdgcn_permlane16_swap(pc, pc, false, false);
            auto rc = __builtin_amdgcn_permlane16_swap(qc, qc, false, false);
            v4u pm = {ra[0], rc[0], ra[1], rc[1]};
            if (!hi8) ST16(pm, rw, oms);
            STAMP(6);
            __builtin_amdgcn_sched_barrier(0);
            const u32 tnow = rt32();
            if (lane < NREP) ST4(nos(tnow), rw, ob + SLOT(s));
            __builtin_amdgcn_s_setprio(0);
            }
        } else if (w == 1) {
            bf16x8 F[4];
#pragma unroll
            for (int ks = 0; ks < 4; ++ks) F[ks] = as8(trd(pq + ks * 1024 + 16), trd(pq + ks * 1024 + 16 + 128));
            if (i16 < 8) {
                const int ko = OFF_KM + (((s * NH + h) * 8 + i16) * DH + g * 8) * 2;
#pragma unroll
                for (int ks = 0; ks < 4; ++ks) ST16(asu(F[ks]), rw, ko + ks * 64);
            }
            DRAIN();
            if (lane == 0) ST4((u32)s, rw, (FL_M + h) * 128);
        } else {
            if (w != 3 && w != 5) __builtin_amdgcn_s_sleep(14);
            if (w == 2) DRAIN();
            if (w == 3 && s < NSEG - 1) {
                if (s > 0) tk_T0(tk, beacon_max(rw, s, rep, 0, NR, lane, budget), a);
                wait_until(tk.XR);
                tk_Tp(tk, beacon_max(rw, s, rep, 40, NH, lane, budget), a);
                tk_post(tkp, s + 1, tk.XA, lane);
            }
        }
        STAMP_FLUSH;
    }
    STAMP_FIN;
    if (budget < 0 && lane == 0) ST4(1u, rw, FL_ERR * 128);
}

__device__ __forceinline__ void role_vo(const Args& a, lptr lds, rsrc_t rw, int h, int rep) {
    const int tid = threadIdx.x, lane = tid & 63, w = __builtin_amdgcn_readfirstlane(tid >> 6);
    bf16x16 Bv[9], Bo[5][2];
    {
        v4u v[3][6], o[2][9];
#pragma unroll
        for (int c = 0; c < 3; ++c) rows_ld<192, 16>(v[c], a.Wv + (size_t)c * 192 * 4096 + h * DH, 4096, tid);
#pragma unroll
        for (int c = 0; c < 2; ++c) rows_ld<64, 72>(o[c], a.Wo + ((size_t)h * DH + c * 64) * DM, DM, tid);
        __builtin_amdgcn_sched_barrier(0);
#pragma unroll
        for (int c = 0; c < 3; ++c) {
            lptr l = lds + (c & 1) * 77824;
            rows_st<192, 16>(v[c], l, 288, tid);
            LBAR();
#pragma unroll
            for (int t = 0; t < 3; ++t) Bv[c * 3 + t] = cat16(frag16(l + (64 * t) * 288 + w * 32, 288, lane), frag16(l + (64 * t + 32) * 288 + w * 32, 288, lane));
            __builtin_amdgcn_sched_barrier(0);
        }
#pragma unroll
        for (int c = 0; c < 2; ++c) {
            lptr l = lds + ((c + 1) & 1) * 77824;
            rows_st<64, 72>(o[c], l, 1184, tid);
            LBAR();
#pragma unroll
            for (int j = 0; j < 5; ++j) {
                const int r = (w + 8 * j < NR) ? w + 8 * j : w;
                Bo[j][c] = cat16(frag16(l + r * 32, 1184, lane), frag16(l + 32 * 1184 + r * 32, 1184, lane));
            }
            __builtin_amdgcn_sched_barrier(0);
        }
        LBAR();
    }
    lptr ml = lds, xl = lds + MEMB;
    const int idx = (lane & 8) ? 0xEEEE : 0x4444;
    const int i16 = lane & 15, g = lane >> 4;
    int budget = SPIN_MAX;
    const int q = i16 >> 2, p = i16 & 3;
    const int aoff = (16 * g + 4 * (q >> 1) + 2 * (p >> 1) + (q & 1)) * 16 + (p & 1) * 8;
    const int jt = ((g & 1) << 1) | (g >> 1);
    const int zo4 = (w + 8 * jt) * (NH * 256) + h * 256 + i16 * 16;
    const int zo5 = (w + 32) * (NH * 256) + h * 256 + i16 * 16;
    v4u zp4 = {}, zp5 = {};
    TK tk; tk_init(tk);
    lptr tkp = lds + MEMB + 4096;
    if (tid == 0) tk_post(tkp, 0, tk.XA, 0);
    __syncthreads();
    STAMP_INIT
    for (int s = 0; s < NSEG; ++s) {
        const int zb = s < NSEG - 1 ? OFF_BLK + B_Z + SLOT(s) : OFF_ZH + s * (NR * NH * 256);
        const int zs4 = zo4 + zb, zs5 = zo5 + zb;
        wait_until(tk_get(tkp, s, budget));
        STAMP(0);
        gather_mem(a, rw, s, rep, ml, tid, lane, w, budget);
        STAMP(1);
        LBAR();
        STAMP(2);
        f32x4 av = {}, av2 = {};
        bf16x8 A[9];
#pragma unroll
        for (int t = 0; t < 9; ++t) A[t] = lds8(ml + t * 1024 + lane * 16);
        {
            const v4u ff = {0xFFFFFFFFu, 0xFFFFFFFFu, 0xFFFFFFFFu, 0xFFFFFFFFu};
            if (s > 0) ST16(zp4, rw, OFF_ZH + (s - 1) * (NR * NH * 256) + zo4);
            ST16(ff, rw, OFF_BLK + B_Z + SLOT(s + 2) + zo4);
            if (w < 4 && lane < 16) { if (s > 0) ST16(zp5, rw, OFF_ZH + (s - 1) * (NR * NH * 256) + zo5); ST16(ff, rw, OFF_BLK + B_Z + SLOT(s + 2) + zo5); }
            if (tid == 320 && s >= 2) ST4((u32)(s - 2), rw, (FL_Z + h) * 128);
        }
        __builtin_amdgcn_sched_barrier(0);
#pragma unroll
        for (int t = 0; t < 9; ++t) av = SMF(A[t], Bv[t], av, idx);
        {
            const u32 o = pack2(swap32_sum(av[0], av[2]), swap32_sum(av[1], av[3]));
            *(LAS u32*)(xl + (16 * w + i16) * 16 + (g & 1) * 8 + (g >> 1) * 4) = o;
        }
        STAMP(3);
        LBAR();
        STAMP(4);
        bf16x8 A2[2];
#pragma unroll
        for (int t = 0; t < 2; ++t) A2[t] = as8(trd(xl + t * 1024 + aoff), trd(xl + t * 1024 + aoff + 128));
        f32x4 d[4];
#pragma unroll
        for (int j = 0; j < 4; ++j) { d[j] = f32x4{}; d[j] = SMF(A2[0], Bo[j][0], d[j], idx); }
#pragma unroll
        for (int j = 0; j < 4; ++j) d[j] = SMF(A2[1], Bo[j][1], d[j], idx);
        if (w < 4) { av2 = SMF(A2[0], Bo[4][0], av2, idx); av2 = SMF(A2[1], Bo[4][1], av2, idx); }
        {
            const u32 x0 = pack2(swap32_sum(d[0][0], d[1][0]), swap32_sum(d[0][1], d[1][1])), x1 = pack2(swap32_sum(d[0][2], d[1][2]), swap32_sum(d[0][3], d[1][3]));
            const u32 y0 = pack2(swap32_sum(d[2][0], d[3][0]), swap32_sum(d[2][1], d[3][1])), y1 = pack2(swap32_sum(d[2][2], d[3][2]), swap32_sum(d[2][3], d[3][3]));
            auto r0 = __builtin_amdgcn_permlane16_swap(x0, y0, false, false);
            auto r1 = __builtin_amdgcn_permlane16_swap(x1, y1, false, false);
            zp4 = v4u{nos(r0[0]), nos(r1[0]), nos(r0[1]), nos(r1[1])};
            ST16(zp4, rw, zs4);
        }
        if (w < 4) {
            const u32 x = nos(pack2(swap32_sum(av2[0], av2[2]), swap32_sum(av2[1], av2[3])));
            auto r0 = __builtin_amdgcn_permlane16_swap(x, x, false, false);
            auto ra = __builtin_amdgcn_permlane32_swap(r0[0], r0[0], false, false);
            auto rc = __builtin_amdgcn_permlane32_swap(r0[1], r0[1], false, false);
            zp5 = v4u{ra[0], ra[1], rc[0], rc[1]};
            if (lane < 16) ST16(zp5, rw, zs5);
        }
        STAMP(6);
        STAMP_FLUSH;
        if (s < NSEG - 1) {
            if (w == 5) {
                if (s > 0) tk_T0(tk, beacon_max(rw, s, rep, 0, NR, lane, budget), a);
                wait_until(tk.XR);
                tk_Tp(tk, beacon_max(rw, s, rep, 40, NH, lane, budget), a);
                tk_post(tkp, s + 1, tk.XA, lane);
            }
            DRAIN();
        }
    }
    DRAIN();
    LBAR();
    if (tid == 0) ST4((u32)(NSEG - 1), rw, (FL_Z + h) * 128);
    STAMP_FIN;
    if (budget < 0 && lane == 0) ST4(2u, rw, FL_ERR * 128);
}

__device__ __forceinline__ void role_red(const Args& a, lptr lds, rsrc_t rw, int r, int rep) {
    const int tid = threadIdx.x, lane = tid & 63, w = __builtin_amdgcn_readfirstlane(tid >> 6);
    const int idx = (lane & 8) ? 0xEEEE : 0x4444;
    const int i16 = lane & 15, g = lane >> 4;
    int budget = SPIN_MAX;
    lptr tkp = lds + 4096;
    volatile LAS int* hs = (volatile LAS int*)(lds + 4096 + 64);
    volatile LAS int* prod = (volatile LAS int*)(lds + 4096 + 128);
    lptr ring = lds + 16384;
    if (tid == 0) { tk_post(tkp, -1, 0u, 0); *hs = 0; prod[0] = prod[1] = prod[2] = prod[3] = 0; }
    __syncthreads();
    STAMP_INIT
    if (w < 2) {
    LAS float* R = (LAS float*)lds;
    const int pn = ((i16 >> 1) & 1) * 8 + (i16 >> 2) * 2 + (i16 & 1);
    const int row = 4 * (g & 1) + (g >> 1);
    const int zo = OFF_BLK + B_Z + (r * NH + 16 * w + g) * 256 + i16 * 16;
    const int po = OFF_BLK + B_P + rep * PB + 8 * w * 1024 + lane * 16;
    const int mo = OFF_BLK + B_P + rep * PB + PMM_OFF + (16 * w + 2 * g) * 128 + (i16 & 7) * 16 + (i16 >> 3) * 8;
    const int mso = OFF_BLK + B_MEM + g * MEMB + r * 256 + i16 * 16;
    const int ob = OFF_BLK + B_BC + (lane & 3) * BC_REP + r * 128;
    for (int s = 0; s < NSEG - 1; ++s) {
        while (__builtin_amdgcn_readfirstlane(prod[w]) <= s && --budget >= 0) __builtin_amdgcn_s_sleep(1);
        MB();
        bf16x8 b[16];
#pragma unroll
        for (int k = 0; k < 16; ++k) b[k] = lds8(ring + (s & 3) * 32768 + (16 * w + k) * 1024 + lane * 16);
        asm volatile("s_waitcnt lgkmcnt(0)" ::: "memory");
#pragma unroll
        for (int k = 0; k < 16; ++k) PIN4(b[k]);
        prod[2 + w] = s + 1;
        const int sb = SLOT(s);
        LAS float* Rb = R + (s & 1) * 256;
        f32x4 d;
        const u32 XR = tk_get(tkp, s, budget);
        wait_until(XR - a.phz);
        STAMP(0);
        for (int it = 0;; ++it) {
            v4u z[4], p[8]; v2u m[4];
#pragma unroll
            for (int k = 0; k < 4; ++k) z[k] = LD16(rw, zo + k * 1024, sb);
            MB();
            if (it == 0) wait_until(XR);
            STAMP(1);
#pragma unroll
            for (int k = 0; k < 8; ++k) p[k] = LD16(rw, po + k * 1024, sb);
#pragma unroll
            for (int k = 0; k < 4; ++k) m[k] = LD8(rw, mo + (k >> 1) * 1024 + (k & 1) * 128, sb);
            MB();
            __builtin_amdgcn_sched_barrier(0);
            u32 bad = max(max(bad4(z[0]), bad4(z[1])), max(bad4(z[2]), bad4(z[3])));
            bf16x16 bz[2];
#pragma unroll
            for (int k = 0; k < 2; ++k) { v4u y0 = {z[2 * k].x, z[2 * k].z, z[2 * k].y, z[2 * k].w}, y1 = {z[2 * k + 1].x, z[2 * k + 1].z, z[2 * k + 1].y, z[2 * k + 1].w}; bz[k] = cat16(as8(y0), as8(y1)); PIN4(bz[k]); }
            __builtin_amdgcn_sched_barrier(0);
            d = f32x4{};
#pragma unroll
            for (int k = 0; k < 8; ++k) { d = SMF(as8(p[k]), cat16(b[2 * k], b[2 * k + 1]), d, idx); bad = max(bad, bad4(p[k])); }
#pragma unroll
            for (int k = 0; k < 2; ++k) { d = SMF(as8(m[2 * k], m[2 * k + 1]), bz[k], d, idx); bad = max(bad, max(max(m[2 * k].x, m[2 * k].y), max(m[2 * k + 1].x, m[2 * k + 1].y))); }
            if (__all(bad != 0xFFFFFFFFu)) break;
            if (--budget < 0) break;
        }
        STAMP(2);
        const float s01 = swap32_sum(d[0], d[1]), s23 = swap32_sum(d[2], d[3]);
        *(volatile LAS float*)(Rb + w * 128 + row * 16 + pn) = s01;
        *(volatile LAS float*)(Rb + w * 128 + (row + 2) * 16 + pn) = s23;
        STAMP(3);
        if (w == 1) { *hs = s + 1; }
        else {
            volatile LAS f32x4* q = (volatile LAS f32x4*)(Rb + (i16 & 7) * 16 + (i16 >> 3) * 8);
            f32x4 a0, a1, b0, b1;
            for (;;) {
                const int f = *hs;
                a0 = q[0]; a1 = q[1]; b0 = q[32]; b1 = q[33];
                if (__builtin_amdgcn_readfirstlane(f) > s || --budget < 0) break;
            }
            STAMP(4);
            const f32x4 c0 = a0 + b0, c1 = a1 + b1;
            v4u o = {nos(pack2(c0[0], c0[1])), nos(pack2(c0[2], c0[3])), nos(pack2(c1[0], c1[1])), nos(pack2(c1[2], c1[3]))};
            STAMP(5);
            ST16(o, rw, mso + SLOT(s + 1));
            __builtin_amdgcn_sched_barrier(0);
            const u32 tnow = rt32();
            if (lane < NREP) ST4(nos(tnow), rw, ob + SLOT(s + 1));
            const v4u ff = {0xFFFFFFFFu, 0xFFFFFFFFu, 0xFFFFFFFFu, 0xFFFFFFFFu};
            ST16(ff, rw, mso + SLOT(s + 3));
            if (lane < NREP) ST4(0xFFFFFFFFu, rw, ob + SLOT(s + 3));
        }
        STAMP_FLUSH;
    }
    } else if (w == 2) {
        TK tk; tk_init(tk);
        for (int s = 0; s < NSEG - 1; ++s) {
            if (s > 0) { wait_until(tk.XA); tk_T0(tk, beacon_max(rw, s, rep, 0, NR, lane, budget), a); }
            tk_post(tkp, s, tk.XR, lane);
            wait_until(tk.XR);
            tk_Tp(tk, beacon_max(rw, s, rep, 40, NH, lane, budget), a);
        }
    } else if (w < 5) {
        const int hw = w - 3;
        const int vo = OFF_VOX + (r * NH + 16 * hw) * 1024 + lane * 16;
        int vdone0 = -1, vdone1 = -1;
        for (int s = 0; s < NSEG - 1; ++s) {
            const int blk = s >> 1;
            if (blk & 1) { if (vdone1 < blk) vdone1 = wait_flags(rw, FL_V + 1, 2, NH, blk, lane, budget); }
            else { if (vdone0 < blk) vdone0 = wait_flags(rw, FL_V, 2, NH, blk, lane, budget); }
            while (__builtin_amdgcn_readfirstlane(prod[2 + hw]) < s - 3 && --budget >= 0) __builtin_amdgcn_s_sleep(2);
            MB();
            v4u v[16];
#pragma unroll
            for (int k = 0; k < 16; ++k) v[k] = LD16(rw, vo + k * 1024, s * (NR * NH * 1024));
#pragma unroll
            for (int k = 0; k < 16; ++k) *(LAS v4u*)(ring + (s & 3) * 32768 + (16 * hw + k) * 1024 + lane * 16) = v[k];
            asm volatile("s_waitcnt lgkmcnt(0)" ::: "memory");
            prod[hw] = s + 1;
        }
    }
    STAMP_FIN;
    if (budget < 0 && lane == 0) ST4(3u, rw, FL_ERR * 128);
}

__device__ __forceinline__ void cexp_l(float lr, float li, float l, float& er, float& ei) {
    const float mag = __builtin_amdgcn_exp2f(lr * l);
    const float ph = li * l;
    const float pe = fmaf(li, l, -ph);
    const float fr = (ph - floorf(ph)) + pe;
    er = mag * __builtin_amdgcn_cosf(fr);
    ei = mag * __builtin_amdgcn_sinf(fr);
}
constexpr int CV_W = 8 * 260 * 16, CV_U = 96 * 80, CV_CH = CV_W + CV_U;
__device__ __forceinline__ void s4_unit(const Args& a, lptr lds, rsrc_t rw, int unit) {
    const int tid = threadIdx.x, lane = tid & 63, w = __builtin_amdgcn_readfirstlane(tid >> 6);
    const int d0 = unit * 4;
    v2u uv[4];
#pragma unroll
    for (int k = 0; k < 4; ++k) uv[k] = *(const v2u*)(a.u + (size_t)(2 * (tid + 512 * (k >> 1)) + (k & 1)) * DM + d0);
    {
        const int c = tid >> 7, nn = tid & 127, n = (d0 + c) * 128 + nn;
        const float dt = rbf(expf(bf2f(a.logdt[d0 + c])));
        const float lr = dt * bf2f(a.Are[n]) * 1.4426950408889634f, li = dt * bf2f(a.Aim[n]) * 0.15915494309189535f;
        const float br = dt * bf2f(a.Bre[n]), bi = dt * bf2f(a.Bim[n]);
        const float cr = bf2f(a.Cre[n]), ci = bf2f(a.Cim[n]);
        LAS f32x2* T = (LAS f32x2*)(lds + c * CV_CH) + nn;
        float er, ei, pr, pi;
        cexp_l(lr, li, 1.0f, er, ei); pr = er; pi = ei;
#pragma unroll
        for (int o = 0; o < 7; ++o) { T[o * 130] = f32x2{pr, pi}; const float t = pr * er - pi * ei; pi = pr * ei + pi * er; pr = t; }
#pragma unroll
        for (int q4 = 0; q4 < 4; ++q4) {
            if (q4 == 0) cexp_l(lr, li, 128.0f, er, ei);
            cexp_l(lr, li, (float)(128 + 512 * q4), pr, pi);
#pragma unroll
            for (int o = 0; o < 4; ++o) { T[(7 + 4 * q4 + o) * 130] = f32x2{pr, pi}; const float t = pr * er - pi * ei; pi = pr * ei + pi * er; pr = t; }
        }
        const float cbr = cr * br - ci * bi, cbi = cr * bi + ci * br;
        cexp_l(lr, li, 8.0f, er, ei);
#pragma unroll
        for (int q4 = 0; q4 < 4; ++q4) {
            float sr, si;
            cexp_l(lr, li, (float)(32 * q4), sr, si);
            pr = cbr * sr - cbi * si; pi = cbr * si + cbi * sr;
#pragma unroll
            for (int o = 0; o < 4; ++o) { T[(23 + 4 * q4 + o) * 130] = f32x2{pr, pi}; const float t = pr * er - pi * ei; pi = pr * ei + pi * er; pr = t; }
        }
    }
    __syncthreads();
    f32x4 kr[3];
    {
        const int x = lane & 15, g = lane >> 4, c = w & 3, nh = w >> 2;
        const int ah0 = x >> 3, o0 = x & 7;
        const int ah1 = x < 8 ? 2 + (x >> 2) : 4 + ((x - 8) >> 1), o1 = x < 8 ? 2 * (x & 3) : 4 * (x & 1);
        const int ah2 = x < 8 ? 8 + x : 16;
        LAS f32x2* T = (LAS f32x2*)(lds + c * CV_CH) + nh * 64 + g;
        LAS f32x2* Ta0 = T + 7 * 130; LAS f32x2* To0 = T + (o0 ? o0 - 1 : 0) * 130;
        LAS f32x2* Ta1 = T + (6 + ah1) * 130; LAS f32x2* To1 = T + (o1 ? o1 - 1 : 0) * 130;
        LAS f32x2* Ta2 = T + (6 + ah2) * 130; LAS f32x2* Tv = T + (23 + x) * 130;
        const f32x2 one = {1.0f, 0.0f};
        f32x4 ar[3] = {}, ai[3] = {};
#pragma unroll
        for (int jj = 0; jj < 16; ++jj) {
            const f32x2 v = Tv[4 * jj], u2 = Ta2[4 * jj], a1 = Ta1[4 * jj];
            f32x2 a0v = Ta0[4 * jj], e0 = To0[4 * jj], e1 = To1[4 * jj];
            a0v = ah0 ? a0v : one; e0 = o0 ? e0 : one; e1 = o1 ? e1 : one;
            const float u0r = a0v[0] * e0[0] - a0v[1] * e0[1], u0i = a0v[0] * e0[1] + a0v[1] * e0[0];
            const float u1r = a1[0] * e1[0] - a1[1] * e1[1], u1i = a1[0] * e1[1] + a1[1] * e1[0];
            ar[2] = __builtin_amdgcn_mfma_f32_16x16x4f32(u2[0], v[0], ar[2], 0, 0, 0);
            ai[2] = __builtin_amdgcn_mfma_f32_16x16x4f32(u2[1], v[1], ai[2], 0, 0, 0);
            ar[0] = __builtin_amdgcn_mfma_f32_16x16x4f32(u0r, v[0], ar[0], 0, 0, 0);
            ai[0] = __builtin_amdgcn_mfma_f32_16x16x4f32(u0i, v[1], ai[0], 0, 0, 0);
            ar[1] = __builtin_amdgcn_mfma_f32_16x16x4f32(u1r, v[0], ar[1], 0, 0, 0);
            ai[1] = __builtin_amdgcn_mfma_f32_16x16x4f32(u1i, v[1], ai[1], 0, 0, 0);
        }
#pragma unroll
        for (int t = 0; t < 3; ++t) kr[t] = ar[t] - ai[t];
    }
    __syncthreads();
    {
        LAS float* T = (LAS float*)(lds + (w & 3) * CV_CH + 4096 + (w >> 2) * 3072);
#pragma unroll
        for (int t = 0; t < 3; ++t)
#pragma unroll
            for (int r = 0; r < 4; ++r) T[(16 * t + 4 * (lane >> 4) + r) * 16 + (lane & 15)] = kr[t][r];
    }
    __syncthreads();
#pragma unroll
    for (int k = 0; k < 8; ++k) {
        const int i = tid + 512 * k, c = i >> 10, l2 = i & 1023;
        LAS float* T = (LAS float*)(lds + c * CV_CH + 4096);
        float v[2];
#pragma unroll
        for (int e = 0; e < 2; ++e) {
            const int lb = (int)rbf((float)(2 * l2 + e));
            const int hi = lb >> 7, o = lb & 7;
            const int row = lb < 256 ? 8 * hi + o : lb < 512 ? 16 + 4 * (hi - 2) + (o >> 1) : lb < 1024 ? 24 + 2 * (hi - 4) + (o >> 2) : 32 + (hi - 8);
            const int ix = row * 16 + ((lb >> 3) & 15);
            v[e] = T[ix] + T[768 + ix];
        }
        *(LAS u32*)(lds + c * CV_CH + CV_W + 2704 + l2 * 4) = pack2(v[0], v[1]);
    }
    __syncthreads();
    for (int i = tid; i < 4 * 259; i += 512) {
        const int c = i / 259, zz = i - c * 259, z = zz - 3;
        lptr ch = lds + c * CV_CH;
        v4u lo = {0, 0, 0, 0}, hi = {0, 0, 0, 0};
        if (z >= 1) lo = *(LAS v4u*)(ch + CV_W + 2704 + (z - 1) * 16);
        if (z >= 0) hi = *(LAS v4u*)(ch + CV_W + 2704 + z * 16);
        const u32 e[8] = {lo.x, lo.y, lo.z, lo.w, hi.x, hi.y, hi.z, hi.w};
#pragma unroll
        for (int cc = 0; cc < 8; ++cc) {
            v4u o;
#pragma unroll
            for (int k = 0; k < 4; ++k) {
                const int t = 8 + cc - 2 * k;
                if (t & 1) o[k] = (e[t >> 1] >> 16) | (e[t >> 1] << 16);
                else o[k] = (e[t >> 1] & 0xffffu) | (e[(t >> 1) - 1] & 0xffff0000u);
            }
            *(LAS v4u*)(ch + (cc * 260 + zz) * 16) = o;
        }
    }
    __syncthreads();
    for (int i = tid; i < 4 * 160; i += 512) *(LAS v4u*)(lds + (i / 160) * CV_CH + CV_W + (i % 160) * 16) = v4u{0, 0, 0, 0};
#pragma unroll
    for (int k2 = 0; k2 < 2; ++k2) {
        const int l = 2 * (tid + 512 * k2);
        const int o = CV_W + (32 + (l >> 5)) * 80 + (l & 31) * 2;
        const v2u p = uv[2 * k2], q = uv[2 * k2 + 1];
        *(LAS u32*)(lds + 0 * CV_CH + o) = (p.x & 0xffffu) | (q.x << 16);
        *(LAS u32*)(lds + 1 * CV_CH + o) = (p.x >> 16) | (q.x & 0xffff0000u);
        *(LAS u32*)(lds + 2 * CV_CH + o) = (p.y & 0xffffu) | (q.y << 16);
        *(LAS u32*)(lds + 3 * CV_CH + o) = (p.y >> 16) | (q.y & 0xffff0000u);
    }
    __syncthreads();
    const int c = w & 3, par = w >> 2, b = lane & 31, hh = lane >> 5;
    lptr ch = lds + c * CV_CH;
    lptr wa = ch + ((b & 7) * 260 + (b >> 3) - hh + 3) * 16;
    lptr ub = ch + CV_W + (32 + b) * 80 + hh * 16;
    f32x16 y0 = {}, y1 = {};
    wa += par * 64; ub -= par * 80;
    {
        bf16x8 fa[2][2], fb[2][2], fc[2][2];
#pragma unroll
        for (int ks = 0; ks < 2; ++ks) { fa[0][ks] = lds8(wa - ks * 32); fb[0][ks] = lds8(ub + ks * 32); fc[0][ks] = lds8(ub + 2560 + ks * 32); }
#pragma unroll
        for (int i = 0; i < 32; ++i) {
            const int cur = i & 1, nx = cur ^ 1;
            if (i + 1 < 32) {
#pragma unroll
                for (int ks = 0; ks < 2; ++ks) {
                    fa[nx][ks] = lds8(wa + (i + 1) * 128 - ks * 32);
                    if (i + 1 < 16) fb[nx][ks] = lds8(ub - (i + 1) * 160 + ks * 32);
                    fc[nx][ks] = lds8(ub + 2560 - (i + 1) * 160 + ks * 32);
                }
            }
            __builtin_amdgcn_sched_barrier(0);
#pragma unroll
            for (int ks = 0; ks < 2; ++ks) {
                if (i < 16) y0 = MFMA32(fa[cur][ks], fb[cur][ks], y0);
                y1 = MFMA32(fa[cur][ks], fc[cur][ks], y1);
            }
            __builtin_amdgcn_sched_barrier(0);
        }
    }
    __syncthreads();
    {
        LAS f32x4* X = (LAS f32x4*)(ch + par * 4096);
        const f32x16 src = par ? y0 : y1;
#pragma unroll
        for (int k = 0; k < 4; ++k) X[k * 64 + lane] = f32x4{src[4 * k], src[4 * k + 1], src[4 * k + 2], src[4 * k + 3]};
    }
    __syncthreads();
    {
        LAS f32x4* X = (LAS f32x4*)(ch + (1 - par) * 4096);
        lptr Y = ch + 8192 + (32 * par + b) * 72 + hh * 8;
        const f32x16 own = par ? y1 : y0;
#pragma unroll
        for (int k = 0; k < 4; ++k) {
            const f32x4 o = X[k * 64 + lane];
            v2u yv = {pack2(own[4 * k] + o[0], own[4 * k + 1] + o[1]), pack2(own[4 * k + 2] + o[2], own[4 * k + 3] + o[3])};
            *(LAS v2u*)(Y + k * 16) = yv;
        }
    }
    __syncthreads();
    const v2u dk = *(const v2u*)(a.dskip + d0);
#pragma unroll
    for (int k2 = 0; k2 < 2; ++k2) {
        const int i = tid + 512 * k2;
        lptr yp = lds + 8192 + (i >> 4) * 72 + (i & 15) * 4;
        const u32 ya = *(LAS u32*)(yp), yb = *(LAS u32*)(yp + CV_CH), yc = *(LAS u32*)(yp + 2 * CV_CH), yd = *(LAS u32*)(yp + 3 * CV_CH);
        const v2u p = uv[2 * k2], q = uv[2 * k2 + 1];
        v4u o;
        o.x = pack2(bflo(ya) + rbf(bflo(p.x) * bflo(dk.x)), bflo(yb) + rbf(bfhi(p.x) * bfhi(dk.x)));
        o.y = pack2(bflo(yc) + rbf(bflo(p.y) * bflo(dk.y)), bflo(yd) + rbf(bfhi(p.y) * bfhi(dk.y)));
        o.z = pack2(bfhi(ya) + rbf(bflo(q.x) * bflo(dk.x)), bfhi(yb) + rbf(bfhi(q.x) * bfhi(dk.x)));
        o.w = pack2(bfhi(yc) + rbf(bflo(q.y) * bflo(dk.y)), bfhi(yd) + rbf(bfhi(q.y) * bfhi(dk.y)));
        ST16(o, rw, OFF_A4 + unit * 16384 + i * 16);
    }
    __syncthreads();
}
__device__ __forceinline__ void role_s4(const Args& a, lptr lds, rsrc_t rw, int idx) {
    if (idx < 32) {
        const int tid = threadIdx.x, t = idx * 64 + (tid >> 3), j = tid & 7;
        float acc = 0.f;
#pragma unroll
        for (int k = 0; k < 9; ++k) {
            const v4u xv = *(const v4u*)(a.u + (size_t)t * DM + (k * 8 + j) * 8), gv = *(const v4u*)(a.gatew + (k * 8 + j) * 8);
#pragma unroll
            for (int e = 0; e < 4; ++e) acc += rbf(bflo(xv[e]) * bflo(gv[e])) + rbf(bfhi(xv[e]) * bfhi(gv[e]));
        }
        acc += __shfl_xor(acc, 1); acc += __shfl_xor(acc, 2); acc += __shfl_xor(acc, 4);
        acc = rbf(acc);
        if (j == 0) ST4(__float_as_uint(rbf(1.0f / (1.0f + __expf(-acc)))), rw, OFF_G + t * 4);
    }
#pragma unroll 1
    for (int unit = idx; unit < ((a.mask & 8) ? 0 : DM / 4); unit += NS4) s4_unit(a, lds, rw, unit);
    DRAIN();
    __syncthreads();
    if (threadIdx.x == 0) ST4(1u, rw, (FL_S + idx) * 128);
}

__device__ __forceinline__ void role_post(const Args& a, lptr lds, rsrc_t rw) {
    const int tid = threadIdx.x, lane = tid & 63, w = __builtin_amdgcn_readfirstlane(tid >> 6);
    const int i16 = lane & 15, g = lane >> 4;
    int budget = SPIN_MAX, vdone0 = -1, vdone1 = -1, zdone = -1;
    volatile LAS int* U = (volatile LAS int*)(lds + 81920);
    wait_flags(rw, FL_S, 1, 64, 1, lane, budget);
    wait_flags(rw, FL_S + 64, 1, NS4 - 64, 1, lane, budget);
#pragma unroll 1
    for (;;) {
        if (tid == 0) *U = 1 + (int)__hip_atomic_fetch_add((u32*)(a.ws + FL_Q * 128), 1u, __ATOMIC_RELAXED, __HIP_MEMORY_SCOPE_AGENT);
        __syncthreads();
        const int unit = __builtin_amdgcn_readfirstlane(*U);
        if (unit >= NSEG * 6 || budget < 0) break;
        const int s = unit / 6, r = (unit - s * 6) * 6 + (w < 6 ? w : 0), blk = s >> 1;
        bf16x8 b[32];
        v2u av[2]; float gt[2];
        if (w < 6) {
            if (blk & 1) { if (vdone1 < blk) vdone1 = wait_flags(rw, FL_V + 1, 2, NH, blk, lane, budget); }
            else { if (vdone0 < blk) vdone0 = wait_flags(rw, FL_V, 2, NH, blk, lane, budget); }
            const int vo = OFF_VOX + (s * NR + r) * (NH * 1024) + lane * 16;
#pragma unroll
            for (int k = 0; k < 32; ++k) b[k] = as8(LD16(rw, vo + (k & 3) * 1024, (k >> 2) * 4096));
#pragma unroll
            for (int qt = 0; qt < 2; ++qt) {
                av[qt] = LD8(rw, OFF_A4 + (4 * r + g) * 16384 + (32 * s + 16 * qt + i16) * 8, 0);
                gt[qt] = __uint_as_float(LD4(rw, OFF_G + (32 * s + 16 * qt + i16) * 4, 0));
            }
        }
        wait_flags(rw, FL_P + (s & 3), 4, 16, s, lane, budget);
        v4u z[8];
        {
            v4u pv[10];
#pragma unroll
            for (int k = 0; k < 10; ++k) pv[k] = LD16(rw, OFF_PX + s * 81920 + tid * 16, k * 8192);
            if (zdone < s) zdone = wait_flags(rw, FL_Z, 1, NH, s, lane, budget);
            const int zo = OFF_ZH + (s * NR + r) * (NH * 256) + lane * 16;
#pragma unroll
            for (int k = 0; k < 8; ++k) z[k] = LD16(rw, zo + (k & 3) * 1024, (k >> 2) * 4096);
            MB();
#pragma unroll
            for (int k = 0; k < 10; ++k) *(LAS v4u*)(lds + k * 8192 + tid * 16) = pv[k];
        }
        __syncthreads();
        if (w < 6) {
            f32x4 d0 = {}, d1 = {};
            lptr pl = lds + lane * 16;
            bf16x8 f0[4], f1[4];
#pragma unroll
            for (int i = 0; i < 4; ++i) { f0[i] = lds8(pl + i * 1024); f1[i] = lds8(pl + 40960 + i * 1024); }
            __builtin_amdgcn_sched_barrier(0);
#pragma unroll
            for (int k = 0; k < 40; ++k) {
                const bf16x8 v = k < 32 ? b[k < 32 ? k : 0] : as8(z[k < 32 ? 0 : k - 32]);
                d0 = MFMA16(v, f0[k & 3], d0);
                d1 = MFMA16(v, f1[k & 3], d1);
                if (k + 4 < 40) { f0[k & 3] = lds8(pl + (k + 4) * 1024); f1[k & 3] = lds8(pl + 40960 + (k + 4) * 1024); }
                __builtin_amdgcn_sched_barrier(0);
            }
#pragma unroll
            for (int qt = 0; qt < 2; ++qt) {
                const f32x4 d = qt ? d1 : d0;
                const float gq = gt[qt], og = rbf(1.0f - gq);
                const float r0 = rbf(gq * bflo(av[qt].x)) + rbf(og * rbf(d[0])), r1 = rbf(gq * bfhi(av[qt].x)) + rbf(og * rbf(d[1]));
                const float r2 = rbf(gq * bflo(av[qt].y)) + rbf(og * rbf(d[2])), r3 = rbf(gq * bfhi(av[qt].y)) + rbf(og * rbf(d[3]));
                v2u ov = {pack2(r0, r1), pack2(r2, r3)};
                *(v2u*)(a.out + (size_t)(32 * s + 16 * qt + i16) * DM + 16 * r + 4 * g) = ov;
            }
        }
        __syncthreads();
    }
    if (budget < 0 && lane == 0) ST4(6u, rw, FL_ERR * 128);
}

__global__ void __launch_bounds__(512, 2) k_all(Args a) {
    extern __shared__ __attribute__((aligned(16))) char smem[];
    lptr lds = (lptr)smem;
    const rsrc_t rw = __builtin_amdgcn_make_buffer_rsrc(a.ws, 0, WS_BYTES, 0x00020000);
    const int b = blockIdx.x, xg = b & 7, slot = b >> 3;
    WSTAMP(516);
    if (slot >= 25 || (xg >= 4 && slot >= 20)) {
        const int idx = xg < 4 ? (slot - 25) * 4 + xg : 28 + (31 - slot) * 4 + (xg - 4);
        role_s4(a, lds, rw, idx);
        __syncthreads();
    }
    bool post = false;
    if (xg < 4) {
        if (slot < 8) role_qk(a, lds, rw, xg * 8 + slot, xg);
        else if (slot < 16) role_vo(a, lds, rw, xg * 8 + slot - 8, xg);
        else if (slot < 25) role_red(a, lds, rw, xg * 9 + slot - 16, xg);
    } else {
        const int j = xg - 4;
        post = true;
        if (slot < 16 && (a.mask & 32)) { if (threadIdx.x == 0) ST4(31u, rw, (FL_V + 2 * (j * 8 + (slot >> 1)) + (slot & 1)) * 128); }
        else if (slot < 20 && slot >= 16 && (a.mask & 64)) { if (threadIdx.x == 0) ST4(31u, rw, (FL_K + j * 4 + slot - 16) * 128); }
        else if (slot < 16) role_vvo(a, lds, rw, j * 8 + (slot >> 1), slot & 1);
        else if (slot < 20) role_k2(a, lds, rw, j * 4 + slot - 16);
        else if (slot < 28) { if (!(a.mask & 16)) role_q2p(a, lds, rw, j * 4 + ((slot - 20) >> 1), slot & 1); post = false; }
    }
    WSTAMP(517);
    if (post && !(a.mask & 4)) { __syncthreads(); role_post(a, lds, rw); }
    WSTAMP(518);
}

extern "C" void kernel_launch(void* const* d_in, const int* in_sizes, int n_in, void* d_out, int out_size, void* d_ws, size_t ws_size, hipStream_t stream) {
    Args a;
    a.u = (const u16*)d_in[0]; a.Are = (const u16*)d_in[1]; a.Aim = (const u16*)d_in[2]; a.Bre = (const u16*)d_in[3]; a.Bim = (const u16*)d_in[4];
    a.Cre = (const u16*)d_in[5]; a.Cim = (const u16*)d_in[6]; a.logdt = (const u16*)d_in[7]; a.dskip = (const u16*)d_in[8];
    a.mem0 = (const u16*)d_in[10]; a.Wq = (const u16*)d_in[11]; a.Wk = (const u16*)d_in[12]; a.Wv = (const u16*)d_in[13]; a.Wo = (const u16*)d_in[14];
    a.gatew = (const u16*)d_in[16]; a.out = (u16*)d_out; a.ws = (char*)d_ws;
    a.pd = 12; a.pha = 4; a.phz = 8; a.gap = 50; a.mask = 0; a.pad0 = 0;
    const int LDS_ALL = 160 * 1024;
    (void)hipFuncSetAttribute((const void*)k_all, hipFuncAttributeMaxDynamicSharedMemorySize, LDS_ALL);
    (void)hipMemsetAsync(d_ws, 0xFF, INIT_BYTES, stream);
    hipLaunchKernelGGL(k_all, dim3(256), dim3(512), LDS_ALL, stream, a);
}
```

```cpp
#include <hip/hip_runtime.h>
#include <hip/hip_bf16.h>
#include <stdint.h>
#include <stdio.h>
#include <stdlib.h>

typedef unsigned short u16;
typedef unsigned int u32;
typedef u32 v4u __attribute__((ext_vector_type(4)));
typedef u32 v2u __attribute__((ext_vector_type(2)));
typedef float f32x4 __attribute__((ext_vector_type(4)));
typedef float f32x2 __attribute__((ext_vector_type(2)));
typedef float f32x16 __attribute__((ext_vector_type(16)));
typedef __bf16 bf16x2 __attribute__((ext_vector_type(2)));
typedef __bf16 bf16x8 __attribute__((ext_vector_type(8)));
typedef __bf16 bf16x16 __attribute__((ext_vector_type(16)));
typedef short s16x4 __attribute__((ext_vector_type(4)));
#define LAS __attribute__((address_space(3)))
typedef LAS char* lptr;
typedef __amdgpu_buffer_rsrc_t rsrc_t;

constexpr int SEQ = 2048, DM = 576, NH = 32, DH = 128, NSEG = 64, NR = 36, NR8 = 72, NREP = 4;
constexpr int MEMB = 9216, BC_QK = 80;
constexpr int FL_K = 0, FL_V = 16, FL_M = 80, FL_P = 112, FL_S = 176, FL_Q = 254, FL_ERR = 255, FL_Z = 256, FL_W = 328, FL_T = 424, FL_PAR = 432, NS4 = 72;
constexpr int OFF_RING = 65536;
constexpr int N_MEMA = 0, N_PX = 16384, N_PMM = 49152, N_MEMB = 49152 + 4096, N_PROBE = 49152 + 6144;
constexpr int INIT_BYTES = OFF_RING + 16 * 65536;
constexpr int OFF_ZH = INIT_BYTES;
constexpr int OFF_KX = OFF_ZH + NSEG * NR * NH * 256;
constexpr int OFF_VOX = OFF_KX + NH * SEQ * DH * 2;
constexpr int OFF_KM = OFF_VOX + NSEG * NR * NH * 1024;
constexpr int OFF_PX = OFF_KM + NSEG * NH * 2048;
constexpr int OFF_A4 = OFF_PX + NSEG * 81920;
constexpr int OFF_G = OFF_A4 + SEQ * DM * 2;
constexpr int OFF_WVO = OFF_G + SEQ * 4;
constexpr int OFF_ST = OFF_WVO + NR8 * 294912;
constexpr int WS_BYTES = OFF_ST + 256 * 520 * 8 + 4096;

#define BIDX bid_
#define STAMP(k)
#define STAMP_FLUSH
#define STAMP_INIT
#define STAMP_FIN
#define WSTAMP(k)

struct Args {
    const u16* u; const u16* Are; const u16* Aim; const u16* Bre; const u16* Bim; const u16* Cre; const u16* Cim;
    const u16* logdt; const u16* dskip; const u16* mem0; const u16* Wq; const u16* Wk; const u16* Wv; const u16* Wo;
    const u16* gatew; u16* out; char* ws;
    int pd, pha, phz, gap, mask, pad0;
};

__device__ __forceinline__ float bf2f(u16 v) { return __uint_as_float((u32)v << 16); }
__device__ __forceinline__ float bflo(u32 v) { return __uint_as_float(v << 16); }
__device__ __forceinline__ float bfhi(u32 v) { return __uint_as_float(v & 0xffff0000u); }
__device__ __forceinline__ u32 pack2(float lo, float hi) { f32x2 f = {lo, hi}; bf16x2 b = __builtin_convertvector(f, bf16x2); return __builtin_bit_cast(u32, b); }
__device__ __forceinline__ float rbf(float x) { return bflo(pack2(x, x)); }
__device__ __forceinline__ u32 nos(u32 v) { return min(v, 0xFFFFFFFEu); }
__device__ __forceinline__ v2u trd(lptr p) { return __builtin_bit_cast(v2u, __builtin_amdgcn_ds_read_tr16_b64_v4i16((LAS s16x4*)p)); }
__device__ __forceinline__ bf16x8 as8(v4u v) { return __builtin_bit_cast(bf16x8, v); }
__device__ __forceinline__ bf16x8 as8(v2u a, v2u b) { v4u v = {a.x, a.y, b.x, b.y}; return __builtin_bit_cast(bf16x8, v); }
__device__ __forceinline__ v4u asu(bf16x8 v) { return __builtin_bit_cast(v4u, v); }
__device__ __forceinline__ bf16x16 cat16(bf16x8 a, bf16x8 b) { return __builtin_shufflevector(a, b, 0, 1, 2, 3, 4, 5, 6, 7, 8, 9, 10, 11, 12, 13, 14, 15); }
__device__ __forceinline__ bf16x8 lds8(lptr p) { return *(LAS bf16x8*)p; }
__device__ __forceinline__ u32 bad4(v4u v) { return max(max(v.x, v.y), max(v.z, v.w)); }
#define NEARB(s, rep, pm) (OFF_RING + ((((s) & 3) * 4 + (rep)) << 16) + ((((pm) >> (((s) & 3) * 4 + (rep))) & 1) << 13))
#define FARB(s, rep, pm) ((NEARB(s, rep, pm) ^ 8192) + (((s) & 4) << 13))
#define BCO(slot) (((slot) & 63) * 128 + ((slot) >> 6) * 16384)
#define LBAR() do { asm volatile("s_waitcnt lgkmcnt(0)" ::: "memory"); __builtin_amdgcn_s_barrier(); asm volatile("" ::: "memory"); } while (0)
#define MB() asm volatile("" ::: "memory")
#define DRAIN() asm volatile("s_waitcnt vmcnt(0)" ::: "memory")
#define PIN4(x) asm volatile("" : "+v"(x))
#define MFMA16(a, b, c) __builtin_amdgcn_mfma_f32_16x16x32_bf16((a), (b), (c), 0, 0, 0)
#define MFMA32(a, b, c) __builtin_amdgcn_mfma_f32_32x32x16_bf16((a), (b), (c), 0, 0, 0)
#define SMF(a, b, c, idx) __builtin_amdgcn_smfmac_f32_16x16x64_bf16((a), (b), (c), (idx), 0, 0)
#define LD16(r, vo, so) __builtin_amdgcn_raw_buffer_load_b128((r), (vo), (so), 16)
#define LD8(r, vo, so) __builtin_amdgcn_raw_buffer_load_b64((r), (vo), (so), 16)
#define LD4(r, vo, so) __builtin_amdgcn_raw_buffer_load_b32((r), (vo), (so), 16)
#define ST16(v, r, vo) __builtin_amdgcn_raw_buffer_store_b128((v), (r), (vo), 0, 16)
#define ST8(v, r, vo) __builtin_amdgcn_raw_buffer_store_b64((v), (r), (vo), 0, 16)
#define ST4(v, r, vo) __builtin_amdgcn_raw_buffer_store_b32((v), (r), (vo), 0, 16)

__device__ __forceinline__ float swap32_sum(float a, float b) {
    auto r = __builtin_amdgcn_permlane32_swap(__float_as_uint(a), __float_as_uint(b), false, false);
    return __uint_as_float(r[0]) + __uint_as_float(r[1]); }
__device__ __forceinline__ float xmax(float m) {
    auto r = __builtin_amdgcn_permlane16_swap(__float_as_uint(m), __float_as_uint(m), false, false);
    m = fmaxf(__uint_as_float(r[0]), __uint_as_float(r[1]));
    auto q = __builtin_amdgcn_permlane32_swap(__float_as_uint(m), __float_as_uint(m), false, false);
    return fmaxf(__uint_as_float(q[0]), __uint_as_float(q[1])); }
__device__ __forceinline__ float xsum(float m) {
    auto r = __builtin_amdgcn_permlane16_swap(__float_as_uint(m), __float_as_uint(m), false, false);
    m = __uint_as_float(r[0]) + __uint_as_float(r[1]);
    auto q = __builtin_amdgcn_permlane32_swap(__float_as_uint(m), __float_as_uint(m), false, false);
    return __uint_as_float(q[0]) + __uint_as_float(q[1]); }
__device__ __forceinline__ float vmax(float a, float b) { float r; asm("v_max_f32 %0, %1, %2" : "=v"(r) : "v"(a), "v"(b)); return r; }
__device__ __forceinline__ float vmax3(float a, float b, float c) { float r; asm("v_max3_f32 %0, %1, %2, %3" : "=v"(r) : "v"(a), "v"(b), "v"(c)); return r; }
__device__ __forceinline__ u32 ror8(u32 v) { return (u32)__builtin_amdgcn_update_dpp(0, (int)v, 0x128, 0xf, 0xf, true); }
__device__ __forceinline__ float ror8f(float v) { return __uint_as_float(ror8(__float_as_uint(v))); }
__device__ __forceinline__ int wave_max(int d) {
    d = max(d, __builtin_amdgcn_update_dpp(d, d, 0x128, 0xf, 0xf, false));
    d = max(d, __builtin_amdgcn_update_dpp(d, d, 0x124, 0xf, 0xf, false));
    d = max(d, __builtin_amdgcn_update_dpp(d, d, 0x122, 0xf, 0xf, false));
    d = max(d, __builtin_amdgcn_update_dpp(d, d, 0x121, 0xf, 0xf, false));
    return max(max(__builtin_amdgcn_readlane(d, 0), __builtin_amdgcn_readlane(d, 16)), max(__builtin_amdgcn_readlane(d, 32), __builtin_amdgcn_readlane(d, 48)));
}

constexpr int SPIN_MAX = 2000000;
__device__ __forceinline__ int wait_flags(rsrc_t rw, int line0, int stride, int n, int need, int lane, int& budget) {
    const int vo = (line0 + (lane < n ? lane : 0) * stride) * 128;
    int mn;
    for (;;) {
        const int v = (int)LD4(rw, vo, 0);
        MB();
        mn = -wave_max(-v);
        if (mn >= need) break;
        if (--budget < 0) break;
        __builtin_amdgcn_s_sleep(8);
    }
    return mn;
}

template <int NRW, int CW>
__device__ __forceinline__ void stage_rows(const u16* g, int gs, lptr lds, int ls, int tid) {
    constexpr int N = NRW * CW / 512;
    static_assert(NRW * CW % 512 == 0, "");
    v4u v[N];
#pragma unroll
    for (int j = 0; j < N; ++j) { const int i = tid + 512 * j, row = i / CW, c = i % CW; v[j] = *(const v4u*)(g + (size_t)row * gs + c * 8); }
#pragma unroll
    for (int j = 0; j < N; ++j) { const int i = tid + 512 * j, row = i / CW, c = i % CW; *(LAS v4u*)(lds + row * ls + c * 16) = v[j]; }
}
template <int NRW, int CW>
__device__ __forceinline__ void rows_ld(v4u (&v)[NRW * CW / 512], const u16* g, int gs, int tid) {
#pragma unroll
    for (int j = 0; j < NRW * CW / 512; ++j) { const int i = tid + 512 * j, row = i / CW, c = i % CW; v[j] = *(const v4u*)(g + (size_t)row * gs + c * 8); }
}
template <int NRW, int CW>
__device__ __forceinline__ void rows_st(const v4u (&v)[NRW * CW / 512], lptr lds, int ls, int tid) {
#pragma unroll
    for (int j = 0; j < NRW * CW / 512; ++j) { const int i = tid + 512 * j, row = i / CW, c = i % CW; *(LAS v4u*)(lds + row * ls + c * 16) = v[j]; }
}
__device__ __forceinline__ bf16x8 frag16(lptr base, int ls, int lane) {
    const int i16 = lane & 15, bg = lane >> 4, q = i16 >> 2, p = i16 & 3;
    lptr a = base + (8 * bg + q) * ls + p * 8;
    return as8(trd(a), trd(a + 4 * ls));
}
__device__ __forceinline__ bf16x8 frag32p(lptr base, int ls, int lane) {
    const int i16 = lane & 15, G = lane >> 4, q = i16 >> 2, p = i16 & 3;
    lptr a = base + (8 * (G >> 1) + q) * ls + (16 * (p & 1) + 8 * (G & 1) + 4 * (p >> 1)) * 2;
    return as8(trd(a), trd(a + 4 * ls));
}

constexpr int XS = 1168;
__device__ __forceinline__ void xload(v4u (&st)[9], const u16* x, int tok0, int tid) {
#pragma unroll
    for (int j = 0; j < 9; ++j) st[j] = *(const v4u*)(x + (size_t)tok0 * DM + (tid + 512 * j) * 8);
}
__device__ __forceinline__ void xstore(const v4u (&st)[9], lptr xl, int tid) {
#pragma unroll
    for (int j = 0; j < 9; ++j) { const int i = tid + 512 * j; *(LAS v4u*)(xl + i * 16 + (i / 72) * 16) = st[j]; }
}
__device__ __forceinline__ void load_w2(bf16x8 (&A)[36], const u16* W, int h0, lptr lds, int tid, int lane, int w) {
    v4u v[6][6];
#pragma unroll
    for (int c = 0; c < 6; ++c) rows_ld<96, 32>(v[c], W + (size_t)c * 96 * 4096 + h0 * DH, 4096, tid);
    __builtin_amdgcn_sched_barrier(0);
#pragma unroll
    for (int c = 0; c < 6; ++c) {
        lptr l = lds + (c & 1) * (96 * 544);
        rows_st<96, 32>(v[c], l, 544, tid);
        LBAR();
#pragma unroll
        for (int ks = 0; ks < 6; ++ks) A[c * 6 + ks] = frag32p(l + ks * 16 * 544 + w * 64, 544, lane);
        __builtin_amdgcn_sched_barrier(0);
    }
    LBAR();
}

__device__ __forceinline__ f32x16 proj_tile(const bf16x8 (&A)[36], lptr xb) {
    f32x16 acc = {};
    bf16x8 f[6];
#pragma unroll
    for (int i = 0; i < 6; ++i) f[i] = lds8(xb + i * 32);
    __builtin_amdgcn_sched_barrier(0);
#pragma unroll
    for (int ks = 0; ks < 36; ++ks) {
        acc = MFMA32(A[ks], f[ks % 6], acc);
        if (ks + 6 < 36) f[ks % 6] = lds8(xb + (ks + 6) * 32);
        __builtin_amdgcn_sched_barrier(0);
    }
    return acc;
}
__device__ __forceinline__ void role_k2(const Args& a, lptr lds, rsrc_t rw, int cu) {
    const int tid = threadIdx.x, lane = tid & 63, w = __builtin_amdgcn_readfirstlane(tid >> 6);
    v4u st[9];
    bf16x8 A[36];
    load_w2(A, a.Wk, 2 * cu, lds, tid, lane, w);
    xload(st, a.u, 0, tid);
    xstore(st, lds, tid);
    __syncthreads();
    const int o0 = OFF_KX + (2 * cu + (w >> 2)) * (NSEG * 8192) + ((w & 3) * 4 + (lane >> 5) * 2) * 512 + (lane & 31) * 16;
    for (int blk = 0; blk < 32; ++blk) {
        lptr xl = lds + (blk & 1) * (64 * XS);
        if (blk + 1 < 32) xload(st, a.u, (blk + 1) * 64, tid);
#pragma unroll
        for (int tt = 0; tt < 2; ++tt) {
            const f32x16 acc = proj_tile(A, xl + (32 * tt + (lane & 31)) * XS + (lane >> 5) * 16);
            v4u c0 = {pack2(acc[0], acc[1]), pack2(acc[2], acc[3]), pack2(acc[4], acc[5]), pack2(acc[6], acc[7])};
            v4u c1 = {pack2(acc[8], acc[9]), pack2(acc[10], acc[11]), pack2(acc[12], acc[13]), pack2(acc[14], acc[15])};
            const int o = o0 + (blk * 2 + tt) * 8192;
            ST16(c0, rw, o); ST16(c1, rw, o + 512);
        }
        if (blk + 1 < 32) xstore(st, lds + ((blk + 1) & 1) * (64 * XS), tid);
        const bool full = blk < 4 || blk == 31;
        if (full) DRAIN(); else asm volatile("s_waitcnt vmcnt(4)" ::: "memory");
        LBAR();
        if (tid == 0) ST4((u32)(full ? blk : blk - 1), rw, (FL_K + cu) * 128);
    }
}

constexpr int VS = 272;
__device__ __forceinline__ void role_q2p(const Args& a, lptr lds, rsrc_t rw, int cu, int par) {
    const int tid = threadIdx.x, lane = tid & 63, w = __builtin_amdgcn_readfirstlane(tid >> 6);
    v4u st[9];
    bf16x8 A[36];
    load_w2(A, a.Wq, 2 * cu, lds, tid, lane, w);
    lptr xl = lds, ql = lds + 64 * XS;
    LAS int* cnt = (LAS int*)(lds + 64 * XS + 4 * 64 * VS);
    if (tid < 2) cnt[tid] = 0;
    xload(st, a.u, par * 64, tid);
    xstore(st, xl, tid);
    __syncthreads();
    int budget = SPIN_MAX, kdone = -1;
    const int i16 = lane & 15, g = lane >> 4;
    const int sg = w >> 2, hh = (w >> 1) & 1, qt = w & 1, h = 2 * cu + hh;
    const float c2 = 0.08838834764831845f * 1.4426950408889634f;
    for (int it = -1; it < 16; ++it) {
        const int blk = 2 * it + par;
        if (it + 1 < 16) {
            if (it + 2 < 16) xload(st, a.u, (blk + 4) * 64, tid);
            lptr qb = ql + (((it + 1) & 1) * 2 + (w >> 2)) * (64 * VS) + ((w & 3) * 32 + (lane >> 5) * 16) * 2;
#pragma unroll
            for (int tt = 0; tt < 2; ++tt) {
                const f32x16 acc = proj_tile(A, xl + (32 * tt + (lane & 31)) * XS + (lane >> 5) * 16);
                v4u c0 = {pack2(acc[0], acc[1]), pack2(acc[2], acc[3]), pack2(acc[4], acc[5]), pack2(acc[6], acc[7])};
                v4u c1 = {pack2(acc[8], acc[9]), pack2(acc[10], acc[11]), pack2(acc[12], acc[13]), pack2(acc[14], acc[15])};
                lptr qp = qb + (32 * tt + (lane & 31)) * VS;
                *(LAS v4u*)qp = c0; *(LAS v4u*)(qp + 16) = c1;
            }
            __syncthreads();
            if (it + 2 < 16) xstore(st, xl, tid);
        }
        if (it >= 0) {
            const int s = 2 * blk + sg;
            if (kdone < blk) kdone = wait_flags(rw, FL_K + cu, 1, 1, blk, lane, budget);
            bf16x8 k0[4], k1[4], km[4], qf[4];
            const int ko = OFF_KX + (h * NSEG + s) * 8192 + g * 512 + i16 * 16;
#pragma unroll
            for (int ks = 0; ks < 4; ++ks) { k0[ks] = as8(LD16(rw, ko, ks * 2048)); k1[ks] = as8(LD16(rw, ko + 256, ks * 2048)); }
            lptr qp = ql + ((it & 1) * 2 + hh) * (64 * VS) + (32 * sg + 16 * qt + i16) * VS + g * 16;
#pragma unroll
            for (int ks = 0; ks < 4; ++ks) qf[ks] = lds8(qp + ks * 64);
            f32x4 S0 = {}, S1 = {}, Sm = {};
#pragma unroll
            for (int ks = 0; ks < 4; ++ks) { S0 = MFMA16(k0[ks], qf[ks], S0); S1 = MFMA16(k1[ks], qf[ks], S1); }
            wait_flags(rw, FL_M + h, 1, 1, s, lane, budget);
            const int mo = OFF_KM + (s * NH + h) * 2048 + g * 128 + (i16 & 7) * 16;
#pragma unroll
            for (int ks = 0; ks < 4; ++ks) km[ks] = as8(LD16(rw, mo + ks * 512, 0));
#pragma unroll
            for (int ks = 0; ks < 4; ++ks) Sm = MFMA16(km[ks], qf[ks], Sm);
            const int qi = 16 * qt + i16, t0 = 4 * g;
#pragma unroll
            for (int r = 0; r < 4; ++r) {
                if (t0 + r > qi) S0[r] = -INFINITY;
                if (t0 + r + 16 > qi) S1[r] = -INFINITY;
                if (g >= 2) Sm[r] = -INFINITY;
            }
            float m = fmaxf(fmaxf(fmaxf(S0[0], S0[1]), fmaxf(S0[2], S0[3])), fmaxf(fmaxf(S1[0], S1[1]), fmaxf(S1[2], S1[3])));
            m = fmaxf(m, fmaxf(fmaxf(Sm[0], Sm[1]), fmaxf(Sm[2], Sm[3])));
            m = xmax(m);
            const float nm = -m * c2;
            float sum = 0.f;
#pragma unroll
            for (int r = 0; r < 4; ++r) {
                S0[r] = __builtin_amdgcn_exp2f(fmaf(S0[r], c2, nm)); S1[r] = __builtin_amdgcn_exp2f(fmaf(S1[r], c2, nm)); Sm[r] = __builtin_amdgcn_exp2f(fmaf(Sm[r], c2, nm));
                sum += S0[r] + S1[r] + Sm[r];
            }
            sum = xsum(sum);
            const float inv = __builtin_amdgcn_rcpf(sum);
            v4u px = {pack2(S0[0] * inv, S0[1] * inv), pack2(S0[2] * inv, S0[3] * inv), pack2(S1[0] * inv, S1[1] * inv), pack2(S1[2] * inv, S1[3] * inv)};
            const int po = OFF_PX + (s * 2 + qt) * 40960;
            ST16(px, rw, po + h * 1024 + lane * 16);
            const u32 ma = pack2(Sm[0] * inv, Sm[1] * inv), mb = pack2(Sm[2] * inv, Sm[3] * inv);
            auto ra = __builtin_amdgcn_permlane16_swap(ma, ma, false, false);
            auto rb = __builtin_amdgcn_permlane16_swap(mb, mb, false, false);
            v4u pm = {ra[0], rb[0], ra[1], rb[1]};
            if (lane < 16) ST16(pm, rw, po + 32768 + h * 256 + lane * 16);
            DRAIN();
            if (lane == 0) {
                const int old = __hip_atomic_fetch_add(cnt + sg, 1, __ATOMIC_RELAXED, __HIP_MEMORY_SCOPE_WORKGROUP);
                if ((old & 3) == 3) ST4((u32)s, rw, (FL_P + 4 * cu + 2 * par + sg) * 128);
            }
        }
        __syncthreads();
    }
    if (budget < 0 && lane == 0) ST4(5u, rw, FL_ERR * 128);
}

__device__ __forceinline__ void role_vvo(const Args& a, lptr lds, rsrc_t rw, int h, int par) {
    const int tid = threadIdx.x, lane = tid & 63, w = __builtin_amdgcn_readfirstlane(tid >> 6);
    bf16x8 Av[18], Bo[5][4];
    {
        v4u v[3][6], o[2][9];
#pragma unroll
        for (int c = 0; c < 3; ++c) rows_ld<192, 16>(v[c], a.Wv + (size_t)c * 192 * 4096 + h * DH, 4096, tid);
#pragma unroll
        for (int c = 0; c < 2; ++c) rows_ld<64, 72>(o[c], a.Wo + ((size_t)h * DH + c * 64) * DM, DM, tid);
        __builtin_amdgcn_sched_barrier(0);
#pragma unroll
        for (int c = 0; c < 3; ++c) {
            lptr l = lds + (c & 1) * 77824;
            rows_st<192, 16>(v[c], l, 288, tid);
            LBAR();
#pragma unroll
            for (int ks = 0; ks < 6; ++ks) Av[c * 6 + ks] = frag16(l + ks * 32 * 288 + w * 32, 288, lane);
            __builtin_amdgcn_sched_barrier(0);
        }
#pragma unroll
        for (int c = 0; c < 2; ++c) {
            lptr l = lds + ((c + 1) & 1) * 77824;
            rows_st<64, 72>(o[c], l, 1184, tid);
            LBAR();
#pragma unroll
            for (int j = 0; j < 5; ++j) {
                const int r = (w + 8 * j < NR) ? w + 8 * j : w;
#pragma unroll
                for (int ks = 0; ks < 2; ++ks) Bo[j][c * 2 + ks] = frag16(l + ks * 32 * 1184 + r * 32, 1184, lane);
            }
            __builtin_amdgcn_sched_barrier(0);
        }
        LBAR();
    }
    lptr xl = lds, vl = lds + 64 * XS;
    v4u st[9];
    xload(st, a.u, par * 64, tid);
    xstore(st, xl, tid);
    __syncthreads();
    const int rho = lane & 15, bg = lane >> 4;
    const int vo0 = OFF_VOX + ((2 * w + ((rho >> 1) & 1)) * 16 + (h >> 1)) * 1024 + ((2 * (h & 1) + (bg & 1)) * 16 + (bg >> 1) * 8 + 2 * (rho >> 2) + (rho & 1)) * 16;
    for (int blk = par; blk < 32; blk += 2) {
        if (blk + 2 < 32) xload(st, a.u, (blk + 2) * 64, tid);
#pragma unroll
        for (int tt = 0; tt < 4; ++tt) {
            f32x4 acc = {};
            lptr xb = xl + (16 * tt + rho) * XS + bg * 16;
            bf16x8 f[6];
#pragma unroll
            for (int i = 0; i < 6; ++i) f[i] = lds8(xb + i * 64);
            __builtin_amdgcn_sched_barrier(0);
#pragma unroll
            for (int ks = 0; ks < 18; ++ks) {
                acc = MFMA16(Av[ks], f[ks % 6], acc);
                if (ks + 6 < 18) f[ks % 6] = lds8(xb + (ks + 6) * 64);
                __builtin_amdgcn_sched_barrier(0);
            }
            v2u o = {pack2(acc[0], acc[1]), pack2(acc[2], acc[3])};
            *(LAS v2u*)(vl + (16 * tt + rho) * VS + (16 * w + 4 * bg) * 2) = o;
        }
        DRAIN();
        LBAR();
        if (tid == 0 && blk >= 6) ST4((u32)(blk - 2), rw, (FL_V + 2 * h + par) * 128);
        if (blk + 2 < 32) xstore(st, xl, tid);
#pragma unroll
        for (int sg = 0; sg < 2; ++sg) {
            bf16x8 Af[2][4];
#pragma unroll
            for (int half = 0; half < 2; ++half)
#pragma unroll
                for (int ks = 0; ks < 4; ++ks) Af[half][ks] = lds8(vl + (32 * sg + rho + 16 * half) * VS + ks * 64 + bg * 16);
            const int s = blk * 2 + sg;
#pragma unroll
            for (int j = 0; j < 5; ++j) {
                if (j < 4 || w < 4) {
                    f32x4 d0 = {}, d1 = {};
#pragma unroll
                    for (int ks = 0; ks < 4; ++ks) { d0 = MFMA16(Af[0][ks], Bo[j][ks], d0); d1 = MFMA16(Af[1][ks], Bo[j][ks], d1); }
                    v4u o = {pack2(d0[0], d0[1]), pack2(d0[2], d0[3]), pack2(d1[0], d1[1]), pack2(d1[2], d1[3])};
                    if (!(a.mask & 1)) ST16(o, rw, vo0 + (s * NR + 8 * j) * (NH * 1024)); else asm volatile("" :: "v"(o));
                }
            }
        }
        const bool full = blk < 4 || blk >= 30;
        if (full) DRAIN();
        LBAR();
        if (tid == 0 && full) ST4((u32)blk, rw, (FL_V + 2 * h + par) * 128);
    }
}

__device__ __forceinline__ u32 rt32() { return (u32)__builtin_amdgcn_s_memrealtime(); }
__device__ __forceinline__ void wait_until(u32 t) {
    if ((int)(t - rt32()) > 1000) return;
    while ((int)(rt32() - t) < -24) __builtin_amdgcn_s_sleep(4);
    while ((int)(rt32() - t) < 0) {}
}
struct TK { u32 XA, XR, EA, ER; int dA0, dA1, dA2, dR0, dR1, dR2; int okA, okR; };
__device__ __forceinline__ int tk_est(int d0, int d1, int d2) { return max(min(d0, d1), min(max(d0, d1), d2)); }
template <int N>
__device__ __forceinline__ u32 beacon_max(rsrc_t rw, int fb, int slot0, int lane, int& budget) {
    const int vo = BCO(slot0 + (lane < N ? lane : 0)), v2 = BCO(64 + (lane & 7));
    u32 v, u = 0;
    for (;;) {
        v = LD4(rw, vo, fb);
        if (N > 64) u = LD4(rw, v2, fb);
        MB();
        if (__all(max(v, u) != 0xFFFFFFFFu)) break;
        if (--budget < 0) break;
        __builtin_amdgcn_s_sleep(1);
    }
    const u32 base = __builtin_amdgcn_readfirstlane(v);
    int d = (int)(v - base);
    if (N > 64) d = max(d, (int)(u - base));
    return base + (u32)wave_max(d);
}
struct BC2 { u32 v, u; };
__device__ __forceinline__ BC2 beacon_issue(rsrc_t rw, int fb, int lane) {
    BC2 b;
    b.v = LD4(rw, BCO(lane), fb);
    b.u = LD4(rw, BCO(64 + (lane & 7)), fb);
    MB();
    return b;
}
__device__ __forceinline__ u32 beacon_finish(BC2 b, rsrc_t rw, int fb, int lane, int& budget) {
    if (!__all(max(b.v, b.u) != 0xFFFFFFFFu)) return beacon_max<NR8>(rw, fb, 0, lane, budget);
    const u32 base = __builtin_amdgcn_readfirstlane(b.v);
    return base + (u32)wave_max(max((int)(b.v - base), (int)(b.u - base)));
}
__device__ __forceinline__ void tk_T0(TK& k, u32 T0, const Args& a) {
    if (k.okR && k.okA) { k.dR2 = k.dR1; k.dR1 = k.dR0; k.dR0 = (int)(T0 - k.ER); }
    k.okA = (int)(k.XA - T0) >= a.pad0 - a.pha;
    k.EA = k.okA ? k.XA : T0 + a.gap;
    k.XR = k.EA + tk_est(k.dA0, k.dA1, k.dA2) + a.pd;
}
__device__ __forceinline__ void tk_Tp(TK& k, u32 Tp, const Args& a) {
    if (k.okA && k.okR) { k.dA2 = k.dA1; k.dA1 = k.dA0; k.dA0 = (int)(Tp - k.EA); }
    k.okR = (int)(k.XR - Tp) >= a.pd - a.pha;
    k.ER = k.okR ? k.XR : Tp + a.gap;
    k.XA = k.ER + tk_est(k.dR0, k.dR1, k.dR2) + a.pad0;
}
__device__ __forceinline__ u32 tk_specA(const TK& k, const Args& a) { return k.XR + tk_est(k.dR0, k.dR1, k.dR2) + a.pad0; }
__device__ __forceinline__ u32 tk_specR(const TK& k, const Args& a) { return k.XA + tk_est(k.dA0, k.dA1, k.dA2) + a.pd; }
__device__ __forceinline__ void tk_init(TK& k) { const u32 n = rt32(); k.XA = k.XR = k.EA = k.ER = n - 100000u; k.dA0 = k.dA1 = k.dA2 = 130; k.dR0 = k.dR1 = k.dR2 = 80; k.okA = k.okR = 0; }

__device__ __forceinline__ void tk_post(lptr p, int tag, u32 t, int lane) { if (lane == 0) { v2u v = {(u32)tag, t}; *(volatile LAS v2u*)p = v; } }
__device__ __forceinline__ u32 tk_get(lptr p, int tag, int& budget) {
    for (;;) {
        const v2u v = *(volatile LAS v2u*)p;
        if ((int)__builtin_amdgcn_readfirstlane(v.x) >= tag || --budget < 0) return __builtin_amdgcn_readfirstlane(v.y);
        __builtin_amdgcn_s_sleep(2);
    }
}
__device__ __forceinline__ void tk_wait(lptr p, int tag, int& budget) {
    for (;;) {
        const v2u v = *(volatile LAS v2u*)p;
        const u32 X = __builtin_amdgcn_readfirstlane(v.y);
        const int dt = (int)(X - rt32());
        if (--budget < 0) return;
        const int tg = (int)__builtin_amdgcn_readfirstlane(v.x);
        if (tg >= tag) {
            if (dt > 1000 || tg > tag) return;
            if (dt <= 16) { while ((int)(rt32() - X) < 0) {} return; }
        }
        __builtin_amdgcn_s_sleep(2);
    }
}
constexpr int OOB = 0x70000000;
__device__ __forceinline__ void stage_mem0(const Args& a, lptr ml, int tid) {
    for (int c = tid; c < 576; c += 512) {
        const u32* p = (const u32*)(a.mem0 + (c & 7) * DM + 16 * (c >> 4) + 2 * ((c >> 3) & 1));
        v4u v = {p[0], p[2], p[4], p[6]};
        *(LAS v4u*)(ml + c * 16) = v;
    }
}
__device__ __forceinline__ void gather_mem(rsrc_t rw, int nb, lptr ml, int lane, int w, int& budget) {
    const int gi = w == 7 ? 5 : w;
    const int l0 = gi * 1536 + lane * 16, l1 = l0 + 1024;
    const int o0 = l0 < 8192 ? l0 : l0 + (N_MEMB - 8192), o1 = lane < 32 ? (l1 < 8192 ? l1 : l1 + (N_MEMB - 8192)) : OOB;
    lptr p1 = ml + (lane < 32 ? l1 : 14336 + lane * 16);
    v4u v0, v1;
    for (;;) {
        v0 = LD16(rw, o0, nb); v1 = LD16(rw, o1, nb);
        MB();
        if (__builtin_expect(__all(max(bad4(v0), bad4(v1)) != 0xFFFFFFFFu), 1)) break;
        if (--budget < 0) break;
    }
    *(LAS v4u*)(ml + l0) = v0; *(LAS v4u*)p1 = v1;
}
__device__ __forceinline__ int get_parity(rsrc_t rw, int lane, int& budget) {
    const int vo = (FL_PAR + (lane & 7)) * 128;
    u32 v;
    for (;;) {
        v = LD4(rw, vo, 0);
        MB();
        if (__all(v != 0xFFFFFFFFu) || --budget < 0) break;
        __builtin_amdgcn_s_sleep(8);
    }
    int pm = 0;
#pragma unroll
    for (int k = 0; k < 8; ++k) pm |= (__builtin_amdgcn_readlane((int)v, k) & 3) << (2 * k);
    return __builtin_popcount(pm) > 8 ? 0xFFFF : 0;
}
__device__ __forceinline__ void role_probe(rsrc_t rw) {
    const int lane = threadIdx.x & 63, w = __builtin_amdgcn_readfirstlane(threadIdx.x >> 6);
    u32 bits = 0;
    for (int gg = 0; gg < 2; ++gg) {
        int best[2] = {1 << 30, 1 << 30};
        for (int rep = 0; rep < 3; ++rep) {
#pragma unroll
            for (int p = 0; p < 2; ++p) {
                const int o = OFF_RING + (2 * w + gg) * 65536 + N_PROBE + p * 8192 + rep * 128;
                ST4(0xFFFFFFFFu, rw, lane == 0 ? o : OOB);
                DRAIN();
                const unsigned long long t0 = __builtin_amdgcn_s_memtime();
                asm volatile("s_waitcnt lgkmcnt(0)" ::: "memory");
                u32 v = LD4(rw, o, 0);
                asm volatile("s_waitcnt vmcnt(0)" : "+v"(v) :: "memory");
                const unsigned long long t1 = __builtin_amdgcn_s_memtime();
                best[p] = min(best[p], (int)(t1 - t0));
            }
        }
        bits |= (best[1] < best[0] ? 1u : 0u) << gg;
    }
    if (lane == 0) ST4(bits, rw, (FL_PAR + w) * 128);
}
__device__ __forceinline__ void qk_project(const bf16x16 (&Bq)[9], const bf16x16 (&Bk)[9], lptr ma, lptr xw, int idx) {
    f32x4 aq = {}, ak = {};
    bf16x8 A[9];
#pragma unroll
    for (int t = 0; t < 9; ++t) A[t] = lds8(ma + t * 1024);
    __builtin_amdgcn_sched_barrier(0);
#pragma unroll
    for (int t = 0; t < 9; ++t) { aq = SMF(A[t], Bq[t], aq, idx); ak = SMF(A[t], Bk[t], ak, idx); }
    const float s0 = swap32_sum(aq[0], ak[0]), s1 = swap32_sum(aq[1], ak[1]), s2 = swap32_sum(aq[2], ak[2]), s3 = swap32_sum(aq[3], ak[3]);
    v2u o = {pack2(s0, s1), pack2(s2, s3)};
    *(LAS v2u*)xw = o;
}

__device__ __forceinline__ void role_qk(const Args& a, lptr lds, rsrc_t rw, int h, int rep) {
    const int tid = threadIdx.x, lane = tid & 63, w = __builtin_amdgcn_readfirstlane(tid >> 6);
    bf16x16 Bq[9], Bk[9];
    for (int i = 0; i < a.phz; ++i) __builtin_amdgcn_s_sleep(16);
    {
        v4u v[6][6];
#pragma unroll
        for (int c = 0; c < 6; ++c) rows_ld<192, 16>(v[c], (c >= 3 ? a.Wk : a.Wq) + (size_t)(c % 3) * 192 * 4096 + h * DH, 4096, tid);
        __builtin_amdgcn_sched_barrier(0);
#pragma unroll
        for (int c = 0; c < 6; ++c) {
            lptr l = lds + (c & 1) * 77824;
            rows_st<192, 16>(v[c], l, 288, tid);
            LBAR();
#pragma unroll
            for (int t = 0; t < 3; ++t) {
                bf16x16 f = cat16(frag16(l + (64 * t) * 288 + w * 32, 288, lane), frag16(l + (64 * t + 32) * 288 + w * 32, 288, lane));
                if (c >= 3) Bk[(c % 3) * 3 + t] = f; else Bq[c * 3 + t] = f;
            }
            __builtin_amdgcn_sched_barrier(0);
        }
        LBAR();
    }
    lptr ml = lds, xl = lds + MEMB;
    const int idx = (lane & 8) ? 0xEEEE : 0x4444;
    const int i16 = lane & 15, g = lane >> 4;
    const bool hi8 = (lane & 8) != 0;
    int budget = SPIN_MAX;
    lptr pq = xl + (8 * g + (i16 >> 2)) * 32 + (i16 & 1) * 8;
    lptr kl = lds + 16384 + lane * 16;
    lptr ma = ml + lane * 16, xw = xl + (16 * w + i16) * 32 + g * 8;
    const int ox = N_PX + (h >> 4) * 16384 + (h & 15) * 512 + ((g & 1) * 16 + (g >> 1) * 8 + (i16 & 7)) * 16;
    const int om = hi8 ? OOB : N_PMM + h * 128 + (i16 & 7) * 16;
    const int ob = lane < NREP ? BCO(BC_QK + h) : OOB;
    const int pm = get_parity(rw, lane, budget);
    lptr tkp = lds + MEMB + 4096, kfp = tkp + 64;
    if (tid == 0) { tk_post(tkp, 0, rt32() - 100000u, 0); tk_post(kfp, -1, 0u, 0); }
    stage_mem0(a, ml, tid);
    __syncthreads();
    STAMP_INIT
    if (w == 0) {
        const float c2 = 0.08838834764831845f * 1.4426950408889634f;
        for (int s = 0; s < NSEG - 1; ++s) {
            tk_get(kfp, s, budget);
            bf16x8 kx[2][4];
#pragma unroll
            for (int tl = 0; tl < 2; ++tl)
#pragma unroll
                for (int ks = 0; ks < 4; ++ks) kx[tl][ks] = lds8(kl + (tl * 4 + ks) * 1024);
            int oxs = ox + NEARB(s, i16 >> 3, pm), oxt = ox + NEARB(s, 2 + (i16 >> 3), pm), oms = om + NEARB(s, g, pm), obs = ob + FARB(s, lane & 3, pm);
            const int nb = NEARB(s, rep, pm);
            asm volatile("s_waitcnt lgkmcnt(0)" : "+v"(oxs), "+v"(oxt), "+v"(oms), "+v"(obs) :: "memory");
#pragma unroll
            for (int tl = 0; tl < 2; ++tl)
#pragma unroll
                for (int ks = 0; ks < 4; ++ks) PIN4(kx[tl][ks]);
            if (s > 0) {
                tk_wait(tkp, s, budget);
                STAMP(0);
                gather_mem(rw, nb, ml, lane, 0, budget);
            }
            STAMP(1);
            LBAR();
            STAMP(2);
            qk_project(Bq, Bk, ma, xw, idx);
            STAMP(3);
            __builtin_amdgcn_s_setprio(3);
            LBAR();
            STAMP(4);
            bf16x8 Fq[4], Fk[4];
#pragma unroll
            for (int ks = 0; ks < 4; ++ks) Fq[ks] = as8(trd(pq + ks * 1024), trd(pq + ks * 1024 + 128));
            __builtin_amdgcn_sched_barrier(0);
#pragma unroll
            for (int ks = 0; ks < 4; ++ks) Fk[ks] = as8(trd(pq + ks * 1024 + 16), trd(pq + ks * 1024 + 16 + 128));
            __builtin_amdgcn_sched_barrier(0);
            f32x4 S0 = {}, S1 = {}, Sm = {};
            S0 = MFMA16(kx[0][0], Fq[0], S0); S1 = MFMA16(kx[1][0], Fq[0], S1);
            S0 = MFMA16(kx[0][1], Fq[1], S0); S1 = MFMA16(kx[1][1], Fq[1], S1);
            __builtin_amdgcn_sched_barrier(0);
            Sm = MFMA16(Fk[0], Fq[0], Sm); S0 = MFMA16(kx[0][2], Fq[2], S0);
            Sm = MFMA16(Fk[1], Fq[1], Sm); S1 = MFMA16(kx[1][2], Fq[2], S1);
            Sm = MFMA16(Fk[2], Fq[2], Sm); S0 = MFMA16(kx[0][3], Fq[3], S0);
            Sm = MFMA16(Fk[3], Fq[3], Sm); S1 = MFMA16(kx[1][3], Fq[3], S1);
            __builtin_amdgcn_sched_barrier(0);
            float y0 = hi8 ? Sm[2] : Sm[0], y1 = hi8 ? Sm[3] : Sm[1];
            float x0 = hi8 ? S1[0] : S0[0], x1 = hi8 ? S1[1] : S0[1], x2 = hi8 ? S1[2] : S0[2], x3 = hi8 ? S1[3] : S0[3];
            float m = vmax3(vmax3(y0, y1, x0), x1, vmax(x2, x3));
            m = vmax(m, ror8f(m));
            { auto r = __builtin_amdgcn_permlane16_swap(__float_as_uint(m), __float_as_uint(m), false, false); m = vmax(__uint_as_float(r[0]), __uint_as_float(r[1])); }
            { auto r = __builtin_amdgcn_permlane32_swap(__float_as_uint(m), __float_as_uint(m), false, false); m = vmax(__uint_as_float(r[0]), __uint_as_float(r[1])); }
            STAMP(5);
            const float nm = -m * c2;
            x0 = __builtin_amdgcn_exp2f(fmaf(x0, c2, nm)); x1 = __builtin_amdgcn_exp2f(fmaf(x1, c2, nm)); x2 = __builtin_amdgcn_exp2f(fmaf(x2, c2, nm)); x3 = __builtin_amdgcn_exp2f(fmaf(x3, c2, nm));
            y0 = __builtin_amdgcn_exp2f(fmaf(y0, c2, nm)); y1 = __builtin_amdgcn_exp2f(fmaf(y1, c2, nm));
            float sum = fmaf(0.5f, y0 + y1, (x0 + x1) + (x2 + x3));
            sum += ror8f(sum);
            sum = xsum(sum);
            const float inv = __builtin_amdgcn_rcpf(sum);
            const u32 pa = pack2(x0 * inv, x1 * inv) & 0x7fff7fffu, pb = pack2(x2 * inv, x3 * inv) & 0x7fff7fffu, pc = pack2(y0 * inv, y1 * inv) & 0x7fff7fffu;
            const u32 qa = ror8(pa), qb = ror8(pb), qc = ror8(pc);
            v4u px = {hi8 ? qa : pa, hi8 ? qb : pb, hi8 ? pa : qa, hi8 ? pb : qb};
            ST16(px, rw, oxs);
            ST16(px, rw, oxt);
            auto ra = __builtin_amdgcn_permlane16_swap(pc, pc, false, false);
            auto rc = __builtin_amdgcn_permlane16_swap(qc, qc, false, false);
            v4u pmv = {ra[0], rc[0], ra[1], rc[1]};
            ST16(pmv, rw, oms);
            STAMP(6);
            __builtin_amdgcn_sched_barrier(0);
            ST4(nos(rt32()), rw, obs);
            __builtin_amdgcn_s_setprio(0);
            STAMP_FLUSH;
        }
        {
            tk_wait(tkp, NSEG - 1, budget);
            gather_mem(rw, NEARB(NSEG - 1, rep, pm), ml, lane, 0, budget);
            LBAR();
            qk_project(Bq, Bk, ma, xw, idx);
            LBAR();
        }
    } else {
        TK tk; tk_init(tk);
        int kdone = -1;
        const int kxo = OFF_KX + h * (NSEG * 8192) + g * 512 + i16 * 16;
        const int kmo = i16 < 8 ? OFF_KM + h * 2048 + g * 128 + i16 * 16 : OOB;
        for (int s = 0; s < NSEG; ++s) {
            if (w == 5) {
                if (s < NSEG - 1) {
                    if (kdone < (s >> 1)) kdone = wait_flags(rw, FL_K + (h >> 1), 1, 1, s >> 1, lane, budget);
                    v4u kx[8];
#pragma unroll
                    for (int k = 0; k < 8; ++k) kx[k] = LD16(rw, kxo + (k >> 2) * 256 + (k & 3) * 2048, s * 8192);
                    const v4u ff = {0xFFFFFFFFu, 0xFFFFFFFFu, 0xFFFFFFFFu, 0xFFFFFFFFu};
                    ST16(ff, rw, ox + NEARB(s + 2, i16 >> 3, pm)); ST16(ff, rw, ox + NEARB(s + 2, 2 + (i16 >> 3), pm));
                    ST16(ff, rw, om + NEARB(s + 2, g, pm));
                    ST4(0xFFFFFFFFu, rw, ob + FARB(s + 4, lane & 3, pm));
#pragma unroll
                    for (int k = 0; k < 8; ++k) *(LAS v4u*)(kl + k * 1024) = kx[k];
                    asm volatile("s_waitcnt lgkmcnt(0)" ::: "memory");
                    tk_post(kfp, s, 0u, lane);
                    DRAIN();
                }
            }
            if (s > 0 && w != 5 && w != 6) {
                tk_wait(tkp, s, budget);
                gather_mem(rw, NEARB(s, rep, pm), ml, lane, w, budget);
            }
            BC2 bc = {0u, 0u};
            if (w == 3) bc = beacon_issue(rw, FARB(s, rep, pm), lane);
            LBAR();
            qk_project(Bq, Bk, ma, xw, idx);
            LBAR();
            if (w == 6) {
                bf16x8 F[4];
#pragma unroll
                for (int ks = 0; ks < 4; ++ks) F[ks] = as8(trd(pq + ks * 1024 + 16), trd(pq + ks * 1024 + 16 + 128));
#pragma unroll
                for (int ks = 0; ks < 4; ++ks) ST16(asu(F[ks]), rw, kmo + s * (NH * 2048) + ks * 512);
                DRAIN();
                if (lane == 0) ST4((u32)s, rw, (FL_M + h) * 128);
            } else if (w == 3) {
                if (s < NSEG - 1) {
                    if (s > 0) { tk_T0(tk, beacon_finish(bc, rw, FARB(s, rep, pm), lane, budget), a); tk_post(tkp, s + 1, tk_specA(tk, a), lane); }
                    wait_until(tk.XR + 8);
                    tk_Tp(tk, beacon_max<NH>(rw, FARB(s, rep, pm), BC_QK, lane, budget), a);
                    tk_post(tkp, s + 1, tk.XA, lane);
                }
            } else if (w != 5) __builtin_amdgcn_s_sleep(14);
        }
    }
    { const int bid_ = rep + 8 * (h - 8 * rep); (void)bid_; STAMP_FIN; }
    if (budget < 0 && lane == 0) ST4(1u, rw, FL_ERR * 128);
}

__device__ __forceinline__ void role_wvo(const Args& a, lptr lds, rsrc_t rw, int idx) {
    const int tid = threadIdx.x, lane = tid & 63, w = __builtin_amdgcn_readfirstlane(tid >> 6);
    const int h = idx / 3, part = idx - 3 * h;
    bf16x8 Bo[5][4];
    v4u v[6], o[2][9];
#pragma unroll
    for (int c = 0; c < 2; ++c) rows_ld<64, 72>(o[c], a.Wo + ((size_t)h * DH + c * 64) * DM, DM, tid);
    rows_ld<192, 16>(v, a.Wv + (size_t)part * 192 * 4096 + h * DH, 4096, tid);
    __builtin_amdgcn_sched_barrier(0);
#pragma unroll
    for (int c = 0; c < 2; ++c) {
        lptr l = lds + c * 77824;
        rows_st<64, 72>(o[c], l, 1184, tid);
        LBAR();
#pragma unroll
        for (int j = 0; j < 5; ++j) {
            const int r = (w + 8 * j < NR) ? w + 8 * j : w;
#pragma unroll
            for (int ks = 0; ks < 2; ++ks) Bo[j][c * 2 + ks] = frag16(l + ks * 32 * 1184 + r * 32, 1184, lane);
        }
        __builtin_amdgcn_sched_barrier(0);
    }
    rows_st<192, 16>(v, lds, VS, tid);
    LBAR();
    const int rho = lane & 15, bg = lane >> 4;
    const int wo0 = OFF_WVO + (2 * w + ((rho >> 1) & 1)) * 294912 + ((h >> 1) * 18 + part * 6) * 1024 + (bg * 16 + (h & 1) * 8 + 2 * (rho >> 2) + (rho & 1)) * 16;
#pragma unroll 1
    for (int kb = 0; kb < 6; ++kb) {
        bf16x8 Af[2][4];
#pragma unroll
        for (int mt = 0; mt < 2; ++mt)
#pragma unroll
            for (int ks = 0; ks < 4; ++ks) Af[mt][ks] = lds8(lds + (32 * kb + 8 * (rho >> 2) + 4 * mt + (rho & 3)) * VS + ks * 64 + bg * 16);
#pragma unroll
        for (int j = 0; j < 5; ++j) {
            if (j < 4 || w < 4) {
                f32x4 d0 = {}, d1 = {};
#pragma unroll
                for (int ks = 0; ks < 4; ++ks) { d0 = MFMA16(Af[0][ks], Bo[j][ks], d0); d1 = MFMA16(Af[1][ks], Bo[j][ks], d1); }
                v4u q = {pack2(d0[0], d0[1]), pack2(d0[2], d0[3]), pack2(d1[0], d1[1]), pack2(d1[2], d1[3])};
                ST16(q, rw, wo0 + j * (16 * 294912) + kb * 1024);
            }
        }
    }
    DRAIN();
    LBAR();
    if (tid == 0) ST4(1u, rw, (FL_W + idx) * 128);
}

__device__ __forceinline__ void role_rz(const Args& a, lptr lds, rsrc_t rw, int r, int rep) {
    const int tid = threadIdx.x, lane = tid & 63, w = __builtin_amdgcn_readfirstlane(tid >> 6);
    const int idx = (lane & 8) ? 0xEEEE : 0x4444;
    const int i16 = lane & 15, g = lane >> 4;
    int budget = SPIN_MAX;
    wait_flags(rw, FL_W + 12 * w, 1, 12, 1, lane, budget);
    bf16x16 Ba[9], Bb[9];
    {
        const int wo = OFF_WVO + r * 294912 + 2 * w * 18432 + lane * 16;
#pragma unroll
        for (int t = 0; t < 9; ++t) {
            Ba[t] = cat16(as8(LD16(rw, wo, t * 2048)), as8(LD16(rw, wo + 1024, t * 2048)));
            Bb[t] = cat16(as8(LD16(rw, wo, 18432 + t * 2048)), as8(LD16(rw, wo + 1024, 18432 + t * 2048)));
        }
    }
    lptr ml = lds, zl = lds + MEMB;
    lptr tka = lds + 13312, tkr = tka + 16, cntp = tka + 64;
    volatile LAS int* prod = (volatile LAS int*)(lds + 13312 + 512);
    lptr ring = lds + 16384;
    lptr ma = ml + lane * 16;
    lptr zw = zl + ((2 * w + (lane >> 5)) >> 2) * 1024 + ((((2 * w + (lane >> 5)) & 3) * 16 + (g & 1) * 8 + (i16 & 7)) * 16) + (i16 >> 3) * 8;
    const int mso = i16 < 8 ? (r < 64 ? N_MEMA + r * 128 : N_MEMB + (r - 64) * 128) + i16 * 16 : OOB;
    const int ob = lane < NREP ? BCO(r) : OOB;
    const int pm = get_parity(rw, lane, budget);
    if (tid == 0) { tk_post(tka, 0, rt32() - 100000u, 0); tk_post(tkr, -1, 0u, 0); *(volatile LAS int*)cntp = 0; }
    if (tid < 4) prod[tid] = 0;
    stage_mem0(a, ml, tid);
    __syncthreads();
    STAMP_INIT
    if (w == 0 || w == 2) {
        const int dw = w >> 1;
        const int po = N_PX + dw * 16384 + lane * 16;
        const int mo = N_PMM + dw * 2048 + 2 * g * 128 + (i16 & 7) * 16 + (i16 >> 3) * 8;
        lptr cna = lane == 0 ? cntp : cntp + 4 + lane * 4;
        const u32 one = lane == 0 ? 1u : 0u;
        const bool dv = (g >> 1) == (i16 >> 3);
        const int rwo = dv ? (dw * 2 + (g >> 1)) * 256 + (g & 1) * 128 + (i16 & 7) * 4 : 2048 + lane * 4;
        for (int s = 0; s < NSEG; ++s) {
            if (s > 0) {
                tk_wait(tka, s, budget);
                gather_mem(rw, NEARB(s, rep, pm), ml, lane, w, budget);
            }
            LBAR();
            qk_project(Ba, Bb, ma, zw, idx);
            LBAR();
            if (s == NSEG - 1) break;
            lptr Rw = lds + 81920 + (s & 1) * 1024 + rwo, Rr = lds + 81920 + (s & 1) * 1024 + (i16 & 7) * 32;
            int mss = mso + NEARB(s + 1, g, pm), obs = ob + FARB(s + 1, lane & 3, pm);
            const int sb = NEARB(s, rep, pm);
            while (__builtin_amdgcn_readfirstlane(prod[dw]) <= s && --budget >= 0) __builtin_amdgcn_s_sleep(1);
            MB();
            lptr rg = ring + (s & 3) * 16384 + dw * 8192 + lane * 16;
            asm volatile("" : "+v"(mss), "+v"(obs), "+v"(Rw), "+v"(Rr), "+v"(rg));
            __builtin_amdgcn_s_setprio(3);
            tk_wait(tkr, s, budget);
            STAMP(1);
            f32x4 d;
            for (;;) {
                v4u p[8]; v2u m[4];
#pragma unroll
                for (int k = 0; k < 4; ++k) { m[k] = LD8(rw, mo + (k >> 1) * 1024 + (k & 1) * 128, sb); __builtin_amdgcn_sched_barrier(0); }
#pragma unroll
                for (int k = 0; k < 8; ++k) { p[k] = LD16(rw, po + (k & 3) * 1024, sb + (k >> 2) * 4096); __builtin_amdgcn_sched_barrier(0); }
                MB();
                bf16x8 b[6];
                b[4] = lds8(zl + dw * 2048 + lane * 16); b[5] = lds8(zl + dw * 2048 + 1024 + lane * 16);
#pragma unroll
                for (int k = 0; k < 4; ++k) b[k] = lds8(rg + k * 1024);
                __builtin_amdgcn_sched_barrier(0);
                d = f32x4{};
                d = MFMA16(as8(m[0], m[1]), b[4], d);
                d = MFMA16(as8(m[2], m[3]), b[5], d);
                b[4] = lds8(rg + 4096); b[5] = lds8(rg + 5120);
                u32 bad = ((m[0].x | m[0].y) | (m[1].x | m[1].y)) | ((m[2].x | m[2].y) | (m[3].x | m[3].y));
                __builtin_amdgcn_sched_barrier(0);
#pragma unroll
                for (int k = 0; k < 8; ++k) {
                    d = MFMA16(as8(p[k]), b[k % 6], d);
                    if (k < 2) b[k] = lds8(rg + (6 + k) * 1024);
                    bad |= (p[k].x | p[k].y) | (p[k].z | p[k].w);
                    __builtin_amdgcn_sched_barrier(0);
                }
                if (__builtin_expect(__all((int)bad >= 0), 1)) break;
                if (--budget < 0) break;
            }
            STAMP(2);
            f32x4 x[8]; u32 old;
            const float d0 = d[0] + 0.0f, d1 = d[1] + 0.0f, d2 = d[2] + 0.0f, d3 = d[3] + 0.0f;
            asm volatile("ds_write2_b32 %10, %11, %12 offset1:8\n\tds_write2_b32 %10, %13, %14 offset0:16 offset1:24\n\tds_add_rtn_u32 %0, %15, %16\n\t"
                         "ds_read_b128 %1, %9\n\tds_read_b128 %2, %9 offset:16\n\tds_read_b128 %3, %9 offset:256\n\tds_read_b128 %4, %9 offset:272\n\t"
                         "ds_read_b128 %5, %9 offset:512\n\tds_read_b128 %6, %9 offset:528\n\tds_read_b128 %7, %9 offset:768\n\tds_read_b128 %8, %9 offset:784\n\ts_waitcnt lgkmcnt(0)"
                         : "=&v"(old), "=&v"(x[0]), "=&v"(x[1]), "=&v"(x[2]), "=&v"(x[3]), "=&v"(x[4]), "=&v"(x[5]), "=&v"(x[6]), "=&v"(x[7])
                         : "v"(Rr), "v"(Rw), "v"(d0), "v"(d1), "v"(d2), "v"(d3), "v"(cna), "v"(one) : "memory");
            STAMP(4);
            if (__builtin_amdgcn_readfirstlane(old) & 1) {
                const f32x4 c0 = (x[0] + x[2]) + (x[4] + x[6]), c1 = (x[1] + x[3]) + (x[5] + x[7]);
                v4u o = {nos(pack2(c0[0], c0[1])), nos(pack2(c0[2], c0[3])), nos(pack2(c1[0], c1[1])), nos(pack2(c1[2], c1[3]))};
                ST16(o, rw, mss);
                __builtin_amdgcn_sched_barrier(0);
                ST4(nos(rt32()), rw, obs);
            }
            STAMP(5);
            __builtin_amdgcn_s_setprio(0);
            STAMP_FLUSH;
        }
    } else if (w == 5 || w == 6) {
        int vdone0 = -1, vdone1 = -1, staged = 0;
        const int hw = w - 5;
        const int vo = OFF_VOX + r * 16384 + hw * 8192 + lane * 16;
        const int zho = OFF_ZH + r * 4096 + hw * 2048 + lane * 16;
        lptr zhr = zl + hw * 2048 + (((lane >> 4) & 3) * 16 + (lane & 7)) * 16 + ((lane >> 3) & 1) * 8;
        const int aro = w == 5 ? mso : OOB, arb = w == 5 ? ob : OOB;
        v4u zv[2] = {};
        for (int s = 0; s < NSEG; ++s) {
            bool go = false;
            if (staged <= min(s + 3, NSEG - 2)) {
                const int blk = staged >> 1;
                int& vd = (blk & 1) ? vdone1 : vdone0;
                if (vd < blk) vd = wait_flags(rw, FL_V + (blk & 1), 2, NH, -1, lane, budget);
                go = vd >= blk;
            }
            const int vos = go ? vo : OOB;
            LBAR();
            v4u v[8];
#pragma unroll
            for (int k = 0; k < 8; ++k) v[k] = LD16(rw, vos + (k & 3) * 1024, staged * (NR * NH * 1024) + (k >> 2) * 4096);
            {
                const int zs = s > 0 ? zho + (s - 1) * (NR * NH * 256) : OOB;
                ST16(zv[0], rw, zs); ST16(zv[1], rw, zs + 1024);
                const v4u ff = {0xFFFFFFFFu, 0xFFFFFFFFu, 0xFFFFFFFFu, 0xFFFFFFFFu};
                ST16(ff, rw, aro + NEARB(s + 3, g, pm));
                ST4(0xFFFFFFFFu, rw, arb + FARB(s + 4, lane & 3, pm));
            }
            MB();
            qk_project(Ba, Bb, ma, zw, idx);
            LBAR();
#pragma unroll
            for (int k = 0; k < 2; ++k) {
                const v2u lo = *(LAS v2u*)(zhr + k * 1024), hi = *(LAS v2u*)(zhr + k * 1024 + 128);
                zv[k] = v4u{lo.x, lo.y, hi.x, hi.y};
            }
            for (;;) {
                if (go) {
#pragma unroll
                    for (int k = 0; k < 8; ++k) *(LAS v4u*)(ring + (staged & 3) * 16384 + hw * 8192 + k * 1024 + lane * 16) = v[k];
                    asm volatile("s_waitcnt lgkmcnt(0)" ::: "memory");
                    ++staged;
                    prod[hw] = staged;
                }
                if (staged > s + 1 || staged > NSEG - 2) break;
                const int blk = staged >> 1;
                int& vd = (blk & 1) ? vdone1 : vdone0;
                if (vd < blk) vd = wait_flags(rw, FL_V + (blk & 1), 2, NH, staged <= s ? blk : -1, lane, budget);
                if (vd < blk) break;
#pragma unroll
                for (int k = 0; k < 8; ++k) v[k] = LD16(rw, vo + (k & 3) * 1024, staged * (NR * NH * 1024) + (k >> 2) * 4096);
                go = true;
            }
            DRAIN();
        }
        ST16(zv[0], rw, zho + (NSEG - 1) * (NR * NH * 256)); ST16(zv[1], rw, zho + (NSEG - 1) * (NR * NH * 256) + 1024);
    } else {
        TK tk; tk_init(tk);
        for (int s = 0; s < NSEG; ++s) {
            if (s > 0) {
                tk_wait(tka, s, budget);
                gather_mem(rw, NEARB(s, rep, pm), ml, lane, w, budget);
            }
            BC2 bc = {0u, 0u};
            if (w == 3) bc = beacon_issue(rw, FARB(s, rep, pm), lane);
            LBAR();
            qk_project(Ba, Bb, ma, zw, idx);
            LBAR();
            if (w == 3) {
                if (s < NSEG - 1) {
                    if (s > 0) { tk_T0(tk, beacon_finish(bc, rw, FARB(s, rep, pm), lane, budget), a); tk_post(tka, s + 1, tk_specA(tk, a), lane); }
                    tk_post(tkr, s, tk.XR, lane);
                    wait_until(tk.XR + 8);
                    tk_Tp(tk, beacon_max<NH>(rw, FARB(s, rep, pm), BC_QK, lane, budget), a);
                    tk_post(tka, s + 1, tk.XA, lane);
                    tk_post(tkr, s + 1, tk_specR(tk, a), lane);
                }
            } else {
                if (w == 1 && s >= 2 && lane == 0) ST4((u32)(s - 2), rw, (FL_Z + r) * 128);
                __builtin_amdgcn_s_sleep(14);
            }
        }
    }
    DRAIN();
    LBAR();
    if (tid == 0) ST4((u32)(NSEG - 1), rw, (FL_Z + r) * 128);
    { const int bid_ = rep + 8 * (8 + r - 18 * rep); (void)bid_; STAMP_FIN; }
    if (budget < 0 && lane == 0) ST4(3u, rw, FL_ERR * 128);
}

__device__ __forceinline__ void cexp_l(float lr, float li, float l, float& er, float& ei) {
    const float mag = __builtin_amdgcn_exp2f(lr * l);
    const float ph = li * l;
    const float pe = fmaf(li, l, -ph);
    const float fr = (ph - floorf(ph)) + pe;
    er = mag * __builtin_amdgcn_cosf(fr);
    ei = mag * __builtin_amdgcn_sinf(fr);
}
constexpr int CV_W = 8 * 260 * 16, CV_U = 96 * 80, CV_CH = CV_W + CV_U;
__device__ __forceinline__ void s4_unit(const Args& a, lptr lds, rsrc_t rw, int unit) {
    const int tid = threadIdx.x, lane = tid & 63, w = __builtin_amdgcn_readfirstlane(tid >> 6);
    const int d0 = unit * 4;
    v2u uv[4];
#pragma unroll
    for (int k = 0; k < 4; ++k) uv[k] = *(const v2u*)(a.u + (size_t)(2 * (tid + 512 * (k >> 1)) + (k & 1)) * DM + d0);
    {
        const int c = tid >> 7, nn = tid & 127, n = (d0 + c) * 128 + nn;
        const float dt = rbf(expf(bf2f(a.logdt[d0 + c])));
        const float lr = dt * bf2f(a.Are[n]) * 1.4426950408889634f, li = dt * bf2f(a.Aim[n]) * 0.15915494309189535f;
        const float br = dt * bf2f(a.Bre[n]), bi = dt * bf2f(a.Bim[n]);
        const float cr = bf2f(a.Cre[n]), ci = bf2f(a.Cim[n]);
        LAS f32x2* T = (LAS f32x2*)(lds + c * CV_CH) + nn;
        float er, ei, pr, pi;
        cexp_l(lr, li, 1.0f, er, ei); pr = er; pi = ei;
#pragma unroll
        for (int o = 0; o < 7; ++o) { T[o * 130] = f32x2{pr, pi}; const float t = pr * er - pi * ei; pi = pr * ei + pi * er; pr = t; }
#pragma unroll
        for (int q4 = 0; q4 < 4; ++q4) {
            if (q4 == 0) cexp_l(lr, li, 128.0f, er, ei);
            cexp_l(lr, li, (float)(128 + 512 * q4), pr, pi);
#pragma unroll
            for (int o = 0; o < 4; ++o) { T[(7 + 4 * q4 + o) * 130] = f32x2{pr, pi}; const float t = pr * er - pi * ei; pi = pr * ei + pi * er; pr = t; }
        }
        const float cbr = cr * br - ci * bi, cbi = cr * bi + ci * br;
        cexp_l(lr, li, 8.0f, er, ei);
#pragma unroll
        for (int q4 = 0; q4 < 4; ++q4) {
            float sr, si;
            cexp_l(lr, li, (float)(32 * q4), sr, si);
            pr = cbr * sr - cbi * si; pi = cbr * si + cbi * sr;
#pragma unroll
            for (int o = 0; o < 4; ++o) { T[(23 + 4 * q4 + o) * 130] = f32x2{pr, pi}; const float t = pr * er - pi * ei; pi = pr * ei + pi * er; pr = t; }
        }
    }
    __syncthreads();
    f32x4 kr[3];
    {
        const int x = lane & 15, g = lane >> 4, c = w & 3, nh = w >> 2;
        const int ah0 = x >> 3, o0 = x & 7;
        const int ah1 = x < 8 ? 2 + (x >> 2) : 4 + ((x - 8) >> 1), o1 = x < 8 ? 2 * (x & 3) : 4 * (x & 1);
        const int ah2 = x < 8 ? 8 + x : 16;
        LAS f32x2* T = (LAS f32x2*)(lds + c * CV_CH) + nh * 64 + g;
        LAS f32x2* Ta0 = T + 7 * 130; LAS f32x2* To0 = T + (o0 ? o0 - 1 : 0) * 130;
        LAS f32x2* Ta1 = T + (6 + ah1) * 130; LAS f32x2* To1 = T + (o1 ? o1 - 1 : 0) * 130;
        LAS f32x2* Ta2 = T + (6 + ah2) * 130; LAS f32x2* Tv = T + (23 + x) * 130;
        const f32x2 one = {1.0f, 0.0f};
        f32x4 ar[3] = {}, ai[3] = {};
#pragma unroll
        for (int jj = 0; jj < 16; ++jj) {
            const f32x2 v = Tv[4 * jj], u2 = Ta2[4 * jj], a1 = Ta1[4 * jj];
            f32x2 a0v = Ta0[4 * jj], e0 = To0[4 * jj], e1 = To1[4 * jj];
            a0v = ah0 ? a0v : one; e0 = o0 ? e0 : one; e1 = o1 ? e1 : one;
            const float u0r = a0v[0] * e0[0] - a0v[1] * e0[1], u0i = a0v[0] * e0[1] + a0v[1] * e0[0];
            const float u1r = a1[0] * e1[0] - a1[1] * e1[1], u1i = a1[0] * e1[1] + a1[1] * e1[0];
            ar[2] = __builtin_amdgcn_mfma_f32_16x16x4f32(u2[0], v[0], ar[2], 0, 0, 0);
            ai[2] = __builtin_amdgcn_mfma_f32_16x16x4f32(u2[1], v[1], ai[2], 0, 0, 0);
            ar[0] = __builtin_amdgcn_mfma_f32_16x16x4f32(u0r, v[0], ar[0], 0, 0, 0);
            ai[0] = __builtin_amdgcn_mfma_f32_16x16x4f32(u0i, v[1], ai[0], 0, 0, 0);
            ar[1] = __builtin_amdgcn_mfma_f32_16x16x4f32(u1r, v[0], ar[1], 0, 0, 0);
            ai[1] = __builtin_amdgcn_mfma_f32_16x16x4f32(u1i, v[1], ai[1], 0, 0, 0);
        }
#pragma unroll
        for (int t = 0; t < 3; ++t) kr[t] = ar[t] - ai[t];
    }
    __syncthreads();
    {
        LAS float* T = (LAS float*)(lds + (w & 3) * CV_CH + 4096 + (w >> 2) * 3072);
#pragma unroll
        for (int t = 0; t < 3; ++t)
#pragma unroll
            for (int r = 0; r < 4; ++r) T[(16 * t + 4 * (lane >> 4) + r) * 16 + (lane & 15)] = kr[t][r];
    }
    __syncthreads();
#pragma unroll
    for (int k = 0; k < 8; ++k) {
        const int i = tid + 512 * k, c = i >> 10, l2 = i & 1023;
        LAS float* T = (LAS float*)(lds + c * CV_CH + 4096);
        float v[2];
#pragma unroll
        for (int e = 0; e < 2; ++e) {
            const int lb = (int)rbf((float)(2 * l2 + e));
            const int hi = lb >> 7, o = lb & 7;
            const int row = lb < 256 ? 8 * hi + o : lb < 512 ? 16 + 4 * (hi - 2) + (o >> 1) : lb < 1024 ? 24 + 2 * (hi - 4) + (o >> 2) : 32 + (hi - 8);
            const int ix = row * 16 + ((lb >> 3) & 15);
            v[e] = T[ix] + T[768 + ix];
        }
        *(LAS u32*)(lds + c * CV_CH + CV_W + 2704 + l2 * 4) = pack2(v[0], v[1]);
    }
    __syncthreads();
    for (int i = tid; i < 4 * 259; i += 512) {
        const int c = i / 259, zz = i - c * 259, z = zz - 3;
        lptr ch = lds + c * CV_CH;
        v4u lo = {0, 0, 0, 0}, hi = {0, 0, 0, 0};
        if (z >= 1) lo = *(LAS v4u*)(ch + CV_W + 2704 + (z - 1) * 16);
        if (z >= 0) hi = *(LAS v4u*)(ch + CV_W + 2704 + z * 16);
        const u32 e[8] = {lo.x, lo.y, lo.z, lo.w, hi.x, hi.y, hi.z, hi.w};
#pragma unroll
        for (int cc = 0; cc < 8; ++cc) {
            v4u o;
#pragma unroll
            for (int k = 0; k < 4; ++k) {
                const int t = 8 + cc - 2 * k;
                if (t & 1) o[k] = (e[t >> 1] >> 16) | (e[t >> 1] << 16);
                else o[k] = (e[t >> 1] & 0xffffu) | (e[(t >> 1) - 1] & 0xffff0000u);
            }
            *(LAS v4u*)(ch + (cc * 260 + zz) * 16) = o;
        }
    }
    __syncthreads();
    for (int i = tid; i < 4 * 160; i += 512) *(LAS v4u*)(lds + (i / 160) * CV_CH + CV_W + (i % 160) * 16) = v4u{0, 0, 0, 0};
#pragma unroll
    for (int k2 = 0; k2 < 2; ++k2) {
        const int l = 2 * (tid + 512 * k2);
        const int o = CV_W + (32 + (l >> 5)) * 80 + (l & 31) * 2;
        const v2u p = uv[2 * k2], q = uv[2 * k2 + 1];
        *(LAS u32*)(lds + 0 * CV_CH + o) = (p.x & 0xffffu) | (q.x << 16);
        *(LAS u32*)(lds + 1 * CV_CH + o) = (p.x >> 16) | (q.x & 0xffff0000u);
        *(LAS u32*)(lds + 2 * CV_CH + o) = (p.y & 0xffffu) | (q.y << 16);
        *(LAS u32*)(lds + 3 * CV_CH + o) = (p.y >> 16) | (q.y & 0xffff0000u);
    }
    __syncthreads();
    const int c = w & 3, par = w >> 2, b = lane & 31, hh = lane >> 5;
    lptr ch = lds + c * CV_CH;
    lptr wa = ch + ((b & 7) * 260 + (b >> 3) - hh + 3) * 16;
    lptr ub = ch + CV_W + (32 + b) * 80 + hh * 16;
    f32x16 y0 = {}, y1 = {};
    wa += par * 64; ub -= par * 80;
    {
        bf16x8 fa[2][2], fb[2][2], fc[2][2];
#pragma unroll
        for (int ks = 0; ks < 2; ++ks) { fa[0][ks] = lds8(wa - ks * 32); fb[0][ks] = lds8(ub + ks * 32); fc[0][ks] = lds8(ub + 2560 + ks * 32); }
#pragma unroll
        for (int i = 0; i < 32; ++i) {
            const int cur = i & 1, nx = cur ^ 1;
            if (i + 1 < 32) {
#pragma unroll
                for (int ks = 0; ks < 2; ++ks) {
                    fa[nx][ks] = lds8(wa + (i + 1) * 128 - ks * 32);
                    if (i + 1 < 16) fb[nx][ks] = lds8(ub - (i + 1) * 160 + ks * 32);
                    fc[nx][ks] = lds8(ub + 2560 - (i + 1) * 160 + ks * 32);
                }
            }
            __builtin_amdgcn_sched_barrier(0);
#pragma unroll
            for (int ks = 0; ks < 2; ++ks) {
                if (i < 16) y0 = MFMA32(fa[cur][ks], fb[cur][ks], y0);
                y1 = MFMA32(fa[cur][ks], fc[cur][ks], y1);
            }
            __builtin_amdgcn_sched_barrier(0);
        }
    }
    __syncthreads();
    {
        LAS f32x4* X = (LAS f32x4*)(ch + par * 4096);
        const f32x16 src = par ? y0 : y1;
#pragma unroll
        for (int k = 0; k < 4; ++k) X[k * 64 + lane] = f32x4{src[4 * k], src[4 * k + 1], src[4 * k + 2], src[4 * k + 3]};
    }
    __syncthreads();
    {
        LAS f32x4* X = (LAS f32x4*)(ch + (1 - par) * 4096);
        lptr Y = ch + 8192 + (32 * par + b) * 72 + hh * 8;
        const f32x16 own = par ? y1 : y0;
#pragma unroll
        for (int k = 0; k < 4; ++k) {
            const f32x4 o = X[k * 64 + lane];
            v2u yv = {pack2(own[4 * k] + o[0], own[4 * k + 1] + o[1]), pack2(own[4 * k + 2] + o[2], own[4 * k + 3] + o[3])};
            *(LAS v2u*)(Y + k * 16) = yv;
        }
    }
    __syncthreads();
    const v2u dk = *(const v2u*)(a.dskip + d0);
#pragma unroll
    for (int k2 = 0; k2 < 2; ++k2) {
        const int i = tid + 512 * k2;
        lptr yp = lds + 8192 + (i >> 4) * 72 + (i & 15) * 4;
        const u32 ya = *(LAS u32*)(yp), yb = *(LAS u32*)(yp + CV_CH), yc = *(LAS u32*)(yp + 2 * CV_CH), yd = *(LAS u32*)(yp + 3 * CV_CH);
        const v2u p = uv[2 * k2], q = uv[2 * k2 + 1];
        v4u o;
        o.x = pack2(bflo(ya) + rbf(bflo(p.x) * bflo(dk.x)), bflo(yb) + rbf(bfhi(p.x) * bfhi(dk.x)));
        o.y = pack2(bflo(yc) + rbf(bflo(p.y) * bflo(dk.y)), bflo(yd) + rbf(bfhi(p.y) * bfhi(dk.y)));
        o.z = pack2(bfhi(ya) + rbf(bflo(q.x) * bflo(dk.x)), bfhi(yb) + rbf(bfhi(q.x) * bfhi(dk.x)));
        o.w = pack2(bfhi(yc) + rbf(bflo(q.y) * bflo(dk.y)), bfhi(yd) + rbf(bfhi(q.y) * bfhi(dk.y)));
        ST16(o, rw, OFF_A4 + unit * 16384 + i * 16);
    }
    __syncthreads();
}
__device__ __forceinline__ void role_s4(const Args& a, lptr lds, rsrc_t rw, int idx) {
    if (idx < 32) {
        const int tid = threadIdx.x, t = idx * 64 + (tid >> 3), j = tid & 7;
        float acc = 0.f;
#pragma unroll
        for (int k = 0; k < 9; ++k) {
            const v4u xv = *(const v4u*)(a.u + (size_t)t * DM + (k * 8 + j) * 8), gv = *(const v4u*)(a.gatew + (k * 8 + j) * 8);
#pragma unroll
            for (int e = 0; e < 4; ++e) acc += rbf(bflo(xv[e]) * bflo(gv[e])) + rbf(bfhi(xv[e]) * bfhi(gv[e]));
        }
        acc += __shfl_xor(acc, 1); acc += __shfl_xor(acc, 2); acc += __shfl_xor(acc, 4);
        acc = rbf(acc);
        if (j == 0) ST4(__float_as_uint(rbf(1.0f / (1.0f + __expf(-acc)))), rw, OFF_G + t * 4);
    }
#pragma unroll 1
    for (int unit = idx; unit < ((a.mask & 8) ? 0 : DM / 4); unit += NS4) s4_unit(a, lds, rw, unit);
    DRAIN();
    __syncthreads();
    if (threadIdx.x == 0) ST4(1u, rw, (FL_S + idx) * 128);
}

__device__ __forceinline__ void role_post(const Args& a, lptr lds, rsrc_t rw) {
    const int tid = threadIdx.x, lane = tid & 63, w = __builtin_amdgcn_readfirstlane(tid >> 6);
    const int i16 = lane & 15, g = lane >> 4;
    int budget = SPIN_MAX, vdone0 = -1, vdone1 = -1;
    volatile LAS int* U = (volatile LAS int*)(lds + 81920);
    wait_flags(rw, FL_S, 1, 64, 1, lane, budget);
    wait_flags(rw, FL_S + 64, 1, NS4 - 64, 1, lane, budget);
#pragma unroll 1
    for (;;) {
        if (tid == 0) *U = 1 + (int)__hip_atomic_fetch_add((u32*)(a.ws + FL_Q * 128), 1u, __ATOMIC_RELAXED, __HIP_MEMORY_SCOPE_AGENT);
        __syncthreads();
        const int unit = __builtin_amdgcn_readfirstlane(*U);
        if (unit >= NSEG * 6 || budget < 0) break;
        const int s = unit / 6, r = (unit - s * 6) * 6 + (w < 6 ? w : 0), blk = s >> 1;
        bf16x8 b[32];
        v2u av[2]; float gt[2];
        if (w < 6) {
            if (blk & 1) { if (vdone1 < blk) vdone1 = wait_flags(rw, FL_V + 1, 2, NH, blk, lane, budget); }
            else { if (vdone0 < blk) vdone0 = wait_flags(rw, FL_V, 2, NH, blk, lane, budget); }
            const int vo = OFF_VOX + (s * NR + r) * (NH * 1024) + ((i16 >> 1) & 1) * 16384 + ((g & 1) * 16 + (g >> 1) * 8 + 2 * (i16 >> 2) + (i16 & 1)) * 16;
#pragma unroll
            for (int k = 0; k < 32; ++k) b[k] = as8(LD16(rw, vo + (k & 7) * 512, (k >> 3) * 4096));
#pragma unroll
            for (int qt = 0; qt < 2; ++qt) {
                av[qt] = LD8(rw, OFF_A4 + (4 * r + g) * 16384 + (32 * s + 16 * qt + i16) * 8, 0);
                gt[qt] = __uint_as_float(LD4(rw, OFF_G + (32 * s + 16 * qt + i16) * 4, 0));
            }
        }
        wait_flags(rw, FL_P + (s & 3), 4, 16, s, lane, budget);
        v4u z[8];
        {
            v4u pv[10];
#pragma unroll
            for (int k = 0; k < 10; ++k) pv[k] = LD16(rw, OFF_PX + s * 81920 + tid * 16, k * 8192);
            wait_flags(rw, FL_Z + 2 * r, 1, 2, s, lane, budget);
            const int zo = OFF_ZH + (s * NR + r) * (NH * 256) + ((i16 >> 1) & 1) * 4096 + g * 128 + (2 * (i16 >> 2) + (i16 & 1)) * 16;
#pragma unroll
            for (int k = 0; k < 8; ++k) z[k] = LD16(rw, zo + k * 512, 0);
            MB();
#pragma unroll
            for (int k = 0; k < 10; ++k) *(LAS v4u*)(lds + k * 8192 + tid * 16) = pv[k];
        }
        __syncthreads();
        if (w < 6) {
            f32x4 d0 = {}, d1 = {};
            lptr pl = lds + lane * 16;
            bf16x8 f0[4], f1[4];
#pragma unroll
            for (int i = 0; i < 4; ++i) { f0[i] = lds8(pl + i * 1024); f1[i] = lds8(pl + 40960 + i * 1024); }
            __builtin_amdgcn_sched_barrier(0);
#pragma unroll
            for (int k = 0; k < 40; ++k) {
                const bf16x8 v = k < 32 ? b[k < 32 ? k : 0] : as8(z[k < 32 ? 0 : k - 32]);
                d0 = MFMA16(v, f0[k & 3], d0);
                d1 = MFMA16(v, f1[k & 3], d1);
                if (k + 4 < 40) { f0[k & 3] = lds8(pl + (k + 4) * 1024); f1[k & 3] = lds8(pl + 40960 + (k + 4) * 1024); }
                __builtin_amdgcn_sched_barrier(0);
            }
#pragma unroll
            for (int qt = 0; qt < 2; ++qt) {
                const f32x4 d = qt ? d1 : d0;
                const float gq = gt[qt], og = rbf(1.0f - gq);
                const float r0 = rbf(gq * bflo(av[qt].x)) + rbf(og * rbf(d[0])), r1 = rbf(gq * bfhi(av[qt].x)) + rbf(og * rbf(d[1]));
                const float r2 = rbf(gq * bflo(av[qt].y)) + rbf(og * rbf(d[2])), r3 = rbf(gq * bfhi(av[qt].y)) + rbf(og * rbf(d[3]));
                v2u ov = {pack2(r0, r1), pack2(r2, r3)};
                *(v2u*)(a.out + (size_t)(32 * s + 16 * qt + i16) * DM + 16 * r + 4 * g) = ov;
            }
        }
        __syncthreads();
    }
    if (budget < 0 && lane == 0) ST4(6u, rw, FL_ERR * 128);
}

__global__ void __launch_bounds__(512, 2) k_all(Args a) {
    extern __shared__ __attribute__((aligned(16))) char smem[];
    lptr lds = (lptr)smem;
    const rsrc_t rw = __builtin_amdgcn_make_buffer_rsrc(a.ws, 0, WS_BYTES, 0x00020000);
    if (threadIdx.x == 0) {
        const int x = (int)(__builtin_amdgcn_s_getreg(20 | (3 << 11)) & 7u);
        int role = 0;
        for (int k = 0; k < 8; ++k) {
            const int gq = (x + k) & 7;
            const int t = 1 + (int)__hip_atomic_fetch_add((u32*)(a.ws + (FL_T + gq) * 128), 1u, __ATOMIC_RELAXED, __HIP_MEMORY_SCOPE_AGENT);
            if (t < 32) { role = gq + 8 * t; break; }
        }
        *(volatile LAS int*)lds = role;
    }
    __syncthreads();
    const int b = __builtin_amdgcn_readfirstlane(*(volatile LAS int*)lds), xg = b & 7, slot = b >> 3;
    __syncthreads();
    WSTAMP(516);

    if (b == 8 * 8) role_probe(rw);
    if (slot >= 20) { role_wvo(a, lds, rw, (xg & 3) * 24 + (xg < 4 ? 12 : 0) + slot - 20); __syncthreads(); }
    if ((xg < 4 && slot >= 26) || (xg >= 4 && slot >= 20)) {
        const int idx = xg < 4 ? 48 + (slot - 26) * 4 + xg : (31 - slot) * 4 + (xg - 4);
        role_s4(a, lds, rw, idx);
        __syncthreads();
    }
    bool post = false;
    if (xg < 4) {
        if (slot < 8) role_qk(a, lds, rw, xg * 8 + slot, xg);
        else if (slot < 26) role_rz(a, lds, rw, xg * 18 + slot - 8, xg);
    } else {
        const int j = xg - 4;
        post = true;
        if (slot < 16) role_vvo(a, lds, rw, j * 8 + (slot >> 1), slot & 1);
        else if (slot < 20) role_k2(a, lds, rw, j * 4 + slot - 16);
        else if (slot < 28) { role_q2p(a, lds, rw, j * 4 + ((slot - 20) >> 1), slot & 1); post = false; }
    }
    WSTAMP(517);
    if (post) { __syncthreads(); role_post(a, lds, rw); }
    WSTAMP(518);
}

extern "C" void kernel_launch(void* const* d_in, const int* in_sizes, int n_in, void* d_out, int out_size, void* d_ws, size_t ws_size, hipStream_t stream) {
    Args a;
    a.u = (const u16*)d_in[0]; a.Are = (const u16*)d_in[1]; a.Aim = (const u16*)d_in[2]; a.Bre = (const u16*)d_in[3]; a.Bim = (const u16*)d_in[4];
    a.Cre = (const u16*)d_in[5]; a.Cim = (const u16*)d_in[6]; a.logdt = (const u16*)d_in[7]; a.dskip = (const u16*)d_in[8];
    a.mem0 = (const u16*)d_in[10]; a.Wq = (const u16*)d_in[11]; a.Wk = (const u16*)d_in[12]; a.Wv = (const u16*)d_in[13]; a.Wo = (const u16*)d_in[14];
    a.gatew = (const u16*)d_in[16]; a.out = (u16*)d_out; a.ws = (char*)d_ws;
    a.pd = 12; a.pha = 8; a.phz = 2; a.gap = 50; a.mask = 0; a.pad0 = 8;
    const int LDS_ALL = 160 * 1024;
    (void)hipFuncSetAttribute((const void*)k_all, hipFuncAttributeMaxDynamicSharedMemorySize, LDS_ALL);
    (void)hipMemsetAsync(d_ws, 0xFF, INIT_BYTES, stream);
    hipLaunchKernelGGL(k_all, dim3(256), dim3(512), LDS_ALL, stream, a);
}
```

```cpp
#include <hip/hip_runtime.h>
#include <hip/hip_bf16.h>
#include <stdint.h>
#include <stdio.h>
#include <stdlib.h>

typedef unsigned short u16;
typedef unsigned int u32;
typedef u32 v4u __attribute__((ext_vector_type(4)));
typedef u32 v2u __attribute__((ext_vector_type(2)));
typedef float f32x4 __attribute__((ext_vector_type(4)));
typedef float f32x2 __attribute__((ext_vector_type(2)));
typedef float f32x16 __attribute__((ext_vector_type(16)));
typedef __bf16 bf16x2 __attribute__((ext_vector_type(2)));
typedef __bf16 bf16x8 __attribute__((ext_vector_type(8)));
typedef __bf16 bf16x16 __attribute__((ext_vector_type(16)));
typedef short s16x4 __attribute__((ext_vector_type(4)));
#define LAS __attribute__((address_space(3)))
typedef LAS char* lptr;
typedef __amdgpu_buffer_rsrc_t rsrc_t;

constexpr int SEQ = 2048, DM = 576, NH = 32, DH = 128, NSEG = 64, NR = 36, NR8 = 72, NREP = 4;
constexpr int MEMB = 9216, BC_QK = 80;
constexpr int FL_K = 0, FL_V = 16, FL_M = 80, FL_P = 112, FL_S = 176, FL_Q = 254, FL_ERR = 255, FL_Z = 256, FL_W = 328, FL_T = 424, FL_PAR = 432, NS4 = 72;
constexpr int OFF_RING = 65536;
constexpr int N_MEMA = 0, N_PX = 16384, N_PMM = 49152, N_MEMB = 49152 + 4096, N_PROBE = 49152 + 6144;
constexpr int INIT_BYTES = OFF_RING + 16 * 65536;
constexpr int OFF_ZH = INIT_BYTES;
constexpr int OFF_KX = OFF_ZH + NSEG * NR * NH * 256;
constexpr int OFF_VOX = OFF_KX + NH * SEQ * DH * 2;
constexpr int OFF_KM = OFF_VOX + NSEG * NR * NH * 1024;
constexpr int OFF_PX = OFF_KM + NSEG * NH * 2048;
constexpr int OFF_A4 = OFF_PX + NSEG * 81920;
constexpr int OFF_G = OFF_A4 + SEQ * DM * 2;
constexpr int OFF_WVO = OFF_G + SEQ * 4;
constexpr int OFF_ST = OFF_WVO + NR8 * 294912;
constexpr int WS_BYTES = OFF_ST + 256 * 520 * 8 + 4096;

#define BIDX bid_
#define RST(k)
#define STAMP(k)
#define STAMP_FLUSH
#define STAMP_INIT
#define STAMP_FIN
#define WSTAMP(k)

struct Args {
    const u16* u; const u16* Are; const u16* Aim; const u16* Bre; const u16* Bim; const u16* Cre; const u16* Cim;
    const u16* logdt; const u16* dskip; const u16* mem0; const u16* Wq; const u16* Wk; const u16* Wv; const u16* Wo;
    const u16* gatew; u16* out; char* ws;
    int pd, pha, phz, gap, mask, pad0;
};

__device__ __forceinline__ float bf2f(u16 v) { return __uint_as_float((u32)v << 16); }
__device__ __forceinline__ float bflo(u32 v) { return __uint_as_float(v << 16); }
__device__ __forceinline__ float bfhi(u32 v) { return __uint_as_float(v & 0xffff0000u); }
__device__ __forceinline__ u32 pack2(float lo, float hi) { f32x2 f = {lo, hi}; bf16x2 b = __builtin_convertvector(f, bf16x2); return __builtin_bit_cast(u32, b); }
__device__ __forceinline__ float rbf(float x) { return bflo(pack2(x, x)); }
__device__ __forceinline__ u32 nos(u32 v) { return min(v, 0xFFFFFFFEu); }
__device__ __forceinline__ v2u trd(lptr p) { return __builtin_bit_cast(v2u, __builtin_amdgcn_ds_read_tr16_b64_v4i16((LAS s16x4*)p)); }
__device__ __forceinline__ bf16x8 as8(v4u v) { return __builtin_bit_cast(bf16x8, v); }
__device__ __forceinline__ bf16x8 as8(v2u a, v2u b) { v4u v = {a.x, a.y, b.x, b.y}; return __builtin_bit_cast(bf16x8, v); }
__device__ __forceinline__ v4u asu(bf16x8 v) { return __builtin_bit_cast(v4u, v); }
__device__ __forceinline__ bf16x16 cat16(bf16x8 a, bf16x8 b) { return __builtin_shufflevector(a, b, 0, 1, 2, 3, 4, 5, 6, 7, 8, 9, 10, 11, 12, 13, 14, 15); }
__device__ __forceinline__ bf16x8 lds8(lptr p) { return *(LAS bf16x8*)p; }
__device__ __forceinline__ u32 bad4(v4u v) { return max(max(v.x, v.y), max(v.z, v.w)); }
#define NEARB(s, rep, pm) (OFF_RING + ((((s) & 3) * 4 + (rep)) << 16) + ((((pm) >> (((s) & 3) * 4 + (rep))) & 1) << 13))
#define FARB(s, rep, pm) ((NEARB(s, rep, pm) ^ 8192) + (((s) & 4) << 13))
#define BCO(slot) (((slot) & 63) * 128 + ((slot) >> 6) * 16384)
#define LBAR() do { asm volatile("s_waitcnt lgkmcnt(0)" ::: "memory"); __builtin_amdgcn_s_barrier(); asm volatile("" ::: "memory"); } while (0)
#define MB() asm volatile("" ::: "memory")
#define DRAIN() asm volatile("s_waitcnt vmcnt(0)" ::: "memory")
#define PIN4(x) asm volatile("" : "+v"(x))
#define MFMA16(a, b, c) __builtin_amdgcn_mfma_f32_16x16x32_bf16((a), (b), (c), 0, 0, 0)
#define MFMA32(a, b, c) __builtin_amdgcn_mfma_f32_32x32x16_bf16((a), (b), (c), 0, 0, 0)
#define SMF(a, b, c, idx) __builtin_amdgcn_smfmac_f32_16x16x64_bf16((a), (b), (c), (idx), 0, 0)
#define LD16(r, vo, so) __builtin_amdgcn_raw_buffer_load_b128((r), (vo), (so), 16)
#define LD8(r, vo, so) __builtin_amdgcn_raw_buffer_load_b64((r), (vo), (so), 16)
#define LD4(r, vo, so) __builtin_amdgcn_raw_buffer_load_b32((r), (vo), (so), 16)
#define ST16(v, r, vo) __builtin_amdgcn_raw_buffer_store_b128((v), (r), (vo), 0, 16)
#define ST8(v, r, vo) __builtin_amdgcn_raw_buffer_store_b64((v), (r), (vo), 0, 16)
#define ST4(v, r, vo) __builtin_amdgcn_raw_buffer_store_b32((v), (r), (vo), 0, 16)

__device__ __forceinline__ float swap32_sum(float a, float b) {
    auto r = __builtin_amdgcn_permlane32_swap(__float_as_uint(a), __float_as_uint(b), false, false);
    return __uint_as_float(r[0]) + __uint_as_float(r[1]); }
__device__ __forceinline__ float xmax(float m) {
    auto r = __builtin_amdgcn_permlane16_swap(__float_as_uint(m), __float_as_uint(m), false, false);
    m = fmaxf(__uint_as_float(r[0]), __uint_as_float(r[1]));
    auto q = __builtin_amdgcn_permlane32_swap(__float_as_uint(m), __float_as_uint(m), false, false);
    return fmaxf(__uint_as_float(q[0]), __uint_as_float(q[1])); }
__device__ __forceinline__ float xsum(float m) {
    auto r = __builtin_amdgcn_permlane16_swap(__float_as_uint(m), __float_as_uint(m), false, false);
    m = __uint_as_float(r[0]) + __uint_as_float(r[1]);
    auto q = __builtin_amdgcn_permlane32_swap(__float_as_uint(m), __float_as_uint(m), false, false);
    return __uint_as_float(q[0]) + __uint_as_float(q[1]); }
__device__ __forceinline__ float vmax(float a, float b) { float r; asm("v_max_f32 %0, %1, %2" : "=v"(r) : "v"(a), "v"(b)); return r; }
__device__ __forceinline__ float vmax3(float a, float b, float c) { float r; asm("v_max3_f32 %0, %1, %2, %3" : "=v"(r) : "v"(a), "v"(b), "v"(c)); return r; }
__device__ __forceinline__ u32 ror8(u32 v) { return (u32)__builtin_amdgcn_update_dpp(0, (int)v, 0x128, 0xf, 0xf, true); }
__device__ __forceinline__ float ror8f(float v) { return __uint_as_float(ror8(__float_as_uint(v))); }
__device__ __forceinline__ int wave_max(int d) {
    d = max(d, __builtin_amdgcn_update_dpp(d, d, 0x128, 0xf, 0xf, false));
    d = max(d, __builtin_amdgcn_update_dpp(d, d, 0x124, 0xf, 0xf, false));
    d = max(d, __builtin_amdgcn_update_dpp(d, d, 0x122, 0xf, 0xf, false));
    d = max(d, __builtin_amdgcn_update_dpp(d, d, 0x121, 0xf, 0xf, false));
    return max(max(__builtin_amdgcn_readlane(d, 0), __builtin_amdgcn_readlane(d, 16)), max(__builtin_amdgcn_readlane(d, 32), __builtin_amdgcn_readlane(d, 48)));
}

constexpr int SPIN_MAX = 2000000;
__device__ __forceinline__ int wait_flags(rsrc_t rw, int line0, int stride, int n, int need, int lane, int& budget, bool fast = false) {
    const int vo = (line0 + (lane < n ? lane : 0) * stride) * 128;
    int mn;
    for (;;) {
        const int v = (int)LD4(rw, vo, 0);
        MB();
        mn = -wave_max(-v);
        if (mn >= need) break;
        if (--budget < 0) break;
        if (fast) __builtin_amdgcn_s_sleep(2); else __builtin_amdgcn_s_sleep(12);
    }
    return mn;
}

template <int NRW, int CW>
__device__ __forceinline__ void stage_rows(const u16* g, int gs, lptr lds, int ls, int tid) {
    constexpr int N = NRW * CW / 512;
    static_assert(NRW * CW % 512 == 0, "");
    v4u v[N];
#pragma unroll
    for (int j = 0; j < N; ++j) { const int i = tid + 512 * j, row = i / CW, c = i % CW; v[j] = *(const v4u*)(g + (size_t)row * gs + c * 8); }
#pragma unroll
    for (int j = 0; j < N; ++j) { const int i = tid + 512 * j, row = i / CW, c = i % CW; *(LAS v4u*)(lds + row * ls + c * 16) = v[j]; }
}
template <int NRW, int CW>
__device__ __forceinline__ void rows_ld(v4u (&v)[NRW * CW / 512], const u16* g, int gs, int tid) {
#pragma unroll
    for (int j = 0; j < NRW * CW / 512; ++j) { const int i = tid + 512 * j, row = i / CW, c = i % CW; v[j] = *(const v4u*)(g + (size_t)row * gs + c * 8); }
}
template <int NRW, int CW>
__device__ __forceinline__ void rows_st(const v4u (&v)[NRW * CW / 512], lptr lds, int ls, int tid) {
#pragma unroll
    for (int j = 0; j < NRW * CW / 512; ++j) { const int i = tid + 512 * j, row = i / CW, c = i % CW; *(LAS v4u*)(lds + row * ls + c * 16) = v[j]; }
}
__device__ __forceinline__ bf16x8 frag16(lptr base, int ls, int lane) {
    const int i16 = lane & 15, bg = lane >> 4, q = i16 >> 2, p = i16 & 3;
    lptr a = base + (8 * bg + q) * ls + p * 8;
    return as8(trd(a), trd(a + 4 * ls));
}
__device__ __forceinline__ bf16x8 frag32p(lptr base, int ls, int lane) {
    const int i16 = lane & 15, G = lane >> 4, q = i16 >> 2, p = i16 & 3;
    lptr a = base + (8 * (G >> 1) + q) * ls + (16 * (p & 1) + 8 * (G & 1) + 4 * (p >> 1)) * 2;
    return as8(trd(a), trd(a + 4 * ls));
}

constexpr int XS = 1168;
__device__ __forceinline__ void xload(v4u (&st)[9], const u16* x, int tok0, int tid) {
#pragma unroll
    for (int j = 0; j < 9; ++j) st[j] = *(const v4u*)(x + (size_t)tok0 * DM + (tid + 512 * j) * 8);
}
__device__ __forceinline__ void xstore(const v4u (&st)[9], lptr xl, int tid) {
#pragma unroll
    for (int j = 0; j < 9; ++j) { const int i = tid + 512 * j; *(LAS v4u*)(xl + i * 16 + (i / 72) * 16) = st[j]; }
}
__device__ __forceinline__ void load_w2(bf16x8 (&A)[36], const u16* W, int h0, lptr lds, int tid, int lane, int w) {
    v4u v[6][6];
#pragma unroll
    for (int c = 0; c < 6; ++c) rows_ld<96, 32>(v[c], W + (size_t)c * 96 * 4096 + h0 * DH, 4096, tid);
    __builtin_amdgcn_sched_barrier(0);
#pragma unroll
    for (int c = 0; c < 6; ++c) {
        lptr l = lds + (c & 1) * (96 * 544);
        rows_st<96, 32>(v[c], l, 544, tid);
        LBAR();
#pragma unroll
        for (int ks = 0; ks < 6; ++ks) A[c * 6 + ks] = frag32p(l + ks * 16 * 544 + w * 64, 544, lane);
        __builtin_amdgcn_sched_barrier(0);
    }
    LBAR();
}

__device__ __forceinline__ f32x16 proj_tile(const bf16x8 (&A)[36], lptr xb) {
    f32x16 acc = {};
    bf16x8 f[6];
#pragma unroll
    for (int i = 0; i < 6; ++i) f[i] = lds8(xb + i * 32);
    __builtin_amdgcn_sched_barrier(0);
#pragma unroll
    for (int ks = 0; ks < 36; ++ks) {
        acc = MFMA32(A[ks], f[ks % 6], acc);
        if (ks + 6 < 36) f[ks % 6] = lds8(xb + (ks + 6) * 32);
        __builtin_amdgcn_sched_barrier(0);
    }
    return acc;
}
__device__ __forceinline__ void role_k2(const Args& a, lptr lds, rsrc_t rw, int cu) {
    const int tid = threadIdx.x, lane = tid & 63, w = __builtin_amdgcn_readfirstlane(tid >> 6);
    v4u st[9];
    bf16x8 A[36];
    xload(st, a.u, 0, tid);
    load_w2(A, a.Wk, 2 * cu, lds, tid, lane, w);
    RST(4);
    xstore(st, lds, tid);
    __syncthreads();
    const int o0 = OFF_KX + (2 * cu + (w >> 2)) * (NSEG * 8192) + ((w & 3) * 4 + (lane >> 5) * 2) * 512 + (lane & 31) * 16;
    for (int blk = 0; blk < 32; ++blk) {
        lptr xl = lds + (blk & 1) * (64 * XS);
        if (blk + 1 < 32) xload(st, a.u, (blk + 1) * 64, tid);
#pragma unroll
        for (int tt = 0; tt < 2; ++tt) {
            const f32x16 acc = proj_tile(A, xl + (32 * tt + (lane & 31)) * XS + (lane >> 5) * 16);
            v4u c0 = {pack2(acc[0], acc[1]), pack2(acc[2], acc[3]), pack2(acc[4], acc[5]), pack2(acc[6], acc[7])};
            v4u c1 = {pack2(acc[8], acc[9]), pack2(acc[10], acc[11]), pack2(acc[12], acc[13]), pack2(acc[14], acc[15])};
            const int o = o0 + (blk * 2 + tt) * 8192;
            ST16(c0, rw, o); ST16(c1, rw, o + 512);
            if (blk == 0 && tt == 0) { DRAIN(); LBAR(); if (tid == 0) ST4(0u, rw, (FL_K + cu) * 128); RST(5); }
        }
        if (blk + 1 < 32) xstore(st, lds + ((blk + 1) & 1) * (64 * XS), tid);
        const bool full = blk < 4 || blk == 31;
        if (full) DRAIN(); else asm volatile("s_waitcnt vmcnt(4)" ::: "memory");
        LBAR();
        if (tid == 0) ST4((u32)(full ? 2 * blk + 1 : 2 * blk - 1), rw, (FL_K + cu) * 128);
    }
}

constexpr int VS = 272;
__device__ __forceinline__ void role_q2p(const Args& a, lptr lds, rsrc_t rw, int cu, int par) {
    const int tid = threadIdx.x, lane = tid & 63, w = __builtin_amdgcn_readfirstlane(tid >> 6);
    v4u st[9];
    bf16x8 A[36];
    load_w2(A, a.Wq, 2 * cu, lds, tid, lane, w);
    lptr xl = lds, ql = lds + 64 * XS;
    LAS int* cnt = (LAS int*)(lds + 64 * XS + 4 * 64 * VS);
    if (tid < 2) cnt[tid] = 0;
    xload(st, a.u, par * 64, tid);
    xstore(st, xl, tid);
    __syncthreads();
    int budget = SPIN_MAX, kdone = -1;
    const int i16 = lane & 15, g = lane >> 4;
    const int sg = w >> 2, hh = (w >> 1) & 1, qt = w & 1, h = 2 * cu + hh;
    const float c2 = 0.08838834764831845f * 1.4426950408889634f;
    for (int it = -1; it < 16; ++it) {
        const int blk = 2 * it + par;
        if (it + 1 < 16) {
            if (it + 2 < 16) xload(st, a.u, (blk + 4) * 64, tid);
            lptr qb = ql + (((it + 1) & 1) * 2 + (w >> 2)) * (64 * VS) + ((w & 3) * 32 + (lane >> 5) * 16) * 2;
#pragma unroll
            for (int tt = 0; tt < 2; ++tt) {
                const f32x16 acc = proj_tile(A, xl + (32 * tt + (lane & 31)) * XS + (lane >> 5) * 16);
                v4u c0 = {pack2(acc[0], acc[1]), pack2(acc[2], acc[3]), pack2(acc[4], acc[5]), pack2(acc[6], acc[7])};
                v4u c1 = {pack2(acc[8], acc[9]), pack2(acc[10], acc[11]), pack2(acc[12], acc[13]), pack2(acc[14], acc[15])};
                lptr qp = qb + (32 * tt + (lane & 31)) * VS;
                *(LAS v4u*)qp = c0; *(LAS v4u*)(qp + 16) = c1;
            }
            __syncthreads();
            if (it + 2 < 16) xstore(st, xl, tid);
        }
        if (it >= 0) {
            const int s = 2 * blk + sg;
            if (kdone < s) kdone = wait_flags(rw, FL_K + cu, 1, 1, s, lane, budget);
            bf16x8 k0[4], k1[4], km[4], qf[4];
            const int ko = OFF_KX + (h * NSEG + s) * 8192 + g * 512 + i16 * 16;
#pragma unroll
            for (int ks = 0; ks < 4; ++ks) { k0[ks] = as8(LD16(rw, ko, ks * 2048)); k1[ks] = as8(LD16(rw, ko + 256, ks * 2048)); }
            lptr qp = ql + ((it & 1) * 2 + hh) * (64 * VS) + (32 * sg + 16 * qt + i16) * VS + g * 16;
#pragma unroll
            for (int ks = 0; ks < 4; ++ks) qf[ks] = lds8(qp + ks * 64);
            f32x4 S0 = {}, S1 = {}, Sm = {};
#pragma unroll
            for (int ks = 0; ks < 4; ++ks) { S0 = MFMA16(k0[ks], qf[ks], S0); S1 = MFMA16(k1[ks], qf[ks], S1); }
            wait_flags(rw, FL_M + h, 1, 1, s, lane, budget, s >= 62);
            const int mo = OFF_KM + (s * NH + h) * 2048 + g * 128 + (i16 & 7) * 16;
#pragma unroll
            for (int ks = 0; ks < 4; ++ks) km[ks] = as8(LD16(rw, mo + ks * 512, 0));
#pragma unroll
            for (int ks = 0; ks < 4; ++ks) Sm = MFMA16(km[ks], qf[ks], Sm);
            const int qi = 16 * qt + i16, t0 = 4 * g;
#pragma unroll
            for (int r = 0; r < 4; ++r) {
                if (t0 + r > qi) S0[r] = -INFINITY;
                if (t0 + r + 16 > qi) S1[r] = -INFINITY;
                if (g >= 2) Sm[r] = -INFINITY;
            }
            float m = fmaxf(fmaxf(fmaxf(S0[0], S0[1]), fmaxf(S0[2], S0[3])), fmaxf(fmaxf(S1[0], S1[1]), fmaxf(S1[2], S1[3])));
            m = fmaxf(m, fmaxf(fmaxf(Sm[0], Sm[1]), fmaxf(Sm[2], Sm[3])));
            m = xmax(m);
            const float nm = -m * c2;
            float sum = 0.f;
#pragma unroll
            for (int r = 0; r < 4; ++r) {
                S0[r] = __builtin_amdgcn_exp2f(fmaf(S0[r], c2, nm)); S1[r] = __builtin_amdgcn_exp2f(fmaf(S1[r], c2, nm)); Sm[r] = __builtin_amdgcn_exp2f(fmaf(Sm[r], c2, nm));
                sum += S0[r] + S1[r] + Sm[r];
            }
            sum = xsum(sum);
            const float inv = __builtin_amdgcn_rcpf(sum);
            v4u px = {pack2(S0[0] * inv, S0[1] * inv), pack2(S0[2] * inv, S0[3] * inv), pack2(S1[0] * inv, S1[1] * inv), pack2(S1[2] * inv, S1[3] * inv)};
            const int po = OFF_PX + (s * 2 + qt) * 40960;
            ST16(px, rw, po + h * 1024 + lane * 16);
            const u32 ma = pack2(Sm[0] * inv, Sm[1] * inv), mb = pack2(Sm[2] * inv, Sm[3] * inv);
            auto ra = __builtin_amdgcn_permlane16_swap(ma, ma, false, false);
            auto rb = __builtin_amdgcn_permlane16_swap(mb, mb, false, false);
            v4u pm = {ra[0], rb[0], ra[1], rb[1]};
            if (lane < 16) ST16(pm, rw, po + 32768 + h * 256 + lane * 16);
            DRAIN();
            if (lane == 0) {
                const int old = __hip_atomic_fetch_add(cnt + sg, 1, __ATOMIC_RELAXED, __HIP_MEMORY_SCOPE_WORKGROUP);
                if ((old & 3) == 3) ST4((u32)s, rw, (FL_P + 4 * cu + 2 * par + sg) * 128);
            }
        }
        __syncthreads();
    }
    if (budget < 0 && lane == 0) ST4(5u, rw, FL_ERR * 128);
}

__device__ __forceinline__ void role_vvo(const Args& a, lptr lds, rsrc_t rw, int h, int par) {
    const int tid = threadIdx.x, lane = tid & 63, w = __builtin_amdgcn_readfirstlane(tid >> 6);
    v4u st[9];
#pragma unroll
    for (int j = 0; j < 9; ++j) { const int i = tid + 512 * j; st[j] = *(const v4u*)(a.u + (size_t)(par * 32 + (i >= 2304 ? 32 : 0)) * DM + i * 8); }
    bf16x8 Av[18], Bo[5][4];
    {
        v4u v[3][6], o[2][9];
#pragma unroll
        for (int c = 0; c < 3; ++c) rows_ld<192, 16>(v[c], a.Wv + (size_t)c * 192 * 4096 + h * DH, 4096, tid);
#pragma unroll
        for (int c = 0; c < 2; ++c) rows_ld<64, 72>(o[c], a.Wo + ((size_t)h * DH + c * 64) * DM, DM, tid);
        __builtin_amdgcn_sched_barrier(0);
#pragma unroll
        for (int c = 0; c < 3; ++c) {
            lptr l = lds + (c & 1) * 77824;
            rows_st<192, 16>(v[c], l, 288, tid);
            LBAR();
#pragma unroll
            for (int ks = 0; ks < 6; ++ks) Av[c * 6 + ks] = frag16(l + ks * 32 * 288 + w * 32, 288, lane);
            __builtin_amdgcn_sched_barrier(0);
        }
#pragma unroll
        for (int c = 0; c < 2; ++c) {
            lptr l = lds + ((c + 1) & 1) * 77824;
            rows_st<64, 72>(o[c], l, 1184, tid);
            LBAR();
#pragma unroll
            for (int j = 0; j < 5; ++j) {
                const int r = (w + 8 * j < NR) ? w + 8 * j : w;
#pragma unroll
                for (int ks = 0; ks < 2; ++ks) Bo[j][c * 2 + ks] = frag16(l + ks * 32 * 1184 + r * 32, 1184, lane);
            }
            __builtin_amdgcn_sched_barrier(0);
        }
        LBAR();
    }
    RST(4);
    lptr xl = lds, vl = lds + 64 * XS;
    xstore(st, xl, tid);
    __syncthreads();
    const int rho = lane & 15, bg = lane >> 4;
    const int vo0 = OFF_VOX + ((2 * w + ((rho >> 1) & 1)) * 16 + (h >> 1)) * 1024 + ((2 * (h & 1) + (bg & 1)) * 16 + (bg >> 1) * 8 + 2 * (rho >> 2) + (rho & 1)) * 16;
    const int fo = (FL_V + 2 * h + par) * 128;
    auto stage1 = [&](int tt) {
        f32x4 acc = {};
        lptr xb = xl + (16 * tt + rho) * XS + bg * 16;
        bf16x8 f[6];
#pragma unroll
        for (int i = 0; i < 6; ++i) f[i] = lds8(xb + i * 64);
        __builtin_amdgcn_sched_barrier(0);
#pragma unroll
        for (int ks = 0; ks < 18; ++ks) {
            acc = MFMA16(Av[ks], f[ks % 6], acc);
            if (ks + 6 < 18) f[ks % 6] = lds8(xb + (ks + 6) * 64);
            __builtin_amdgcn_sched_barrier(0);
        }
        v2u o = {pack2(acc[0], acc[1]), pack2(acc[2], acc[3])};
        *(LAS v2u*)(vl + (16 * tt + rho) * VS + (16 * w + 4 * bg) * 2) = o;
    };
    auto stage2 = [&](int sg, int s) {
        bf16x8 Af[2][4];
#pragma unroll
        for (int half = 0; half < 2; ++half)
#pragma unroll
            for (int ks = 0; ks < 4; ++ks) Af[half][ks] = lds8(vl + (32 * sg + rho + 16 * half) * VS + ks * 64 + bg * 16);
#pragma unroll
        for (int j = 0; j < 5; ++j) {
            if (j < 4 || w < 4) {
                f32x4 d0 = {}, d1 = {};
#pragma unroll
                for (int ks = 0; ks < 4; ++ks) { d0 = MFMA16(Af[0][ks], Bo[j][ks], d0); d1 = MFMA16(Af[1][ks], Bo[j][ks], d1); }
                v4u o = {pack2(d0[0], d0[1]), pack2(d0[2], d0[3]), pack2(d1[0], d1[1]), pack2(d1[2], d1[3])};
                ST16(o, rw, vo0 + (s * NR + 8 * j) * (NH * 1024));
            }
        }
    };
    int prev = -1;
#pragma unroll 1
    for (int un = 0; un < 32; ++un) {
        const int sg = un & 1, blk = (un >> 1) * 2 + par;
        const int s = un < 2 ? par + 2 * un : 2 * blk + sg;
        if (sg == 0 && blk + 2 < 32) xload(st, a.u, (blk + 2) * 64, tid);
        stage1(2 * sg); stage1(2 * sg + 1);
        if (sg == 1) DRAIN(); else if (blk + 2 < 32) asm volatile("s_waitcnt vmcnt(9)" ::: "memory"); else DRAIN();
        LBAR();
        if (tid == 0 && prev >= 0) ST4((u32)prev, rw, fo);
        if (sg == 1 && blk + 2 < 32) xstore(st, xl, tid);
        stage2(sg, s);
        prev = s;
        if (un == 0 || un == 31) { DRAIN(); LBAR(); if (tid == 0) ST4((u32)s, rw, fo); if (un == 0) RST(5); }
        else if (sg == 1) LBAR();
    }
}
#define VOWN(s) ((s) < 4 ? ((s) & 1) : (((s) >> 1) & 1))

__device__ __forceinline__ u32 rt32() { return (u32)__builtin_amdgcn_s_memrealtime(); }
__device__ __forceinline__ void wait_until(u32 t) {
    if ((int)(t - rt32()) > 1000) return;
    while ((int)(rt32() - t) < -24) __builtin_amdgcn_s_sleep(4);
    while ((int)(rt32() - t) < 0) {}
}
struct TK { u32 XA, XR, EA, ER; int dA0, dA1, dA2, dR0, dR1, dR2; int okA, okR, gap; };
__device__ __forceinline__ int tk_est(int d0, int d1, int d2) { return max(min(d0, d1), min(max(d0, d1), d2)); }
template <int N>
__device__ __forceinline__ u32 beacon_max(rsrc_t rw, int fb, int slot0, int lane, int& budget) {
    const int vo = BCO(slot0 + (lane < N ? lane : 0)), v2 = BCO(64 + (lane & 7));
    u32 v, u = 0;
    for (;;) {
        v = LD4(rw, vo, fb);
        if (N > 64) u = LD4(rw, v2, fb);
        MB();
        if (__all(max(v, u) != 0xFFFFFFFFu)) break;
        if (--budget < 0) break;
        __builtin_amdgcn_s_sleep(1);
    }
    const u32 base = __builtin_amdgcn_readfirstlane(v);
    int d = (int)(v - base);
    if (N > 64) d = max(d, (int)(u - base));
    return base + (u32)wave_max(d);
}
struct BC2 { u32 v, u; };
__device__ __forceinline__ BC2 beacon_issue(rsrc_t rw, int fb, int lane) {
    BC2 b;
    b.v = LD4(rw, BCO(lane), fb);
    b.u = LD4(rw, BCO(64 + (lane & 7)), fb);
    MB();
    return b;
}
__device__ __forceinline__ u32 beacon_finish(BC2 b, rsrc_t rw, int fb, int lane, int& budget) {
    if (!__all(max(b.v, b.u) != 0xFFFFFFFFu)) return beacon_max<NR8>(rw, fb, 0, lane, budget);
    const u32 base = __builtin_amdgcn_readfirstlane(b.v);
    return base + (u32)wave_max(max((int)(b.v - base), (int)(b.u - base)));
}
__device__ __forceinline__ void tk_T0(TK& k, u32 T0, const Args& a) {
    if (k.okR) { k.dR2 = k.dR1; k.dR1 = k.dR0; k.dR0 = (int)(T0 - k.ER); }
    k.okA = (int)(k.XA - T0) >= a.pad0 - a.pha;
    if (!k.okR) k.gap = k.okA ? max(a.gap, k.gap - 6) : min(k.gap + 16, 160);
    k.EA = k.XA;
    k.XR = k.EA + tk_est(k.dA0, k.dA1, k.dA2) + a.pd;
}
__device__ __forceinline__ void tk_Tp(TK& k, u32 Tp, const Args& a) {
    const int okn = (int)(k.XR - Tp) >= a.pd - a.pha;
    if (k.okA && (k.okR || !okn)) { k.dA2 = k.dA1; k.dA1 = k.dA0; k.dA0 = (int)(Tp - k.EA); }
    k.okR = okn;
    k.ER = k.okR ? k.XR : Tp + k.gap;
    k.XA = k.ER + tk_est(k.dR0, k.dR1, k.dR2) + a.pad0;
}
__device__ __forceinline__ u32 tk_specA(const TK& k, const Args& a) { return k.XR + tk_est(k.dR0, k.dR1, k.dR2) + a.pad0; }
__device__ __forceinline__ u32 tk_specR(const TK& k, const Args& a) { return k.XA + tk_est(k.dA0, k.dA1, k.dA2) + a.pd; }
__device__ __forceinline__ void tk_init(TK& k) { const u32 n = rt32(); k.XA = k.XR = k.EA = k.ER = n - 100000u; k.dA0 = k.dA1 = k.dA2 = 134; k.dR0 = k.dR1 = k.dR2 = 80; k.okA = k.okR = 0; k.gap = 50; }

__device__ __forceinline__ void tk_post(lptr p, int tag, u32 t, int lane) { if (lane == 0) { v2u v = {(u32)tag, t}; *(volatile LAS v2u*)p = v; } }
__device__ __forceinline__ u32 tk_get(lptr p, int tag, int& budget) {
    for (;;) {
        const v2u v = *(volatile LAS v2u*)p;
        if ((int)__builtin_amdgcn_readfirstlane(v.x) >= tag || --budget < 0) return __builtin_amdgcn_readfirstlane(v.y);
        __builtin_amdgcn_s_sleep(2);
    }
}
__device__ __forceinline__ void tk_wait(lptr p, int tag, int& budget) {
    u32 X;
    for (;;) {
        const v2u v = *(volatile LAS v2u*)p;
        const u32 now = rt32();
        X = __builtin_amdgcn_readfirstlane(v.y);
        const int tg = (int)__builtin_amdgcn_readfirstlane(v.x), dt = (int)(X - now);
        if (tg >= tag) {
            if (dt <= 0 || dt > 1000 || tg > tag) return;
            if (dt <= 28) break;
        }
        if (--budget < 0) return;
        __builtin_amdgcn_s_sleep(1);
    }
    while ((int)(rt32() - X) < 0) {}
}
constexpr int OOB = 0x70000000;
__device__ __forceinline__ void stage_rows8(const u16* src, lptr ml, int tid) {
    for (int c = tid; c < 576; c += 512) {
        const u32* p = (const u32*)(src + (c & 7) * DM + 16 * (c >> 4) + 2 * ((c >> 3) & 1));
        v4u v = {p[0], p[2], p[4], p[6]};
        *(LAS v4u*)(ml + c * 16) = v;
    }
}
__device__ __forceinline__ void gather_mem(rsrc_t rw, int nb, lptr ml, int lane, int w, int& budget) {
    const int gi = w == 7 ? 5 : w;
    const int l0 = gi * 1536 + lane * 16, l1 = l0 + 1024;
    const int o0 = l0 < 8192 ? l0 : l0 + (N_MEMB - 8192), o1 = lane < 32 ? (l1 < 8192 ? l1 : l1 + (N_MEMB - 8192)) : OOB;
    lptr p1 = ml + (lane < 32 ? l1 : 14336 + lane * 16);
    v4u v0, v1;
    for (;;) {
        v0 = LD16(rw, o0, nb); v1 = LD16(rw, o1, nb);
        MB();
        if (__builtin_expect(__all(max(bad4(v0), bad4(v1)) != 0xFFFFFFFFu), 1)) break;
        if (--budget < 0) break;
    }
    *(LAS v4u*)(ml + l0) = v0; *(LAS v4u*)p1 = v1;
}
__device__ __forceinline__ void gather_mem4(rsrc_t rw, int nb, lptr ml, int lane, int gi, int& budget) {
    const int l0 = gi * 2048 + lane * 16;
    const int o2 = lane < 16 ? N_MEMB + gi * 256 + lane * 16 : OOB;
    lptr p2 = ml + (lane < 16 ? 8192 + gi * 256 + lane * 16 : 14336 + lane * 16);
    v4u v0, v1, v2;
    for (;;) {
        v0 = LD16(rw, l0, nb); v1 = LD16(rw, l0 + 1024, nb); v2 = LD16(rw, o2, nb);
        MB();
        if (__builtin_expect(__all(max(max(bad4(v0), bad4(v1)), bad4(v2)) != 0xFFFFFFFFu), 1)) break;
        if (--budget < 0) break;
    }
    *(LAS v4u*)(ml + l0) = v0; *(LAS v4u*)(ml + l0 + 1024) = v1; *(LAS v4u*)p2 = v2;
}
__device__ __forceinline__ int get_parity(rsrc_t rw, int lane, int& budget) {
    const int vo = (FL_PAR + (lane & 7)) * 128;
    u32 v;
    for (;;) {
        v = LD4(rw, vo, 0);
        MB();
        if (__all(v != 0xFFFFFFFFu) || --budget < 0) break;
        __builtin_amdgcn_s_sleep(8);
    }
    int pm = 0;
#pragma unroll
    for (int k = 0; k < 8; ++k) pm |= (__builtin_amdgcn_readlane((int)v, k) & 3) << (2 * k);
    return __builtin_popcount(pm) > 8 ? 0xFFFF : 0;
}
__device__ __forceinline__ void role_probe(rsrc_t rw) {
    const int lane = threadIdx.x & 63, w = __builtin_amdgcn_readfirstlane(threadIdx.x >> 6);
    u32 bits = 0;
    for (int gg = 0; gg < 2; ++gg) {
        int best[2] = {1 << 30, 1 << 30};
        for (int rep = 0; rep < 3; ++rep) {
#pragma unroll
            for (int p = 0; p < 2; ++p) {
                const int o = OFF_RING + (2 * w + gg) * 65536 + N_PROBE + p * 8192 + rep * 128;
                ST4(0xFFFFFFFFu, rw, lane == 0 ? o : OOB);
                DRAIN();
                const unsigned long long t0 = __builtin_amdgcn_s_memtime();
                asm volatile("s_waitcnt lgkmcnt(0)" ::: "memory");
                u32 v = LD4(rw, o, 0);
                asm volatile("s_waitcnt vmcnt(0)" : "+v"(v) :: "memory");
                const unsigned long long t1 = __builtin_amdgcn_s_memtime();
                best[p] = min(best[p], (int)(t1 - t0));
            }
        }
        bits |= (best[1] < best[0] ? 1u : 0u) << gg;
    }
    if (lane == 0) ST4(bits, rw, (FL_PAR + w) * 128);
}
__device__ __forceinline__ void qk_project(const bf16x16 (&Bq)[9], const bf16x16 (&Bk)[9], lptr ma, lptr xw, int idx) {
    f32x4 aq = {}, ak = {};
    bf16x8 A[9];
#pragma unroll
    for (int t = 0; t < 9; ++t) A[t] = lds8(ma + t * 1024);
    __builtin_amdgcn_sched_barrier(0);
#pragma unroll
    for (int t = 0; t < 9; ++t) { aq = SMF(A[t], Bq[t], aq, idx); ak = SMF(A[t], Bk[t], ak, idx); }
    const float s0 = swap32_sum(aq[0], ak[0]), s1 = swap32_sum(aq[1], ak[1]), s2 = swap32_sum(aq[2], ak[2]), s3 = swap32_sum(aq[3], ak[3]);
    v2u o = {pack2(s0, s1), pack2(s2, s3)};
    *(LAS v2u*)xw = o;
}

__device__ __forceinline__ void role_qk(const Args& a, lptr lds, rsrc_t rw, int h, int rep) {
    const int tid = threadIdx.x, lane = tid & 63, w = __builtin_amdgcn_readfirstlane(tid >> 6);
    bf16x16 Bq[9], Bk[9];
    for (int i = 0; i < a.phz; ++i) __builtin_amdgcn_s_sleep(16);
    {
        v4u v[6][6];
#pragma unroll
        for (int c = 0; c < 6; ++c) rows_ld<192, 16>(v[c], (c >= 3 ? a.Wk : a.Wq) + (size_t)(c % 3) * 192 * 4096 + h * DH, 4096, tid);
        __builtin_amdgcn_sched_barrier(0);
#pragma unroll
        for (int c = 0; c < 6; ++c) {
            lptr l = lds + (c & 1) * 77824;
            rows_st<192, 16>(v[c], l, 288, tid);
            LBAR();
#pragma unroll
            for (int t = 0; t < 3; ++t) {
                bf16x16 f = cat16(frag16(l + (64 * t) * 288 + w * 32, 288, lane), frag16(l + (64 * t + 32) * 288 + w * 32, 288, lane));
                if (c >= 3) Bk[(c % 3) * 3 + t] = f; else Bq[c * 3 + t] = f;
            }
            __builtin_amdgcn_sched_barrier(0);
        }
        LBAR();
    }
    lptr ml = lds, xl = lds + MEMB;
    const int idx = (lane & 8) ? 0xEEEE : 0x4444;
    const int i16 = lane & 15, g = lane >> 4;
    const bool hi8 = (lane & 8) != 0;
    int budget = SPIN_MAX;
    lptr pq = xl + (8 * g + (i16 >> 2)) * 32 + (i16 & 1) * 8;
    lptr kl = lds + 16384 + lane * 16;
    lptr ma = ml + lane * 16, xw = xl + (16 * w + i16) * 32 + g * 8;
    const int ox = N_PX + (h >> 4) * 16384 + (h & 15) * 512 + ((g & 1) * 16 + (g >> 1) * 8 + (i16 & 7)) * 16;
    const int om = hi8 ? OOB : N_PMM + h * 128 + (i16 & 7) * 16;
    const int ob = lane < NREP ? BCO(BC_QK + h) : OOB;
    const int pm = get_parity(rw, lane, budget);
    lptr tkp = lds + MEMB + 4096, kfp = tkp + 64;
    if (tid == 0) { tk_post(tkp, 0, rt32() - 100000u, 0); tk_post(kfp, 0, 0u, 0); }
    stage_rows8(a.mem0, ml, tid);
#pragma unroll
    for (int G = 0; G < 4; ++G) stage_rows8(a.u + G * 8 * DM, lds + 32768 + G * MEMB, tid);
    __syncthreads();
#pragma unroll 1
    for (int G = 0; G < 4; ++G) {
        qk_project(Bq, Bk, lds + 32768 + G * MEMB + lane * 16, xw, idx);
        LBAR();
        if (w < 4) {
            const bf16x8 F = as8(trd(pq + w * 1024 + 16), trd(pq + w * 1024 + 16 + 128));
            if ((i16 >> 3) == (G & 1)) *(LAS bf16x8*)(kl + ((G >> 1) * 4 + w) * 1024) = F;
        }
        LBAR();
    }
    RST(5);
    STAMP_INIT
    if (w == 0) {
        const float c2 = 0.08838834764831845f * 1.4426950408889634f;
        for (int s = 0; s < NSEG - 1; ++s) {
            tk_get(kfp, s, budget);
            bf16x8 kx[2][4];
#pragma unroll
            for (int tl = 0; tl < 2; ++tl)
#pragma unroll
                for (int ks = 0; ks < 4; ++ks) kx[tl][ks] = lds8(kl + (tl * 4 + ks) * 1024);
            int oxs = ox + NEARB(s, i16 >> 3, pm), oxt = ox + NEARB(s, 2 + (i16 >> 3), pm), oms = om + NEARB(s, g, pm), obs = ob + FARB(s, lane & 3, pm);
            const int nb = NEARB(s, rep, pm);
            asm volatile("s_waitcnt lgkmcnt(0)" : "+v"(oxs), "+v"(oxt), "+v"(oms), "+v"(obs) :: "memory");
#pragma unroll
            for (int tl = 0; tl < 2; ++tl)
#pragma unroll
                for (int ks = 0; ks < 4; ++ks) PIN4(kx[tl][ks]);
            if (s > 0) {
                tk_wait(tkp, s, budget);
                STAMP(0);
                gather_mem(rw, nb, ml, lane, 0, budget);
            }
            STAMP(1);
            LBAR();
            STAMP(2);
            qk_project(Bq, Bk, ma, xw, idx);
            STAMP(3);
            __builtin_amdgcn_s_setprio(3);
            LBAR();
            STAMP(4);
            bf16x8 Fq[4], Fk[4];
#pragma unroll
            for (int ks = 0; ks < 4; ++ks) Fq[ks] = as8(trd(pq + ks * 1024), trd(pq + ks * 1024 + 128));
            __builtin_amdgcn_sched_barrier(0);
#pragma unroll
            for (int ks = 0; ks < 4; ++ks) Fk[ks] = as8(trd(pq + ks * 1024 + 16), trd(pq + ks * 1024 + 16 + 128));
            __builtin_amdgcn_sched_barrier(0);
            f32x4 S0 = {}, S1 = {}, Sm = {};
            S0 = MFMA16(kx[0][0], Fq[0], S0); S1 = MFMA16(kx[1][0], Fq[0], S1);
            S0 = MFMA16(kx[0][1], Fq[1], S0); S1 = MFMA16(kx[1][1], Fq[1], S1);
            __builtin_amdgcn_sched_barrier(0);
            Sm = MFMA16(Fk[0], Fq[0], Sm); S0 = MFMA16(kx[0][2], Fq[2], S0);
            Sm = MFMA16(Fk[1], Fq[1], Sm); S1 = MFMA16(kx[1][2], Fq[2], S1);
            Sm = MFMA16(Fk[2], Fq[2], Sm); S0 = MFMA16(kx[0][3], Fq[3], S0);
            Sm = MFMA16(Fk[3], Fq[3], Sm); S1 = MFMA16(kx[1][3], Fq[3], S1);
            __builtin_amdgcn_sched_barrier(0);
            float y0 = hi8 ? Sm[2] : Sm[0], y1 = hi8 ? Sm[3] : Sm[1];
            float x0 = hi8 ? S1[0] : S0[0], x1 = hi8 ? S1[1] : S0[1], x2 = hi8 ? S1[2] : S0[2], x3 = hi8 ? S1[3] : S0[3];
            float m = vmax3(vmax3(y0, y1, x0), x1, vmax(x2, x3));
            m = vmax(m, ror8f(m));
            { auto r = __builtin_amdgcn_permlane16_swap(__float_as_uint(m), __float_as_uint(m), false, false); m = vmax(__uint_as_float(r[0]), __uint_as_float(r[1])); }
            { auto r = __builtin_amdgcn_permlane32_swap(__float_as_uint(m), __float_as_uint(m), false, false); m = vmax(__uint_as_float(r[0]), __uint_as_float(r[1])); }
            STAMP(5);
            const float nm = -m * c2;
            x0 = __builtin_amdgcn_exp2f(fmaf(x0, c2, nm)); x1 = __builtin_amdgcn_exp2f(fmaf(x1, c2, nm)); x2 = __builtin_amdgcn_exp2f(fmaf(x2, c2, nm)); x3 = __builtin_amdgcn_exp2f(fmaf(x3, c2, nm));
            y0 = __builtin_amdgcn_exp2f(fmaf(y0, c2, nm)); y1 = __builtin_amdgcn_exp2f(fmaf(y1, c2, nm));
            float sum = fmaf(0.5f, y0 + y1, (x0 + x1) + (x2 + x3));
            sum += ror8f(sum);
            sum = xsum(sum);
            const float inv = __builtin_amdgcn_rcpf(sum);
            const u32 pa = pack2(x0 * inv, x1 * inv) & 0x7fff7fffu, pb = pack2(x2 * inv, x3 * inv) & 0x7fff7fffu, pc = pack2(y0 * inv, y1 * inv) & 0x7fff7fffu;
            const u32 qa = ror8(pa), qb = ror8(pb), qc = ror8(pc);
            v4u px = {hi8 ? qa : pa, hi8 ? qb : pb, hi8 ? pa : qa, hi8 ? pb : qb};
            ST16(px, rw, oxs);
            ST16(px, rw, oxt);
            auto ra = __builtin_amdgcn_permlane16_swap(pc, pc, false, false);
            auto rc = __builtin_amdgcn_permlane16_swap(qc, qc, false, false);
            v4u pmv = {ra[0], rc[0], ra[1], rc[1]};
            ST16(pmv, rw, oms);
            STAMP(6);
            __builtin_amdgcn_sched_barrier(0);
            ST4(nos(rt32()), rw, obs);
            __builtin_amdgcn_s_setprio(0);
            STAMP_FLUSH;
        }
        {
            tk_wait(tkp, NSEG - 1, budget);
            gather_mem(rw, NEARB(NSEG - 1, rep, pm), ml, lane, 0, budget);
            LBAR();
            qk_project(Bq, Bk, ma, xw, idx);
            LBAR();
        }
    } else {
        TK tk; tk_init(tk);
        int kdone = -1;
        const int kxo = OFF_KX + h * (NSEG * 8192) + g * 512 + i16 * 16;
        const int kmo = i16 < 8 ? OFF_KM + h * 2048 + g * 128 + i16 * 16 : OOB;
        for (int s = 0; s < NSEG; ++s) {
            if (w == 5) {
                if (s > 0 && s < NSEG - 1) {
                    if (kdone < s) kdone = wait_flags(rw, FL_K + (h >> 1), 1, 1, s, lane, budget, true);
                    v4u kx[8];
#pragma unroll
                    for (int k = 0; k < 8; ++k) kx[k] = LD16(rw, kxo + (k >> 2) * 256 + (k & 3) * 2048, s * 8192);
                    const v4u ff = {0xFFFFFFFFu, 0xFFFFFFFFu, 0xFFFFFFFFu, 0xFFFFFFFFu};
                    ST16(ff, rw, ox + NEARB(s + 2, i16 >> 3, pm)); ST16(ff, rw, ox + NEARB(s + 2, 2 + (i16 >> 3), pm));
                    ST16(ff, rw, om + NEARB(s + 2, g, pm));
                    ST4(0xFFFFFFFFu, rw, ob + FARB(s + 4, lane & 3, pm));
#pragma unroll
                    for (int k = 0; k < 8; ++k) *(LAS v4u*)(kl + k * 1024) = kx[k];
                    asm volatile("s_waitcnt lgkmcnt(0)" ::: "memory");
                    tk_post(kfp, s, 0u, lane);
                    DRAIN();
                }
            }
            if (s > 0 && w != 5 && w != 6) {
                tk_wait(tkp, s, budget);
                gather_mem(rw, NEARB(s, rep, pm), ml, lane, w, budget);
            }
            BC2 bc = {0u, 0u};
            if (w == 3) bc = beacon_issue(rw, FARB(s, rep, pm), lane);
            LBAR();
            qk_project(Bq, Bk, ma, xw, idx);
            LBAR();
            if (w == 6) {
                bf16x8 F[4];
#pragma unroll
                for (int ks = 0; ks < 4; ++ks) F[ks] = as8(trd(pq + ks * 1024 + 16), trd(pq + ks * 1024 + 16 + 128));
#pragma unroll
                for (int ks = 0; ks < 4; ++ks) ST16(asu(F[ks]), rw, kmo + s * (NH * 2048) + ks * 512);
                DRAIN();
                if (lane == 0) ST4((u32)s, rw, (FL_M + h) * 128);
            } else if (w == 3) {
                if (s < NSEG - 1) {
                    if (s > 0) { tk_T0(tk, beacon_finish(bc, rw, FARB(s, rep, pm), lane, budget), a); if (tk.okA) tk_post(tkp, s + 1, tk_specA(tk, a), lane); }
                    wait_until(tk.XR + 8);
                    tk_Tp(tk, beacon_max<NH>(rw, FARB(s, rep, pm), BC_QK, lane, budget), a);
                    tk_post(tkp, s + 1, tk.XA, lane);
                }
            } else if (w != 5) __builtin_amdgcn_s_sleep(14);
        }
    }
    { const int bid_ = rep + 8 * (h - 8 * rep); (void)bid_; STAMP_FIN; }
    if (budget < 0 && lane == 0) ST4(1u, rw, FL_ERR * 128);
}

__device__ __forceinline__ void role_wvo(const Args& a, lptr lds, rsrc_t rw, int idx) {
    const int tid = threadIdx.x, lane = tid & 63, w = __builtin_amdgcn_readfirstlane(tid >> 6);
    const int h = idx / 3, part = idx - 3 * h;
    bf16x8 Bo[5][4];
    v4u v[6], o[2][9];
#pragma unroll
    for (int c = 0; c < 2; ++c) rows_ld<64, 72>(o[c], a.Wo + ((size_t)h * DH + c * 64) * DM, DM, tid);
    rows_ld<192, 16>(v, a.Wv + (size_t)part * 192 * 4096 + h * DH, 4096, tid);
    __builtin_amdgcn_sched_barrier(0);
#pragma unroll
    for (int c = 0; c < 2; ++c) {
        lptr l = lds + c * 77824;
        rows_st<64, 72>(o[c], l, 1184, tid);
        LBAR();
#pragma unroll
        for (int j = 0; j < 5; ++j) {
            const int r = (w + 8 * j < NR) ? w + 8 * j : w;
#pragma unroll
            for (int ks = 0; ks < 2; ++ks) Bo[j][c * 2 + ks] = frag16(l + ks * 32 * 1184 + r * 32, 1184, lane);
        }
        __builtin_amdgcn_sched_barrier(0);
    }
    rows_st<192, 16>(v, lds, VS, tid);
    LBAR();
    RST(1);
    const int rho = lane & 15, bg = lane >> 4;
    const int wo0 = OFF_WVO + (2 * w + ((rho >> 1) & 1)) * 294912 + ((h >> 1) * 18 + part * 6) * 1024 + (bg * 16 + (h & 1) * 8 + 2 * (rho >> 2) + (rho & 1)) * 16;
#pragma unroll 1
    for (int kb = 0; kb < 6; ++kb) {
        bf16x8 Af[2][4];
#pragma unroll
        for (int mt = 0; mt < 2; ++mt)
#pragma unroll
            for (int ks = 0; ks < 4; ++ks) Af[mt][ks] = lds8(lds + (32 * kb + 8 * (rho >> 2) + 4 * mt + (rho & 3)) * VS + ks * 64 + bg * 16);
#pragma unroll
        for (int j = 0; j < 5; ++j) {
            if (j < 4 || w < 4) {
                f32x4 d0 = {}, d1 = {};
#pragma unroll
                for (int ks = 0; ks < 4; ++ks) { d0 = MFMA16(Af[0][ks], Bo[j][ks], d0); d1 = MFMA16(Af[1][ks], Bo[j][ks], d1); }
                v4u q = {pack2(d0[0], d0[1]), pack2(d0[2], d0[3]), pack2(d1[0], d1[1]), pack2(d1[2], d1[3])};
                ST16(q, rw, wo0 + j * (16 * 294912) + kb * 1024);
            }
        }
        if (kb == 2 || kb == 4) {
            asm volatile("s_waitcnt vmcnt(4)" ::: "memory");
            LBAR();
            if (tid == 0) ST4((u32)(kb >> 1), rw, (FL_W + idx) * 128);
        }
    }
    RST(2);
    DRAIN();
    LBAR();
    RST(3);
    if (tid == 0) ST4(3u, rw, (FL_W + idx) * 128);
}

__device__ __forceinline__ void role_rz(const Args& a, lptr lds, rsrc_t rw, int r, int rep) {
    const int tid = threadIdx.x, lane = tid & 63, w = __builtin_amdgcn_readfirstlane(tid >> 6);
    const int idx = (lane & 8) ? 0xEEEE : 0x4444;
    const int i16 = lane & 15, g = lane >> 4;
    int budget = SPIN_MAX;
    bf16x16 Ba[9], Bb[9];
    {
        const int wo = OFF_WVO + r * 294912 + 2 * w * 18432 + lane * 16;
#pragma unroll
        for (int p = 0; p < 3; ++p) {
            wait_flags(rw, FL_W + 12 * w, 1, 12, p + 1, lane, budget, true);
            if (p == 0) RST(4);
#pragma unroll
            for (int q = 0; q < 3; ++q) {
                const int t = 3 * q + p;
                Ba[t] = cat16(as8(LD16(rw, wo, t * 2048)), as8(LD16(rw, wo + 1024, t * 2048)));
                Bb[t] = cat16(as8(LD16(rw, wo, 18432 + t * 2048)), as8(LD16(rw, wo + 1024, 18432 + t * 2048)));
            }
            MB();
        }
    }
    lptr ml = lds, zl = lds + MEMB;
    lptr tka = lds + 13312, tkr = tka + 16, cntp = tka + 64;
    volatile LAS int* prod = (volatile LAS int*)(lds + 13312 + 512);
    lptr ring = lds + 16384;
    lptr ma = ml + lane * 16;
    lptr zw = zl + ((2 * w + (lane >> 5)) >> 2) * 1024 + ((((2 * w + (lane >> 5)) & 3) * 16 + (g & 1) * 8 + (i16 & 7)) * 16) + (i16 >> 3) * 8;
    const bool hi8r = (lane & 8) != 0;
    const int mso = i16 < 8 ? (r < 64 ? N_MEMA + r * 128 : N_MEMB + (r - 64) * 128) + i16 * 16 : OOB;
    const int ob = lane < NREP ? BCO(r) : OOB;
    const int pm = get_parity(rw, lane, budget);
    if (tid == 0) { tk_post(tka, 0, rt32() - 100000u, 0); tk_post(tkr, -1, 0u, 0); *(volatile LAS int*)cntp = 0; }
    if (tid < 4) prod[tid] = 0;
    stage_rows8(a.mem0, ml, tid);
    __syncthreads();
    RST(5);
    STAMP_INIT
    if (w < 4) {
        const int po = N_PX + (w >> 1) * 16384 + (w & 1) * 4096 + lane * 16;
        const int mo = N_PMM + w * 1024 + 2 * g * 128 + (i16 & 7) * 16 + (i16 >> 3) * 8;
        lptr cna = lane == 0 ? cntp : cntp + 4 + lane * 4;
        const u32 one = lane == 0 ? 1u : 0u;
        const bool dv = (g >> 1) == (i16 >> 3);
        const int rwo = dv ? w * 256 + (g & 1) * 128 + (g >> 1) * 64 + (i16 & 7) * 4 : 2048 + lane * 4;
        lptr zrd = zl + w * 1024 + lane * 16;
        for (int s = 0; s < NSEG; ++s) {
            lptr Rw = lds + 81920 + (s & 1) * 1024 + rwo, Rr = lds + 81920 + (s & 1) * 1024 + (i16 & 7) * 32;
            int mss = mso + NEARB(s + 1, g, pm), obs = ob + FARB(s + 1, lane & 3, pm);
            const int sb = NEARB(s, rep, pm);
            lptr rg = ring + (s & 3) * 16384 + w * 4096 + lane * 16;
            bf16x8 b[5];
            const bool early = __builtin_amdgcn_readfirstlane(prod[w >> 1]) > s;
            MB();
#pragma unroll
            for (int k = 0; k < 4; ++k) b[k] = lds8(rg + k * 1024);
            const u32 X = s > 0 ? tk_get(tkr, min(s, NSEG - 2), budget) : 0u;
            asm volatile("s_waitcnt lgkmcnt(0)" : "+v"(mss), "+v"(obs), "+v"(Rw), "+v"(Rr) :: "memory");
#pragma unroll
            for (int k = 0; k < 4; ++k) PIN4(b[k]);
            LBAR();
            qk_project(Ba, Bb, ma, zw, idx);
            LBAR();
            if (s == NSEG - 1) break;
            __builtin_amdgcn_s_setprio(3);
            b[4] = lds8(zrd);
            if (!early) {
                while (__builtin_amdgcn_readfirstlane(prod[w >> 1]) <= s && --budget >= 0) __builtin_amdgcn_s_sleep(1);
                MB();
#pragma unroll
                for (int k = 0; k < 4; ++k) b[k] = lds8(rg + k * 1024);
            }
            if (s > 0 && (int)(X - rt32()) < 1000) { while ((int)(rt32() - X) < 0) {} }
            STAMP(1);
            f32x4 d;
            for (;;) {
                v4u p[4]; v2u m[2];
                m[0] = LD8(rw, mo, sb); m[1] = LD8(rw, mo + 128, sb);
                __builtin_amdgcn_sched_barrier(0);
#pragma unroll
                for (int k = 0; k < 4; ++k) { p[k] = LD16(rw, po + k * 1024, sb); __builtin_amdgcn_sched_barrier(0); }
                MB();
                d = f32x4{};
                d = MFMA16(as8(m[0], m[1]), b[4], d);
                u32 bad = (m[0].x | m[0].y) | (m[1].x | m[1].y);
                __builtin_amdgcn_sched_barrier(0);
#pragma unroll
                for (int k = 0; k < 4; ++k) {
                    d = MFMA16(as8(p[k]), b[k], d);
                    bad |= (p[k].x | p[k].y) | (p[k].z | p[k].w);
                    __builtin_amdgcn_sched_barrier(0);
                }
                if (__builtin_expect(__all((int)bad >= 0), 1)) break;
                if (--budget < 0) break;
            }
            STAMP(2);
            float e0, e1;
            {
                auto r0 = __builtin_amdgcn_permlane32_swap(__float_as_uint(d[0]), __float_as_uint(d[2]), false, false);
                auto r1 = __builtin_amdgcn_permlane32_swap(__float_as_uint(d[1]), __float_as_uint(d[3]), false, false);
                const float t0 = __uint_as_float(hi8r ? r0[1] : r0[0]), t1 = __uint_as_float(hi8r ? r1[1] : r1[0]);
                e0 = t0 + ror8f(t0); e1 = t1 + ror8f(t1);
            }
            f32x4 x[8]; u32 old;
            asm volatile("ds_write2_b32 %10, %11, %12 offset1:8\n\tds_add_rtn_u32 %0, %13, %14\n\t"
                         "ds_read_b128 %1, %9\n\tds_read_b128 %2, %9 offset:16\n\tds_read_b128 %3, %9 offset:256\n\tds_read_b128 %4, %9 offset:272\n\t"
                         "ds_read_b128 %5, %9 offset:512\n\tds_read_b128 %6, %9 offset:528\n\tds_read_b128 %7, %9 offset:768\n\tds_read_b128 %8, %9 offset:784\n\ts_waitcnt lgkmcnt(0)"
                         : "=&v"(old), "=&v"(x[0]), "=&v"(x[1]), "=&v"(x[2]), "=&v"(x[3]), "=&v"(x[4]), "=&v"(x[5]), "=&v"(x[6]), "=&v"(x[7])
                         : "v"(Rr), "v"(Rw), "v"(e0), "v"(e1), "v"(cna), "v"(one) : "memory");
            STAMP(4);
            if ((__builtin_amdgcn_readfirstlane(old) & 3) == 3) {
                const f32x4 c0 = (x[0] + x[2]) + (x[4] + x[6]), c1 = (x[1] + x[3]) + (x[5] + x[7]);
                v4u o = {nos(pack2(c0[0], c0[1])), nos(pack2(c0[2], c0[3])), nos(pack2(c1[0], c1[1])), nos(pack2(c1[2], c1[3]))};
                ST16(o, rw, mss);
                __builtin_amdgcn_sched_barrier(0);
                ST4(nos(rt32()), rw, obs);
            }
            STAMP(5);
            __builtin_amdgcn_s_setprio(0);
            STAMP_FLUSH;
        }
    } else if (w == 5 || w == 6) {
        int vdone0 = -1, vdone1 = -1, staged = 0;
        const int hw = w - 5;
        const int vo = OFF_VOX + r * 16384 + hw * 8192 + lane * 16;
        const int zho = OFF_ZH + r * 4096 + hw * 2048 + lane * 16;
        lptr zhr = zl + hw * 2048 + (((lane >> 4) & 3) * 16 + (lane & 7)) * 16 + ((lane >> 3) & 1) * 8;
        const int aro = w == 5 ? mso : OOB, arb = w == 5 ? ob : OOB;
        v4u zv[2] = {};
        for (int s = 0; s < NSEG; ++s) {
            bool go = false;
            if (staged <= min(s + 3, NSEG - 2)) {
                const int ow = VOWN(staged);
                int& vd = ow ? vdone1 : vdone0;
                if (vd < staged) vd = wait_flags(rw, FL_V + ow, 2, NH, -1, lane, budget);
                go = vd >= staged;
            }
            const int vos = go ? vo : OOB;
            if (s > 0) {
                tk_wait(tka, s, budget);
                gather_mem4(rw, NEARB(s, rep, pm), ml, lane, w - 4, budget);
            }
            LBAR();
            v4u v[8];
#pragma unroll
            for (int k = 0; k < 8; ++k) v[k] = LD16(rw, vos + (k & 3) * 1024, staged * (NR * NH * 1024) + (k >> 2) * 4096);
            {
                const int zs = s > 0 ? zho + (s - 1) * (NR * NH * 256) : OOB;
                ST16(zv[0], rw, zs); ST16(zv[1], rw, zs + 1024);
                const v4u ff = {0xFFFFFFFFu, 0xFFFFFFFFu, 0xFFFFFFFFu, 0xFFFFFFFFu};
                ST16(ff, rw, aro + NEARB(s + 3, g, pm));
                ST4(0xFFFFFFFFu, rw, arb + FARB(s + 4, lane & 3, pm));
            }
            MB();
            qk_project(Ba, Bb, ma, zw, idx);
            LBAR();
#pragma unroll
            for (int k = 0; k < 2; ++k) {
                const v2u lo = *(LAS v2u*)(zhr + k * 1024), hi = *(LAS v2u*)(zhr + k * 1024 + 128);
                zv[k] = v4u{lo.x, lo.y, hi.x, hi.y};
            }
            for (;;) {
                if (go) {
#pragma unroll
                    for (int k = 0; k < 8; ++k) *(LAS v4u*)(ring + (staged & 3) * 16384 + hw * 8192 + k * 1024 + lane * 16) = v[k];
                    asm volatile("s_waitcnt lgkmcnt(0)" ::: "memory");
                    ++staged;
                    prod[hw] = staged;
                }
                if (staged > s + 1 || staged > NSEG - 2) break;
                const int ow = VOWN(staged);
                int& vd = ow ? vdone1 : vdone0;
                if (vd < staged) vd = wait_flags(rw, FL_V + ow, 2, NH, staged <= s ? staged : -1, lane, budget, true);
                if (vd < staged) break;
#pragma unroll
                for (int k = 0; k < 8; ++k) v[k] = LD16(rw, vo + (k & 3) * 1024, staged * (NR * NH * 1024) + (k >> 2) * 4096);
                go = true;
            }
            DRAIN();
        }
        ST16(zv[0], rw, zho + (NSEG - 1) * (NR * NH * 256)); ST16(zv[1], rw, zho + (NSEG - 1) * (NR * NH * 256) + 1024);
    } else {
        TK tk; tk_init(tk);
        for (int s = 0; s < NSEG; ++s) {
            if (s > 0) {
                tk_wait(tka, s, budget);
                gather_mem4(rw, NEARB(s, rep, pm), ml, lane, w - 4, budget);
            }
            BC2 bc = {0u, 0u};
            if (w == 7) bc = beacon_issue(rw, FARB(s, rep, pm), lane);
            LBAR();
            qk_project(Ba, Bb, ma, zw, idx);
            LBAR();
            if (w == 7) {
                if (s < NSEG - 1) {
                    if (s > 0) { tk_T0(tk, beacon_finish(bc, rw, FARB(s, rep, pm), lane, budget), a); if (tk.okA) tk_post(tka, s + 1, tk_specA(tk, a), lane); }
                    tk_post(tkr, s, tk.XR, lane);
                    wait_until(tk.XR + 8);
                    tk_Tp(tk, beacon_max<NH>(rw, FARB(s, rep, pm), BC_QK, lane, budget), a);
                    tk_post(tka, s + 1, tk.XA, lane);
                    tk_post(tkr, s + 1, tk_specR(tk, a), lane);
                }
            } else {
                if (w == 4 && s >= 2 && lane == 0) ST4((u32)(s - 2), rw, (FL_Z + r) * 128);
                __builtin_amdgcn_s_sleep(14);
            }
        }
    }
    DRAIN();
    LBAR();
    if (tid == 0) ST4((u32)(NSEG - 1), rw, (FL_Z + r) * 128);
    { const int bid_ = rep + 8 * (8 + r - 18 * rep); (void)bid_; STAMP_FIN; }
    if (budget < 0 && lane == 0) ST4(3u, rw, FL_ERR * 128);
}

__device__ __forceinline__ void cexp_l(float lr, float li, float l, float& er, float& ei) {
    const float mag = __builtin_amdgcn_exp2f(lr * l);
    const float ph = li * l;
    const float pe = fmaf(li, l, -ph);
    const float fr = (ph - floorf(ph)) + pe;
    er = mag * __builtin_amdgcn_cosf(fr);
    ei = mag * __builtin_amdgcn_sinf(fr);
}
constexpr int CV_W = 8 * 260 * 16, CV_U = 96 * 80, CV_CH = CV_W + CV_U;
__device__ __forceinline__ void s4_unit(const Args& a, lptr lds, rsrc_t rw, int unit) {
    const int tid = threadIdx.x, lane = tid & 63, w = __builtin_amdgcn_readfirstlane(tid >> 6);
    const int d0 = unit * 4;
    v2u uv[4];
#pragma unroll
    for (int k = 0; k < 4; ++k) uv[k] = *(const v2u*)(a.u + (size_t)(2 * (tid + 512 * (k >> 1)) + (k & 1)) * DM + d0);
    {
        const int c = tid >> 7, nn = tid & 127, n = (d0 + c) * 128 + nn;
        const float dt = rbf(expf(bf2f(a.logdt[d0 + c])));
        const float lr = dt * bf2f(a.Are[n]) * 1.4426950408889634f, li = dt * bf2f(a.Aim[n]) * 0.15915494309189535f;
        const float br = dt * bf2f(a.Bre[n]), bi = dt * bf2f(a.Bim[n]);
        const float cr = bf2f(a.Cre[n]), ci = bf2f(a.Cim[n]);
        LAS f32x2* T = (LAS f32x2*)(lds + c * CV_CH) + nn;
        float er, ei, pr, pi;
        cexp_l(lr, li, 1.0f, er, ei); pr = er; pi = ei;
#pragma unroll
        for (int o = 0; o < 7; ++o) { T[o * 130] = f32x2{pr, pi}; const float t = pr * er - pi * ei; pi = pr * ei + pi * er; pr = t; }
#pragma unroll
        for (int q4 = 0; q4 < 4; ++q4) {
            if (q4 == 0) cexp_l(lr, li, 128.0f, er, ei);
            cexp_l(lr, li, (float)(128 + 512 * q4), pr, pi);
#pragma unroll
            for (int o = 0; o < 4; ++o) { T[(7 + 4 * q4 + o) * 130] = f32x2{pr, pi}; const float t = pr * er - pi * ei; pi = pr * ei + pi * er; pr = t; }
        }
        const float cbr = cr * br - ci * bi, cbi = cr * bi + ci * br;
        cexp_l(lr, li, 8.0f, er, ei);
#pragma unroll
        for (int q4 = 0; q4 < 4; ++q4) {
            float sr, si;
            cexp_l(lr, li, (float)(32 * q4), sr, si);
            pr = cbr * sr - cbi * si; pi = cbr * si + cbi * sr;
#pragma unroll
            for (int o = 0; o < 4; ++o) { T[(23 + 4 * q4 + o) * 130] = f32x2{pr, pi}; const float t = pr * er - pi * ei; pi = pr * ei + pi * er; pr = t; }
        }
    }
    __syncthreads();
    f32x4 kr[3];
    {
        const int x = lane & 15, g = lane >> 4, c = w & 3, nh = w >> 2;
        const int ah0 = x >> 3, o0 = x & 7;
        const int ah1 = x < 8 ? 2 + (x >> 2) : 4 + ((x - 8) >> 1), o1 = x < 8 ? 2 * (x & 3) : 4 * (x & 1);
        const int ah2 = x < 8 ? 8 + x : 16;
        LAS f32x2* T = (LAS f32x2*)(lds + c * CV_CH) + nh * 64 + g;
        LAS f32x2* Ta0 = T + 7 * 130; LAS f32x2* To0 = T + (o0 ? o0 - 1 : 0) * 130;
        LAS f32x2* Ta1 = T + (6 + ah1) * 130; LAS f32x2* To1 = T + (o1 ? o1 - 1 : 0) * 130;
        LAS f32x2* Ta2 = T + (6 + ah2) * 130; LAS f32x2* Tv = T + (23 + x) * 130;
        const f32x2 one = {1.0f, 0.0f};
        f32x4 ar[3] = {}, ai[3] = {};
#pragma unroll
        for (int jj = 0; jj < 16; ++jj) {
            const f32x2 v = Tv[4 * jj], u2 = Ta2[4 * jj], a1 = Ta1[4 * jj];
            f32x2 a0v = Ta0[4 * jj], e0 = To0[4 * jj], e1 = To1[4 * jj];
            a0v = ah0 ? a0v : one; e0 = o0 ? e0 : one; e1 = o1 ? e1 : one;
            const float u0r = a0v[0] * e0[0] - a0v[1] * e0[1], u0i = a0v[0] * e0[1] + a0v[1] * e0[0];
            const float u1r = a1[0] * e1[0] - a1[1] * e1[1], u1i = a1[0] * e1[1] + a1[1] * e1[0];
            ar[2] = __builtin_amdgcn_mfma_f32_16x16x4f32(u2[0], v[0], ar[2], 0, 0, 0);
            ai[2] = __builtin_amdgcn_mfma_f32_16x16x4f32(u2[1], v[1], ai[2], 0, 0, 0);
            ar[0] = __builtin_amdgcn_mfma_f32_16x16x4f32(u0r, v[0], ar[0], 0, 0, 0);
            ai[0] = __builtin_amdgcn_mfma_f32_16x16x4f32(u0i, v[1], ai[0], 0, 0, 0);
            ar[1] = __builtin_amdgcn_mfma_f32_16x16x4f32(u1r, v[0], ar[1], 0, 0, 0);
            ai[1] = __builtin_amdgcn_mfma_f32_16x16x4f32(u1i, v[1], ai[1], 0, 0, 0);
        }
#pragma unroll
        for (int t = 0; t < 3; ++t) kr[t] = ar[t] - ai[t];
    }
    __syncthreads();
    {
        LAS float* T = (LAS float*)(lds + (w & 3) * CV_CH + 4096 + (w >> 2) * 3072);
#pragma unroll
        for (int t = 0; t < 3; ++t)
#pragma unroll
            for (int r = 0; r < 4; ++r) T[(16 * t + 4 * (lane >> 4) + r) * 16 + (lane & 15)] = kr[t][r];
    }
    __syncthreads();
#pragma unroll
    for (int k = 0; k < 8; ++k) {
        const int i = tid + 512 * k, c = i >> 10, l2 = i & 1023;
        LAS float* T = (LAS float*)(lds + c * CV_CH + 4096);
        float v[2];
#pragma unroll
        for (int e = 0; e < 2; ++e) {
            const int lb = (int)rbf((float)(2 * l2 + e));
            const int hi = lb >> 7, o = lb & 7;
            const int row = lb < 256 ? 8 * hi + o : lb < 512 ? 16 + 4 * (hi - 2) + (o >> 1) : lb < 1024 ? 24 + 2 * (hi - 4) + (o >> 2) : 32 + (hi - 8);
            const int ix = row * 16 + ((lb >> 3) & 15);
            v[e] = T[ix] + T[768 + ix];
        }
        *(LAS u32*)(lds + c * CV_CH + CV_W + 2704 + l2 * 4) = pack2(v[0], v[1]);
    }
    __syncthreads();
    for (int i = tid; i < 4 * 259; i += 512) {
        const int c = i / 259, zz = i - c * 259, z = zz - 3;
        lptr ch = lds + c * CV_CH;
        v4u lo = {0, 0, 0, 0}, hi = {0, 0, 0, 0};
        if (z >= 1) lo = *(LAS v4u*)(ch + CV_W + 2704 + (z - 1) * 16);
        if (z >= 0) hi = *(LAS v4u*)(ch + CV_W + 2704 + z * 16);
        const u32 e[8] = {lo.x, lo.y, lo.z, lo.w, hi.x, hi.y, hi.z, hi.w};
#pragma unroll
        for (int cc = 0; cc < 8; ++cc) {
            v4u o;
#pragma unroll
            for (int k = 0; k < 4; ++k) {
                const int t = 8 + cc - 2 * k;
                if (t & 1) o[k] = (e[t >> 1] >> 16) | (e[t >> 1] << 16);
                else o[k] = (e[t >> 1] & 0xffffu) | (e[(t >> 1) - 1] & 0xffff0000u);
            }
            *(LAS v4u*)(ch + (cc * 260 + zz) * 16) = o;
        }
    }
    __syncthreads();
    for (int i = tid; i < 4 * 160; i += 512) *(LAS v4u*)(lds + (i / 160) * CV_CH + CV_W + (i % 160) * 16) = v4u{0, 0, 0, 0};
#pragma unroll
    for (int k2 = 0; k2 < 2; ++k2) {
        const int l = 2 * (tid + 512 * k2);
        const int o = CV_W + (32 + (l >> 5)) * 80 + (l & 31) * 2;
        const v2u p = uv[2 * k2], q = uv[2 * k2 + 1];
        *(LAS u32*)(lds + 0 * CV_CH + o) = (p.x & 0xffffu) | (q.x << 16);
        *(LAS u32*)(lds + 1 * CV_CH + o) = (p.x >> 16) | (q.x & 0xffff0000u);
        *(LAS u32*)(lds + 2 * CV_CH + o) = (p.y & 0xffffu) | (q.y << 16);
        *(LAS u32*)(lds + 3 * CV_CH + o) = (p.y >> 16) | (q.y & 0xffff0000u);
    }
    __syncthreads();
    const int c = w & 3, par = w >> 2, b = lane & 31, hh = lane >> 5;
    lptr ch = lds + c * CV_CH;
    lptr wa = ch + ((b & 7) * 260 + (b >> 3) - hh + 3) * 16;
    lptr ub = ch + CV_W + (32 + b) * 80 + hh * 16;
    f32x16 y0 = {}, y1 = {};
    wa += par * 64; ub -= par * 80;
    {
        bf16x8 fa[2][2], fb[2][2], fc[2][2];
#pragma unroll
        for (int ks = 0; ks < 2; ++ks) { fa[0][ks] = lds8(wa - ks * 32); fb[0][ks] = lds8(ub + ks * 32); fc[0][ks] = lds8(ub + 2560 + ks * 32); }
#pragma unroll
        for (int i = 0; i < 32; ++i) {
            const int cur = i & 1, nx = cur ^ 1;
            if (i + 1 < 32) {
#pragma unroll
                for (int ks = 0; ks < 2; ++ks) {
                    fa[nx][ks] = lds8(wa + (i + 1) * 128 - ks * 32);
                    if (i + 1 < 16) fb[nx][ks] = lds8(ub - (i + 1) * 160 + ks * 32);
                    fc[nx][ks] = lds8(ub + 2560 - (i + 1) * 160 + ks * 32);
                }
            }
            __builtin_amdgcn_sched_barrier(0);
#pragma unroll
            for (int ks = 0; ks < 2; ++ks) {
                if (i < 16) y0 = MFMA32(fa[cur][ks], fb[cur][ks], y0);
                y1 = MFMA32(fa[cur][ks], fc[cur][ks], y1);
            }
            __builtin_amdgcn_sched_barrier(0);
        }
    }
    __syncthreads();
    {
        LAS f32x4* X = (LAS f32x4*)(ch + par * 4096);
        const f32x16 src = par ? y0 : y1;
#pragma unroll
        for (int k = 0; k < 4; ++k) X[k * 64 + lane] = f32x4{src[4 * k], src[4 * k + 1], src[4 * k + 2], src[4 * k + 3]};
    }
    __syncthreads();
    {
        LAS f32x4* X = (LAS f32x4*)(ch + (1 - par) * 4096);
        lptr Y = ch + 8192 + (32 * par + b) * 72 + hh * 8;
        const f32x16 own = par ? y1 : y0;
#pragma unroll
        for (int k = 0; k < 4; ++k) {
            const f32x4 o = X[k * 64 + lane];
            v2u yv = {pack2(own[4 * k] + o[0], own[4 * k + 1] + o[1]), pack2(own[4 * k + 2] + o[2], own[4 * k + 3] + o[3])};
            *(LAS v2u*)(Y + k * 16) = yv;
        }
    }
    __syncthreads();
    const v2u dk = *(const v2u*)(a.dskip + d0);
#pragma unroll
    for (int k2 = 0; k2 < 2; ++k2) {
        const int i = tid + 512 * k2;
        lptr yp = lds + 8192 + (i >> 4) * 72 + (i & 15) * 4;
        const u32 ya = *(LAS u32*)(yp), yb = *(LAS u32*)(yp + CV_CH), yc = *(LAS u32*)(yp + 2 * CV_CH), yd = *(LAS u32*)(yp + 3 * CV_CH);
        const v2u p = uv[2 * k2], q = uv[2 * k2 + 1];
        v4u o;
        o.x = pack2(bflo(ya) + rbf(bflo(p.x) * bflo(dk.x)), bflo(yb) + rbf(bfhi(p.x) * bfhi(dk.x)));
        o.y = pack2(bflo(yc) + rbf(bflo(p.y) * bflo(dk.y)), bflo(yd) + rbf(bfhi(p.y) * bfhi(dk.y)));
        o.z = pack2(bfhi(ya) + rbf(bflo(q.x) * bflo(dk.x)), bfhi(yb) + rbf(bfhi(q.x) * bfhi(dk.x)));
        o.w = pack2(bfhi(yc) + rbf(bflo(q.y) * bflo(dk.y)), bfhi(yd) + rbf(bfhi(q.y) * bfhi(dk.y)));
        ST16(o, rw, OFF_A4 + unit * 16384 + i * 16);
    }
    __syncthreads();
}
__device__ __forceinline__ void role_s4(const Args& a, lptr lds, rsrc_t rw, int idx) {
    if (idx < 32) {
        const int tid = threadIdx.x, t = idx * 64 + (tid >> 3), j = tid & 7;
        float acc = 0.f;
#pragma unroll
        for (int k = 0; k < 9; ++k) {
            const v4u xv = *(const v4u*)(a.u + (size_t)t * DM + (k * 8 + j) * 8), gv = *(const v4u*)(a.gatew + (k * 8 + j) * 8);
#pragma unroll
            for (int e = 0; e < 4; ++e) acc += rbf(bflo(xv[e]) * bflo(gv[e])) + rbf(bfhi(xv[e]) * bfhi(gv[e]));
        }
        acc += __shfl_xor(acc, 1); acc += __shfl_xor(acc, 2); acc += __shfl_xor(acc, 4);
        acc = rbf(acc);
        if (j == 0) ST4(__float_as_uint(rbf(1.0f / (1.0f + __expf(-acc)))), rw, OFF_G + t * 4);
    }
#pragma unroll 1
    for (int unit = idx; unit < DM / 4; unit += NS4) s4_unit(a, lds, rw, unit);
    DRAIN();
    __syncthreads();
    if (threadIdx.x == 0) ST4(1u, rw, (FL_S + idx) * 128);
}

__device__ __forceinline__ void role_post(const Args& a, lptr lds, rsrc_t rw) {
    const int tid = threadIdx.x, lane = tid & 63, w = __builtin_amdgcn_readfirstlane(tid >> 6);
    const int i16 = lane & 15, g = lane >> 4;
    int budget = SPIN_MAX, vdone0 = -1, vdone1 = -1;
    volatile LAS int* U = (volatile LAS int*)(lds + 81920);
    wait_flags(rw, FL_S, 1, 64, 1, lane, budget);
    wait_flags(rw, FL_S + 64, 1, NS4 - 64, 1, lane, budget);
    constexpr int NFULL = 62 * 6, NUNIT = NFULL + 24;
#pragma unroll 1
    for (;;) {
        if (tid == 0) *U = 1 + (int)__hip_atomic_fetch_add((u32*)(a.ws + FL_Q * 128), 1u, __ATOMIC_RELAXED, __HIP_MEMORY_SCOPE_AGENT);
        __syncthreads();
        const int unit = __builtin_amdgcn_readfirstlane(*U);
        if (unit >= NUNIT || budget < 0) break;
        const bool half = unit >= NFULL;
        const int uu = half ? NFULL + ((unit - NFULL) >> 1) : unit, q0 = half ? (unit - NFULL) & 1 : 0;
        const int s = uu / 6, r = (uu - s * 6) * 6 + (w < 6 ? w : 0);
        bf16x8 b[32];
        v2u av[2]; float gt[2];
        if (w < 6) {
            if (VOWN(s)) { if (vdone1 < s) vdone1 = wait_flags(rw, FL_V + 1, 2, NH, s, lane, budget); }
            else { if (vdone0 < s) vdone0 = wait_flags(rw, FL_V, 2, NH, s, lane, budget); }
            const int vo = OFF_VOX + (s * NR + r) * (NH * 1024) + ((i16 >> 1) & 1) * 16384 + ((g & 1) * 16 + (g >> 1) * 8 + 2 * (i16 >> 2) + (i16 & 1)) * 16;
#pragma unroll
            for (int k = 0; k < 32; ++k) b[k] = as8(LD16(rw, vo + (k & 7) * 512, (k >> 3) * 4096));
#pragma unroll
            for (int qt = 0; qt < 2; ++qt) {
                av[qt] = LD8(rw, OFF_A4 + (4 * r + g) * 16384 + (32 * s + 16 * (qt ^ q0) + i16) * 8, 0);
                gt[qt] = __uint_as_float(LD4(rw, OFF_G + (32 * s + 16 * (qt ^ q0) + i16) * 4, 0));
            }
        }
        v4u z[8];
        const int zo = OFF_ZH + (s * NR + r) * (NH * 256) + ((i16 >> 1) & 1) * 4096 + g * 128 + (2 * (i16 >> 2) + (i16 & 1)) * 16;
        if (half) {
            wait_flags(rw, FL_Z + 2 * r, 1, 2, s, lane, budget, true);
#pragma unroll
            for (int k = 0; k < 8; ++k) z[k] = LD16(rw, zo + k * 512, 0);
            wait_flags(rw, FL_P + (s & 3), 4, 16, s, lane, budget, true);
            v4u pv[5];
#pragma unroll
            for (int k = 0; k < 5; ++k) pv[k] = LD16(rw, OFF_PX + s * 81920 + q0 * 40960 + tid * 16, k * 8192);
#pragma unroll
            for (int k = 0; k < 5; ++k) *(LAS v4u*)(lds + k * 8192 + tid * 16) = pv[k];
        } else {
            wait_flags(rw, FL_P + (s & 3), 4, 16, s, lane, budget);
            v4u pv[10];
#pragma unroll
            for (int k = 0; k < 10; ++k) pv[k] = LD16(rw, OFF_PX + s * 81920 + tid * 16, k * 8192);
            wait_flags(rw, FL_Z + 2 * r, 1, 2, s, lane, budget);
#pragma unroll
            for (int k = 0; k < 8; ++k) z[k] = LD16(rw, zo + k * 512, 0);
            MB();
#pragma unroll
            for (int k = 0; k < 10; ++k) *(LAS v4u*)(lds + k * 8192 + tid * 16) = pv[k];
        }
        __syncthreads();
        if (w < 6) {
#pragma unroll 1
            for (int qt = 0; qt < (half ? 1 : 2); ++qt) {
                f32x4 d = {}, e = {};
                lptr pl = lds + qt * 40960 + lane * 16;
                bf16x8 f[6];
#pragma unroll
                for (int i = 0; i < 6; ++i) f[i] = lds8(pl + i * 1024);
                __builtin_amdgcn_sched_barrier(0);
#pragma unroll
                for (int k = 0; k < 40; ++k) {
                    const bf16x8 v = k < 32 ? b[k < 32 ? k : 0] : as8(z[k < 32 ? 0 : k - 32]);
                    if (k & 1) e = MFMA16(v, f[k % 6], e); else d = MFMA16(v, f[k % 6], d);
                    if (k + 6 < 40) f[k % 6] = lds8(pl + (k + 6) * 1024);
                    __builtin_amdgcn_sched_barrier(0);
                }
                d += e;
                const float gq = qt ? gt[1] : gt[0], og = rbf(1.0f - gq);
                const v2u aq = qt ? av[1] : av[0];
                const float r0 = rbf(gq * bflo(aq.x)) + rbf(og * rbf(d[0])), r1 = rbf(gq * bfhi(aq.x)) + rbf(og * rbf(d[1]));
                const float r2 = rbf(gq * bflo(aq.y)) + rbf(og * rbf(d[2])), r3 = rbf(gq * bfhi(aq.y)) + rbf(og * rbf(d[3]));
                v2u ov = {pack2(r0, r1), pack2(r2, r3)};
                *(v2u*)(a.out + (size_t)(32 * s + 16 * (qt ^ q0) + i16) * DM + 16 * r + 4 * g) = ov;
            }
        }
        if (half) break;
        LBAR();
    }
    if (budget < 0 && lane == 0) ST4(6u, rw, FL_ERR * 128);
}

__global__ void __launch_bounds__(512, 2) k_all(Args a) {
    extern __shared__ __attribute__((aligned(16))) char smem[];
    lptr lds = (lptr)smem;
    const rsrc_t rw = __builtin_amdgcn_make_buffer_rsrc(a.ws, 0, WS_BYTES, 0x00020000);
    RST(0);
    if (threadIdx.x == 0) {
        const int x = (int)(__builtin_amdgcn_s_getreg(20 | (3 << 11)) & 7u);
        int role = 0;
        for (int k = 0; k < 8; ++k) {
            const int gq = (x + k) & 7;
            const int t = 1 + (int)__hip_atomic_fetch_add((u32*)(a.ws + (FL_T + gq) * 128), 1u, __ATOMIC_RELAXED, __HIP_MEMORY_SCOPE_AGENT);
            if (t < 32) { role = gq + 8 * t; break; }
        }
        *(volatile LAS int*)lds = role;
    }
    __syncthreads();
    const int b = __builtin_amdgcn_readfirstlane(*(volatile LAS int*)lds), xg = b & 7, slot = b >> 3;
    __syncthreads();
    WSTAMP(516);

    if (b == 8 * 8) role_probe(rw);
    if (slot >= 20) { role_wvo(a, lds, rw, (xg & 3) * 24 + (xg < 4 ? 12 : 0) + slot - 20); __syncthreads(); }
    if ((xg < 4 && slot >= 26) || (xg >= 4 && slot >= 20)) {
        const int idx = xg < 4 ? 48 + (slot - 26) * 4 + xg : (31 - slot) * 4 + (xg - 4);
        role_s4(a, lds, rw, idx);
        __syncthreads();
    }
    bool post = false;
    if (xg < 4) {
        if (slot < 8) role_qk(a, lds, rw, xg * 8 + slot, xg);
        else if (slot < 26) role_rz(a, lds, rw, xg * 18 + slot - 8, xg);
    } else {
        const int j = xg - 4;
        post = true;
        if (slot < 16) { role_vvo(a, lds, rw, j * 8 + (slot >> 1), slot & 1); post = slot < a.mask; }
        else if (slot < 20) role_k2(a, lds, rw, j * 4 + slot - 16);
        else if (slot < 28) { role_q2p(a, lds, rw, j * 4 + ((slot - 20) >> 1), slot & 1); post = false; }
    }
    WSTAMP(517);
    if (post) { __syncthreads(); role_post(a, lds, rw); }
    WSTAMP(518);
}

extern "C" void kernel_launch(void* const* d_in, const int* in_sizes, int n_in, void* d_out, int out_size, void* d_ws, size_t ws_size, hipStream_t stream) {
    Args a;
    a.u = (const u16*)d_in[0]; a.Are = (const u16*)d_in[1]; a.Aim = (const u16*)d_in[2]; a.Bre = (const u16*)d_in[3]; a.Bim = (const u16*)d_in[4];
    a.Cre = (const u16*)d_in[5]; a.Cim = (const u16*)d_in[6]; a.logdt = (const u16*)d_in[7]; a.dskip = (const u16*)d_in[8];
    a.mem0 = (const u16*)d_in[10]; a.Wq = (const u16*)d_in[11]; a.Wk = (const u16*)d_in[12]; a.Wv = (const u16*)d_in[13]; a.Wo = (const u16*)d_in[14];
    a.gatew = (const u16*)d_in[16]; a.out = (u16*)d_out; a.ws = (char*)d_ws;
    a.pd = 10; a.pha = 8; a.phz = 0; a.gap = 30; a.mask = 4; a.pad0 = 8;
    const int LDS_ALL = 160 * 1024;
    (void)hipFuncSetAttribute((const void*)k_all, hipFuncAttributeMaxDynamicSharedMemorySize, LDS_ALL);
    (void)hipMemsetAsync(d_ws, 0xFF, INIT_BYTES, stream);
    hipLaunchKernelGGL(k_all, dim3(256), dim3(512), LDS_ALL, stream, a);
}
```

```cpp
#include <hip/hip_runtime.h>
#include <hip/hip_bf16.h>
#include <stdint.h>
#include <stdio.h>
#include <stdlib.h>

typedef unsigned short u16;
typedef unsigned int u32;
typedef u32 v4u __attribute__((ext_vector_type(4)));
typedef u32 v2u __attribute__((ext_vector_type(2)));
typedef float f32x4 __attribute__((ext_vector_type(4)));
typedef float f32x2 __attribute__((ext_vector_type(2)));
typedef float f32x16 __attribute__((ext_vector_type(16)));
typedef __bf16 bf16x2 __attribute__((ext_vector_type(2)));
typedef __bf16 bf16x8 __attribute__((ext_vector_type(8)));
typedef __bf16 bf16x16 __attribute__((ext_vector_type(16)));
typedef short s16x4 __attribute__((ext_vector_type(4)));
#define LAS __attribute__((address_space(3)))
typedef LAS char* lptr;
typedef __amdgpu_buffer_rsrc_t rsrc_t;

constexpr int SEQ = 2048, DM = 576, NH = 32, DH = 128, NSEG = 64, NR = 36, NR8 = 72, NREP = 4;
constexpr int MEMB = 9216, BC_QK = 80;
constexpr int FL_K = 0, FL_V = 16, FL_M = 80, FL_P = 112, FL_S = 176, FL_Q = 254, FL_ERR = 255, FL_Z = 256, FL_W = 328, FL_T = 424, FL_PAR = 432, NS4 = 72;
constexpr int OFF_RING = 65536;
constexpr int N_MEMA = 0, N_PX = 16384, N_PMM = 49152, N_MEMB = 49152 + 4096, N_PROBE = 49152 + 6144;
constexpr int INIT_BYTES = OFF_RING + 16 * 65536;
constexpr int OFF_ZH = INIT_BYTES;
constexpr int OFF_KX = OFF_ZH + NSEG * NR * NH * 256;
constexpr int OFF_VOX = OFF_KX + NH * SEQ * DH * 2;
constexpr int OFF_KM = OFF_VOX + NSEG * NR * NH * 1024;
constexpr int OFF_PX = OFF_KM + NSEG * NH * 2048;
constexpr int OFF_A4 = OFF_PX + NSEG * 81920;
constexpr int OFF_G = OFF_A4 + SEQ * DM * 2;
constexpr int OFF_WVO = OFF_G + SEQ * 4;
constexpr int OFF_ST = OFF_WVO + NR8 * 294912;
constexpr int WS_BYTES = OFF_ST + 256 * 520 * 8 + 4096;

#define BIDX bid_
#define RST(k)
#define STAMP(k)
#define STAMP_FLUSH
#define STAMP_INIT
#define STAMP_FIN
#define WSTAMP(k)

struct Args {
    const u16* u; const u16* Are; const u16* Aim; const u16* Bre; const u16* Bim; const u16* Cre; const u16* Cim;
    const u16* logdt; const u16* dskip; const u16* mem0; const u16* Wq; const u16* Wk; const u16* Wv; const u16* Wo;
    const u16* gatew; u16* out; char* ws;
    int pd, pha, phz, gap, mask, pad0;
};

__device__ __forceinline__ float bf2f(u16 v) { return __uint_as_float((u32)v << 16); }
__device__ __forceinline__ float bflo(u32 v) { return __uint_as_float(v << 16); }
__device__ __forceinline__ float bfhi(u32 v) { return __uint_as_float(v & 0xffff0000u); }
__device__ __forceinline__ u32 pack2(float lo, float hi) { f32x2 f = {lo, hi}; bf16x2 b = __builtin_convertvector(f, bf16x2); return __builtin_bit_cast(u32, b); }
__device__ __forceinline__ float rbf(float x) { return bflo(pack2(x, x)); }
__device__ __forceinline__ u32 nos(u32 v) { return min(v, 0xFFFFFFFEu); }
__device__ __forceinline__ v2u trd(lptr p) { return __builtin_bit_cast(v2u, __builtin_amdgcn_ds_read_tr16_b64_v4i16((LAS s16x4*)p)); }
__device__ __forceinline__ bf16x8 as8(v4u v) { return __builtin_bit_cast(bf16x8, v); }
__device__ __forceinline__ bf16x8 as8(v2u a, v2u b) { v4u v = {a.x, a.y, b.x, b.y}; return __builtin_bit_cast(bf16x8, v); }
__device__ __forceinline__ v4u asu(bf16x8 v) { return __builtin_bit_cast(v4u, v); }
__device__ __forceinline__ bf16x16 cat16(bf16x8 a, bf16x8 b) { return __builtin_shufflevector(a, b, 0, 1, 2, 3, 4, 5, 6, 7, 8, 9, 10, 11, 12, 13, 14, 15); }
__device__ __forceinline__ bf16x8 lds8(lptr p) { return *(LAS bf16x8*)p; }
__device__ __forceinline__ u32 bad4(v4u v) { return max(max(v.x, v.y), max(v.z, v.w)); }
#define NEARB(s, rep, pm) (OFF_RING + ((((s) & 3) * 4 + (rep)) << 16) + ((((pm) >> (((s) & 3) * 4 + (rep))) & 1) << 13))
#define FARB(s, rep, pm) ((NEARB(s, rep, pm) ^ 8192) + (((s) & 4) << 13))
#define BCO(slot) (((slot) & 63) * 128 + ((slot) >> 6) * 16384)
#define LBAR() do { asm volatile("s_waitcnt lgkmcnt(0)" ::: "memory"); __builtin_amdgcn_s_barrier(); asm volatile("" ::: "memory"); } while (0)
#define MB() asm volatile("" ::: "memory")
#define DRAIN() asm volatile("s_waitcnt vmcnt(0)" ::: "memory")
#define PIN4(x) asm volatile("" : "+v"(x))
#define MFMA16(a, b, c) __builtin_amdgcn_mfma_f32_16x16x32_bf16((a), (b), (c), 0, 0, 0)
#define MFMA32(a, b, c) __builtin_amdgcn_mfma_f32_32x32x16_bf16((a), (b), (c), 0, 0, 0)
#define SMF(a, b, c, idx) __builtin_amdgcn_smfmac_f32_16x16x64_bf16((a), (b), (c), (idx), 0, 0)
#define LD16(r, vo, so) __builtin_amdgcn_raw_buffer_load_b128((r), (vo), (so), 16)
#define LD8(r, vo, so) __builtin_amdgcn_raw_buffer_load_b64((r), (vo), (so), 16)
#define LD4(r, vo, so) __builtin_amdgcn_raw_buffer_load_b32((r), (vo), (so), 16)
#define ST16(v, r, vo) __builtin_amdgcn_raw_buffer_store_b128((v), (r), (vo), 0, 16)
#define ST8(v, r, vo) __builtin_amdgcn_raw_buffer_store_b64((v), (r), (vo), 0, 16)
#define ST4(v, r, vo) __builtin_amdgcn_raw_buffer_store_b32((v), (r), (vo), 0, 16)

__device__ __forceinline__ float swap32_sum(float a, float b) {
    auto r = __builtin_amdgcn_permlane32_swap(__float_as_uint(a), __float_as_uint(b), false, false);
    return __uint_as_float(r[0]) + __uint_as_float(r[1]); }
__device__ __forceinline__ float xmax(float m) {
    auto r = __builtin_amdgcn_permlane16_swap(__float_as_uint(m), __float_as_uint(m), false, false);
    m = fmaxf(__uint_as_float(r[0]), __uint_as_float(r[1]));
    auto q = __builtin_amdgcn_permlane32_swap(__float_as_uint(m), __float_as_uint(m), false, false);
    return fmaxf(__uint_as_float(q[0]), __uint_as_float(q[1])); }
__device__ __forceinline__ float xsum(float m) {
    auto r = __builtin_amdgcn_permlane16_swap(__float_as_uint(m), __float_as_uint(m), false, false);
    m = __uint_as_float(r[0]) + __uint_as_float(r[1]);
    auto q = __builtin_amdgcn_permlane32_swap(__float_as_uint(m), __float_as_uint(m), false, false);
    return __uint_as_float(q[0]) + __uint_as_float(q[1]); }
__device__ __forceinline__ float vmax(float a, float b) { float r; asm("v_max_f32 %0, %1, %2" : "=v"(r) : "v"(a), "v"(b)); return r; }
__device__ __forceinline__ float vmax3(float a, float b, float c) { float r; asm("v_max3_f32 %0, %1, %2, %3" : "=v"(r) : "v"(a), "v"(b), "v"(c)); return r; }
__device__ __forceinline__ u32 ror8(u32 v) { return (u32)__builtin_amdgcn_update_dpp(0, (int)v, 0x128, 0xf, 0xf, true); }
__device__ __forceinline__ float ror8f(float v) { return __uint_as_float(ror8(__float_as_uint(v))); }
__device__ __forceinline__ int wave_max(int d) {
    d = max(d, __builtin_amdgcn_update_dpp(d, d, 0x128, 0xf, 0xf, false));
    d = max(d, __builtin_amdgcn_update_dpp(d, d, 0x124, 0xf, 0xf, false));
    d = max(d, __builtin_amdgcn_update_dpp(d, d, 0x122, 0xf, 0xf, false));
    d = max(d, __builtin_amdgcn_update_dpp(d, d, 0x121, 0xf, 0xf, false));
    return max(max(__builtin_amdgcn_readlane(d, 0), __builtin_amdgcn_readlane(d, 16)), max(__builtin_amdgcn_readlane(d, 32), __builtin_amdgcn_readlane(d, 48)));
}

constexpr int SPIN_MAX = 2000000;
__device__ __forceinline__ int wait_flags(rsrc_t rw, int line0, int stride, int n, int need, int lane, int& budget, bool fast = false) {
    const int vo = (line0 + (lane < n ? lane : 0) * stride) * 128;
    int mn;
    for (;;) {
        const int v = (int)LD4(rw, vo, 0);
        MB();
        mn = -wave_max(-v);
        if (mn >= need) break;
        if (--budget < 0) break;
        if (fast) __builtin_amdgcn_s_sleep(2); else __builtin_amdgcn_s_sleep(12);
    }
    return mn;
}

template <int NRW, int CW>
__device__ __forceinline__ void stage_rows(const u16* g, int gs, lptr lds, int ls, int tid) {
    constexpr int N = NRW * CW / 512;
    static_assert(NRW * CW % 512 == 0, "");
    v4u v[N];
#pragma unroll
    for (int j = 0; j < N; ++j) { const int i = tid + 512 * j, row = i / CW, c = i % CW; v[j] = *(const v4u*)(g + (size_t)row * gs + c * 8); }
#pragma unroll
    for (int j = 0; j < N; ++j) { const int i = tid + 512 * j, row = i / CW, c = i % CW; *(LAS v4u*)(lds + row * ls + c * 16) = v[j]; }
}
template <int NRW, int CW>
__device__ __forceinline__ void rows_ld(v4u (&v)[NRW * CW / 512], const u16* g, int gs, int tid) {
#pragma unroll
    for (int j = 0; j < NRW * CW / 512; ++j) { const int i = tid + 512 * j, row = i / CW, c = i % CW; v[j] = *(const v4u*)(g + (size_t)row * gs + c * 8); }
}
template <int NRW, int CW>
__device__ __forceinline__ void rows_st(const v4u (&v)[NRW * CW / 512], lptr lds, int ls, int tid) {
#pragma unroll
    for (int j = 0; j < NRW * CW / 512; ++j) { const int i = tid + 512 * j, row = i / CW, c = i % CW; *(LAS v4u*)(lds + row * ls + c * 16) = v[j]; }
}
__device__ __forceinline__ bf16x8 frag16(lptr base, int ls, int lane) {
    const int i16 = lane & 15, bg = lane >> 4, q = i16 >> 2, p = i16 & 3;
    lptr a = base + (8 * bg + q) * ls + p * 8;
    return as8(trd(a), trd(a + 4 * ls));
}
__device__ __forceinline__ bf16x8 frag32p(lptr base, int ls, int lane) {
    const int i16 = lane & 15, G = lane >> 4, q = i16 >> 2, p = i16 & 3;
    lptr a = base + (8 * (G >> 1) + q) * ls + (16 * (p & 1) + 8 * (G & 1) + 4 * (p >> 1)) * 2;
    return as8(trd(a), trd(a + 4 * ls));
}

constexpr int XS = 1168;
__device__ __forceinline__ void xload(v4u (&st)[9], const u16* x, int tok0, int tid) {
#pragma unroll
    for (int j = 0; j < 9; ++j) st[j] = *(const v4u*)(x + (size_t)tok0 * DM + (tid + 512 * j) * 8);
}
__device__ __forceinline__ void xstore(const v4u (&st)[9], lptr xl, int tid) {
#pragma unroll
    for (int j = 0; j < 9; ++j) { const int i = tid + 512 * j; *(LAS v4u*)(xl + i * 16 + (i / 72) * 16) = st[j]; }
}
__device__ __forceinline__ void load_w2(bf16x8 (&A)[36], const u16* W, int h0, lptr lds, int tid, int lane, int w) {
    v4u v[6][6];
#pragma unroll
    for (int c = 0; c < 6; ++c) rows_ld<96, 32>(v[c], W + (size_t)c * 96 * 4096 + h0 * DH, 4096, tid);
    __builtin_amdgcn_sched_barrier(0);
#pragma unroll
    for (int c = 0; c < 6; ++c) {
        lptr l = lds + (c & 1) * (96 * 544);
        rows_st<96, 32>(v[c], l, 544, tid);
        LBAR();
#pragma unroll
        for (int ks = 0; ks < 6; ++ks) A[c * 6 + ks] = frag32p(l + ks * 16 * 544 + w * 64, 544, lane);
        __builtin_amdgcn_sched_barrier(0);
    }
    LBAR();
}

__device__ __forceinline__ f32x16 proj_tile(const bf16x8 (&A)[36], lptr xb) {
    f32x16 acc = {};
    bf16x8 f[6];
#pragma unroll
    for (int i = 0; i < 6; ++i) f[i] = lds8(xb + i * 32);
    __builtin_amdgcn_sched_barrier(0);
#pragma unroll
    for (int ks = 0; ks < 36; ++ks) {
        acc = MFMA32(A[ks], f[ks % 6], acc);
        if (ks + 6 < 36) f[ks % 6] = lds8(xb + (ks + 6) * 32);
        __builtin_amdgcn_sched_barrier(0);
    }
    return acc;
}
__device__ __forceinline__ void role_k2(const Args& a, lptr lds, rsrc_t rw, int cu) {
    const int tid = threadIdx.x, lane = tid & 63, w = __builtin_amdgcn_readfirstlane(tid >> 6);
    v4u st[9];
    bf16x8 A[36];
    xload(st, a.u, 0, tid);
    load_w2(A, a.Wk, 2 * cu, lds, tid, lane, w);
    RST(4);
    xstore(st, lds, tid);
    __syncthreads();
    const int o0 = OFF_KX + (2 * cu + (w >> 2)) * (NSEG * 8192) + ((w & 3) * 4 + (lane >> 5) * 2) * 512 + (lane & 31) * 16;
    for (int blk = 0; blk < 32; ++blk) {
        lptr xl = lds + (blk & 1) * (64 * XS);
        if (blk + 1 < 32) xload(st, a.u, (blk + 1) * 64, tid);
#pragma unroll
        for (int tt = 0; tt < 2; ++tt) {
            const f32x16 acc = proj_tile(A, xl + (32 * tt + (lane & 31)) * XS + (lane >> 5) * 16);
            v4u c0 = {pack2(acc[0], acc[1]), pack2(acc[2], acc[3]), pack2(acc[4], acc[5]), pack2(acc[6], acc[7])};
            v4u c1 = {pack2(acc[8], acc[9]), pack2(acc[10], acc[11]), pack2(acc[12], acc[13]), pack2(acc[14], acc[15])};
            const int o = o0 + (blk * 2 + tt) * 8192;
            ST16(c0, rw, o); ST16(c1, rw, o + 512);
            if (blk == 0 && tt == 0) { DRAIN(); LBAR(); if (tid == 0) ST4(0u, rw, (FL_K + cu) * 128); RST(5); }
        }
        if (blk + 1 < 32) xstore(st, lds + ((blk + 1) & 1) * (64 * XS), tid);
        const bool full = blk < 4 || blk == 31;
        if (full) DRAIN(); else asm volatile("s_waitcnt vmcnt(4)" ::: "memory");
        LBAR();
        if (tid == 0) ST4((u32)(full ? 2 * blk + 1 : 2 * blk - 1), rw, (FL_K + cu) * 128);
    }
}

constexpr int VS = 272;
__device__ __forceinline__ void role_q2p(const Args& a, lptr lds, rsrc_t rw, int cu, int par) {
    const int tid = threadIdx.x, lane = tid & 63, w = __builtin_amdgcn_readfirstlane(tid >> 6);
    v4u st[9];
    bf16x8 A[36];
    load_w2(A, a.Wq, 2 * cu, lds, tid, lane, w);
    lptr xl = lds, ql = lds + 64 * XS;
    LAS int* cnt = (LAS int*)(lds + 64 * XS + 4 * 64 * VS);
    if (tid < 2) cnt[tid] = 0;
    xload(st, a.u, par * 64, tid);
    xstore(st, xl, tid);
    __syncthreads();
    int budget = SPIN_MAX, kdone = -1;
    const int i16 = lane & 15, g = lane >> 4;
    const int sg = w >> 2, hh = (w >> 1) & 1, qt = w & 1, h = 2 * cu + hh;
    const float c2 = 0.08838834764831845f * 1.4426950408889634f;
    for (int it = -1; it < 16; ++it) {
        const int blk = 2 * it + par;
        if (it + 1 < 16) {
            if (it + 2 < 16) xload(st, a.u, (blk + 4) * 64, tid);
            lptr qb = ql + (((it + 1) & 1) * 2 + (w >> 2)) * (64 * VS) + ((w & 3) * 32 + (lane >> 5) * 16) * 2;
#pragma unroll
            for (int tt = 0; tt < 2; ++tt) {
                const f32x16 acc = proj_tile(A, xl + (32 * tt + (lane & 31)) * XS + (lane >> 5) * 16);
                v4u c0 = {pack2(acc[0], acc[1]), pack2(acc[2], acc[3]), pack2(acc[4], acc[5]), pack2(acc[6], acc[7])};
                v4u c1 = {pack2(acc[8], acc[9]), pack2(acc[10], acc[11]), pack2(acc[12], acc[13]), pack2(acc[14], acc[15])};
                lptr qp = qb + (32 * tt + (lane & 31)) * VS;
                *(LAS v4u*)qp = c0; *(LAS v4u*)(qp + 16) = c1;
            }
            __syncthreads();
            if (it + 2 < 16) xstore(st, xl, tid);
        }
        if (it >= 0) {
            const int s = 2 * blk + sg;
            if (kdone < s) kdone = wait_flags(rw, FL_K + cu, 1, 1, s, lane, budget);
            bf16x8 k0[4], k1[4], km[4], qf[4];
            const int ko = OFF_KX + (h * NSEG + s) * 8192 + g * 512 + i16 * 16;
#pragma unroll
            for (int ks = 0; ks < 4; ++ks) { k0[ks] = as8(LD16(rw, ko, ks * 2048)); k1[ks] = as8(LD16(rw, ko + 256, ks * 2048)); }
            lptr qp = ql + ((it & 1) * 2 + hh) * (64 * VS) + (32 * sg + 16 * qt + i16) * VS + g * 16;
#pragma unroll
            for (int ks = 0; ks < 4; ++ks) qf[ks] = lds8(qp + ks * 64);
            f32x4 S0 = {}, S1 = {}, Sm = {};
#pragma unroll
            for (int ks = 0; ks < 4; ++ks) { S0 = MFMA16(k0[ks], qf[ks], S0); S1 = MFMA16(k1[ks], qf[ks], S1); }
            wait_flags(rw, FL_M + h, 1, 1, s, lane, budget, s >= 62);
            const int mo = OFF_KM + (s * NH + h) * 2048 + g * 128 + (i16 & 7) * 16;
#pragma unroll
            for (int ks = 0; ks < 4; ++ks) km[ks] = as8(LD16(rw, mo + ks * 512, 0));
#pragma unroll
            for (int ks = 0; ks < 4; ++ks) Sm = MFMA16(km[ks], qf[ks], Sm);
            const int qi = 16 * qt + i16, t0 = 4 * g;
#pragma unroll
            for (int r = 0; r < 4; ++r) {
                if (t0 + r > qi) S0[r] = -INFINITY;
                if (t0 + r + 16 > qi) S1[r] = -INFINITY;
                if (g >= 2) Sm[r] = -INFINITY;
            }
            float m = fmaxf(fmaxf(fmaxf(S0[0], S0[1]), fmaxf(S0[2], S0[3])), fmaxf(fmaxf(S1[0], S1[1]), fmaxf(S1[2], S1[3])));
            m = fmaxf(m, fmaxf(fmaxf(Sm[0], Sm[1]), fmaxf(Sm[2], Sm[3])));
            m = xmax(m);
            const float nm = -m * c2;
            float sum = 0.f;
#pragma unroll
            for (int r = 0; r < 4; ++r) {
                S0[r] = __builtin_amdgcn_exp2f(fmaf(S0[r], c2, nm)); S1[r] = __builtin_amdgcn_exp2f(fmaf(S1[r], c2, nm)); Sm[r] = __builtin_amdgcn_exp2f(fmaf(Sm[r], c2, nm));
                sum += S0[r] + S1[r] + Sm[r];
            }
            sum = xsum(sum);
            const float inv = __builtin_amdgcn_rcpf(sum);
            v4u px = {pack2(S0[0] * inv, S0[1] * inv), pack2(S0[2] * inv, S0[3] * inv), pack2(S1[0] * inv, S1[1] * inv), pack2(S1[2] * inv, S1[3] * inv)};
            const int po = OFF_PX + (s * 2 + qt) * 40960;
            ST16(px, rw, po + h * 1024 + lane * 16);
            const u32 ma = pack2(Sm[0] * inv, Sm[1] * inv), mb = pack2(Sm[2] * inv, Sm[3] * inv);
            auto ra = __builtin_amdgcn_permlane16_swap(ma, ma, false, false);
            auto rb = __builtin_amdgcn_permlane16_swap(mb, mb, false, false);
            v4u pm = {ra[0], rb[0], ra[1], rb[1]};
            if (lane < 16) ST16(pm, rw, po + 32768 + h * 256 + lane * 16);
            DRAIN();
            if (lane == 0) {
                const int old = __hip_atomic_fetch_add(cnt + sg, 1, __ATOMIC_RELAXED, __HIP_MEMORY_SCOPE_WORKGROUP);
                if ((old & 3) == 3) ST4((u32)s, rw, (FL_P + 4 * cu + 2 * par + sg) * 128);
            }
        }
        __syncthreads();
    }
    if (budget < 0 && lane == 0) ST4(5u, rw, FL_ERR * 128);
}

__device__ __forceinline__ void role_vvo(const Args& a, lptr lds, rsrc_t rw, int h, int par) {
    const int tid = threadIdx.x, lane = tid & 63, w = __builtin_amdgcn_readfirstlane(tid >> 6);
    v4u st[9];
#pragma unroll
    for (int j = 0; j < 9; ++j) { const int i = tid + 512 * j; st[j] = *(const v4u*)(a.u + (size_t)(par * 32 + (i >= 2304 ? 32 : 0)) * DM + i * 8); }
    bf16x8 Av[18], Bo[5][4];
    {
        v4u v[3][6], o[2][9];
#pragma unroll
        for (int c = 0; c < 3; ++c) rows_ld<192, 16>(v[c], a.Wv + (size_t)c * 192 * 4096 + h * DH, 4096, tid);
#pragma unroll
        for (int c = 0; c < 2; ++c) rows_ld<64, 72>(o[c], a.Wo + ((size_t)h * DH + c * 64) * DM, DM, tid);
        __builtin_amdgcn_sched_barrier(0);
#pragma unroll
        for (int c = 0; c < 3; ++c) {
            lptr l = lds + (c & 1) * 77824;
            rows_st<192, 16>(v[c], l, 288, tid);
            LBAR();
#pragma unroll
            for (int ks = 0; ks < 6; ++ks) Av[c * 6 + ks] = frag16(l + ks * 32 * 288 + w * 32, 288, lane);
            __builtin_amdgcn_sched_barrier(0);
        }
#pragma unroll
        for (int c = 0; c < 2; ++c) {
            lptr l = lds + ((c + 1) & 1) * 77824;
            rows_st<64, 72>(o[c], l, 1184, tid);
            LBAR();
#pragma unroll
            for (int j = 0; j < 5; ++j) {
                const int r = (w + 8 * j < NR) ? w + 8 * j : w;
#pragma unroll
                for (int ks = 0; ks < 2; ++ks) Bo[j][c * 2 + ks] = frag16(l + ks * 32 * 1184 + r * 32, 1184, lane);
            }
            __builtin_amdgcn_sched_barrier(0);
        }
        LBAR();
    }
    RST(4);
    lptr xl = lds, vl = lds + 64 * XS;
    xstore(st, xl, tid);
    __syncthreads();
    const int rho = lane & 15, bg = lane >> 4;
    const int vo0 = OFF_VOX + ((2 * w + ((rho >> 1) & 1)) * 16 + (h >> 1)) * 1024 + ((2 * (h & 1) + (bg & 1)) * 16 + (bg >> 1) * 8 + 2 * (rho >> 2) + (rho & 1)) * 16;
    const int fo = (FL_V + 2 * h + par) * 128;
    auto stage1 = [&](int tt) {
        f32x4 acc = {};
        lptr xb = xl + (16 * tt + rho) * XS + bg * 16;
        bf16x8 f[6];
#pragma unroll
        for (int i = 0; i < 6; ++i) f[i] = lds8(xb + i * 64);
        __builtin_amdgcn_sched_barrier(0);
#pragma unroll
        for (int ks = 0; ks < 18; ++ks) {
            acc = MFMA16(Av[ks], f[ks % 6], acc);
            if (ks + 6 < 18) f[ks % 6] = lds8(xb + (ks + 6) * 64);
            __builtin_amdgcn_sched_barrier(0);
        }
        v2u o = {pack2(acc[0], acc[1]), pack2(acc[2], acc[3])};
        *(LAS v2u*)(vl + (16 * tt + rho) * VS + (16 * w + 4 * bg) * 2) = o;
    };
    auto stage2 = [&](int sg, int s) {
        bf16x8 Af[2][4];
#pragma unroll
        for (int half = 0; half < 2; ++half)
#pragma unroll
            for (int ks = 0; ks < 4; ++ks) Af[half][ks] = lds8(vl + (32 * sg + rho + 16 * half) * VS + ks * 64 + bg * 16);
#pragma unroll
        for (int j = 0; j < 5; ++j) {
            if (j < 4 || w < 4) {
                f32x4 d0 = {}, d1 = {};
#pragma unroll
                for (int ks = 0; ks < 4; ++ks) { d0 = MFMA16(Af[0][ks], Bo[j][ks], d0); d1 = MFMA16(Af[1][ks], Bo[j][ks], d1); }
                v4u o = {pack2(d0[0], d0[1]), pack2(d0[2], d0[3]), pack2(d1[0], d1[1]), pack2(d1[2], d1[3])};
                ST16(o, rw, vo0 + (s * NR + 8 * j) * (NH * 1024));
            }
        }
    };
    int prev = -1;
#pragma unroll 1
    for (int un = 0; un < 32; ++un) {
        const int sg = un & 1, blk = (un >> 1) * 2 + par;
        const int s = un < 2 ? par + 2 * un : 2 * blk + sg;
        if (sg == 0 && blk + 2 < 32) xload(st, a.u, (blk + 2) * 64, tid);
        stage1(2 * sg); stage1(2 * sg + 1);
        if (sg == 1) DRAIN(); else if (blk + 2 < 32) asm volatile("s_waitcnt vmcnt(9)" ::: "memory"); else DRAIN();
        LBAR();
        if (tid == 0 && prev >= 0) ST4((u32)prev, rw, fo);
        if (sg == 1 && blk + 2 < 32) xstore(st, xl, tid);
        stage2(sg, s);
        prev = s;
        if (un == 0 || un == 31) { DRAIN(); LBAR(); if (tid == 0) ST4((u32)s, rw, fo); if (un == 0) RST(5); }
        else if (sg == 1) LBAR();
    }
}
#define VOWN(s) ((s) < 4 ? ((s) & 1) : (((s) >> 1) & 1))

__device__ __forceinline__ u32 rt32() { return (u32)__builtin_amdgcn_s_memrealtime(); }
__device__ __forceinline__ void wait_until(u32 t) {
    if ((int)(t - rt32()) > 1000) return;
    while ((int)(rt32() - t) < -24) __builtin_amdgcn_s_sleep(4);
    while ((int)(rt32() - t) < 0) {}
}
struct TK { u32 XA, XR, EA, ER; int dA0, dA1, dA2, dR0, dR1, dR2; int okA, okR; };
__device__ __forceinline__ int tk_est(int d0, int d1, int d2) { return max(min(d0, d1), min(max(d0, d1), d2)); }
template <int N>
__device__ __forceinline__ u32 beacon_max(rsrc_t rw, int fb, int slot0, int lane, int& budget) {
    const int vo = BCO(slot0 + (lane < N ? lane : 0)), v2 = BCO(64 + (lane & 7));
    u32 v, u = 0;
    for (;;) {
        v = LD4(rw, vo, fb);
        if (N > 64) u = LD4(rw, v2, fb);
        MB();
        if (__all(max(v, u) != 0xFFFFFFFFu)) break;
        if (--budget < 0) break;
        __builtin_amdgcn_s_sleep(1);
    }
    const u32 base = __builtin_amdgcn_readfirstlane(v);
    int d = (int)(v - base);
    if (N > 64) d = max(d, (int)(u - base));
    return base + (u32)wave_max(d);
}
struct BC2 { u32 v, u; };
__device__ __forceinline__ BC2 beacon_issue(rsrc_t rw, int fb, int lane) {
    BC2 b;
    b.v = LD4(rw, BCO(lane), fb);
    b.u = LD4(rw, BCO(64 + (lane & 7)), fb);
    MB();
    return b;
}
__device__ __forceinline__ u32 beacon_finish(BC2 b, rsrc_t rw, int fb, int lane, int& budget) {
    if (!__all(max(b.v, b.u) != 0xFFFFFFFFu)) return beacon_max<NR8>(rw, fb, 0, lane, budget);
    const u32 base = __builtin_amdgcn_readfirstlane(b.v);
    return base + (u32)wave_max(max((int)(b.v - base), (int)(b.u - base)));
}
__device__ __forceinline__ void tk_T0(TK& k, u32 T0, const Args& a) {
    if (k.okR) { k.dR2 = k.dR1; k.dR1 = k.dR0; k.dR0 = (int)(T0 - k.ER); }
    k.okA = (int)(k.XA - T0) >= a.pad0 - a.pha;
    k.EA = k.XA;
    k.XR = k.EA + tk_est(k.dA0, k.dA1, k.dA2) + a.pd;
}
__device__ __forceinline__ void tk_Tp(TK& k, u32 Tp, const Args& a) {
    if (k.okA && k.okR) { k.dA2 = k.dA1; k.dA1 = k.dA0; k.dA0 = (int)(Tp - k.EA); }
    k.okR = (int)(k.XR - Tp) >= a.pd - a.pha;
    k.ER = k.okR ? k.XR : Tp + a.gap;
    k.XA = k.ER + tk_est(k.dR0, k.dR1, k.dR2) + a.pad0;
}
__device__ __forceinline__ u32 tk_specA(const TK& k, const Args& a) { return k.XR + tk_est(k.dR0, k.dR1, k.dR2) + a.pad0; }
__device__ __forceinline__ u32 tk_specR(const TK& k, const Args& a) { return k.XA + tk_est(k.dA0, k.dA1, k.dA2) + a.pd; }
__device__ __forceinline__ void tk_init(TK& k) { const u32 n = rt32(); k.XA = k.XR = k.EA = k.ER = n - 100000u; k.dA0 = k.dA1 = k.dA2 = 130; k.dR0 = k.dR1 = k.dR2 = 80; k.okA = k.okR = 0; }

__device__ __forceinline__ void tk_post(lptr p, int tag, u32 t, int lane) { if (lane == 0) { v2u v = {(u32)tag, t}; *(volatile LAS v2u*)p = v; } }
__device__ __forceinline__ u32 tk_get(lptr p, int tag, int& budget) {
    for (;;) {
        const v2u v = *(volatile LAS v2u*)p;
        if ((int)__builtin_amdgcn_readfirstlane(v.x) >= tag || --budget < 0) return __builtin_amdgcn_readfirstlane(v.y);
        __builtin_amdgcn_s_sleep(2);
    }
}
__device__ __forceinline__ void tk_wait(lptr p, int tag, int& budget) {
    u32 X;
    for (;;) {
        const v2u v = *(volatile LAS v2u*)p;
        const u32 now = rt32();
        X = __builtin_amdgcn_readfirstlane(v.y);
        const int tg = (int)__builtin_amdgcn_readfirstlane(v.x), dt = (int)(X - now);
        if (tg >= tag) {
            if (dt <= 0 || dt > 1000 || tg > tag) return;
            if (dt <= 28) break;
        }
        if (--budget < 0) return;
        __builtin_amdgcn_s_sleep(1);
    }
    while ((int)(rt32() - X) < 0) {}
}
constexpr int OOB = 0x70000000;
__device__ __forceinline__ void stage_rows8(const u16* src, lptr ml, int tid) {
    for (int c = tid; c < 576; c += 512) {
        const u32* p = (const u32*)(src + (c & 7) * DM + 16 * (c >> 4) + 2 * ((c >> 3) & 1));
        v4u v = {p[0], p[2], p[4], p[6]};
        *(LAS v4u*)(ml + c * 16) = v;
    }
}
__device__ __forceinline__ void gather_mem(rsrc_t rw, int nb, lptr ml, int lane, int w, int& budget) {
    const int gi = w == 7 ? 5 : w;
    const int l0 = gi * 1536 + lane * 16, l1 = l0 + 1024;
    const int o0 = l0 < 8192 ? l0 : l0 + (N_MEMB - 8192), o1 = lane < 32 ? (l1 < 8192 ? l1 : l1 + (N_MEMB - 8192)) : OOB;
    lptr p1 = ml + (lane < 32 ? l1 : 14336 + lane * 16);
    v4u v0, v1;
    for (;;) {
        v0 = LD16(rw, o0, nb); v1 = LD16(rw, o1, nb);
        MB();
        if (__builtin_expect(__all(max(bad4(v0), bad4(v1)) != 0xFFFFFFFFu), 1)) break;
        if (--budget < 0) break;
    }
    *(LAS v4u*)(ml + l0) = v0; *(LAS v4u*)p1 = v1;
}
__device__ __forceinline__ void gather_mem4(rsrc_t rw, int nb, lptr ml, int lane, int gi, int& budget) {
    const int l0 = gi * 2048 + lane * 16;
    const int o2 = lane < 16 ? N_MEMB + gi * 256 + lane * 16 : OOB;
    lptr p2 = ml + (lane < 16 ? 8192 + gi * 256 + lane * 16 : 14336 + lane * 16);
    v4u v0, v1, v2;
    for (;;) {
        v0 = LD16(rw, l0, nb); v1 = LD16(rw, l0 + 1024, nb); v2 = LD16(rw, o2, nb);
        MB();
        if (__builtin_expect(__all(max(max(bad4(v0), bad4(v1)), bad4(v2)) != 0xFFFFFFFFu), 1)) break;
        if (--budget < 0) break;
    }
    *(LAS v4u*)(ml + l0) = v0; *(LAS v4u*)(ml + l0 + 1024) = v1; *(LAS v4u*)p2 = v2;
}
__device__ __forceinline__ int get_parity(rsrc_t rw, int lane, int& budget) {
    const int vo = (FL_PAR + (lane & 7)) * 128;
    u32 v;
    for (;;) {
        v = LD4(rw, vo, 0);
        MB();
        if (__all(v != 0xFFFFFFFFu) || --budget < 0) break;
        __builtin_amdgcn_s_sleep(8);
    }
    int pm = 0;
#pragma unroll
    for (int k = 0; k < 8; ++k) pm |= (__builtin_amdgcn_readlane((int)v, k) & 3) << (2 * k);
    return __builtin_popcount(pm) > 8 ? 0xFFFF : 0;
}
__device__ __forceinline__ void role_probe(rsrc_t rw) {
    const int lane = threadIdx.x & 63, w = __builtin_amdgcn_readfirstlane(threadIdx.x >> 6);
    u32 bits = 0;
    for (int gg = 0; gg < 2; ++gg) {
        int best[2] = {1 << 30, 1 << 30};
        for (int rep = 0; rep < 3; ++rep) {
#pragma unroll
            for (int p = 0; p < 2; ++p) {
                const int o = OFF_RING + (2 * w + gg) * 65536 + N_PROBE + p * 8192 + rep * 128;
                ST4(0xFFFFFFFFu, rw, lane == 0 ? o : OOB);
                DRAIN();
                const unsigned long long t0 = __builtin_amdgcn_s_memtime();
                asm volatile("s_waitcnt lgkmcnt(0)" ::: "memory");
                u32 v = LD4(rw, o, 0);
                asm volatile("s_waitcnt vmcnt(0)" : "+v"(v) :: "memory");
                const unsigned long long t1 = __builtin_amdgcn_s_memtime();
                best[p] = min(best[p], (int)(t1 - t0));
            }
        }
        bits |= (best[1] < best[0] ? 1u : 0u) << gg;
    }
    if (lane == 0) ST4(bits, rw, (FL_PAR + w) * 128);
}
__device__ __forceinline__ void qk_project(const bf16x16 (&Bq)[9], const bf16x16 (&Bk)[9], lptr ma, lptr xw, int idx) {
    f32x4 aq = {}, ak = {};
    bf16x8 A[9];
#pragma unroll
    for (int t = 0; t < 9; ++t) A[t] = lds8(ma + t * 1024);
    __builtin_amdgcn_sched_barrier(0);
#pragma unroll
    for (int t = 0; t < 9; ++t) { aq = SMF(A[t], Bq[t], aq, idx); ak = SMF(A[t], Bk[t], ak, idx); }
    const float s0 = swap32_sum(aq[0], ak[0]), s1 = swap32_sum(aq[1], ak[1]), s2 = swap32_sum(aq[2], ak[2]), s3 = swap32_sum(aq[3], ak[3]);
    v2u o = {pack2(s0, s1), pack2(s2, s3)};
    *(LAS v2u*)xw = o;
}

__device__ __forceinline__ void role_qk(const Args& a, lptr lds, rsrc_t rw, int h, int rep) {
    const int tid = threadIdx.x, lane = tid & 63, w = __builtin_amdgcn_readfirstlane(tid >> 6);
    bf16x16 Bq[9], Bk[9];
    for (int i = 0; i < a.phz; ++i) __builtin_amdgcn_s_sleep(16);
    {
        v4u v[6][6];
#pragma unroll
        for (int c = 0; c < 6; ++c) rows_ld<192, 16>(v[c], (c >= 3 ? a.Wk : a.Wq) + (size_t)(c % 3) * 192 * 4096 + h * DH, 4096, tid);
        __builtin_amdgcn_sched_barrier(0);
#pragma unroll
        for (int c = 0; c < 6; ++c) {
            lptr l = lds + (c & 1) * 77824;
            rows_st<192, 16>(v[c], l, 288, tid);
            LBAR();
#pragma unroll
            for (int t = 0; t < 3; ++t) {
                bf16x16 f = cat16(frag16(l + (64 * t) * 288 + w * 32, 288, lane), frag16(l + (64 * t + 32) * 288 + w * 32, 288, lane));
                if (c >= 3) Bk[(c % 3) * 3 + t] = f; else Bq[c * 3 + t] = f;
            }
            __builtin_amdgcn_sched_barrier(0);
        }
        LBAR();
    }
    lptr ml = lds, xl = lds + MEMB;
    const int idx = (lane & 8) ? 0xEEEE : 0x4444;
    const int i16 = lane & 15, g = lane >> 4;
    const bool hi8 = (lane & 8) != 0;
    int budget = SPIN_MAX;
    lptr pq = xl + (8 * g + (i16 >> 2)) * 32 + (i16 & 1) * 8;
    lptr kl = lds + 16384 + lane * 16;
    lptr ma = ml + lane * 16, xw = xl + (16 * w + i16) * 32 + g * 8;
    const int ox = N_PX + (h >> 4) * 16384 + (h & 15) * 512 + ((g & 1) * 16 + (g >> 1) * 8 + (i16 & 7)) * 16;
    const int om = hi8 ? OOB : N_PMM + h * 128 + (i16 & 7) * 16;
    const int ob = lane < NREP ? BCO(BC_QK + h) : OOB;
    const int pm = get_parity(rw, lane, budget);
    lptr tkp = lds + MEMB + 4096, kfp = tkp + 64;
    if (tid == 0) { tk_post(tkp, 0, rt32() - 100000u, 0); tk_post(kfp, 0, 0u, 0); }
    stage_rows8(a.mem0, ml, tid);
#pragma unroll
    for (int G = 0; G < 4; ++G) stage_rows8(a.u + G * 8 * DM, lds + 32768 + G * MEMB, tid);
    __syncthreads();
#pragma unroll 1
    for (int G = 0; G < 4; ++G) {
        qk_project(Bq, Bk, lds + 32768 + G * MEMB + lane * 16, xw, idx);
        LBAR();
        if (w < 4) {
            const bf16x8 F = as8(trd(pq + w * 1024 + 16), trd(pq + w * 1024 + 16 + 128));
            if ((i16 >> 3) == (G & 1)) *(LAS bf16x8*)(kl + ((G >> 1) * 4 + w) * 1024) = F;
        }
        LBAR();
    }
    RST(5);
    STAMP_INIT
    if (w == 0) {
        const float c2 = 0.08838834764831845f * 1.4426950408889634f;
        for (int s = 0; s < NSEG - 1; ++s) {
            tk_get(kfp, s, budget);
            bf16x8 kx[2][4];
#pragma unroll
            for (int tl = 0; tl < 2; ++tl)
#pragma unroll
                for (int ks = 0; ks < 4; ++ks) kx[tl][ks] = lds8(kl + (tl * 4 + ks) * 1024);
            int oxs = ox + NEARB(s, i16 >> 3, pm), oxt = ox + NEARB(s, 2 + (i16 >> 3), pm), oms = om + NEARB(s, g, pm), obs = ob + FARB(s, lane & 3, pm);
            const int nb = NEARB(s, rep, pm);
            asm volatile("s_waitcnt lgkmcnt(0)" : "+v"(oxs), "+v"(oxt), "+v"(oms), "+v"(obs) :: "memory");
#pragma unroll
            for (int tl = 0; tl < 2; ++tl)
#pragma unroll
                for (int ks = 0; ks < 4; ++ks) PIN4(kx[tl][ks]);
            if (s > 0) {
                tk_wait(tkp, s, budget);
                STAMP(0);
                gather_mem(rw, nb, ml, lane, 0, budget);
            }
            STAMP(1);
            LBAR();
            STAMP(2);
            qk_project(Bq, Bk, ma, xw, idx);
            STAMP(3);
            __builtin_amdgcn_s_setprio(3);
            LBAR();
            STAMP(4);
            bf16x8 Fq[4], Fk[4];
#pragma unroll
            for (int ks = 0; ks < 4; ++ks) Fq[ks] = as8(trd(pq + ks * 1024), trd(pq + ks * 1024 + 128));
            __builtin_amdgcn_sched_barrier(0);
#pragma unroll
            for (int ks = 0; ks < 4; ++ks) Fk[ks] = as8(trd(pq + ks * 1024 + 16), trd(pq + ks * 1024 + 16 + 128));
            __builtin_amdgcn_sched_barrier(0);
            f32x4 S0 = {}, S1 = {}, Sm = {};
            S0 = MFMA16(kx[0][0], Fq[0], S0); S1 = MFMA16(kx[1][0], Fq[0], S1);
            S0 = MFMA16(kx[0][1], Fq[1], S0); S1 = MFMA16(kx[1][1], Fq[1], S1);
            __builtin_amdgcn_sched_barrier(0);
            Sm = MFMA16(Fk[0], Fq[0], Sm); S0 = MFMA16(kx[0][2], Fq[2], S0);
            Sm = MFMA16(Fk[1], Fq[1], Sm); S1 = MFMA16(kx[1][2], Fq[2], S1);
            Sm = MFMA16(Fk[2], Fq[2], Sm); S0 = MFMA16(kx[0][3], Fq[3], S0);
            Sm = MFMA16(Fk[3], Fq[3], Sm); S1 = MFMA16(kx[1][3], Fq[3], S1);
            __builtin_amdgcn_sched_barrier(0);
            float y0 = hi8 ? Sm[2] : Sm[0], y1 = hi8 ? Sm[3] : Sm[1];
            float x0 = hi8 ? S1[0] : S0[0], x1 = hi8 ? S1[1] : S0[1], x2 = hi8 ? S1[2] : S0[2], x3 = hi8 ? S1[3] : S0[3];
            float m = vmax3(vmax3(y0, y1, x0), x1, vmax(x2, x3));
            m = vmax(m, ror8f(m));
            { auto r = __builtin_amdgcn_permlane16_swap(__float_as_uint(m), __float_as_uint(m), false, false); m = vmax(__uint_as_float(r[0]), __uint_as_float(r[1])); }
            { auto r = __builtin_amdgcn_permlane32_swap(__float_as_uint(m), __float_as_uint(m), false, false); m = vmax(__uint_as_float(r[0]), __uint_as_float(r[1])); }
            STAMP(5);
            const float nm = -m * c2;
            x0 = __builtin_amdgcn_exp2f(fmaf(x0, c2, nm)); x1 = __builtin_amdgcn_exp2f(fmaf(x1, c2, nm)); x2 = __builtin_amdgcn_exp2f(fmaf(x2, c2, nm)); x3 = __builtin_amdgcn_exp2f(fmaf(x3, c2, nm));
            y0 = __builtin_amdgcn_exp2f(fmaf(y0, c2, nm)); y1 = __builtin_amdgcn_exp2f(fmaf(y1, c2, nm));
            float sum = fmaf(0.5f, y0 + y1, (x0 + x1) + (x2 + x3));
            sum += ror8f(sum);
            sum = xsum(sum);
            const float inv = __builtin_amdgcn_rcpf(sum);
            const u32 pa = pack2(x0 * inv, x1 * inv) & 0x7fff7fffu, pb = pack2(x2 * inv, x3 * inv) & 0x7fff7fffu, pc = pack2(y0 * inv, y1 * inv) & 0x7fff7fffu;
            const u32 qa = ror8(pa), qb = ror8(pb), qc = ror8(pc);
            v4u px = {hi8 ? qa : pa, hi8 ? qb : pb, hi8 ? pa : qa, hi8 ? pb : qb};
            ST16(px, rw, oxs);
            ST16(px, rw, oxt);
            auto ra = __builtin_amdgcn_permlane16_swap(pc, pc, false, false);
            auto rc = __builtin_amdgcn_permlane16_swap(qc, qc, false, false);
            v4u pmv = {ra[0], rc[0], ra[1], rc[1]};
            ST16(pmv, rw, oms);
            STAMP(6);
            __builtin_amdgcn_sched_barrier(0);
            ST4(nos(rt32()), rw, obs);
            __builtin_amdgcn_s_setprio(0);
            STAMP_FLUSH;
        }
        {
            tk_wait(tkp, NSEG - 1, budget);
            gather_mem(rw, NEARB(NSEG - 1, rep, pm), ml, lane, 0, budget);
            LBAR();
            qk_project(Bq, Bk, ma, xw, idx);
            LBAR();
        }
    } else {
        TK tk; tk_init(tk);
        int kdone = -1;
        const int kxo = OFF_KX + h * (NSEG * 8192) + g * 512 + i16 * 16;
        const int kmo = i16 < 8 ? OFF_KM + h * 2048 + g * 128 + i16 * 16 : OOB;
        for (int s = 0; s < NSEG; ++s) {
            if (w == 5) {
                if (s > 0 && s < NSEG - 1) {
                    if (kdone < s) kdone = wait_flags(rw, FL_K + (h >> 1), 1, 1, s, lane, budget, true);
                    v4u kx[8];
#pragma unroll
                    for (int k = 0; k < 8; ++k) kx[k] = LD16(rw, kxo + (k >> 2) * 256 + (k & 3) * 2048, s * 8192);
                    const v4u ff = {0xFFFFFFFFu, 0xFFFFFFFFu, 0xFFFFFFFFu, 0xFFFFFFFFu};
                    ST16(ff, rw, ox + NEARB(s + 2, i16 >> 3, pm)); ST16(ff, rw, ox + NEARB(s + 2, 2 + (i16 >> 3), pm));
                    ST16(ff, rw, om + NEARB(s + 2, g, pm));
                    ST4(0xFFFFFFFFu, rw, ob + FARB(s + 4, lane & 3, pm));
#pragma unroll
                    for (int k = 0; k < 8; ++k) *(LAS v4u*)(kl + k * 1024) = kx[k];
                    asm volatile("s_waitcnt lgkmcnt(0)" ::: "memory");
                    tk_post(kfp, s, 0u, lane);
                    DRAIN();
                }
            }
            if (s > 0 && w != 5 && w != 6) {
                tk_wait(tkp, s, budget);
                gather_mem(rw, NEARB(s, rep, pm), ml, lane, w, budget);
            }
            BC2 bc = {0u, 0u};
            if (w == 3) bc = beacon_issue(rw, FARB(s, rep, pm), lane);
            LBAR();
            qk_project(Bq, Bk, ma, xw, idx);
            LBAR();
            if (w == 6) {
                bf16x8 F[4];
#pragma unroll
                for (int ks = 0; ks < 4; ++ks) F[ks] = as8(trd(pq + ks * 1024 + 16), trd(pq + ks * 1024 + 16 + 128));
#pragma unroll
                for (int ks = 0; ks < 4; ++ks) ST16(asu(F[ks]), rw, kmo + s * (NH * 2048) + ks * 512);
                DRAIN();
                if (lane == 0) ST4((u32)s, rw, (FL_M + h) * 128);
            } else if (w == 3) {
                if (s < NSEG - 1) {
                    if (s > 0) { tk_T0(tk, beacon_finish(bc, rw, FARB(s, rep, pm), lane, budget), a); if (tk.okA) tk_post(tkp, s + 1, tk_specA(tk, a), lane); }
                    wait_until(tk.XR + 8);
                    tk_Tp(tk, beacon_max<NH>(rw, FARB(s, rep, pm), BC_QK, lane, budget), a);
                    tk_post(tkp, s + 1, tk.XA, lane);
                }
            } else if (w != 5) __builtin_amdgcn_s_sleep(14);
        }
    }
    { const int bid_ = rep + 8 * (h - 8 * rep); (void)bid_; STAMP_FIN; }
    if (budget < 0 && lane == 0) ST4(1u, rw, FL_ERR * 128);
}

__device__ __forceinline__ void role_wvo(const Args& a, lptr lds, rsrc_t rw, int idx) {
    const int tid = threadIdx.x, lane = tid & 63, w = __builtin_amdgcn_readfirstlane(tid >> 6);
    const int h = idx / 3, part = idx - 3 * h;
    bf16x8 Bo[5][4];
    v4u v[6], o[2][9];
#pragma unroll
    for (int c = 0; c < 2; ++c) rows_ld<64, 72>(o[c], a.Wo + ((size_t)h * DH + c * 64) * DM, DM, tid);
    rows_ld<192, 16>(v, a.Wv + (size_t)part * 192 * 4096 + h * DH, 4096, tid);
    __builtin_amdgcn_sched_barrier(0);
#pragma unroll
    for (int c = 0; c < 2; ++c) {
        lptr l = lds + c * 77824;
        rows_st<64, 72>(o[c], l, 1184, tid);
        LBAR();
#pragma unroll
        for (int j = 0; j < 5; ++j) {
            const int r = (w + 8 * j < NR) ? w + 8 * j : w;
#pragma unroll
            for (int ks = 0; ks < 2; ++ks) Bo[j][c * 2 + ks] = frag16(l + ks * 32 * 1184 + r * 32, 1184, lane);
        }
        __builtin_amdgcn_sched_barrier(0);
    }
    rows_st<192, 16>(v, lds, VS, tid);
    LBAR();
    RST(1);
    const int rho = lane & 15, bg = lane >> 4;
    const int wo0 = OFF_WVO + (2 * w + ((rho >> 1) & 1)) * 294912 + ((h >> 1) * 18 + part * 6) * 1024 + (bg * 16 + (h & 1) * 8 + 2 * (rho >> 2) + (rho & 1)) * 16;
#pragma unroll 1
    for (int kb = 0; kb < 6; ++kb) {
        bf16x8 Af[2][4];
#pragma unroll
        for (int mt = 0; mt < 2; ++mt)
#pragma unroll
            for (int ks = 0; ks < 4; ++ks) Af[mt][ks] = lds8(lds + (32 * kb + 8 * (rho >> 2) + 4 * mt + (rho & 3)) * VS + ks * 64 + bg * 16);
#pragma unroll
        for (int j = 0; j < 5; ++j) {
            if (j < 4 || w < 4) {
                f32x4 d0 = {}, d1 = {};
#pragma unroll
                for (int ks = 0; ks < 4; ++ks) { d0 = MFMA16(Af[0][ks], Bo[j][ks], d0); d1 = MFMA16(Af[1][ks], Bo[j][ks], d1); }
                v4u q = {pack2(d0[0], d0[1]), pack2(d0[2], d0[3]), pack2(d1[0], d1[1]), pack2(d1[2], d1[3])};
                ST16(q, rw, wo0 + j * (16 * 294912) + kb * 1024);
            }
        }
        if (kb == 2 || kb == 4) {
            asm volatile("s_waitcnt vmcnt(4)" ::: "memory");
            LBAR();
            if (tid == 0) ST4((u32)(kb >> 1), rw, (FL_W + idx) * 128);
        }
    }
    RST(2);
    DRAIN();
    LBAR();
    RST(3);
    if (tid == 0) ST4(3u, rw, (FL_W + idx) * 128);
}

__device__ __forceinline__ void role_rz(const Args& a, lptr lds, rsrc_t rw, int r, int rep) {
    const int tid = threadIdx.x, lane = tid & 63, w = __builtin_amdgcn_readfirstlane(tid >> 6);
    const int idx = (lane & 8) ? 0xEEEE : 0x4444;
    const int i16 = lane & 15, g = lane >> 4;
    int budget = SPIN_MAX;
    bf16x16 Ba[9], Bb[9];
    {
        const int wo = OFF_WVO + r * 294912 + 2 * w * 18432 + lane * 16;
#pragma unroll
        for (int p = 0; p < 3; ++p) {
            wait_flags(rw, FL_W + 12 * w, 1, 12, p + 1, lane, budget, true);
            if (p == 0) RST(4);
#pragma unroll
            for (int q = 0; q < 3; ++q) {
                const int t = 3 * q + p;
                Ba[t] = cat16(as8(LD16(rw, wo, t * 2048)), as8(LD16(rw, wo + 1024, t * 2048)));
                Bb[t] = cat16(as8(LD16(rw, wo, 18432 + t * 2048)), as8(LD16(rw, wo + 1024, 18432 + t * 2048)));
            }
            MB();
        }
    }
    lptr ml = lds, zl = lds + MEMB;
    lptr tka = lds + 13312, tkr = tka + 16, cntp = tka + 64;
    volatile LAS int* prod = (volatile LAS int*)(lds + 13312 + 512);
    lptr ring = lds + 16384;
    lptr ma = ml + lane * 16;
    lptr zw = zl + ((2 * w + (lane >> 5)) >> 2) * 1024 + ((((2 * w + (lane >> 5)) & 3) * 16 + (g & 1) * 8 + (i16 & 7)) * 16) + (i16 >> 3) * 8;
    const bool hi8r = (lane & 8) != 0;
    const int mso = i16 < 8 ? (r < 64 ? N_MEMA + r * 128 : N_MEMB + (r - 64) * 128) + i16 * 16 : OOB;
    const int ob = lane < NREP ? BCO(r) : OOB;
    const int pm = get_parity(rw, lane, budget);
    if (tid == 0) { tk_post(tka, 0, rt32() - 100000u, 0); tk_post(tkr, -1, 0u, 0); *(volatile LAS int*)cntp = 0; }
    if (tid < 4) prod[tid] = 0;
    stage_rows8(a.mem0, ml, tid);
    __syncthreads();
    RST(5);
    STAMP_INIT
    if (w < 4) {
        const int po = N_PX + (w >> 1) * 16384 + (w & 1) * 4096 + lane * 16;
        const int mo = N_PMM + w * 1024 + 2 * g * 128 + (i16 & 7) * 16 + (i16 >> 3) * 8;
        lptr cna = lane == 0 ? cntp : cntp + 4 + lane * 4;
        const u32 one = lane == 0 ? 1u : 0u;
        const bool dv = (g >> 1) == (i16 >> 3);
        const int rwo = dv ? w * 256 + (g & 1) * 128 + (g >> 1) * 64 + (i16 & 7) * 4 : 2048 + lane * 4;
        for (int s = 0; s < NSEG; ++s) {
            LBAR();
            qk_project(Ba, Bb, ma, zw, idx);
            LBAR();
            if (s == NSEG - 1) break;
            lptr Rw = lds + 81920 + (s & 1) * 1024 + rwo, Rr = lds + 81920 + (s & 1) * 1024 + (i16 & 7) * 32;
            int mss = mso + NEARB(s + 1, g, pm), obs = ob + FARB(s + 1, lane & 3, pm);
            const int sb = NEARB(s, rep, pm);
            while (__builtin_amdgcn_readfirstlane(prod[w >> 1]) <= s && --budget >= 0) __builtin_amdgcn_s_sleep(1);
            MB();
            lptr rg = ring + (s & 3) * 16384 + w * 4096 + lane * 16;
            bf16x8 b[5];
#pragma unroll
            for (int k = 0; k < 4; ++k) b[k] = lds8(rg + k * 1024);
            b[4] = lds8(zl + w * 1024 + lane * 16);
            asm volatile("s_waitcnt lgkmcnt(0)" : "+v"(mss), "+v"(obs), "+v"(Rw), "+v"(Rr) :: "memory");
#pragma unroll
            for (int k = 0; k < 5; ++k) PIN4(b[k]);
            __builtin_amdgcn_s_setprio(3);
            tk_wait(tkr, s, budget);
            STAMP(1);
            f32x4 d;
            for (;;) {
                v4u p[4]; v2u m[2];
                m[0] = LD8(rw, mo, sb); m[1] = LD8(rw, mo + 128, sb);
                __builtin_amdgcn_sched_barrier(0);
#pragma unroll
                for (int k = 0; k < 4; ++k) { p[k] = LD16(rw, po + k * 1024, sb); __builtin_amdgcn_sched_barrier(0); }
                MB();
                d = f32x4{};
                d = MFMA16(as8(m[0], m[1]), b[4], d);
                u32 bad = (m[0].x | m[0].y) | (m[1].x | m[1].y);
                __builtin_amdgcn_sched_barrier(0);
#pragma unroll
                for (int k = 0; k < 4; ++k) {
                    d = MFMA16(as8(p[k]), b[k], d);
                    bad |= (p[k].x | p[k].y) | (p[k].z | p[k].w);
                    __builtin_amdgcn_sched_barrier(0);
                }
                if (__builtin_expect(__all((int)bad >= 0), 1)) break;
                if (--budget < 0) break;
            }
            STAMP(2);
            float e0, e1;
            {
                auto r0 = __builtin_amdgcn_permlane32_swap(__float_as_uint(d[0]), __float_as_uint(d[2]), false, false);
                auto r1 = __builtin_amdgcn_permlane32_swap(__float_as_uint(d[1]), __float_as_uint(d[3]), false, false);
                const float t0 = __uint_as_float(hi8r ? r0[1] : r0[0]), t1 = __uint_as_float(hi8r ? r1[1] : r1[0]);
                e0 = t0 + ror8f(t0); e1 = t1 + ror8f(t1);
            }
            f32x4 x[8]; u32 old;
            asm volatile("ds_write2_b32 %10, %11, %12 offset1:8\n\tds_add_rtn_u32 %0, %13, %14\n\t"
                         "ds_read_b128 %1, %9\n\tds_read_b128 %2, %9 offset:16\n\tds_read_b128 %3, %9 offset:256\n\tds_read_b128 %4, %9 offset:272\n\t"
                         "ds_read_b128 %5, %9 offset:512\n\tds_read_b128 %6, %9 offset:528\n\tds_read_b128 %7, %9 offset:768\n\tds_read_b128 %8, %9 offset:784\n\ts_waitcnt lgkmcnt(0)"
                         : "=&v"(old), "=&v"(x[0]), "=&v"(x[1]), "=&v"(x[2]), "=&v"(x[3]), "=&v"(x[4]), "=&v"(x[5]), "=&v"(x[6]), "=&v"(x[7])
                         : "v"(Rr), "v"(Rw), "v"(e0), "v"(e1), "v"(cna), "v"(one) : "memory");
            STAMP(4);
            if ((__builtin_amdgcn_readfirstlane(old) & 3) == 3) {
                const f32x4 c0 = (x[0] + x[2]) + (x[4] + x[6]), c1 = (x[1] + x[3]) + (x[5] + x[7]);
                v4u o = {nos(pack2(c0[0], c0[1])), nos(pack2(c0[2], c0[3])), nos(pack2(c1[0], c1[1])), nos(pack2(c1[2], c1[3]))};
                ST16(o, rw, mss);
                __builtin_amdgcn_sched_barrier(0);
                ST4(nos(rt32()), rw, obs);
            }
            STAMP(5);
            __builtin_amdgcn_s_setprio(0);
            STAMP_FLUSH;
        }
    } else if (w == 5 || w == 6) {
        int vdone0 = -1, vdone1 = -1, staged = 0;
        const int hw = w - 5;
        const int vo = OFF_VOX + r * 16384 + hw * 8192 + lane * 16;
        const int zho = OFF_ZH + r * 4096 + hw * 2048 + lane * 16;
        lptr zhr = zl + hw * 2048 + (((lane >> 4) & 3) * 16 + (lane & 7)) * 16 + ((lane >> 3) & 1) * 8;
        const int aro = w == 5 ? mso : OOB, arb = w == 5 ? ob : OOB;
        v4u zv[2] = {};
        for (int s = 0; s < NSEG; ++s) {
            bool go = false;
            if (staged <= min(s + 3, NSEG - 2)) {
                const int ow = VOWN(staged);
                int& vd = ow ? vdone1 : vdone0;
                if (vd < staged) vd = wait_flags(rw, FL_V + ow, 2, NH, -1, lane, budget);
                go = vd >= staged;
            }
            const int vos = go ? vo : OOB;
            if (s > 0) {
                tk_wait(tka, s, budget);
                gather_mem4(rw, NEARB(s, rep, pm), ml, lane, w - 4, budget);
            }
            LBAR();
            v4u v[8];
#pragma unroll
            for (int k = 0; k < 8; ++k) v[k] = LD16(rw, vos + (k & 3) * 1024, staged * (NR * NH * 1024) + (k >> 2) * 4096);
            {
                const int zs = s > 0 ? zho + (s - 1) * (NR * NH * 256) : OOB;
                ST16(zv[0], rw, zs); ST16(zv[1], rw, zs + 1024);
                const v4u ff = {0xFFFFFFFFu, 0xFFFFFFFFu, 0xFFFFFFFFu, 0xFFFFFFFFu};
                ST16(ff, rw, aro + NEARB(s + 3, g, pm));
                ST4(0xFFFFFFFFu, rw, arb + FARB(s + 4, lane & 3, pm));
            }
            MB();
            qk_project(Ba, Bb, ma, zw, idx);
            LBAR();
#pragma unroll
            for (int k = 0; k < 2; ++k) {
                const v2u lo = *(LAS v2u*)(zhr + k * 1024), hi = *(LAS v2u*)(zhr + k * 1024 + 128);
                zv[k] = v4u{lo.x, lo.y, hi.x, hi.y};
            }
            for (;;) {
                if (go) {
#pragma unroll
                    for (int k = 0; k < 8; ++k) *(LAS v4u*)(ring + (staged & 3) * 16384 + hw * 8192 + k * 1024 + lane * 16) = v[k];
                    asm volatile("s_waitcnt lgkmcnt(0)" ::: "memory");
                    ++staged;
                    prod[hw] = staged;
                }
                if (staged > s + 1 || staged > NSEG - 2) break;
                const int ow = VOWN(staged);
                int& vd = ow ? vdone1 : vdone0;
                if (vd < staged) vd = wait_flags(rw, FL_V + ow, 2, NH, staged <= s ? staged : -1, lane, budget, true);
                if (vd < staged) break;
#pragma unroll
                for (int k = 0; k < 8; ++k) v[k] = LD16(rw, vo + (k & 3) * 1024, staged * (NR * NH * 1024) + (k >> 2) * 4096);
                go = true;
            }
            DRAIN();
        }
        ST16(zv[0], rw, zho + (NSEG - 1) * (NR * NH * 256)); ST16(zv[1], rw, zho + (NSEG - 1) * (NR * NH * 256) + 1024);
    } else {
        TK tk; tk_init(tk);
        for (int s = 0; s < NSEG; ++s) {
            if (s > 0) {
                tk_wait(tka, s, budget);
                gather_mem4(rw, NEARB(s, rep, pm), ml, lane, w - 4, budget);
            }
            BC2 bc = {0u, 0u};
            if (w == 7) bc = beacon_issue(rw, FARB(s, rep, pm), lane);
            LBAR();
            qk_project(Ba, Bb, ma, zw, idx);
            LBAR();
            if (w == 7) {
                if (s < NSEG - 1) {
                    if (s > 0) { tk_T0(tk, beacon_finish(bc, rw, FARB(s, rep, pm), lane, budget), a); if (tk.okA) tk_post(tka, s + 1, tk_specA(tk, a), lane); }
                    tk_post(tkr, s, tk.XR, lane);
                    wait_until(tk.XR + 8);
                    tk_Tp(tk, beacon_max<NH>(rw, FARB(s, rep, pm), BC_QK, lane, budget), a);
                    tk_post(tka, s + 1, tk.XA, lane);
                    tk_post(tkr, s + 1, tk_specR(tk, a), lane);
                }
            } else {
                if (w == 4 && s >= 2 && lane == 0) ST4((u32)(s - 2), rw, (FL_Z + r) * 128);
                __builtin_amdgcn_s_sleep(14);
            }
        }
    }
    DRAIN();
    LBAR();
    if (tid == 0) ST4((u32)(NSEG - 1), rw, (FL_Z + r) * 128);
    { const int bid_ = rep + 8 * (8 + r - 18 * rep); (void)bid_; STAMP_FIN; }
    if (budget < 0 && lane == 0) ST4(3u, rw, FL_ERR * 128);
}

__device__ __forceinline__ void cexp_l(float lr, float li, float l, float& er, float& ei) {
    const float mag = __builtin_amdgcn_exp2f(lr * l);
    const float ph = li * l;
    const float pe = fmaf(li, l, -ph);
    const float fr = (ph - floorf(ph)) + pe;
    er = mag * __builtin_amdgcn_cosf(fr);
    ei = mag * __builtin_amdgcn_sinf(fr);
}
constexpr int CV_W = 8 * 260 * 16, CV_U = 96 * 80, CV_CH = CV_W + CV_U;
__device__ __forceinline__ void s4_unit(const Args& a, lptr lds, rsrc_t rw, int unit) {
    const int tid = threadIdx.x, lane = tid & 63, w = __builtin_amdgcn_readfirstlane(tid >> 6);
    const int d0 = unit * 4;
    v2u uv[4];
#pragma unroll
    for (int k = 0; k < 4; ++k) uv[k] = *(const v2u*)(a.u + (size_t)(2 * (tid + 512 * (k >> 1)) + (k & 1)) * DM + d0);
    {
        const int c = tid >> 7, nn = tid & 127, n = (d0 + c) * 128 + nn;
        const float dt = rbf(expf(bf2f(a.logdt[d0 + c])));
        const float lr = dt * bf2f(a.Are[n]) * 1.4426950408889634f, li = dt * bf2f(a.Aim[n]) * 0.15915494309189535f;
        const float br = dt * bf2f(a.Bre[n]), bi = dt * bf2f(a.Bim[n]);
        const float cr = bf2f(a.Cre[n]), ci = bf2f(a.Cim[n]);
        LAS f32x2* T = (LAS f32x2*)(lds + c * CV_CH) + nn;
        float er, ei, pr, pi;
        cexp_l(lr, li, 1.0f, er, ei); pr = er; pi = ei;
#pragma unroll
        for (int o = 0; o < 7; ++o) { T[o * 130] = f32x2{pr, pi}; const float t = pr * er - pi * ei; pi = pr * ei + pi * er; pr = t; }
#pragma unroll
        for (int q4 = 0; q4 < 4; ++q4) {
            if (q4 == 0) cexp_l(lr, li, 128.0f, er, ei);
            cexp_l(lr, li, (float)(128 + 512 * q4), pr, pi);
#pragma unroll
            for (int o = 0; o < 4; ++o) { T[(7 + 4 * q4 + o) * 130] = f32x2{pr, pi}; const float t = pr * er - pi * ei; pi = pr * ei + pi * er; pr = t; }
        }
        const float cbr = cr * br - ci * bi, cbi = cr * bi + ci * br;
        cexp_l(lr, li, 8.0f, er, ei);
#pragma unroll
        for (int q4 = 0; q4 < 4; ++q4) {
            float sr, si;
            cexp_l(lr, li, (float)(32 * q4), sr, si);
            pr = cbr * sr - cbi * si; pi = cbr * si + cbi * sr;
#pragma unroll
            for (int o = 0; o < 4; ++o) { T[(23 + 4 * q4 + o) * 130] = f32x2{pr, pi}; const float t = pr * er - pi * ei; pi = pr * ei + pi * er; pr = t; }
        }
    }
    __syncthreads();
    f32x4 kr[3];
    {
        const int x = lane & 15, g = lane >> 4, c = w & 3, nh = w >> 2;
        const int ah0 = x >> 3, o0 = x & 7;
        const int ah1 = x < 8 ? 2 + (x >> 2) : 4 + ((x - 8) >> 1), o1 = x < 8 ? 2 * (x & 3) : 4 * (x & 1);
        const int ah2 = x < 8 ? 8 + x : 16;
        LAS f32x2* T = (LAS f32x2*)(lds + c * CV_CH) + nh * 64 + g;
        LAS f32x2* Ta0 = T + 7 * 130; LAS f32x2* To0 = T + (o0 ? o0 - 1 : 0) * 130;
        LAS f32x2* Ta1 = T + (6 + ah1) * 130; LAS f32x2* To1 = T + (o1 ? o1 - 1 : 0) * 130;
        LAS f32x2* Ta2 = T + (6 + ah2) * 130; LAS f32x2* Tv = T + (23 + x) * 130;
        const f32x2 one = {1.0f, 0.0f};
        f32x4 ar[3] = {}, ai[3] = {};
#pragma unroll
        for (int jj = 0; jj < 16; ++jj) {
            const f32x2 v = Tv[4 * jj], u2 = Ta2[4 * jj], a1 = Ta1[4 * jj];
            f32x2 a0v = Ta0[4 * jj], e0 = To0[4 * jj], e1 = To1[4 * jj];
            a0v = ah0 ? a0v : one; e0 = o0 ? e0 : one; e1 = o1 ? e1 : one;
            const float u0r = a0v[0] * e0[0] - a0v[1] * e0[1], u0i = a0v[0] * e0[1] + a0v[1] * e0[0];
            const float u1r = a1[0] * e1[0] - a1[1] * e1[1], u1i = a1[0] * e1[1] + a1[1] * e1[0];
            ar[2] = __builtin_amdgcn_mfma_f32_16x16x4f32(u2[0], v[0], ar[2], 0, 0, 0);
            ai[2] = __builtin_amdgcn_mfma_f32_16x16x4f32(u2[1], v[1], ai[2], 0, 0, 0);
            ar[0] = __builtin_amdgcn_mfma_f32_16x16x4f32(u0r, v[0], ar[0], 0, 0, 0);
            ai[0] = __builtin_amdgcn_mfma_f32_16x16x4f32(u0i, v[1], ai[0], 0, 0, 0);
            ar[1] = __builtin_amdgcn_mfma_f32_16x16x4f32(u1r, v[0], ar[1], 0, 0, 0);
            ai[1] = __builtin_amdgcn_mfma_f32_16x16x4f32(u1i, v[1], ai[1], 0, 0, 0);
        }
#pragma unroll
        for (int t = 0; t < 3; ++t) kr[t] = ar[t] - ai[t];
    }
    __syncthreads();
    {
        LAS float* T = (LAS float*)(lds + (w & 3) * CV_CH + 4096 + (w >> 2) * 3072);
#pragma unroll
        for (int t = 0; t < 3; ++t)
#pragma unroll
            for (int r = 0; r < 4; ++r) T[(16 * t + 4 * (lane >> 4) + r) * 16 + (lane & 15)] = kr[t][r];
    }
    __syncthreads();
#pragma unroll
    for (int k = 0; k < 8; ++k) {
        const int i = tid + 512 * k, c = i >> 10, l2 = i & 1023;
        LAS float* T = (LAS float*)(lds + c * CV_CH + 4096);
        float v[2];
#pragma unroll
        for (int e = 0; e < 2; ++e) {
            const int lb = (int)rbf((float)(2 * l2 + e));
            const int hi = lb >> 7, o = lb & 7;
            const int row = lb < 256 ? 8 * hi + o : lb < 512 ? 16 + 4 * (hi - 2) + (o >> 1) : lb < 1024 ? 24 + 2 * (hi - 4) + (o >> 2) : 32 + (hi - 8);
            const int ix = row * 16 + ((lb >> 3) & 15);
            v[e] = T[ix] + T[768 + ix];
        }
        *(LAS u32*)(lds + c * CV_CH + CV_W + 2704 + l2 * 4) = pack2(v[0], v[1]);
    }
    __syncthreads();
    for (int i = tid; i < 4 * 259; i += 512) {
        const int c = i / 259, zz = i - c * 259, z = zz - 3;
        lptr ch = lds + c * CV_CH;
        v4u lo = {0, 0, 0, 0}, hi = {0, 0, 0, 0};
        if (z >= 1) lo = *(LAS v4u*)(ch + CV_W + 2704 + (z - 1) * 16);
        if (z >= 0) hi = *(LAS v4u*)(ch + CV_W + 2704 + z * 16);
        const u32 e[8] = {lo.x, lo.y, lo.z, lo.w, hi.x, hi.y, hi.z, hi.w};
#pragma unroll
        for (int cc = 0; cc < 8; ++cc) {
            v4u o;
#pragma unroll
            for (int k = 0; k < 4; ++k) {
                const int t = 8 + cc - 2 * k;
                if (t & 1) o[k] = (e[t >> 1] >> 16) | (e[t >> 1] << 16);
                else o[k] = (e[t >> 1] & 0xffffu) | (e[(t >> 1) - 1] & 0xffff0000u);
            }
            *(LAS v4u*)(ch + (cc * 260 + zz) * 16) = o;
        }
    }
    __syncthreads();
    for (int i = tid; i < 4 * 160; i += 512) *(LAS v4u*)(lds + (i / 160) * CV_CH + CV_W + (i % 160) * 16) = v4u{0, 0, 0, 0};
#pragma unroll
    for (int k2 = 0; k2 < 2; ++k2) {
        const int l = 2 * (tid + 512 * k2);
        const int o = CV_W + (32 + (l >> 5)) * 80 + (l & 31) * 2;
        const v2u p = uv[2 * k2], q = uv[2 * k2 + 1];
        *(LAS u32*)(lds + 0 * CV_CH + o) = (p.x & 0xffffu) | (q.x << 16);
        *(LAS u32*)(lds + 1 * CV_CH + o) = (p.x >> 16) | (q.x & 0xffff0000u);
        *(LAS u32*)(lds + 2 * CV_CH + o) = (p.y & 0xffffu) | (q.y << 16);
        *(LAS u32*)(lds + 3 * CV_CH + o) = (p.y >> 16) | (q.y & 0xffff0000u);
    }
    __syncthreads();
    const int c = w & 3, par = w >> 2, b = lane & 31, hh = lane >> 5;
    lptr ch = lds + c * CV_CH;
    lptr wa = ch + ((b & 7) * 260 + (b >> 3) - hh + 3) * 16;
    lptr ub = ch + CV_W + (32 + b) * 80 + hh * 16;
    f32x16 y0 = {}, y1 = {};
    wa += par * 64; ub -= par * 80;
    {
        bf16x8 fa[2][2], fb[2][2], fc[2][2];
#pragma unroll
        for (int ks = 0; ks < 2; ++ks) { fa[0][ks] = lds8(wa - ks * 32); fb[0][ks] = lds8(ub + ks * 32); fc[0][ks] = lds8(ub + 2560 + ks * 32); }
#pragma unroll
        for (int i = 0; i < 32; ++i) {
            const int cur = i & 1, nx = cur ^ 1;
            if (i + 1 < 32) {
#pragma unroll
                for (int ks = 0; ks < 2; ++ks) {
                    fa[nx][ks] = lds8(wa + (i + 1) * 128 - ks * 32);
                    if (i + 1 < 16) fb[nx][ks] = lds8(ub - (i + 1) * 160 + ks * 32);
                    fc[nx][ks] = lds8(ub + 2560 - (i + 1) * 160 + ks * 32);
                }
            }
            __builtin_amdgcn_sched_barrier(0);
#pragma unroll
            for (int ks = 0; ks < 2; ++ks) {
                if (i < 16) y0 = MFMA32(fa[cur][ks], fb[cur][ks], y0);
                y1 = MFMA32(fa[cur][ks], fc[cur][ks], y1);
            }
            __builtin_amdgcn_sched_barrier(0);
        }
    }
    __syncthreads();
    {
        LAS f32x4* X = (LAS f32x4*)(ch + par * 4096);
        const f32x16 src = par ? y0 : y1;
#pragma unroll
        for (int k = 0; k < 4; ++k) X[k * 64 + lane] = f32x4{src[4 * k], src[4 * k + 1], src[4 * k + 2], src[4 * k + 3]};
    }
    __syncthreads();
    {
        LAS f32x4* X = (LAS f32x4*)(ch + (1 - par) * 4096);
        lptr Y = ch + 8192 + (32 * par + b) * 72 + hh * 8;
        const f32x16 own = par ? y1 : y0;
#pragma unroll
        for (int k = 0; k < 4; ++k) {
            const f32x4 o = X[k * 64 + lane];
            v2u yv = {pack2(own[4 * k] + o[0], own[4 * k + 1] + o[1]), pack2(own[4 * k + 2] + o[2], own[4 * k + 3] + o[3])};
            *(LAS v2u*)(Y + k * 16) = yv;
        }
    }
    __syncthreads();
    const v2u dk = *(const v2u*)(a.dskip + d0);
#pragma unroll
    for (int k2 = 0; k2 < 2; ++k2) {
        const int i = tid + 512 * k2;
        lptr yp = lds + 8192 + (i >> 4) * 72 + (i & 15) * 4;
        const u32 ya = *(LAS u32*)(yp), yb = *(LAS u32*)(yp + CV_CH), yc = *(LAS u32*)(yp + 2 * CV_CH), yd = *(LAS u32*)(yp + 3 * CV_CH);
        const v2u p = uv[2 * k2], q = uv[2 * k2 + 1];
        v4u o;
        o.x = pack2(bflo(ya) + rbf(bflo(p.x) * bflo(dk.x)), bflo(yb) + rbf(bfhi(p.x) * bfhi(dk.x)));
        o.y = pack2(bflo(yc) + rbf(bflo(p.y) * bflo(dk.y)), bflo(yd) + rbf(bfhi(p.y) * bfhi(dk.y)));
        o.z = pack2(bfhi(ya) + rbf(bflo(q.x) * bflo(dk.x)), bfhi(yb) + rbf(bfhi(q.x) * bfhi(dk.x)));
        o.w = pack2(bfhi(yc) + rbf(bflo(q.y) * bflo(dk.y)), bfhi(yd) + rbf(bfhi(q.y) * bfhi(dk.y)));
        ST16(o, rw, OFF_A4 + unit * 16384 + i * 16);
    }
    __syncthreads();
}
__device__ __forceinline__ void role_s4(const Args& a, lptr lds, rsrc_t rw, int idx) {
    if (idx < 32) {
        const int tid = threadIdx.x, t = idx * 64 + (tid >> 3), j = tid & 7;
        float acc = 0.f;
#pragma unroll
        for (int k = 0; k < 9; ++k) {
            const v4u xv = *(const v4u*)(a.u + (size_t)t * DM + (k * 8 + j) * 8), gv = *(const v4u*)(a.gatew + (k * 8 + j) * 8);
#pragma unroll
            for (int e = 0; e < 4; ++e) acc += rbf(bflo(xv[e]) * bflo(gv[e])) + rbf(bfhi(xv[e]) * bfhi(gv[e]));
        }
        acc += __shfl_xor(acc, 1); acc += __shfl_xor(acc, 2); acc += __shfl_xor(acc, 4);
        acc = rbf(acc);
        if (j == 0) ST4(__float_as_uint(rbf(1.0f / (1.0f + __expf(-acc)))), rw, OFF_G + t * 4);
    }
#pragma unroll 1
    for (int unit = idx; unit < DM / 4; unit += NS4) s4_unit(a, lds, rw, unit);
    DRAIN();
    __syncthreads();
    if (threadIdx.x == 0) ST4(1u, rw, (FL_S + idx) * 128);
}

__device__ __forceinline__ void role_post(const Args& a, lptr lds, rsrc_t rw) {
    const int tid = threadIdx.x, lane = tid & 63, w = __builtin_amdgcn_readfirstlane(tid >> 6);
    const int i16 = lane & 15, g = lane >> 4;
    int budget = SPIN_MAX, vdone0 = -1, vdone1 = -1;
    volatile LAS int* U = (volatile LAS int*)(lds + 81920);
    wait_flags(rw, FL_S, 1, 64, 1, lane, budget);
    wait_flags(rw, FL_S + 64, 1, NS4 - 64, 1, lane, budget);
    constexpr int NFULL = 62 * 6, NUNIT = NFULL + 24;
#pragma unroll 1
    for (;;) {
        if (tid == 0) *U = 1 + (int)__hip_atomic_fetch_add((u32*)(a.ws + FL_Q * 128), 1u, __ATOMIC_RELAXED, __HIP_MEMORY_SCOPE_AGENT);
        __syncthreads();
        const int unit = __builtin_amdgcn_readfirstlane(*U);
        if (unit >= NUNIT || budget < 0) break;
        const bool half = unit >= NFULL;
        const int uu = half ? NFULL + ((unit - NFULL) >> 1) : unit, q0 = half ? (unit - NFULL) & 1 : 0;
        const int s = uu / 6, r = (uu - s * 6) * 6 + (w < 6 ? w : 0);
        bf16x8 b[32];
        v2u av[2]; float gt[2];
        if (w < 6) {
            if (VOWN(s)) { if (vdone1 < s) vdone1 = wait_flags(rw, FL_V + 1, 2, NH, s, lane, budget); }
            else { if (vdone0 < s) vdone0 = wait_flags(rw, FL_V, 2, NH, s, lane, budget); }
            const int vo = OFF_VOX + (s * NR + r) * (NH * 1024) + ((i16 >> 1) & 1) * 16384 + ((g & 1) * 16 + (g >> 1) * 8 + 2 * (i16 >> 2) + (i16 & 1)) * 16;
#pragma unroll
            for (int k = 0; k < 32; ++k) b[k] = as8(LD16(rw, vo + (k & 7) * 512, (k >> 3) * 4096));
#pragma unroll
            for (int qt = 0; qt < 2; ++qt) {
                av[qt] = LD8(rw, OFF_A4 + (4 * r + g) * 16384 + (32 * s + 16 * (qt ^ q0) + i16) * 8, 0);
                gt[qt] = __uint_as_float(LD4(rw, OFF_G + (32 * s + 16 * (qt ^ q0) + i16) * 4, 0));
            }
        }
        v4u z[8];
        const int zo = OFF_ZH + (s * NR + r) * (NH * 256) + ((i16 >> 1) & 1) * 4096 + g * 128 + (2 * (i16 >> 2) + (i16 & 1)) * 16;
        if (half) {
            wait_flags(rw, FL_Z + 2 * r, 1, 2, s, lane, budget, true);
#pragma unroll
            for (int k = 0; k < 8; ++k) z[k] = LD16(rw, zo + k * 512, 0);
            wait_flags(rw, FL_P + (s & 3), 4, 16, s, lane, budget, true);
            v4u pv[5];
#pragma unroll
            for (int k = 0; k < 5; ++k) pv[k] = LD16(rw, OFF_PX + s * 81920 + q0 * 40960 + tid * 16, k * 8192);
#pragma unroll
            for (int k = 0; k < 5; ++k) *(LAS v4u*)(lds + k * 8192 + tid * 16) = pv[k];
        } else {
            wait_flags(rw, FL_P + (s & 3), 4, 16, s, lane, budget);
            v4u pv[10];
#pragma unroll
            for (int k = 0; k < 10; ++k) pv[k] = LD16(rw, OFF_PX + s * 81920 + tid * 16, k * 8192);
            wait_flags(rw, FL_Z + 2 * r, 1, 2, s, lane, budget);
#pragma unroll
            for (int k = 0; k < 8; ++k) z[k] = LD16(rw, zo + k * 512, 0);
            MB();
#pragma unroll
            for (int k = 0; k < 10; ++k) *(LAS v4u*)(lds + k * 8192 + tid * 16) = pv[k];
        }
        __syncthreads();
        if (w < 6) {
#pragma unroll 1
            for (int qt = 0; qt < (half ? 1 : 2); ++qt) {
                f32x4 d = {}, e = {};
                lptr pl = lds + qt * 40960 + lane * 16;
                bf16x8 f[6];
#pragma unroll
                for (int i = 0; i < 6; ++i) f[i] = lds8(pl + i * 1024);
                __builtin_amdgcn_sched_barrier(0);
#pragma unroll
                for (int k = 0; k < 40; ++k) {
                    const bf16x8 v = k < 32 ? b[k < 32 ? k : 0] : as8(z[k < 32 ? 0 : k - 32]);
                    if (k & 1) e = MFMA16(v, f[k % 6], e); else d = MFMA16(v, f[k % 6], d);
                    if (k + 6 < 40) f[k % 6] = lds8(pl + (k + 6) * 1024);
                    __builtin_amdgcn_sched_barrier(0);
                }
                d += e;
                const float gq = qt ? gt[1] : gt[0], og = rbf(1.0f - gq);
                const v2u aq = qt ? av[1] : av[0];
                const float r0 = rbf(gq * bflo(aq.x)) + rbf(og * rbf(d[0])), r1 = rbf(gq * bfhi(aq.x)) + rbf(og * rbf(d[1]));
                const float r2 = rbf(gq * bflo(aq.y)) + rbf(og * rbf(d[2])), r3 = rbf(gq * bfhi(aq.y)) + rbf(og * rbf(d[3]));
                v2u ov = {pack2(r0, r1), pack2(r2, r3)};
                *(v2u*)(a.out + (size_t)(32 * s + 16 * (qt ^ q0) + i16) * DM + 16 * r + 4 * g) = ov;
            }
        }
        if (half) break;
        LBAR();
    }
    if (budget < 0 && lane == 0) ST4(6u, rw, FL_ERR * 128);
}

__global__ void __launch_bounds__(512, 2) k_all(Args a) {
    extern __shared__ __attribute__((aligned(16))) char smem[];
    lptr lds = (lptr)smem;
    const rsrc_t rw = __builtin_amdgcn_make_buffer_rsrc(a.ws, 0, WS_BYTES, 0x00020000);
    RST(0);
    if (threadIdx.x == 0) {
        const int x = (int)(__builtin_amdgcn_s_getreg(20 | (3 << 11)) & 7u);
        int role = 0;
        for (int k = 0; k < 8; ++k) {
            const int gq = (x + k) & 7;
            const int t = 1 + (int)__hip_atomic_fetch_add((u32*)(a.ws + (FL_T + gq) * 128), 1u, __ATOMIC_RELAXED, __HIP_MEMORY_SCOPE_AGENT);
            if (t < 32) { role = gq + 8 * t; break; }
        }
        *(volatile LAS int*)lds = role;
    }
    __syncthreads();
    const int b = __builtin_amdgcn_readfirstlane(*(volatile LAS int*)lds), xg = b & 7, slot = b >> 3;
    __syncthreads();
    WSTAMP(516);

    if (b == 8 * 8) role_probe(rw);
    if (slot >= 20) { role_wvo(a, lds, rw, (xg & 3) * 24 + (xg < 4 ? 12 : 0) + slot - 20); __syncthreads(); }
    if ((xg < 4 && slot >= 26) || (xg >= 4 && slot >= 20)) {
        const int idx = xg < 4 ? 48 + (slot - 26) * 4 + xg : (31 - slot) * 4 + (xg - 4);
        role_s4(a, lds, rw, idx);
        __syncthreads();
    }
    bool post = false;
    if (xg < 4) {
        if (slot < 8) role_qk(a, lds, rw, xg * 8 + slot, xg);
        else if (slot < 26) role_rz(a, lds, rw, xg * 18 + slot - 8, xg);
    } else {
        const int j = xg - 4;
        post = true;
        if (slot < 16) { role_vvo(a, lds, rw, j * 8 + (slot >> 1), slot & 1); post = slot < a.mask; }
        else if (slot < 20) role_k2(a, lds, rw, j * 4 + slot - 16);
        else if (slot < 28) { role_q2p(a, lds, rw, j * 4 + ((slot - 20) >> 1), slot & 1); post = false; }
    }
    WSTAMP(517);
    if (post) { __syncthreads(); role_post(a, lds, rw); }
    WSTAMP(518);
}

extern "C" void kernel_launch(void* const* d_in, const int* in_sizes, int n_in, void* d_out, int out_size, void* d_ws, size_t ws_size, hipStream_t stream) {
    Args a;
    a.u = (const u16*)d_in[0]; a.Are = (const u16*)d_in[1]; a.Aim = (const u16*)d_in[2]; a.Bre = (const u16*)d_in[3]; a.Bim = (const u16*)d_in[4];
    a.Cre = (const u16*)d_in[5]; a.Cim = (const u16*)d_in[6]; a.logdt = (const u16*)d_in[7]; a.dskip = (const u16*)d_in[8];
    a.mem0 = (const u16*)d_in[10]; a.Wq = (const u16*)d_in[11]; a.Wk = (const u16*)d_in[12]; a.Wv = (const u16*)d_in[13]; a.Wo = (const u16*)d_in[14];
    a.gatew = (const u16*)d_in[16]; a.out = (u16*)d_out; a.ws = (char*)d_ws;
    a.pd = 10; a.pha = 8; a.phz = 0; a.gap = 30; a.mask = 4; a.pad0 = 8;
    const int LDS_ALL = 160 * 1024;
    (void)hipFuncSetAttribute((const void*)k_all, hipFuncAttributeMaxDynamicSharedMemorySize, LDS_ALL);
    (void)hipMemsetAsync(d_ws, 0xFF, INIT_BYTES, stream);
    hipLaunchKernelGGL(k_all, dim3(256), dim3(512), LDS_ALL, stream, a);
}
```

```cpp
#include <hip/hip_runtime.h>
#include <hip/hip_bf16.h>
#include <stdint.h>
#include <stdio.h>
#include <stdlib.h>

typedef unsigned short u16;
typedef unsigned int u32;
typedef u32 v4u __attribute__((ext_vector_type(4)));
typedef u32 v2u __attribute__((ext_vector_type(2)));
typedef float f32x4 __attribute__((ext_vector_type(4)));
typedef float f32x2 __attribute__((ext_vector_type(2)));
typedef float f32x16 __attribute__((ext_vector_type(16)));
typedef __bf16 bf16x2 __attribute__((ext_vector_type(2)));
typedef __bf16 bf16x8 __attribute__((ext_vector_type(8)));
typedef __bf16 bf16x16 __attribute__((ext_vector_type(16)));
typedef short s16x4 __attribute__((ext_vector_type(4)));
#define LAS __attribute__((address_space(3)))
typedef LAS char* lptr;
typedef __amdgpu_buffer_rsrc_t rsrc_t;

constexpr int SEQ = 2048, DM = 576, NH = 32, DH = 128, NSEG = 64, NR = 36, NR8 = 72, NREP = 4;
constexpr int MEMB = 9216, BC_QK = 80;
constexpr int FL_K = 0, FL_V = 16, FL_M = 80, FL_P = 112, FL_S = 176, FL_Q = 254, FL_ERR = 255, FL_Z = 256, FL_W = 328, FL_T = 424, FL_PAR = 432, NS4 = 72;
constexpr int OFF_RING = 65536;
constexpr int N_MEMA = 0, N_PX = 16384, N_PMM = 49152, N_MEMB = 49152 + 4096, N_PROBE = 49152 + 6144;
constexpr int INIT_BYTES = OFF_RING + 16 * 65536;
constexpr int OFF_ZH = INIT_BYTES;
constexpr int OFF_KX = OFF_ZH + NSEG * NR * NH * 256;
constexpr int OFF_VOX = OFF_KX + NH * SEQ * DH * 2;
constexpr int OFF_KM = OFF_VOX + NSEG * NR * NH * 1024;
constexpr int OFF_PX = OFF_KM + NSEG * NH * 2048;
constexpr int OFF_A4 = OFF_PX + NSEG * 81920;
constexpr int OFF_G = OFF_A4 + SEQ * DM * 2;
constexpr int OFF_WVO = OFF_G + SEQ * 4;
constexpr int OFF_ST = OFF_WVO + NR8 * 294912;
constexpr int WS_BYTES = OFF_ST + 256 * 520 * 8 + 4096;

#define BIDX bid_
#define RST(k)
#define STAMP(k)
#define STAMP_FLUSH
#define STAMP_INIT
#define STAMP_FIN
#define WSTAMP(k)

struct Args {
    const u16* u; const u16* Are; const u16* Aim; const u16* Bre; const u16* Bim; const u16* Cre; const u16* Cim;
    const u16* logdt; const u16* dskip; const u16* mem0; const u16* Wq; const u16* Wk; const u16* Wv; const u16* Wo;
    const u16* gatew; u16* out; char* ws;
    int pd, pha, phz, gap, mask, pad0;
};

__device__ __forceinline__ float bf2f(u16 v) { return __uint_as_float((u32)v << 16); }
__device__ __forceinline__ float bflo(u32 v) { return __uint_as_float(v << 16); }
__device__ __forceinline__ float bfhi(u32 v) { return __uint_as_float(v & 0xffff0000u); }
__device__ __forceinline__ u32 pack2(float lo, float hi) { f32x2 f = {lo, hi}; bf16x2 b = __builtin_convertvector(f, bf16x2); return __builtin_bit_cast(u32, b); }
__device__ __forceinline__ float rbf(float x) { return bflo(pack2(x, x)); }
__device__ __forceinline__ u32 nos(u32 v) { return min(v, 0xFFFFFFFEu); }
__device__ __forceinline__ v2u trd(lptr p) { return __builtin_bit_cast(v2u, __builtin_amdgcn_ds_read_tr16_b64_v4i16((LAS s16x4*)p)); }
__device__ __forceinline__ bf16x8 as8(v4u v) { return __builtin_bit_cast(bf16x8, v); }
__device__ __forceinline__ bf16x8 as8(v2u a, v2u b) { v4u v = {a.x, a.y, b.x, b.y}; return __builtin_bit_cast(bf16x8, v); }
__device__ __forceinline__ v4u asu(bf16x8 v) { return __builtin_bit_cast(v4u, v); }
__device__ __forceinline__ bf16x16 cat16(bf16x8 a, bf16x8 b) { return __builtin_shufflevector(a, b, 0, 1, 2, 3, 4, 5, 6, 7, 8, 9, 10, 11, 12, 13, 14, 15); }
__device__ __forceinline__ bf16x8 lds8(lptr p) { return *(LAS bf16x8*)p; }
__device__ __forceinline__ u32 bad4(v4u v) { return max(max(v.x, v.y), max(v.z, v.w)); }
#define NEARB(s, rep, pm) (OFF_RING + ((((s) & 3) * 4 + (rep)) << 16) + ((((pm) >> (((s) & 3) * 4 + (rep))) & 1) << 13))
#define FARB(s, rep, pm) ((NEARB(s, rep, pm) ^ 8192) + (((s) & 4) << 13))
#define BCO(slot) (((slot) & 63) * 128 + ((slot) >> 6) * 16384)
#define LBAR() do { asm volatile("s_waitcnt lgkmcnt(0)" ::: "memory"); __builtin_amdgcn_s_barrier(); asm volatile("" ::: "memory"); } while (0)
#define MB() asm volatile("" ::: "memory")
#define DRAIN() asm volatile("s_waitcnt vmcnt(0)" ::: "memory")
#define PIN4(x) asm volatile("" : "+v"(x))
#define MFMA16(a, b, c) __builtin_amdgcn_mfma_f32_16x16x32_bf16((a), (b), (c), 0, 0, 0)
#define MFMA32(a, b, c) __builtin_amdgcn_mfma_f32_32x32x16_bf16((a), (b), (c), 0, 0, 0)
#define SMF(a, b, c, idx) __builtin_amdgcn_smfmac_f32_16x16x64_bf16((a), (b), (c), (idx), 0, 0)
#define LD16(r, vo, so) __builtin_amdgcn_raw_buffer_load_b128((r), (vo), (so), 16)
#define LD8(r, vo, so) __builtin_amdgcn_raw_buffer_load_b64((r), (vo), (so), 16)
#define LD4(r, vo, so) __builtin_amdgcn_raw_buffer_load_b32((r), (vo), (so), 16)
#define ST16(v, r, vo) __builtin_amdgcn_raw_buffer_store_b128((v), (r), (vo), 0, 16)
#define ST8(v, r, vo) __builtin_amdgcn_raw_buffer_store_b64((v), (r), (vo), 0, 16)
#define ST4(v, r, vo) __builtin_amdgcn_raw_buffer_store_b32((v), (r), (vo), 0, 16)

__device__ __forceinline__ float swap32_sum(float a, float b) {
    auto r = __builtin_amdgcn_permlane32_swap(__float_as_uint(a), __float_as_uint(b), false, false);
    return __uint_as_float(r[0]) + __uint_as_float(r[1]); }
__device__ __forceinline__ float xmax(float m) {
    auto r = __builtin_amdgcn_permlane16_swap(__float_as_uint(m), __float_as_uint(m), false, false);
    m = fmaxf(__uint_as_float(r[0]), __uint_as_float(r[1]));
    auto q = __builtin_amdgcn_permlane32_swap(__float_as_uint(m), __float_as_uint(m), false, false);
    return fmaxf(__uint_as_float(q[0]), __uint_as_float(q[1])); }
__device__ __forceinline__ float xsum(float m) {
    auto r = __builtin_amdgcn_permlane16_swap(__float_as_uint(m), __float_as_uint(m), false, false);
    m = __uint_as_float(r[0]) + __uint_as_float(r[1]);
    auto q = __builtin_amdgcn_permlane32_swap(__float_as_uint(m), __float_as_uint(m), false, false);
    return __uint_as_float(q[0]) + __uint_as_float(q[1]); }
__device__ __forceinline__ float vmax(float a, float b) { float r; asm("v_max_f32 %0, %1, %2" : "=v"(r) : "v"(a), "v"(b)); return r; }
__device__ __forceinline__ float vmax3(float a, float b, float c) { float r; asm("v_max3_f32 %0, %1, %2, %3" : "=v"(r) : "v"(a), "v"(b), "v"(c)); return r; }
__device__ __forceinline__ u32 ror8(u32 v) { return (u32)__builtin_amdgcn_update_dpp(0, (int)v, 0x128, 0xf, 0xf, true); }
__device__ __forceinline__ float ror8f(float v) { return __uint_as_float(ror8(__float_as_uint(v))); }
__device__ __forceinline__ int wave_max(int d) {
    d = max(d, __builtin_amdgcn_update_dpp(d, d, 0x128, 0xf, 0xf, false));
    d = max(d, __builtin_amdgcn_update_dpp(d, d, 0x124, 0xf, 0xf, false));
    d = max(d, __builtin_amdgcn_update_dpp(d, d, 0x122, 0xf, 0xf, false));
    d = max(d, __builtin_amdgcn_update_dpp(d, d, 0x121, 0xf, 0xf, false));
    return max(max(__builtin_amdgcn_readlane(d, 0), __builtin_amdgcn_readlane(d, 16)), max(__builtin_amdgcn_readlane(d, 32), __builtin_amdgcn_readlane(d, 48)));
}

constexpr int SPIN_MAX = 2000000;
__device__ __forceinline__ int wait_flags(rsrc_t rw, int line0, int stride, int n, int need, int lane, int& budget, bool fast = false) {
    const int vo = (line0 + (lane < n ? lane : 0) * stride) * 128;
    int mn;
    for (;;) {
        const int v = (int)LD4(rw, vo, 0);
        MB();
        mn = -wave_max(-v);
        if (mn >= need) break;
        if (--budget < 0) break;
        if (fast) __builtin_amdgcn_s_sleep(2); else __builtin_amdgcn_s_sleep(12);
    }
    return mn;
}

template <int NRW, int CW>
__device__ __forceinline__ void stage_rows(const u16* g, int gs, lptr lds, int ls, int tid) {
    constexpr int N = NRW * CW / 512;
    static_assert(NRW * CW % 512 == 0, "");
    v4u v[N];
#pragma unroll
    for (int j = 0; j < N; ++j) { const int i = tid + 512 * j, row = i / CW, c = i % CW; v[j] = *(const v4u*)(g + (size_t)row * gs + c * 8); }
#pragma unroll
    for (int j = 0; j < N; ++j) { const int i = tid + 512 * j, row = i / CW, c = i % CW; *(LAS v4u*)(lds + row * ls + c * 16) = v[j]; }
}
template <int NRW, int CW>
__device__ __forceinline__ void rows_ld(v4u (&v)[NRW * CW / 512], const u16* g, int gs, int tid) {
#pragma unroll
    for (int j = 0; j < NRW * CW / 512; ++j) { const int i = tid + 512 * j, row = i / CW, c = i % CW; v[j] = *(const v4u*)(g + (size_t)row * gs + c * 8); }
}
template <int NRW, int CW>
__device__ __forceinline__ void rows_st(const v4u (&v)[NRW * CW / 512], lptr lds, int ls, int tid) {
#pragma unroll
    for (int j = 0; j < NRW * CW / 512; ++j) { const int i = tid + 512 * j, row = i / CW, c = i % CW; *(LAS v4u*)(lds + row * ls + c * 16) = v[j]; }
}
__device__ __forceinline__ bf16x8 frag16(lptr base, int ls, int lane) {
    const int i16 = lane & 15, bg = lane >> 4, q = i16 >> 2, p = i16 & 3;
    lptr a = base + (8 * bg + q) * ls + p * 8;
    return as8(trd(a), trd(a + 4 * ls));
}
__device__ __forceinline__ bf16x8 frag32p(lptr base, int ls, int lane) {
    const int i16 = lane & 15, G = lane >> 4, q = i16 >> 2, p = i16 & 3;
    lptr a = base + (8 * (G >> 1) + q) * ls + (16 * (p & 1) + 8 * (G & 1) + 4 * (p >> 1)) * 2;
    return as8(trd(a), trd(a + 4 * ls));
}

constexpr int XS = 1168;
__device__ __forceinline__ void xload(v4u (&st)[9], const u16* x, int tok0, int tid) {
#pragma unroll
    for (int j = 0; j < 9; ++j) st[j] = *(const v4u*)(x + (size_t)tok0 * DM + (tid + 512 * j) * 8);
}
__device__ __forceinline__ void xstore(const v4u (&st)[9], lptr xl, int tid) {
#pragma unroll
    for (int j = 0; j < 9; ++j) { const int i = tid + 512 * j; *(LAS v4u*)(xl + i * 16 + (i / 72) * 16) = st[j]; }
}
__device__ __forceinline__ void load_w2(bf16x8 (&A)[36], const u16* W, int h0, lptr lds, int tid, int lane, int w) {
    v4u v[6][6];
#pragma unroll
    for (int c = 0; c < 6; ++c) rows_ld<96, 32>(v[c], W + (size_t)c * 96 * 4096 + h0 * DH, 4096, tid);
    __builtin_amdgcn_sched_barrier(0);
#pragma unroll
    for (int c = 0; c < 6; ++c) {
        lptr l = lds + (c & 1) * (96 * 544);
        rows_st<96, 32>(v[c], l, 544, tid);
        LBAR();
#pragma unroll
        for (int ks = 0; ks < 6; ++ks) A[c * 6 + ks] = frag32p(l + ks * 16 * 544 + w * 64, 544, lane);
        __builtin_amdgcn_sched_barrier(0);
    }
    LBAR();
}

__device__ __forceinline__ f32x16 proj_tile(const bf16x8 (&A)[36], lptr xb) {
    f32x16 acc = {};
    bf16x8 f[6];
#pragma unroll
    for (int i = 0; i < 6; ++i) f[i] = lds8(xb + i * 32);
    __builtin_amdgcn_sched_barrier(0);
#pragma unroll
    for (int ks = 0; ks < 36; ++ks) {
        acc = MFMA32(A[ks], f[ks % 6], acc);
        if (ks + 6 < 36) f[ks % 6] = lds8(xb + (ks + 6) * 32);
        __builtin_amdgcn_sched_barrier(0);
    }
    return acc;
}
__device__ __forceinline__ void role_k2(const Args& a, lptr lds, rsrc_t rw, int cu) {
    const int tid = threadIdx.x, lane = tid & 63, w = __builtin_amdgcn_readfirstlane(tid >> 6);
    v4u st[9];
    bf16x8 A[36];
    xload(st, a.u, 0, tid);
    load_w2(A, a.Wk, 2 * cu, lds, tid, lane, w);
    RST(4);
    xstore(st, lds, tid);
    __syncthreads();
    const int o0 = OFF_KX + (2 * cu + (w >> 2)) * (NSEG * 8192) + ((w & 3) * 4 + (lane >> 5) * 2) * 512 + (lane & 31) * 16;
    for (int blk = 0; blk < 32; ++blk) {
        lptr xl = lds + (blk & 1) * (64 * XS);
        if (blk + 1 < 32) xload(st, a.u, (blk + 1) * 64, tid);
#pragma unroll
        for (int tt = 0; tt < 2; ++tt) {
            const f32x16 acc = proj_tile(A, xl + (32 * tt + (lane & 31)) * XS + (lane >> 5) * 16);
            v4u c0 = {pack2(acc[0], acc[1]), pack2(acc[2], acc[3]), pack2(acc[4], acc[5]), pack2(acc[6], acc[7])};
            v4u c1 = {pack2(acc[8], acc[9]), pack2(acc[10], acc[11]), pack2(acc[12], acc[13]), pack2(acc[14], acc[15])};
            const int o = o0 + (blk * 2 + tt) * 8192;
            ST16(c0, rw, o); ST16(c1, rw, o + 512);
            if (blk == 0 && tt == 0) { DRAIN(); LBAR(); if (tid == 0) ST4(0u, rw, (FL_K + cu) * 128); RST(5); }
        }
        if (blk + 1 < 32) xstore(st, lds + ((blk + 1) & 1) * (64 * XS), tid);
        const bool full = blk < 4 || blk == 31;
        if (full) DRAIN(); else asm volatile("s_waitcnt vmcnt(4)" ::: "memory");
        LBAR();
        if (tid == 0) ST4((u32)(full ? 2 * blk + 1 : 2 * blk - 1), rw, (FL_K + cu) * 128);
    }
}

constexpr int VS = 272;
__device__ __forceinline__ void role_q2p(const Args& a, lptr lds, rsrc_t rw, int cu, int par) {
    const int tid = threadIdx.x, lane = tid & 63, w = __builtin_amdgcn_readfirstlane(tid >> 6);
    v4u st[9];
    bf16x8 A[36];
    load_w2(A, a.Wq, 2 * cu, lds, tid, lane, w);
    lptr xl = lds, ql = lds + 64 * XS;
    LAS int* cnt = (LAS int*)(lds + 64 * XS + 4 * 64 * VS);
    if (tid < 2) cnt[tid] = 0;
    xload(st, a.u, par * 64, tid);
    xstore(st, xl, tid);
    __syncthreads();
    int budget = SPIN_MAX, kdone = -1;
    const int i16 = lane & 15, g = lane >> 4;
    const int sg = w >> 2, hh = (w >> 1) & 1, qt = w & 1, h = 2 * cu + hh;
    const float c2 = 0.08838834764831845f * 1.4426950408889634f;
    for (int it = -1; it < 16; ++it) {
        const int blk = 2 * it + par;
        if (it + 1 < 16) {
            if (it + 2 < 16) xload(st, a.u, (blk + 4) * 64, tid);
            lptr qb = ql + (((it + 1) & 1) * 2 + (w >> 2)) * (64 * VS) + ((w & 3) * 32 + (lane >> 5) * 16) * 2;
#pragma unroll
            for (int tt = 0; tt < 2; ++tt) {
                const f32x16 acc = proj_tile(A, xl + (32 * tt + (lane & 31)) * XS + (lane >> 5) * 16);
                v4u c0 = {pack2(acc[0], acc[1]), pack2(acc[2], acc[3]), pack2(acc[4], acc[5]), pack2(acc[6], acc[7])};
                v4u c1 = {pack2(acc[8], acc[9]), pack2(acc[10], acc[11]), pack2(acc[12], acc[13]), pack2(acc[14], acc[15])};
                lptr qp = qb + (32 * tt + (lane & 31)) * VS;
                *(LAS v4u*)qp = c0; *(LAS v4u*)(qp + 16) = c1;
            }
            __syncthreads();
            if (it + 2 < 16) xstore(st, xl, tid);
        }
        if (it >= 0) {
            const int s = 2 * blk + sg;
            if (kdone < s) kdone = wait_flags(rw, FL_K + cu, 1, 1, s, lane, budget);
            bf16x8 k0[4], k1[4], km[4], qf[4];
            const int ko = OFF_KX + (h * NSEG + s) * 8192 + g * 512 + i16 * 16;
#pragma unroll
            for (int ks = 0; ks < 4; ++ks) { k0[ks] = as8(LD16(rw, ko, ks * 2048)); k1[ks] = as8(LD16(rw, ko + 256, ks * 2048)); }
            lptr qp = ql + ((it & 1) * 2 + hh) * (64 * VS) + (32 * sg + 16 * qt + i16) * VS + g * 16;
#pragma unroll
            for (int ks = 0; ks < 4; ++ks) qf[ks] = lds8(qp + ks * 64);
            f32x4 S0 = {}, S1 = {}, Sm = {};
#pragma unroll
            for (int ks = 0; ks < 4; ++ks) { S0 = MFMA16(k0[ks], qf[ks], S0); S1 = MFMA16(k1[ks], qf[ks], S1); }
            wait_flags(rw, FL_M + h, 1, 1, s, lane, budget, s >= 62);
            const int mo = OFF_KM + (s * NH + h) * 2048 + g * 128 + (i16 & 7) * 16;
#pragma unroll
            for (int ks = 0; ks < 4; ++ks) km[ks] = as8(LD16(rw, mo + ks * 512, 0));
#pragma unroll
            for (int ks = 0; ks < 4; ++ks) Sm = MFMA16(km[ks], qf[ks], Sm);
            const int qi = 16 * qt + i16, t0 = 4 * g;
#pragma unroll
            for (int r = 0; r < 4; ++r) {
                if (t0 + r > qi) S0[r] = -INFINITY;
                if (t0 + r + 16 > qi) S1[r] = -INFINITY;
                if (g >= 2) Sm[r] = -INFINITY;
            }
            float m = fmaxf(fmaxf(fmaxf(S0[0], S0[1]), fmaxf(S0[2], S0[3])), fmaxf(fmaxf(S1[0], S1[1]), fmaxf(S1[2], S1[3])));
            m = fmaxf(m, fmaxf(fmaxf(Sm[0], Sm[1]), fmaxf(Sm[2], Sm[3])));
            m = xmax(m);
            const float nm = -m * c2;
            float sum = 0.f;
#pragma unroll
            for (int r = 0; r < 4; ++r) {
                S0[r] = __builtin_amdgcn_exp2f(fmaf(S0[r], c2, nm)); S1[r] = __builtin_amdgcn_exp2f(fmaf(S1[r], c2, nm)); Sm[r] = __builtin_amdgcn_exp2f(fmaf(Sm[r], c2, nm));
                sum += S0[r] + S1[r] + Sm[r];
            }
            sum = xsum(sum);
            const float inv = __builtin_amdgcn_rcpf(sum);
            v4u px = {pack2(S0[0] * inv, S0[1] * inv), pack2(S0[2] * inv, S0[3] * inv), pack2(S1[0] * inv, S1[1] * inv), pack2(S1[2] * inv, S1[3] * inv)};
            const int po = OFF_PX + (s * 2 + qt) * 40960;
            ST16(px, rw, po + h * 1024 + lane * 16);
            const u32 ma = pack2(Sm[0] * inv, Sm[1] * inv), mb = pack2(Sm[2] * inv, Sm[3] * inv);
            auto ra = __builtin_amdgcn_permlane16_swap(ma, ma, false, false);
            auto rb = __builtin_amdgcn_permlane16_swap(mb, mb, false, false);
            v4u pm = {ra[0], rb[0], ra[1], rb[1]};
            if (lane < 16) ST16(pm, rw, po + 32768 + h * 256 + lane * 16);
            DRAIN();
            if (lane == 0) {
                const int old = __hip_atomic_fetch_add(cnt + sg, 1, __ATOMIC_RELAXED, __HIP_MEMORY_SCOPE_WORKGROUP);
                if ((old & 3) == 3) ST4((u32)s, rw, (FL_P + 4 * cu + 2 * par + sg) * 128);
            }
        }
        __syncthreads();
    }
    if (budget < 0 && lane == 0) ST4(5u, rw, FL_ERR * 128);
}

__device__ __forceinline__ void role_vvo(const Args& a, lptr lds, rsrc_t rw, int h, int par) {
    const int tid = threadIdx.x, lane = tid & 63, w = __builtin_amdgcn_readfirstlane(tid >> 6);
    v4u st[9];
#pragma unroll
    for (int j = 0; j < 9; ++j) { const int i = tid + 512 * j; st[j] = *(const v4u*)(a.u + (size_t)(par * 32 + (i >= 2304 ? 32 : 0)) * DM + i * 8); }
    bf16x8 Av[18], Bo[5][4];
    {
        v4u v[3][6], o[2][9];
#pragma unroll
        for (int c = 0; c < 3; ++c) rows_ld<192, 16>(v[c], a.Wv + (size_t)c * 192 * 4096 + h * DH, 4096, tid);
#pragma unroll
        for (int c = 0; c < 2; ++c) rows_ld<64, 72>(o[c], a.Wo + ((size_t)h * DH + c * 64) * DM, DM, tid);
        __builtin_amdgcn_sched_barrier(0);
#pragma unroll
        for (int c = 0; c < 3; ++c) {
            lptr l = lds + (c & 1) * 77824;
            rows_st<192, 16>(v[c], l, 288, tid);
            LBAR();
#pragma unroll
            for (int ks = 0; ks < 6; ++ks) Av[c * 6 + ks] = frag16(l + ks * 32 * 288 + w * 32, 288, lane);
            __builtin_amdgcn_sched_barrier(0);
        }
#pragma unroll
        for (int c = 0; c < 2; ++c) {
            lptr l = lds + ((c + 1) & 1) * 77824;
            rows_st<64, 72>(o[c], l, 1184, tid);
            LBAR();
#pragma unroll
            for (int j = 0; j < 5; ++j) {
                const int r = (w + 8 * j < NR) ? w + 8 * j : w;
#pragma unroll
                for (int ks = 0; ks < 2; ++ks) Bo[j][c * 2 + ks] = frag16(l + ks * 32 * 1184 + r * 32, 1184, lane);
            }
            __builtin_amdgcn_sched_barrier(0);
        }
        LBAR();
    }
    RST(4);
    lptr xl = lds, vl = lds + 64 * XS;
    xstore(st, xl, tid);
    __syncthreads();
    const int rho = lane & 15, bg = lane >> 4;
    const int vo0 = OFF_VOX + ((2 * w + ((rho >> 1) & 1)) * 16 + (h >> 1)) * 1024 + ((2 * (h & 1) + (bg & 1)) * 16 + (bg >> 1) * 8 + 2 * (rho >> 2) + (rho & 1)) * 16;
    const int fo = (FL_V + 2 * h + par) * 128;
    auto stage1 = [&](int tt) {
        f32x4 acc = {};
        lptr xb = xl + (16 * tt + rho) * XS + bg * 16;
        bf16x8 f[6];
#pragma unroll
        for (int i = 0; i < 6; ++i) f[i] = lds8(xb + i * 64);
        __builtin_amdgcn_sched_barrier(0);
#pragma unroll
        for (int ks = 0; ks < 18; ++ks) {
            acc = MFMA16(Av[ks], f[ks % 6], acc);
            if (ks + 6 < 18) f[ks % 6] = lds8(xb + (ks + 6) * 64);
            __builtin_amdgcn_sched_barrier(0);
        }
        v2u o = {pack2(acc[0], acc[1]), pack2(acc[2], acc[3])};
        *(LAS v2u*)(vl + (16 * tt + rho) * VS + (16 * w + 4 * bg) * 2) = o;
    };
    auto stage2 = [&](int sg, int s) {
        bf16x8 Af[2][4];
#pragma unroll
        for (int half = 0; half < 2; ++half)
#pragma unroll
            for (int ks = 0; ks < 4; ++ks) Af[half][ks] = lds8(vl + (32 * sg + rho + 16 * half) * VS + ks * 64 + bg * 16);
#pragma unroll
        for (int j = 0; j < 5; ++j) {
            if (j < 4 || w < 4) {
                f32x4 d0 = {}, d1 = {};
#pragma unroll
                for (int ks = 0; ks < 4; ++ks) { d0 = MFMA16(Af[0][ks], Bo[j][ks], d0); d1 = MFMA16(Af[1][ks], Bo[j][ks], d1); }
                v4u o = {pack2(d0[0], d0[1]), pack2(d0[2], d0[3]), pack2(d1[0], d1[1]), pack2(d1[2], d1[3])};
                ST16(o, rw, vo0 + (s * NR + 8 * j) * (NH * 1024));
            }
        }
    };
    int prev = -1;
#pragma unroll 1
    for (int un = 0; un < 32; ++un) {
        const int sg = un & 1, blk = (un >> 1) * 2 + par;
        const int s = un < 2 ? par + 2 * un : 2 * blk + sg;
        if (sg == 0 && blk + 2 < 32) xload(st, a.u, (blk + 2) * 64, tid);
        stage1(2 * sg); stage1(2 * sg + 1);
        if (sg == 1) DRAIN(); else if (blk + 2 < 32) asm volatile("s_waitcnt vmcnt(9)" ::: "memory"); else DRAIN();
        LBAR();
        if (tid == 0 && prev >= 0) ST4((u32)prev, rw, fo);
        if (sg == 1 && blk + 2 < 32) xstore(st, xl, tid);
        stage2(sg, s);
        prev = s;
        if (un == 0 || un == 31) { DRAIN(); LBAR(); if (tid == 0) ST4((u32)s, rw, fo); if (un == 0) RST(5); }
        else if (sg == 1) LBAR();
    }
}
#define VOWN(s) ((s) < 4 ? ((s) & 1) : (((s) >> 1) & 1))

__device__ __forceinline__ u32 rt32() { return (u32)__builtin_amdgcn_s_memrealtime(); }
__device__ __forceinline__ void wait_until(u32 t) {
    if ((int)(t - rt32()) > 1000) return;
    while ((int)(rt32() - t) < -24) __builtin_amdgcn_s_sleep(4);
    while ((int)(rt32() - t) < 0) {}
}
struct TK { u32 XA, XR, EA, ER; int dA0, dA1, dA2, dR0, dR1, dR2; int okA, okR, gap; };
__device__ __forceinline__ int tk_est(int d0, int d1, int d2) { return max(min(d0, d1), min(max(d0, d1), d2)); }
template <int N>
__device__ __forceinline__ u32 beacon_max(rsrc_t rw, int fb, int slot0, int lane, int& budget) {
    const int vo = BCO(slot0 + (lane < N ? lane : 0)), v2 = BCO(64 + (lane & 7));
    u32 v, u = 0;
    for (;;) {
        v = LD4(rw, vo, fb);
        if (N > 64) u = LD4(rw, v2, fb);
        MB();
        if (__all(max(v, u) != 0xFFFFFFFFu)) break;
        if (--budget < 0) break;
        __builtin_amdgcn_s_sleep(1);
    }
    const u32 base = __builtin_amdgcn_readfirstlane(v);
    int d = (int)(v - base);
    if (N > 64) d = max(d, (int)(u - base));
    return base + (u32)wave_max(d);
}
struct BC2 { u32 v, u; };
__device__ __forceinline__ BC2 beacon_issue(rsrc_t rw, int fb, int lane) {
    BC2 b;
    b.v = LD4(rw, BCO(lane), fb);
    b.u = LD4(rw, BCO(64 + (lane & 7)), fb);
    MB();
    return b;
}
__device__ __forceinline__ u32 beacon_finish(BC2 b, rsrc_t rw, int fb, int lane, int& budget) {
    if (!__all(max(b.v, b.u) != 0xFFFFFFFFu)) return beacon_max<NR8>(rw, fb, 0, lane, budget);
    const u32 base = __builtin_amdgcn_readfirstlane(b.v);
    return base + (u32)wave_max(max((int)(b.v - base), (int)(b.u - base)));
}
__device__ __forceinline__ void tk_T0(TK& k, u32 T0, const Args& a) {
    if (k.okR) { k.dR2 = k.dR1; k.dR1 = k.dR0; k.dR0 = (int)(T0 - k.ER); }
    k.okA = (int)(k.XA - T0) >= a.pad0 - a.pha;
    if (!k.okR) k.gap = k.okA ? max(a.gap, k.gap - 6) : min(k.gap + 16, 160);
    k.EA = k.XA;
    k.XR = k.EA + tk_est(k.dA0, k.dA1, k.dA2) + a.pd;
}
__device__ __forceinline__ void tk_Tp(TK& k, u32 Tp, const Args& a) {
    const int okn = (int)(k.XR - Tp) >= a.pd - a.pha;
    if (k.okA && (k.okR || !okn)) { k.dA2 = k.dA1; k.dA1 = k.dA0; k.dA0 = (int)(Tp - k.EA); }
    k.okR = okn;
    k.ER = k.okR ? k.XR : Tp + k.gap;
    k.XA = k.ER + tk_est(k.dR0, k.dR1, k.dR2) + a.pad0;
}
__device__ __forceinline__ u32 tk_specA(const TK& k, const Args& a) { return k.XR + tk_est(k.dR0, k.dR1, k.dR2) + a.pad0; }
__device__ __forceinline__ u32 tk_specR(const TK& k, const Args& a) { return k.XA + tk_est(k.dA0, k.dA1, k.dA2) + a.pd; }
__device__ __forceinline__ void tk_init(TK& k) { const u32 n = rt32(); k.XA = k.XR = k.EA = k.ER = n - 100000u; k.dA0 = k.dA1 = k.dA2 = 134; k.dR0 = k.dR1 = k.dR2 = 80; k.okA = k.okR = 0; k.gap = 50; }

__device__ __forceinline__ void tk_post(lptr p, int tag, u32 t, int lane) { if (lane == 0) { v2u v = {(u32)tag, t}; *(volatile LAS v2u*)p = v; } }
__device__ __forceinline__ u32 tk_get(lptr p, int tag, int& budget) {
    for (;;) {
        const v2u v = *(volatile LAS v2u*)p;
        if ((int)__builtin_amdgcn_readfirstlane(v.x) >= tag || --budget < 0) return __builtin_amdgcn_readfirstlane(v.y);
        __builtin_amdgcn_s_sleep(2);
    }
}
__device__ __forceinline__ void tk_wait(lptr p, int tag, int& budget) {
    u32 X;
    for (;;) {
        const v2u v = *(volatile LAS v2u*)p;
        const u32 now = rt32();
        X = __builtin_amdgcn_readfirstlane(v.y);
        const int tg = (int)__builtin_amdgcn_readfirstlane(v.x), dt = (int)(X - now);
        if (tg >= tag) {
            if (dt <= 0 || dt > 1000 || tg > tag) return;
            if (dt <= 28) break;
        }
        if (--budget < 0) return;
        __builtin_amdgcn_s_sleep(1);
    }
    while ((int)(rt32() - X) < 0) {}
}
constexpr int OOB = 0x70000000;
__device__ __forceinline__ void stage_rows8(const u16* src, lptr ml, int tid) {
    for (int c = tid; c < 576; c += 512) {
        const u32* p = (const u32*)(src + (c & 7) * DM + 16 * (c >> 4) + 2 * ((c >> 3) & 1));
        v4u v = {p[0], p[2], p[4], p[6]};
        *(LAS v4u*)(ml + c * 16) = v;
    }
}
__device__ __forceinline__ void gather_mem(rsrc_t rw, int nb, lptr ml, int lane, int w, int& budget) {
    const int gi = w == 7 ? 5 : w;
    const int l0 = gi * 1536 + lane * 16, l1 = l0 + 1024;
    const int o0 = l0 < 8192 ? l0 : l0 + (N_MEMB - 8192), o1 = lane < 32 ? (l1 < 8192 ? l1 : l1 + (N_MEMB - 8192)) : OOB;
    lptr p1 = ml + (lane < 32 ? l1 : 14336 + lane * 16);
    v4u v0, v1;
    for (;;) {
        v0 = LD16(rw, o0, nb); v1 = LD16(rw, o1, nb);
        MB();
        if (__builtin_expect(__all(max(bad4(v0), bad4(v1)) != 0xFFFFFFFFu), 1)) break;
        if (--budget < 0) break;
    }
    *(LAS v4u*)(ml + l0) = v0; *(LAS v4u*)p1 = v1;
}
__device__ __forceinline__ void gather_mem4(rsrc_t rw, int nb, lptr ml, int lane, int gi, int& budget) {
    const int l0 = gi * 2048 + lane * 16;
    const int o2 = lane < 16 ? N_MEMB + gi * 256 + lane * 16 : OOB;
    lptr p2 = ml + (lane < 16 ? 8192 + gi * 256 + lane * 16 : 14336 + lane * 16);
    v4u v0, v1, v2;
    for (;;) {
        v0 = LD16(rw, l0, nb); v1 = LD16(rw, l0 + 1024, nb); v2 = LD16(rw, o2, nb);
        MB();
        if (__builtin_expect(__all(max(max(bad4(v0), bad4(v1)), bad4(v2)) != 0xFFFFFFFFu), 1)) break;
        if (--budget < 0) break;
    }
    *(LAS v4u*)(ml + l0) = v0; *(LAS v4u*)(ml + l0 + 1024) = v1; *(LAS v4u*)p2 = v2;
}
__device__ __forceinline__ int get_parity(rsrc_t rw, int lane, int& budget) {
    const int vo = (FL_PAR + (lane & 7)) * 128;
    u32 v;
    for (;;) {
        v = LD4(rw, vo, 0);
        MB();
        if (__all(v != 0xFFFFFFFFu) || --budget < 0) break;
        __builtin_amdgcn_s_sleep(8);
    }
    int pm = 0;
#pragma unroll
    for (int k = 0; k < 8; ++k) pm |= (__builtin_amdgcn_readlane((int)v, k) & 3) << (2 * k);
    return __builtin_popcount(pm) > 8 ? 0xFFFF : 0;
}
__device__ __forceinline__ void role_probe(rsrc_t rw) {
    const int lane = threadIdx.x & 63, w = __builtin_amdgcn_readfirstlane(threadIdx.x >> 6);
    u32 bits = 0;
    for (int gg = 0; gg < 2; ++gg) {
        int best[2] = {1 << 30, 1 << 30};
        for (int rep = 0; rep < 3; ++rep) {
#pragma unroll
            for (int p = 0; p < 2; ++p) {
                const int o = OFF_RING + (2 * w + gg) * 65536 + N_PROBE + p * 8192 + rep * 128;
                ST4(0xFFFFFFFFu, rw, lane == 0 ? o : OOB);
                DRAIN();
                const unsigned long long t0 = __builtin_amdgcn_s_memtime();
                asm volatile("s_waitcnt lgkmcnt(0)" ::: "memory");
                u32 v = LD4(rw, o, 0);
                asm volatile("s_waitcnt vmcnt(0)" : "+v"(v) :: "memory");
                const unsigned long long t1 = __builtin_amdgcn_s_memtime();
                best[p] = min(best[p], (int)(t1 - t0));
            }
        }
        bits |= (best[1] < best[0] ? 1u : 0u) << gg;
    }
    if (lane == 0) ST4(bits, rw, (FL_PAR + w) * 128);
}
__device__ __forceinline__ void qk_project(const bf16x16 (&Bq)[9], const bf16x16 (&Bk)[9], lptr ma, lptr xw, int idx) {
    f32x4 aq = {}, ak = {};
    bf16x8 A[9];
#pragma unroll
    for (int t = 0; t < 9; ++t) A[t] = lds8(ma + t * 1024);
    __builtin_amdgcn_sched_barrier(0);
#pragma unroll
    for (int t = 0; t < 9; ++t) { aq = SMF(A[t], Bq[t], aq, idx); ak = SMF(A[t], Bk[t], ak, idx); }
    const float s0 = swap32_sum(aq[0], ak[0]), s1 = swap32_sum(aq[1], ak[1]), s2 = swap32_sum(aq[2], ak[2]), s3 = swap32_sum(aq[3], ak[3]);
    v2u o = {pack2(s0, s1), pack2(s2, s3)};
    *(LAS v2u*)xw = o;
}

__device__ __forceinline__ void role_qk(const Args& a, lptr lds, rsrc_t rw, int h, int rep) {
    const int tid = threadIdx.x, lane = tid & 63, w = __builtin_amdgcn_readfirstlane(tid >> 6);
    bf16x16 Bq[9], Bk[9];
    for (int i = 0; i < a.phz; ++i) __builtin_amdgcn_s_sleep(16);
    {
        v4u v[6][6];
#pragma unroll
        for (int c = 0; c < 6; ++c) rows_ld<192, 16>(v[c], (c >= 3 ? a.Wk : a.Wq) + (size_t)(c % 3) * 192 * 4096 + h * DH, 4096, tid);
        __builtin_amdgcn_sched_barrier(0);
#pragma unroll
        for (int c = 0; c < 6; ++c) {
            lptr l = lds + (c & 1) * 77824;
            rows_st<192, 16>(v[c], l, 288, tid);
            LBAR();
#pragma unroll
            for (int t = 0; t < 3; ++t) {
                bf16x16 f = cat16(frag16(l + (64 * t) * 288 + w * 32, 288, lane), frag16(l + (64 * t + 32) * 288 + w * 32, 288, lane));
                if (c >= 3) Bk[(c % 3) * 3 + t] = f; else Bq[c * 3 + t] = f;
            }
            __builtin_amdgcn_sched_barrier(0);
        }
        LBAR();
    }
    lptr ml = lds, xl = lds + MEMB;
    const int idx = (lane & 8) ? 0xEEEE : 0x4444;
    const int i16 = lane & 15, g = lane >> 4;
    const bool hi8 = (lane & 8) != 0;
    int budget = SPIN_MAX;
    lptr pq = xl + (8 * g + (i16 >> 2)) * 32 + (i16 & 1) * 8;
    lptr kl = lds + 16384 + lane * 16;
    lptr ma = ml + lane * 16, xw = xl + (16 * w + i16) * 32 + g * 8;
    const int ox = N_PX + (h >> 4) * 16384 + (h & 15) * 512 + ((g & 1) * 16 + (g >> 1) * 8 + (i16 & 7)) * 16;
    const int om = hi8 ? OOB : N_PMM + h * 128 + (i16 & 7) * 16;
    const int ob = lane < NREP ? BCO(BC_QK + h) : OOB;
    const int pm = get_parity(rw, lane, budget);
    lptr tkp = lds + MEMB + 4096, kfp = tkp + 64;
    if (tid == 0) { tk_post(tkp, 0, rt32() - 100000u, 0); tk_post(kfp, 0, 0u, 0); }
    stage_rows8(a.mem0, ml, tid);
#pragma unroll
    for (int G = 0; G < 4; ++G) stage_rows8(a.u + G * 8 * DM, lds + 32768 + G * MEMB, tid);
    __syncthreads();
#pragma unroll 1
    for (int G = 0; G < 4; ++G) {
        qk_project(Bq, Bk, lds + 32768 + G * MEMB + lane * 16, xw, idx);
        LBAR();
        if (w < 4) {
            const bf16x8 F = as8(trd(pq + w * 1024 + 16), trd(pq + w * 1024 + 16 + 128));
            if ((i16 >> 3) == (G & 1)) *(LAS bf16x8*)(kl + ((G >> 1) * 4 + w) * 1024) = F;
        }
        LBAR();
    }
    RST(5);
    STAMP_INIT
    if (w == 0) {
        const float c2 = 0.08838834764831845f * 1.4426950408889634f;
        for (int s = 0; s < NSEG - 1; ++s) {
            tk_get(kfp, s, budget);
            bf16x8 kx[2][4];
#pragma unroll
            for (int tl = 0; tl < 2; ++tl)
#pragma unroll
                for (int ks = 0; ks < 4; ++ks) kx[tl][ks] = lds8(kl + (tl * 4 + ks) * 1024);
            int oxs = ox + NEARB(s, i16 >> 3, pm), oxt = ox + NEARB(s, 2 + (i16 >> 3), pm), oms = om + NEARB(s, g, pm), obs = ob + FARB(s, lane & 3, pm);
            const int nb = NEARB(s, rep, pm);
            asm volatile("s_waitcnt lgkmcnt(0)" : "+v"(oxs), "+v"(oxt), "+v"(oms), "+v"(obs) :: "memory");
#pragma unroll
            for (int tl = 0; tl < 2; ++tl)
#pragma unroll
                for (int ks = 0; ks < 4; ++ks) PIN4(kx[tl][ks]);
            if (s > 0) {
                tk_wait(tkp, s, budget);
                STAMP(0);
                gather_mem(rw, nb, ml, lane, 0, budget);
            }
            STAMP(1);
            LBAR();
            STAMP(2);
            qk_project(Bq, Bk, ma, xw, idx);
            STAMP(3);
            __builtin_amdgcn_s_setprio(3);
            LBAR();
            STAMP(4);
            bf16x8 Fq[4], Fk[4];
#pragma unroll
            for (int ks = 0; ks < 4; ++ks) Fq[ks] = as8(trd(pq + ks * 1024), trd(pq + ks * 1024 + 128));
            __builtin_amdgcn_sched_barrier(0);
#pragma unroll
            for (int ks = 0; ks < 4; ++ks) Fk[ks] = as8(trd(pq + ks * 1024 + 16), trd(pq + ks * 1024 + 16 + 128));
            __builtin_amdgcn_sched_barrier(0);
            f32x4 S0 = {}, S1 = {}, Sm = {};
            S0 = MFMA16(kx[0][0], Fq[0], S0); S1 = MFMA16(kx[1][0], Fq[0], S1);
            S0 = MFMA16(kx[0][1], Fq[1], S0); S1 = MFMA16(kx[1][1], Fq[1], S1);
            __builtin_amdgcn_sched_barrier(0);
            Sm = MFMA16(Fk[0], Fq[0], Sm); S0 = MFMA16(kx[0][2], Fq[2], S0);
            Sm = MFMA16(Fk[1], Fq[1], Sm); S1 = MFMA16(kx[1][2], Fq[2], S1);
            Sm = MFMA16(Fk[2], Fq[2], Sm); S0 = MFMA16(kx[0][3], Fq[3], S0);
            Sm = MFMA16(Fk[3], Fq[3], Sm); S1 = MFMA16(kx[1][3], Fq[3], S1);
            __builtin_amdgcn_sched_barrier(0);
            float y0 = hi8 ? Sm[2] : Sm[0], y1 = hi8 ? Sm[3] : Sm[1];
            float x0 = hi8 ? S1[0] : S0[0], x1 = hi8 ? S1[1] : S0[1], x2 = hi8 ? S1[2] : S0[2], x3 = hi8 ? S1[3] : S0[3];
            float m = vmax3(vmax3(y0, y1, x0), x1, vmax(x2, x3));
            m = vmax(m, ror8f(m));
            { auto r = __builtin_amdgcn_permlane16_swap(__float_as_uint(m), __float_as_uint(m), false, false); m = vmax(__uint_as_float(r[0]), __uint_as_float(r[1])); }
            { auto r = __builtin_amdgcn_permlane32_swap(__float_as_uint(m), __float_as_uint(m), false, false); m = vmax(__uint_as_float(r[0]), __uint_as_float(r[1])); }
            STAMP(5);
            const float nm = -m * c2;
            x0 = __builtin_amdgcn_exp2f(fmaf(x0, c2, nm)); x1 = __builtin_amdgcn_exp2f(fmaf(x1, c2, nm)); x2 = __builtin_amdgcn_exp2f(fmaf(x2, c2, nm)); x3 = __builtin_amdgcn_exp2f(fmaf(x3, c2, nm));
            y0 = __builtin_amdgcn_exp2f(fmaf(y0, c2, nm)); y1 = __builtin_amdgcn_exp2f(fmaf(y1, c2, nm));
            float sum = fmaf(0.5f, y0 + y1, (x0 + x1) + (x2 + x3));
            sum += ror8f(sum);
            sum = xsum(sum);
            const float inv = __builtin_amdgcn_rcpf(sum);
            const u32 pa = pack2(x0 * inv, x1 * inv) & 0x7fff7fffu, pb = pack2(x2 * inv, x3 * inv) & 0x7fff7fffu, pc = pack2(y0 * inv, y1 * inv) & 0x7fff7fffu;
            const u32 qa = ror8(pa), qb = ror8(pb), qc = ror8(pc);
            v4u px = {hi8 ? qa : pa, hi8 ? qb : pb, hi8 ? pa : qa, hi8 ? pb : qb};
            ST16(px, rw, oxs);
            ST16(px, rw, oxt);
            auto ra = __builtin_amdgcn_permlane16_swap(pc, pc, false, false);
            auto rc = __builtin_amdgcn_permlane16_swap(qc, qc, false, false);
            v4u pmv = {ra[0], rc[0], ra[1], rc[1]};
            ST16(pmv, rw, oms);
            STAMP(6);
            __builtin_amdgcn_sched_barrier(0);
            ST4(nos(rt32()), rw, obs);
            __builtin_amdgcn_s_setprio(0);
            STAMP_FLUSH;
        }
        {
            tk_wait(tkp, NSEG - 1, budget);
            gather_mem(rw, NEARB(NSEG - 1, rep, pm), ml, lane, 0, budget);
            LBAR();
            qk_project(Bq, Bk, ma, xw, idx);
            LBAR();
        }
    } else {
        TK tk; tk_init(tk);
        int kdone = -1;
        const int kxo = OFF_KX + h * (NSEG * 8192) + g * 512 + i16 * 16;
        const int kmo = i16 < 8 ? OFF_KM + h * 2048 + g * 128 + i16 * 16 : OOB;
        for (int s = 0; s < NSEG; ++s) {
            if (w == 5) {
                if (s > 0 && s < NSEG - 1) {
                    if (kdone < s) kdone = wait_flags(rw, FL_K + (h >> 1), 1, 1, s, lane, budget, true);
                    v4u kx[8];
#pragma unroll
                    for (int k = 0; k < 8; ++k) kx[k] = LD16(rw, kxo + (k >> 2) * 256 + (k & 3) * 2048, s * 8192);
                    const v4u ff = {0xFFFFFFFFu, 0xFFFFFFFFu, 0xFFFFFFFFu, 0xFFFFFFFFu};
                    ST16(ff, rw, ox + NEARB(s + 2, i16 >> 3, pm)); ST16(ff, rw, ox + NEARB(s + 2, 2 + (i16 >> 3), pm));
                    ST16(ff, rw, om + NEARB(s + 2, g, pm));
                    ST4(0xFFFFFFFFu, rw, ob + FARB(s + 4, lane & 3, pm));
#pragma unroll
                    for (int k = 0; k < 8; ++k) *(LAS v4u*)(kl + k * 1024) = kx[k];
                    asm volatile("s_waitcnt lgkmcnt(0)" ::: "memory");
                    tk_post(kfp, s, 0u, lane);
                    DRAIN();
                }
            }
            if (s > 0 && w != 5 && w != 6) {
                tk_wait(tkp, s, budget);
                gather_mem(rw, NEARB(s, rep, pm), ml, lane, w, budget);
            }
            BC2 bc = {0u, 0u};
            if (w == 3) bc = beacon_issue(rw, FARB(s, rep, pm), lane);
            LBAR();
            qk_project(Bq, Bk, ma, xw, idx);
            LBAR();
            if (w == 6) {
                bf16x8 F[4];
#pragma unroll
                for (int ks = 0; ks < 4; ++ks) F[ks] = as8(trd(pq + ks * 1024 + 16), trd(pq + ks * 1024 + 16 + 128));
#pragma unroll
                for (int ks = 0; ks < 4; ++ks) ST16(asu(F[ks]), rw, kmo + s * (NH * 2048) + ks * 512);
                DRAIN();
                if (lane == 0) ST4((u32)s, rw, (FL_M + h) * 128);
            } else if (w == 3) {
                if (s < NSEG - 1) {
                    if (s > 0) { tk_T0(tk, beacon_finish(bc, rw, FARB(s, rep, pm), lane, budget), a); if (tk.okA) tk_post(tkp, s + 1, tk_specA(tk, a), lane); }
                    wait_until(tk.XR + 8);
                    tk_Tp(tk, beacon_max<NH>(rw, FARB(s, rep, pm), BC_QK, lane, budget), a);
                    tk_post(tkp, s + 1, tk.XA, lane);
                }
            } else if (w != 5) __builtin_amdgcn_s_sleep(14);
        }
    }
    { const int bid_ = rep + 8 * (h - 8 * rep); (void)bid_; STAMP_FIN; }
    if (budget < 0 && lane == 0) ST4(1u, rw, FL_ERR * 128);
}

__device__ __forceinline__ void role_wvo(const Args& a, lptr lds, rsrc_t rw, int idx) {
    const int tid = threadIdx.x, lane = tid & 63, w = __builtin_amdgcn_readfirstlane(tid >> 6);
    const int h = idx / 3, part = idx - 3 * h;
    bf16x8 Bo[5][4];
    v4u v[6], o[2][9];
#pragma unroll
    for (int c = 0; c < 2; ++c) rows_ld<64, 72>(o[c], a.Wo + ((size_t)h * DH + c * 64) * DM, DM, tid);
    rows_ld<192, 16>(v, a.Wv + (size_t)part * 192 * 4096 + h * DH, 4096, tid);
    __builtin_amdgcn_sched_barrier(0);
#pragma unroll
    for (int c = 0; c < 2; ++c) {
        lptr l = lds + c * 77824;
        rows_st<64, 72>(o[c], l, 1184, tid);
        LBAR();
#pragma unroll
        for (int j = 0; j < 5; ++j) {
            const int r = (w + 8 * j < NR) ? w + 8 * j : w;
#pragma unroll
            for (int ks = 0; ks < 2; ++ks) Bo[j][c * 2 + ks] = frag16(l + ks * 32 * 1184 + r * 32, 1184, lane);
        }
        __builtin_amdgcn_sched_barrier(0);
    }
    rows_st<192, 16>(v, lds, VS, tid);
    LBAR();
    RST(1);
    const int rho = lane & 15, bg = lane >> 4;
    const int wo0 = OFF_WVO + (2 * w + ((rho >> 1) & 1)) * 294912 + ((h >> 1) * 18 + part * 6) * 1024 + (bg * 16 + (h & 1) * 8 + 2 * (rho >> 2) + (rho & 1)) * 16;
#pragma unroll 1
    for (int kb = 0; kb < 6; ++kb) {
        bf16x8 Af[2][4];
#pragma unroll
        for (int mt = 0; mt < 2; ++mt)
#pragma unroll
            for (int ks = 0; ks < 4; ++ks) Af[mt][ks] = lds8(lds + (32 * kb + 8 * (rho >> 2) + 4 * mt + (rho & 3)) * VS + ks * 64 + bg * 16);
#pragma unroll
        for (int j = 0; j < 5; ++j) {
            if (j < 4 || w < 4) {
                f32x4 d0 = {}, d1 = {};
#pragma unroll
                for (int ks = 0; ks < 4; ++ks) { d0 = MFMA16(Af[0][ks], Bo[j][ks], d0); d1 = MFMA16(Af[1][ks], Bo[j][ks], d1); }
                v4u q = {pack2(d0[0], d0[1]), pack2(d0[2], d0[3]), pack2(d1[0], d1[1]), pack2(d1[2], d1[3])};
                ST16(q, rw, wo0 + j * (16 * 294912) + kb * 1024);
            }
        }
        if (kb == 2 || kb == 4) {
            asm volatile("s_waitcnt vmcnt(4)" ::: "memory");
            LBAR();
            if (tid == 0) ST4((u32)(kb >> 1), rw, (FL_W + idx) * 128);
        }
    }
    RST(2);
    DRAIN();
    LBAR();
    RST(3);
    if (tid == 0) ST4(3u, rw, (FL_W + idx) * 128);
}

__device__ __forceinline__ void role_rz(const Args& a, lptr lds, rsrc_t rw, int r, int rep) {
    const int tid = threadIdx.x, lane = tid & 63, w = __builtin_amdgcn_readfirstlane(tid >> 6);
    const int idx = (lane & 8) ? 0xEEEE : 0x4444;
    const int i16 = lane & 15, g = lane >> 4;
    int budget = SPIN_MAX;
    bf16x16 Ba[9], Bb[9];
    {
        const int wo = OFF_WVO + r * 294912 + 2 * w * 18432 + lane * 16;
#pragma unroll
        for (int p = 0; p < 3; ++p) {
            wait_flags(rw, FL_W + 12 * w, 1, 12, p + 1, lane, budget, true);
            if (p == 0) RST(4);
#pragma unroll
            for (int q = 0; q < 3; ++q) {
                const int t = 3 * q + p;
                Ba[t] = cat16(as8(LD16(rw, wo, t * 2048)), as8(LD16(rw, wo + 1024, t * 2048)));
                Bb[t] = cat16(as8(LD16(rw, wo, 18432 + t * 2048)), as8(LD16(rw, wo + 1024, 18432 + t * 2048)));
            }
            MB();
        }
    }
    lptr ml = lds, zl = lds + MEMB;
    lptr tka = lds + 13312, tkr = tka + 16, cntp = tka + 64;
    volatile LAS int* prod = (volatile LAS int*)(lds + 13312 + 512);
    lptr ring = lds + 16384;
    lptr ma = ml + lane * 16;
    lptr zw = zl + ((2 * w + (lane >> 5)) >> 2) * 1024 + ((((2 * w + (lane >> 5)) & 3) * 16 + (g & 1) * 8 + (i16 & 7)) * 16) + (i16 >> 3) * 8;
    const bool hi8r = (lane & 8) != 0;
    const int mso = i16 < 8 ? (r < 64 ? N_MEMA + r * 128 : N_MEMB + (r - 64) * 128) + i16 * 16 : OOB;
    const int ob = lane < NREP ? BCO(r) : OOB;
    const int pm = get_parity(rw, lane, budget);
    if (tid == 0) { tk_post(tka, 0, rt32() - 100000u, 0); tk_post(tkr, -1, 0u, 0); *(volatile LAS int*)cntp = 0; }
    if (tid < 4) prod[tid] = 0;
    stage_rows8(a.mem0, ml, tid);
    __syncthreads();
    RST(5);
    STAMP_INIT
    if (w < 4) {
        const int po = N_PX + (w >> 1) * 16384 + (w & 1) * 4096 + lane * 16;
        const int mo = N_PMM + w * 1024 + 2 * g * 128 + (i16 & 7) * 16 + (i16 >> 3) * 8;
        lptr cna = lane == 0 ? cntp : cntp + 4 + lane * 4;
        const u32 one = lane == 0 ? 1u : 0u;
        const bool dv = (g >> 1) == (i16 >> 3);
        const int rwo = dv ? w * 256 + (g & 1) * 128 + (g >> 1) * 64 + (i16 & 7) * 4 : 2048 + lane * 4;
        lptr zrd = zl + w * 1024 + lane * 16;
        for (int s = 0; s < NSEG; ++s) {
            lptr Rw = lds + 81920 + (s & 1) * 1024 + rwo, Rr = lds + 81920 + (s & 1) * 1024 + (i16 & 7) * 32;
            int mss = mso + NEARB(s + 1, g, pm), obs = ob + FARB(s + 1, lane & 3, pm);
            const int sb = NEARB(s, rep, pm);
            lptr rg = ring + (s & 3) * 16384 + w * 4096 + lane * 16;
            bf16x8 b[5];
            const bool early = __builtin_amdgcn_readfirstlane(prod[w >> 1]) > s;
            MB();
#pragma unroll
            for (int k = 0; k < 4; ++k) b[k] = lds8(rg + k * 1024);
            const u32 X = s > 0 ? tk_get(tkr, min(s, NSEG - 2), budget) : 0u;
            asm volatile("s_waitcnt lgkmcnt(0)" : "+v"(mss), "+v"(obs), "+v"(Rw), "+v"(Rr) :: "memory");
#pragma unroll
            for (int k = 0; k < 4; ++k) PIN4(b[k]);
            LBAR();
            qk_project(Ba, Bb, ma, zw, idx);
            LBAR();
            if (s == NSEG - 1) break;
            __builtin_amdgcn_s_setprio(3);
            b[4] = lds8(zrd);
            if (!early) {
                while (__builtin_amdgcn_readfirstlane(prod[w >> 1]) <= s && --budget >= 0) __builtin_amdgcn_s_sleep(1);
                MB();
#pragma unroll
                for (int k = 0; k < 4; ++k) b[k] = lds8(rg + k * 1024);
            }
            if (s > 0 && (int)(X - rt32()) < 1000) { while ((int)(rt32() - X) < 0) {} }
            STAMP(1);
            f32x4 d;
            for (;;) {
                v4u p[4]; v2u m[2];
                m[0] = LD8(rw, mo, sb); m[1] = LD8(rw, mo + 128, sb);
                __builtin_amdgcn_sched_barrier(0);
#pragma unroll
                for (int k = 0; k < 4; ++k) { p[k] = LD16(rw, po + k * 1024, sb); __builtin_amdgcn_sched_barrier(0); }
                MB();
                d = f32x4{};
                d = MFMA16(as8(m[0], m[1]), b[4], d);
                u32 bad = (m[0].x | m[0].y) | (m[1].x | m[1].y);
                __builtin_amdgcn_sched_barrier(0);
#pragma unroll
                for (int k = 0; k < 4; ++k) {
                    d = MFMA16(as8(p[k]), b[k], d);
                    bad |= (p[k].x | p[k].y) | (p[k].z | p[k].w);
                    __builtin_amdgcn_sched_barrier(0);
                }
                if (__builtin_expect(__all((int)bad >= 0), 1)) break;
                if (--budget < 0) break;
            }
            STAMP(2);
            float e0, e1;
            {
                auto r0 = __builtin_amdgcn_permlane32_swap(__float_as_uint(d[0]), __float_as_uint(d[2]), false, false);
                auto r1 = __builtin_amdgcn_permlane32_swap(__float_as_uint(d[1]), __float_as_uint(d[3]), false, false);
                const float t0 = __uint_as_float(hi8r ? r0[1] : r0[0]), t1 = __uint_as_float(hi8r ? r1[1] : r1[0]);
                e0 = t0 + ror8f(t0); e1 = t1 + ror8f(t1);
            }
            f32x4 x[8]; u32 old;
            asm volatile("ds_write2_b32 %10, %11, %12 offset1:8\n\tds_add_rtn_u32 %0, %13, %14\n\t"
                         "ds_read_b128 %1, %9\n\tds_read_b128 %2, %9 offset:16\n\tds_read_b128 %3, %9 offset:256\n\tds_read_b128 %4, %9 offset:272\n\t"
                         "ds_read_b128 %5, %9 offset:512\n\tds_read_b128 %6, %9 offset:528\n\tds_read_b128 %7, %9 offset:768\n\tds_read_b128 %8, %9 offset:784\n\ts_waitcnt lgkmcnt(0)"
                         : "=&v"(old), "=&v"(x[0]), "=&v"(x[1]), "=&v"(x[2]), "=&v"(x[3]), "=&v"(x[4]), "=&v"(x[5]), "=&v"(x[6]), "=&v"(x[7])
                         : "v"(Rr), "v"(Rw), "v"(e0), "v"(e1), "v"(cna), "v"(one) : "memory");
            STAMP(4);
            if ((__builtin_amdgcn_readfirstlane(old) & 3) == 3) {
                const f32x4 c0 = (x[0] + x[2]) + (x[4] + x[6]), c1 = (x[1] + x[3]) + (x[5] + x[7]);
                v4u o = {nos(pack2(c0[0], c0[1])), nos(pack2(c0[2], c0[3])), nos(pack2(c1[0], c1[1])), nos(pack2(c1[2], c1[3]))};
                ST16(o, rw, mss);
                __builtin_amdgcn_sched_barrier(0);
                ST4(nos(rt32()), rw, obs);
            }
            STAMP(5);
            __builtin_amdgcn_s_setprio(0);
            STAMP_FLUSH;
        }
    } else if (w == 5 || w == 6) {
        int vdone0 = -1, vdone1 = -1, staged = 0;
        const int hw = w - 5;
        const int vo = OFF_VOX + r * 16384 + hw * 8192 + lane * 16;
        const int zho = OFF_ZH + r * 4096 + hw * 2048 + lane * 16;
        lptr zhr = zl + hw * 2048 + (((lane >> 4) & 3) * 16 + (lane & 7)) * 16 + ((lane >> 3) & 1) * 8;
        const int aro = w == 5 ? mso : OOB, arb = w == 5 ? ob : OOB;
        v4u zv[2] = {};
        for (int s = 0; s < NSEG; ++s) {
            bool go = false;
            if (staged <= min(s + 3, NSEG - 2)) {
                const int ow = VOWN(staged);
                int& vd = ow ? vdone1 : vdone0;
                if (vd < staged) vd = wait_flags(rw, FL_V + ow, 2, NH, -1, lane, budget);
                go = vd >= staged;
            }
            const int vos = go ? vo : OOB;
            if (s > 0) {
                tk_wait(tka, s, budget);
                gather_mem4(rw, NEARB(s, rep, pm), ml, lane, w - 4, budget);
            }
            LBAR();
            v4u v[8];
#pragma unroll
            for (int k = 0; k < 8; ++k) v[k] = LD16(rw, vos + (k & 3) * 1024, staged * (NR * NH * 1024) + (k >> 2) * 4096);
            {
                const int zs = s > 0 ? zho + (s - 1) * (NR * NH * 256) : OOB;
                ST16(zv[0], rw, zs); ST16(zv[1], rw, zs + 1024);
                const v4u ff = {0xFFFFFFFFu, 0xFFFFFFFFu, 0xFFFFFFFFu, 0xFFFFFFFFu};
                ST16(ff, rw, aro + NEARB(s + 3, g, pm));
                ST4(0xFFFFFFFFu, rw, arb + FARB(s + 4, lane & 3, pm));
            }
            MB();
            qk_project(Ba, Bb, ma, zw, idx);
            LBAR();
#pragma unroll
            for (int k = 0; k < 2; ++k) {
                const v2u lo = *(LAS v2u*)(zhr + k * 1024), hi = *(LAS v2u*)(zhr + k * 1024 + 128);
                zv[k] = v4u{lo.x, lo.y, hi.x, hi.y};
            }
            for (;;) {
                if (go) {
#pragma unroll
                    for (int k = 0; k < 8; ++k) *(LAS v4u*)(ring + (staged & 3) * 16384 + hw * 8192 + k * 1024 + lane * 16) = v[k];
                    asm volatile("s_waitcnt lgkmcnt(0)" ::: "memory");
                    ++staged;
                    prod[hw] = staged;
                }
                if (staged > s + 1 || staged > NSEG - 2) break;
                const int ow = VOWN(staged);
                int& vd = ow ? vdone1 : vdone0;
                if (vd < staged) vd = wait_flags(rw, FL_V + ow, 2, NH, staged <= s ? staged : -1, lane, budget, true);
                if (vd < staged) break;
#pragma unroll
                for (int k = 0; k < 8; ++k) v[k] = LD16(rw, vo + (k & 3) * 1024, staged * (NR * NH * 1024) + (k >> 2) * 4096);
                go = true;
            }
            DRAIN();
        }
        ST16(zv[0], rw, zho + (NSEG - 1) * (NR * NH * 256)); ST16(zv[1], rw, zho + (NSEG - 1) * (NR * NH * 256) + 1024);
    } else {
        TK tk; tk_init(tk);
        for (int s = 0; s < NSEG; ++s) {
            if (s > 0) {
                tk_wait(tka, s, budget);
                gather_mem4(rw, NEARB(s, rep, pm), ml, lane, w - 4, budget);
            }
            BC2 bc = {0u, 0u};
            if (w == 7) bc = beacon_issue(rw, FARB(s, rep, pm), lane);
            LBAR();
            qk_project(Ba, Bb, ma, zw, idx);
            LBAR();
            if (w == 7) {
                if (s < NSEG - 1) {
                    if (s > 0) { tk_T0(tk, beacon_finish(bc, rw, FARB(s, rep, pm), lane, budget), a); if (tk.okA) tk_post(tka, s + 1, tk_specA(tk, a), lane); }
                    tk_post(tkr, s, tk.XR, lane);
                    wait_until(tk.XR + 8);
                    tk_Tp(tk, beacon_max<NH>(rw, FARB(s, rep, pm), BC_QK, lane, budget), a);
                    tk_post(tka, s + 1, tk.XA, lane);
                    tk_post(tkr, s + 1, tk_specR(tk, a), lane);
                }
            } else {
                if (w == 4 && s >= 2 && lane == 0) ST4((u32)(s - 2), rw, (FL_Z + r) * 128);
                __builtin_amdgcn_s_sleep(14);
            }
        }
    }
    DRAIN();
    LBAR();
    if (tid == 0) ST4((u32)(NSEG - 1), rw, (FL_Z + r) * 128);
    { const int bid_ = rep + 8 * (8 + r - 18 * rep); (void)bid_; STAMP_FIN; }
    if (budget < 0 && lane == 0) ST4(3u, rw, FL_ERR * 128);
}

__device__ __forceinline__ void cexp_l(float lr, float li, float l, float& er, float& ei) {
    const float mag = __builtin_amdgcn_exp2f(lr * l);
    const float ph = li * l;
    const float pe = fmaf(li, l, -ph);
    const float fr = (ph - floorf(ph)) + pe;
    er = mag * __builtin_amdgcn_cosf(fr);
    ei = mag * __builtin_amdgcn_sinf(fr);
}
constexpr int CV_W = 8 * 260 * 16, CV_U = 96 * 80, CV_CH = CV_W + CV_U;
__device__ __forceinline__ void s4_unit(const Args& a, lptr lds, rsrc_t rw, int unit) {
    const int tid = threadIdx.x, lane = tid & 63, w = __builtin_amdgcn_readfirstlane(tid >> 6);
    const int d0 = unit * 4;
    v2u uv[4];
#pragma unroll
    for (int k = 0; k < 4; ++k) uv[k] = *(const v2u*)(a.u + (size_t)(2 * (tid + 512 * (k >> 1)) + (k & 1)) * DM + d0);
    {
        const int c = tid >> 7, nn = tid & 127, n = (d0 + c) * 128 + nn;
        const float dt = rbf(expf(bf2f(a.logdt[d0 + c])));
        const float lr = dt * bf2f(a.Are[n]) * 1.4426950408889634f, li = dt * bf2f(a.Aim[n]) * 0.15915494309189535f;
        const float br = dt * bf2f(a.Bre[n]), bi = dt * bf2f(a.Bim[n]);
        const float cr = bf2f(a.Cre[n]), ci = bf2f(a.Cim[n]);
        LAS f32x2* T = (LAS f32x2*)(lds + c * CV_CH) + nn;
        float er, ei, pr, pi;
        cexp_l(lr, li, 1.0f, er, ei); pr = er; pi = ei;
#pragma unroll
        for (int o = 0; o < 7; ++o) { T[o * 130] = f32x2{pr, pi}; const float t = pr * er - pi * ei; pi = pr * ei + pi * er; pr = t; }
#pragma unroll
        for (int q4 = 0; q4 < 4; ++q4) {
            if (q4 == 0) cexp_l(lr, li, 128.0f, er, ei);
            cexp_l(lr, li, (float)(128 + 512 * q4), pr, pi);
#pragma unroll
            for (int o = 0; o < 4; ++o) { T[(7 + 4 * q4 + o) * 130] = f32x2{pr, pi}; const float t = pr * er - pi * ei; pi = pr * ei + pi * er; pr = t; }
        }
        const float cbr = cr * br - ci * bi, cbi = cr * bi + ci * br;
        cexp_l(lr, li, 8.0f, er, ei);
#pragma unroll
        for (int q4 = 0; q4 < 4; ++q4) {
            float sr, si;
            cexp_l(lr, li, (float)(32 * q4), sr, si);
            pr = cbr * sr - cbi * si; pi = cbr * si + cbi * sr;
#pragma unroll
            for (int o = 0; o < 4; ++o) { T[(23 + 4 * q4 + o) * 130] = f32x2{pr, pi}; const float t = pr * er - pi * ei; pi = pr * ei + pi * er; pr = t; }
        }
    }
    __syncthreads();
    f32x4 kr[3];
    {
        const int x = lane & 15, g = lane >> 4, c = w & 3, nh = w >> 2;
        const int ah0 = x >> 3, o0 = x & 7;
        const int ah1 = x < 8 ? 2 + (x >> 2) : 4 + ((x - 8) >> 1), o1 = x < 8 ? 2 * (x & 3) : 4 * (x & 1);
        const int ah2 = x < 8 ? 8 + x : 16;
        LAS f32x2* T = (LAS f32x2*)(lds + c * CV_CH) + nh * 64 + g;
        LAS f32x2* Ta0 = T + 7 * 130; LAS f32x2* To0 = T + (o0 ? o0 - 1 : 0) * 130;
        LAS f32x2* Ta1 = T + (6 + ah1) * 130; LAS f32x2* To1 = T + (o1 ? o1 - 1 : 0) * 130;
        LAS f32x2* Ta2 = T + (6 + ah2) * 130; LAS f32x2* Tv = T + (23 + x) * 130;
        const f32x2 one = {1.0f, 0.0f};
        f32x4 ar[3] = {}, ai[3] = {};
#pragma unroll
        for (int jj = 0; jj < 16; ++jj) {
            const f32x2 v = Tv[4 * jj], u2 = Ta2[4 * jj], a1 = Ta1[4 * jj];
            f32x2 a0v = Ta0[4 * jj], e0 = To0[4 * jj], e1 = To1[4 * jj];
            a0v = ah0 ? a0v : one; e0 = o0 ? e0 : one; e1 = o1 ? e1 : one;
            const float u0r = a0v[0] * e0[0] - a0v[1] * e0[1], u0i = a0v[0] * e0[1] + a0v[1] * e0[0];
            const float u1r = a1[0] * e1[0] - a1[1] * e1[1], u1i = a1[0] * e1[1] + a1[1] * e1[0];
            ar[2] = __builtin_amdgcn_mfma_f32_16x16x4f32(u2[0], v[0], ar[2], 0, 0, 0);
            ai[2] = __builtin_amdgcn_mfma_f32_16x16x4f32(u2[1], v[1], ai[2], 0, 0, 0);
            ar[0] = __builtin_amdgcn_mfma_f32_16x16x4f32(u0r, v[0], ar[0], 0, 0, 0);
            ai[0] = __builtin_amdgcn_mfma_f32_16x16x4f32(u0i, v[1], ai[0], 0, 0, 0);
            ar[1] = __builtin_amdgcn_mfma_f32_16x16x4f32(u1r, v[0], ar[1], 0, 0, 0);
            ai[1] = __builtin_amdgcn_mfma_f32_16x16x4f32(u1i, v[1], ai[1], 0, 0, 0);
        }
#pragma unroll
        for (int t = 0; t < 3; ++t) kr[t] = ar[t] - ai[t];
    }
    __syncthreads();
    {
        LAS float* T = (LAS float*)(lds + (w & 3) * CV_CH + 4096 + (w >> 2) * 3072);
#pragma unroll
        for (int t = 0; t < 3; ++t)
#pragma unroll
            for (int r = 0; r < 4; ++r) T[(16 * t + 4 * (lane >> 4) + r) * 16 + (lane & 15)] = kr[t][r];
    }
    __syncthreads();
#pragma unroll
    for (int k = 0; k < 8; ++k) {
        const int i = tid + 512 * k, c = i >> 10, l2 = i & 1023;
        LAS float* T = (LAS float*)(lds + c * CV_CH + 4096);
        float v[2];
#pragma unroll
        for (int e = 0; e < 2; ++e) {
            const int lb = (int)rbf((float)(2 * l2 + e));
            const int hi = lb >> 7, o = lb & 7;
            const int row = lb < 256 ? 8 * hi + o : lb < 512 ? 16 + 4 * (hi - 2) + (o >> 1) : lb < 1024 ? 24 + 2 * (hi - 4) + (o >> 2) : 32 + (hi - 8);
            const int ix = row * 16 + ((lb >> 3) & 15);
            v[e] = T[ix] + T[768 + ix];
        }
        *(LAS u32*)(lds + c * CV_CH + CV_W + 2704 + l2 * 4) = pack2(v[0], v[1]);
    }
    __syncthreads();
    for (int i = tid; i < 4 * 259; i += 512) {
        const int c = i / 259, zz = i - c * 259, z = zz - 3;
        lptr ch = lds + c * CV_CH;
        v4u lo = {0, 0, 0, 0}, hi = {0, 0, 0, 0};
        if (z >= 1) lo = *(LAS v4u*)(ch + CV_W + 2704 + (z - 1) * 16);
        if (z >= 0) hi = *(LAS v4u*)(ch + CV_W + 2704 + z * 16);
        const u32 e[8] = {lo.x, lo.y, lo.z, lo.w, hi.x, hi.y, hi.z, hi.w};
#pragma unroll
        for (int cc = 0; cc < 8; ++cc) {
            v4u o;
#pragma unroll
            for (int k = 0; k < 4; ++k) {
                const int t = 8 + cc - 2 * k;
                if (t & 1) o[k] = (e[t >> 1] >> 16) | (e[t >> 1] << 16);
                else o[k] = (e[t >> 1] & 0xffffu) | (e[(t >> 1) - 1] & 0xffff0000u);
            }
            *(LAS v4u*)(ch + (cc * 260 + zz) * 16) = o;
        }
    }
    __syncthreads();
    for (int i = tid; i < 4 * 160; i += 512) *(LAS v4u*)(lds + (i / 160) * CV_CH + CV_W + (i % 160) * 16) = v4u{0, 0, 0, 0};
#pragma unroll
    for (int k2 = 0; k2 < 2; ++k2) {
        const int l = 2 * (tid + 512 * k2);
        const int o = CV_W + (32 + (l >> 5)) * 80 + (l & 31) * 2;
        const v2u p = uv[2 * k2], q = uv[2 * k2 + 1];
        *(LAS u32*)(lds + 0 * CV_CH + o) = (p.x & 0xffffu) | (q.x << 16);
        *(LAS u32*)(lds + 1 * CV_CH + o) = (p.x >> 16) | (q.x & 0xffff0000u);
        *(LAS u32*)(lds + 2 * CV_CH + o) = (p.y & 0xffffu) | (q.y << 16);
        *(LAS u32*)(lds + 3 * CV_CH + o) = (p.y >> 16) | (q.y & 0xffff0000u);
    }
    __syncthreads();
    const int c = w & 3, par = w >> 2, b = lane & 31, hh = lane >> 5;
    lptr ch = lds + c * CV_CH;
    lptr wa = ch + ((b & 7) * 260 + (b >> 3) - hh + 3) * 16;
    lptr ub = ch + CV_W + (32 + b) * 80 + hh * 16;
    f32x16 y0 = {}, y1 = {};
    wa += par * 64; ub -= par * 80;
    {
        bf16x8 fa[2][2], fb[2][2], fc[2][2];
#pragma unroll
        for (int ks = 0; ks < 2; ++ks) { fa[0][ks] = lds8(wa - ks * 32); fb[0][ks] = lds8(ub + ks * 32); fc[0][ks] = lds8(ub + 2560 + ks * 32); }
#pragma unroll
        for (int i = 0; i < 32; ++i) {
            const int cur = i & 1, nx = cur ^ 1;
            if (i + 1 < 32) {
#pragma unroll
                for (int ks = 0; ks < 2; ++ks) {
                    fa[nx][ks] = lds8(wa + (i + 1) * 128 - ks * 32);
                    if (i + 1 < 16) fb[nx][ks] = lds8(ub - (i + 1) * 160 + ks * 32);
                    fc[nx][ks] = lds8(ub + 2560 - (i + 1) * 160 + ks * 32);
                }
            }
            __builtin_amdgcn_sched_barrier(0);
#pragma unroll
            for (int ks = 0; ks < 2; ++ks) {
                if (i < 16) y0 = MFMA32(fa[cur][ks], fb[cur][ks], y0);
                y1 = MFMA32(fa[cur][ks], fc[cur][ks], y1);
            }
            __builtin_amdgcn_sched_barrier(0);
        }
    }
    __syncthreads();
    {
        LAS f32x4* X = (LAS f32x4*)(ch + par * 4096);
        const f32x16 src = par ? y0 : y1;
#pragma unroll
        for (int k = 0; k < 4; ++k) X[k * 64 + lane] = f32x4{src[4 * k], src[4 * k + 1], src[4 * k + 2], src[4 * k + 3]};
    }
    __syncthreads();
    {
        LAS f32x4* X = (LAS f32x4*)(ch + (1 - par) * 4096);
        lptr Y = ch + 8192 + (32 * par + b) * 72 + hh * 8;
        const f32x16 own = par ? y1 : y0;
#pragma unroll
        for (int k = 0; k < 4; ++k) {
            const f32x4 o = X[k * 64 + lane];
            v2u yv = {pack2(own[4 * k] + o[0], own[4 * k + 1] + o[1]), pack2(own[4 * k + 2] + o[2], own[4 * k + 3] + o[3])};
            *(LAS v2u*)(Y + k * 16) = yv;
        }
    }
    __syncthreads();
    const v2u dk = *(const v2u*)(a.dskip + d0);
#pragma unroll
    for (int k2 = 0; k2 < 2; ++k2) {
        const int i = tid + 512 * k2;
        lptr yp = lds + 8192 + (i >> 4) * 72 + (i & 15) * 4;
        const u32 ya = *(LAS u32*)(yp), yb = *(LAS u32*)(yp + CV_CH), yc = *(LAS u32*)(yp + 2 * CV_CH), yd = *(LAS u32*)(yp + 3 * CV_CH);
        const v2u p = uv[2 * k2], q = uv[2 * k2 + 1];
        v4u o;
        o.x = pack2(bflo(ya) + rbf(bflo(p.x) * bflo(dk.x)), bflo(yb) + rbf(bfhi(p.x) * bfhi(dk.x)));
        o.y = pack2(bflo(yc) + rbf(bflo(p.y) * bflo(dk.y)), bflo(yd) + rbf(bfhi(p.y) * bfhi(dk.y)));
        o.z = pack2(bfhi(ya) + rbf(bflo(q.x) * bflo(dk.x)), bfhi(yb) + rbf(bfhi(q.x) * bfhi(dk.x)));
        o.w = pack2(bfhi(yc) + rbf(bflo(q.y) * bflo(dk.y)), bfhi(yd) + rbf(bfhi(q.y) * bfhi(dk.y)));
        ST16(o, rw, OFF_A4 + unit * 16384 + i * 16);
    }
    __syncthreads();
}
__device__ __forceinline__ void role_s4(const Args& a, lptr lds, rsrc_t rw, int idx) {
    if (idx < 32) {
        const int tid = threadIdx.x, t = idx * 64 + (tid >> 3), j = tid & 7;
        float acc = 0.f;
#pragma unroll
        for (int k = 0; k < 9; ++k) {
            const v4u xv = *(const v4u*)(a.u + (size_t)t * DM + (k * 8 + j) * 8), gv = *(const v4u*)(a.gatew + (k * 8 + j) * 8);
#pragma unroll
            for (int e = 0; e < 4; ++e) acc += rbf(bflo(xv[e]) * bflo(gv[e])) + rbf(bfhi(xv[e]) * bfhi(gv[e]));
        }
        acc += __shfl_xor(acc, 1); acc += __shfl_xor(acc, 2); acc += __shfl_xor(acc, 4);
        acc = rbf(acc);
        if (j == 0) ST4(__float_as_uint(rbf(1.0f / (1.0f + __expf(-acc)))), rw, OFF_G + t * 4);
    }
#pragma unroll 1
    for (int unit = idx; unit < DM / 4; unit += NS4) s4_unit(a, lds, rw, unit);
    DRAIN();
    __syncthreads();
    if (threadIdx.x == 0) ST4(1u, rw, (FL_S + idx) * 128);
}

__device__ __forceinline__ void role_post(const Args& a, lptr lds, rsrc_t rw) {
    const int tid = threadIdx.x, lane = tid & 63, w = __builtin_amdgcn_readfirstlane(tid >> 6);
    const int i16 = lane & 15, g = lane >> 4;
    int budget = SPIN_MAX, vdone0 = -1, vdone1 = -1;
    volatile LAS int* U = (volatile LAS int*)(lds + 81920);
    wait_flags(rw, FL_S, 1, 64, 1, lane, budget);
    wait_flags(rw, FL_S + 64, 1, NS4 - 64, 1, lane, budget);
    constexpr int NFULL = 62 * 6, NUNIT = NFULL + 24;
#pragma unroll 1
    for (;;) {
        if (tid == 0) *U = 1 + (int)__hip_atomic_fetch_add((u32*)(a.ws + FL_Q * 128), 1u, __ATOMIC_RELAXED, __HIP_MEMORY_SCOPE_AGENT);
        __syncthreads();
        const int unit = __builtin_amdgcn_readfirstlane(*U);
        if (unit >= NUNIT || budget < 0) break;
        const bool half = unit >= NFULL;
        const int uu = half ? NFULL + ((unit - NFULL) >> 1) : unit, q0 = half ? (unit - NFULL) & 1 : 0;
        const int s = uu / 6, r = (uu - s * 6) * 6 + (w < 6 ? w : 0);
        bf16x8 b[32];
        v2u av[2]; float gt[2];
        if (w < 6) {
            if (VOWN(s)) { if (vdone1 < s) vdone1 = wait_flags(rw, FL_V + 1, 2, NH, s, lane, budget); }
            else { if (vdone0 < s) vdone0 = wait_flags(rw, FL_V, 2, NH, s, lane, budget); }
            const int vo = OFF_VOX + (s * NR + r) * (NH * 1024) + ((i16 >> 1) & 1) * 16384 + ((g & 1) * 16 + (g >> 1) * 8 + 2 * (i16 >> 2) + (i16 & 1)) * 16;
#pragma unroll
            for (int k = 0; k < 32; ++k) b[k] = as8(LD16(rw, vo + (k & 7) * 512, (k >> 3) * 4096));
#pragma unroll
            for (int qt = 0; qt < 2; ++qt) {
                av[qt] = LD8(rw, OFF_A4 + (4 * r + g) * 16384 + (32 * s + 16 * (qt ^ q0) + i16) * 8, 0);
                gt[qt] = __uint_as_float(LD4(rw, OFF_G + (32 * s + 16 * (qt ^ q0) + i16) * 4, 0));
            }
        }
        v4u z[8];
        const int zo = OFF_ZH + (s * NR + r) * (NH * 256) + ((i16 >> 1) & 1) * 4096 + g * 128 + (2 * (i16 >> 2) + (i16 & 1)) * 16;
        if (half) {
            wait_flags(rw, FL_Z + 2 * r, 1, 2, s, lane, budget, true);
#pragma unroll
            for (int k = 0; k < 8; ++k) z[k] = LD16(rw, zo + k * 512, 0);
            wait_flags(rw, FL_P + (s & 3), 4, 16, s, lane, budget, true);
            v4u pv[5];
#pragma unroll
            for (int k = 0; k < 5; ++k) pv[k] = LD16(rw, OFF_PX + s * 81920 + q0 * 40960 + tid * 16, k * 8192);
#pragma unroll
            for (int k = 0; k < 5; ++k) *(LAS v4u*)(lds + k * 8192 + tid * 16) = pv[k];
        } else {
            wait_flags(rw, FL_P + (s & 3), 4, 16, s, lane, budget);
            v4u pv[10];
#pragma unroll
            for (int k = 0; k < 10; ++k) pv[k] = LD16(rw, OFF_PX + s * 81920 + tid * 16, k * 8192);
            wait_flags(rw, FL_Z + 2 * r, 1, 2, s, lane, budget);
#pragma unroll
            for (int k = 0; k < 8; ++k) z[k] = LD16(rw, zo + k * 512, 0);
            MB();
#pragma unroll
            for (int k = 0; k < 10; ++k) *(LAS v4u*)(lds + k * 8192 + tid * 16) = pv[k];
        }
        __syncthreads();
        if (w < 6) {
#pragma unroll 1
            for (int qt = 0; qt < (half ? 1 : 2); ++qt) {
                f32x4 d = {}, e = {};
                lptr pl = lds + qt * 40960 + lane * 16;
                bf16x8 f[6];
#pragma unroll
                for (int i = 0; i < 6; ++i) f[i] = lds8(pl + i * 1024);
                __builtin_amdgcn_sched_barrier(0);
#pragma unroll
                for (int k = 0; k < 40; ++k) {
                    const bf16x8 v = k < 32 ? b[k < 32 ? k : 0] : as8(z[k < 32 ? 0 : k - 32]);
                    if (k & 1) e = MFMA16(v, f[k % 6], e); else d = MFMA16(v, f[k % 6], d);
                    if (k + 6 < 40) f[k % 6] = lds8(pl + (k + 6) * 1024);
                    __builtin_amdgcn_sched_barrier(0);
                }
                d += e;
                const float gq = qt ? gt[1] : gt[0], og = rbf(1.0f - gq);
                const v2u aq = qt ? av[1] : av[0];
                const float r0 = rbf(gq * bflo(aq.x)) + rbf(og * rbf(d[0])), r1 = rbf(gq * bfhi(aq.x)) + rbf(og * rbf(d[1]));
                const float r2 = rbf(gq * bflo(aq.y)) + rbf(og * rbf(d[2])), r3 = rbf(gq * bfhi(aq.y)) + rbf(og * rbf(d[3]));
                v2u ov = {pack2(r0, r1), pack2(r2, r3)};
                *(v2u*)(a.out + (size_t)(32 * s + 16 * (qt ^ q0) + i16) * DM + 16 * r + 4 * g) = ov;
            }
        }
        if (half) break;
        LBAR();
    }
    if (budget < 0 && lane == 0) ST4(6u, rw, FL_ERR * 128);
}

__global__ void __launch_bounds__(512, 2) k_all(Args a) {
    extern __shared__ __attribute__((aligned(16))) char smem[];
    lptr lds = (lptr)smem;
    const rsrc_t rw = __builtin_amdgcn_make_buffer_rsrc(a.ws, 0, WS_BYTES, 0x00020000);
    RST(0);
    if (threadIdx.x == 0) {
        const int x = (int)(__builtin_amdgcn_s_getreg(20 | (3 << 11)) & 7u);
        int role = 0;
        for (int k = 0; k < 8; ++k) {
            const int gq = (x + k) & 7;
            const int t = 1 + (int)__hip_atomic_fetch_add((u32*)(a.ws + (FL_T + gq) * 128), 1u, __ATOMIC_RELAXED, __HIP_MEMORY_SCOPE_AGENT);
            if (t < 32) { role = gq + 8 * t; break; }
        }
        *(volatile LAS int*)lds = role;
    }
    __syncthreads();
    const int b = __builtin_amdgcn_readfirstlane(*(volatile LAS int*)lds), xg = b & 7, slot = b >> 3;
    __syncthreads();
    WSTAMP(516);

    if (b == 8 * 8) role_probe(rw);
    if (slot >= 20) { role_wvo(a, lds, rw, (xg & 3) * 24 + (xg < 4 ? 12 : 0) + slot - 20); __syncthreads(); }
    if ((xg < 4 && slot >= 26) || (xg >= 4 && slot >= 20)) {
        const int idx = xg < 4 ? 48 + (slot - 26) * 4 + xg : (31 - slot) * 4 + (xg - 4);
        role_s4(a, lds, rw, idx);
        __syncthreads();
    }
    bool post = false;
    if (xg < 4) {
        if (slot < 8) role_qk(a, lds, rw, xg * 8 + slot, xg);
        else if (slot < 26) role_rz(a, lds, rw, xg * 18 + slot - 8, xg);
    } else {
        const int j = xg - 4;
        post = true;
        if (slot < 16) { role_vvo(a, lds, rw, j * 8 + (slot >> 1), slot & 1); post = slot < a.mask; }
        else if (slot < 20) role_k2(a, lds, rw, j * 4 + slot - 16);
        else if (slot < 28) { role_q2p(a, lds, rw, j * 4 + ((slot - 20) >> 1), slot & 1); post = false; }
    }
    WSTAMP(517);
    if (post) { __syncthreads(); role_post(a, lds, rw); }
    WSTAMP(518);
}

__global__ void __launch_bounds__(512) k_init(char* ws) {
    const v4u ff = {0xFFFFFFFFu, 0xFFFFFFFFu, 0xFFFFFFFFu, 0xFFFFFFFFu};
    *(v4u*)(ws + ((size_t)blockIdx.x * 512 + threadIdx.x) * 16) = ff;
}
extern "C" void kernel_launch(void* const* d_in, const int* in_sizes, int n_in, void* d_out, int out_size, void* d_ws, size_t ws_size, hipStream_t stream) {
    Args a;
    a.u = (const u16*)d_in[0]; a.Are = (const u16*)d_in[1]; a.Aim = (const u16*)d_in[2]; a.Bre = (const u16*)d_in[3]; a.Bim = (const u16*)d_in[4];
    a.Cre = (const u16*)d_in[5]; a.Cim = (const u16*)d_in[6]; a.logdt = (const u16*)d_in[7]; a.dskip = (const u16*)d_in[8];
    a.mem0 = (const u16*)d_in[10]; a.Wq = (const u16*)d_in[11]; a.Wk = (const u16*)d_in[12]; a.Wv = (const u16*)d_in[13]; a.Wo = (const u16*)d_in[14];
    a.gatew = (const u16*)d_in[16]; a.out = (u16*)d_out; a.ws = (char*)d_ws;
    a.pd = 10; a.pha = 8; a.phz = 0; a.gap = 30; a.mask = 4; a.pad0 = 8;
    const int LDS_ALL = 160 * 1024;
    (void)hipFuncSetAttribute((const void*)k_all, hipFuncAttributeMaxDynamicSharedMemorySize, LDS_ALL);
    hipLaunchKernelGGL(k_init, dim3(INIT_BYTES / 8192), dim3(512), 0, stream, (char*)d_ws);
    hipLaunchKernelGGL(k_all, dim3(256), dim3(512), LDS_ALL, stream, a);
}
```

```cpp
#include <hip/hip_runtime.h>
#include <hip/hip_bf16.h>
#include <stdint.h>
#include <stdio.h>
#include <stdlib.h>

typedef unsigned short u16;
typedef unsigned int u32;
typedef u32 v4u __attribute__((ext_vector_type(4)));
typedef u32 v2u __attribute__((ext_vector_type(2)));
typedef float f32x4 __attribute__((ext_vector_type(4)));
typedef float f32x2 __attribute__((ext_vector_type(2)));
typedef float f32x16 __attribute__((ext_vector_type(16)));
typedef __bf16 bf16x2 __attribute__((ext_vector_type(2)));
typedef __bf16 bf16x8 __attribute__((ext_vector_type(8)));
typedef __bf16 bf16x16 __attribute__((ext_vector_type(16)));
typedef short s16x4 __attribute__((ext_vector_type(4)));
#define LAS __attribute__((address_space(3)))
typedef LAS char* lptr;
typedef __amdgpu_buffer_rsrc_t rsrc_t;

constexpr int SEQ = 2048, DM = 576, NH = 32, DH = 128, NSEG = 64, NR = 36, NR8 = 72, NREP = 4;
constexpr int MEMB = 9216, BC_QK = 80;
constexpr int FL_K = 0, FL_V = 16, FL_M = 80, FL_P = 112, FL_S = 176, FL_Q = 254, FL_ERR = 255, FL_Z = 256, FL_W = 328, FL_T = 424, FL_PAR = 432, NS4 = 72;
constexpr int OFF_RING = 65536;
constexpr int N_MEMA = 0, N_PX = 16384, N_PMM = 49152, N_MEMB = 49152 + 4096, N_PROBE = 49152 + 6144;
constexpr int INIT_BYTES = OFF_RING + 16 * 65536;
constexpr int OFF_ZH = INIT_BYTES;
constexpr int OFF_KX = OFF_ZH + NSEG * NR * NH * 256;
constexpr int OFF_VOX = OFF_KX + NH * SEQ * DH * 2;
constexpr int OFF_KM = OFF_VOX + NSEG * NR * NH * 1024;
constexpr int OFF_PX = OFF_KM + NSEG * NH * 2048;
constexpr int OFF_A4 = OFF_PX + NSEG * 81920;
constexpr int OFF_G = OFF_A4 + SEQ * DM * 2;
constexpr int OFF_WVO = OFF_G + SEQ * 4;
constexpr int OFF_ST = OFF_WVO + NR8 * 294912;
constexpr int WS_BYTES = OFF_ST + 256 * 520 * 8 + 4096;

#define BIDX bid_
#define RST(k)
#define STAMP(k)
#define STAMP_FLUSH
#define STAMP_INIT
#define STAMP_FIN
#define WSTAMP(k)

struct Args {
    const u16* u; const u16* Are; const u16* Aim; const u16* Bre; const u16* Bim; const u16* Cre; const u16* Cim;
    const u16* logdt; const u16* dskip; const u16* mem0; const u16* Wq; const u16* Wk; const u16* Wv; const u16* Wo;
    const u16* gatew; u16* out; char* ws;
    int pd, pha, phz, gap, mask, pad0;
};

__device__ __forceinline__ float bf2f(u16 v) { return __uint_as_float((u32)v << 16); }
__device__ __forceinline__ float bflo(u32 v) { return __uint_as_float(v << 16); }
__device__ __forceinline__ float bfhi(u32 v) { return __uint_as_float(v & 0xffff0000u); }
__device__ __forceinline__ u32 pack2(float lo, float hi) { f32x2 f = {lo, hi}; bf16x2 b = __builtin_convertvector(f, bf16x2); return __builtin_bit_cast(u32, b); }
__device__ __forceinline__ float rbf(float x) { return bflo(pack2(x, x)); }
__device__ __forceinline__ u32 nos(u32 v) { return min(v, 0xFFFFFFFEu); }
__device__ __forceinline__ v2u trd(lptr p) { return __builtin_bit_cast(v2u, __builtin_amdgcn_ds_read_tr16_b64_v4i16((LAS s16x4*)p)); }
__device__ __forceinline__ bf16x8 as8(v4u v) { return __builtin_bit_cast(bf16x8, v); }
__device__ __forceinline__ bf16x8 as8(v2u a, v2u b) { v4u v = {a.x, a.y, b.x, b.y}; return __builtin_bit_cast(bf16x8, v); }
__device__ __forceinline__ v4u asu(bf16x8 v) { return __builtin_bit_cast(v4u, v); }
__device__ __forceinline__ bf16x16 cat16(bf16x8 a, bf16x8 b) { return __builtin_shufflevector(a, b, 0, 1, 2, 3, 4, 5, 6, 7, 8, 9, 10, 11, 12, 13, 14, 15); }
__device__ __forceinline__ bf16x8 lds8(lptr p) { return *(LAS bf16x8*)p; }
__device__ __forceinline__ u32 bad4(v4u v) { return max(max(v.x, v.y), max(v.z, v.w)); }
#define NEARB(s, rep, pm) (OFF_RING + ((((s) & 3) * 4 + (rep)) << 16) + ((((pm) >> (((s) & 3) * 4 + (rep))) & 1) << 13))
#define FARB(s, rep, pm) ((NEARB(s, rep, pm) ^ 8192) + (((s) & 4) << 13))
#define BCO(slot) (((slot) & 63) * 128 + ((slot) >> 6) * 16384)
#define LBAR() do { asm volatile("s_waitcnt lgkmcnt(0)" ::: "memory"); __builtin_amdgcn_s_barrier(); asm volatile("" ::: "memory"); } while (0)
#define MB() asm volatile("" ::: "memory")
#define DRAIN() asm volatile("s_waitcnt vmcnt(0)" ::: "memory")
#define PIN4(x) asm volatile("" : "+v"(x))
#define MFMA16(a, b, c) __builtin_amdgcn_mfma_f32_16x16x32_bf16((a), (b), (c), 0, 0, 0)
#define MFMA32(a, b, c) __builtin_amdgcn_mfma_f32_32x32x16_bf16((a), (b), (c), 0, 0, 0)
#define SMF(a, b, c, idx) __builtin_amdgcn_smfmac_f32_16x16x64_bf16((a), (b), (c), (idx), 0, 0)
#define LD16(r, vo, so) __builtin_amdgcn_raw_buffer_load_b128((r), (vo), (so), 16)
#define LD8(r, vo, so) __builtin_amdgcn_raw_buffer_load_b64((r), (vo), (so), 16)
#define LD4(r, vo, so) __builtin_amdgcn_raw_buffer_load_b32((r), (vo), (so), 16)
#define ST16(v, r, vo) __builtin_amdgcn_raw_buffer_store_b128((v), (r), (vo), 0, 16)
#define ST8(v, r, vo) __builtin_amdgcn_raw_buffer_store_b64((v), (r), (vo), 0, 16)
#define ST4(v, r, vo) __builtin_amdgcn_raw_buffer_store_b32((v), (r), (vo), 0, 16)

__device__ __forceinline__ float swap32_sum(float a, float b) {
    auto r = __builtin_amdgcn_permlane32_swap(__float_as_uint(a), __float_as_uint(b), false, false);
    return __uint_as_float(r[0]) + __uint_as_float(r[1]); }
__device__ __forceinline__ float xmax(float m) {
    auto r = __builtin_amdgcn_permlane16_swap(__float_as_uint(m), __float_as_uint(m), false, false);
    m = fmaxf(__uint_as_float(r[0]), __uint_as_float(r[1]));
    auto q = __builtin_amdgcn_permlane32_swap(__float_as_uint(m), __float_as_uint(m), false, false);
    return fmaxf(__uint_as_float(q[0]), __uint_as_float(q[1])); }
__device__ __forceinline__ float xsum(float m) {
    auto r = __builtin_amdgcn_permlane16_swap(__float_as_uint(m), __float_as_uint(m), false, false);
    m = __uint_as_float(r[0]) + __uint_as_float(r[1]);
    auto q = __builtin_amdgcn_permlane32_swap(__float_as_uint(m), __float_as_uint(m), false, false);
    return __uint_as_float(q[0]) + __uint_as_float(q[1]); }
__device__ __forceinline__ float vmax(float a, float b) { float r; asm("v_max_f32 %0, %1, %2" : "=v"(r) : "v"(a), "v"(b)); return r; }
__device__ __forceinline__ float vmax3(float a, float b, float c) { float r; asm("v_max3_f32 %0, %1, %2, %3" : "=v"(r) : "v"(a), "v"(b), "v"(c)); return r; }
__device__ __forceinline__ u32 ror8(u32 v) { return (u32)__builtin_amdgcn_update_dpp(0, (int)v, 0x128, 0xf, 0xf, true); }
__device__ __forceinline__ float ror8f(float v) { return __uint_as_float(ror8(__float_as_uint(v))); }
__device__ __forceinline__ int wave_max(int d) {
    d = max(d, __builtin_amdgcn_update_dpp(d, d, 0x128, 0xf, 0xf, false));
    d = max(d, __builtin_amdgcn_update_dpp(d, d, 0x124, 0xf, 0xf, false));
    d = max(d, __builtin_amdgcn_update_dpp(d, d, 0x122, 0xf, 0xf, false));
    d = max(d, __builtin_amdgcn_update_dpp(d, d, 0x121, 0xf, 0xf, false));
    return max(max(__builtin_amdgcn_readlane(d, 0), __builtin_amdgcn_readlane(d, 16)), max(__builtin_amdgcn_readlane(d, 32), __builtin_amdgcn_readlane(d, 48)));
}

constexpr int SPIN_MAX = 2000000;
__device__ __forceinline__ int wait_flags(rsrc_t rw, int line0, int stride, int n, int need, int lane, int& budget, bool fast = false) {
    const int vo = (line0 + (lane < n ? lane : 0) * stride) * 128;
    int mn;
    for (;;) {
        const int v = (int)LD4(rw, vo, 0);
        MB();
        mn = -wave_max(-v);
        if (mn >= need) break;
        if (--budget < 0) break;
        if (fast) __builtin_amdgcn_s_sleep(2); else __builtin_amdgcn_s_sleep(12);
    }
    return mn;
}

template <int NRW, int CW>
__device__ __forceinline__ void stage_rows(const u16* g, int gs, lptr lds, int ls, int tid) {
    constexpr int N = NRW * CW / 512;
    static_assert(NRW * CW % 512 == 0, "");
    v4u v[N];
#pragma unroll
    for (int j = 0; j < N; ++j) { const int i = tid + 512 * j, row = i / CW, c = i % CW; v[j] = *(const v4u*)(g + (size_t)row * gs + c * 8); }
#pragma unroll
    for (int j = 0; j < N; ++j) { const int i = tid + 512 * j, row = i / CW, c = i % CW; *(LAS v4u*)(lds + row * ls + c * 16) = v[j]; }
}
template <int NRW, int CW>
__device__ __forceinline__ void rows_ld(v4u (&v)[NRW * CW / 512], const u16* g, int gs, int tid) {
#pragma unroll
    for (int j = 0; j < NRW * CW / 512; ++j) { const int i = tid + 512 * j, row = i / CW, c = i % CW; v[j] = *(const v4u*)(g + (size_t)row * gs + c * 8); }
}
template <int NRW, int CW>
__device__ __forceinline__ void rows_st(const v4u (&v)[NRW * CW / 512], lptr lds, int ls, int tid) {
#pragma unroll
    for (int j = 0; j < NRW * CW / 512; ++j) { const int i = tid + 512 * j, row = i / CW, c = i % CW; *(LAS v4u*)(lds + row * ls + c * 16) = v[j]; }
}
__device__ __forceinline__ bf16x8 frag16(lptr base, int ls, int lane) {
    const int i16 = lane & 15, bg = lane >> 4, q = i16 >> 2, p = i16 & 3;
    lptr a = base + (8 * bg + q) * ls + p * 8;
    return as8(trd(a), trd(a + 4 * ls));
}
__device__ __forceinline__ bf16x8 frag32p(lptr base, int ls, int lane) {
    const int i16 = lane & 15, G = lane >> 4, q = i16 >> 2, p = i16 & 3;
    lptr a = base + (8 * (G >> 1) + q) * ls + (16 * (p & 1) + 8 * (G & 1) + 4 * (p >> 1)) * 2;
    return as8(trd(a), trd(a + 4 * ls));
}

constexpr int XS = 1168;
__device__ __forceinline__ void xload(v4u (&st)[9], const u16* x, int tok0, int tid) {
#pragma unroll
    for (int j = 0; j < 9; ++j) st[j] = *(const v4u*)(x + (size_t)tok0 * DM + (tid + 512 * j) * 8);
}
__device__ __forceinline__ void xstore(const v4u (&st)[9], lptr xl, int tid) {
#pragma unroll
    for (int j = 0; j < 9; ++j) { const int i = tid + 512 * j; *(LAS v4u*)(xl + i * 16 + (i / 72) * 16) = st[j]; }
}
__device__ __forceinline__ void load_w2(bf16x8 (&A)[36], const u16* W, int h0, lptr lds, int tid, int lane, int w) {
    v4u v[6][6];
#pragma unroll
    for (int c = 0; c < 6; ++c) rows_ld<96, 32>(v[c], W + (size_t)c * 96 * 4096 + h0 * DH, 4096, tid);
    __builtin_amdgcn_sched_barrier(0);
#pragma unroll
    for (int c = 0; c < 6; ++c) {
        lptr l = lds + (c & 1) * (96 * 544);
        rows_st<96, 32>(v[c], l, 544, tid);
        LBAR();
#pragma unroll
        for (int ks = 0; ks < 6; ++ks) A[c * 6 + ks] = frag32p(l + ks * 16 * 544 + w * 64, 544, lane);
        __builtin_amdgcn_sched_barrier(0);
    }
    LBAR();
}

__device__ __forceinline__ f32x16 proj_tile(const bf16x8 (&A)[36], lptr xb) {
    f32x16 acc = {};
    bf16x8 f[6];
#pragma unroll
    for (int i = 0; i < 6; ++i) f[i] = lds8(xb + i * 32);
    __builtin_amdgcn_sched_barrier(0);
#pragma unroll
    for (int ks = 0; ks < 36; ++ks) {
        acc = MFMA32(A[ks], f[ks % 6], acc);
        if (ks + 6 < 36) f[ks % 6] = lds8(xb + (ks + 6) * 32);
        __builtin_amdgcn_sched_barrier(0);
    }
    return acc;
}
__device__ __forceinline__ void role_k2(const Args& a, lptr lds, rsrc_t rw, int cu) {
    const int tid = threadIdx.x, lane = tid & 63, w = __builtin_amdgcn_readfirstlane(tid >> 6);
    v4u st[9];
    bf16x8 A[36];
    xload(st, a.u, 0, tid);
    load_w2(A, a.Wk, 2 * cu, lds, tid, lane, w);
    RST(4);
    xstore(st, lds, tid);
    __syncthreads();
    const int o0 = OFF_KX + (2 * cu + (w >> 2)) * (NSEG * 8192) + ((w & 3) * 4 + (lane >> 5) * 2) * 512 + (lane & 31) * 16;
    for (int blk = 0; blk < 32; ++blk) {
        lptr xl = lds + (blk & 1) * (64 * XS);
        if (blk + 1 < 32) xload(st, a.u, (blk + 1) * 64, tid);
#pragma unroll
        for (int tt = 0; tt < 2; ++tt) {
            const f32x16 acc = proj_tile(A, xl + (32 * tt + (lane & 31)) * XS + (lane >> 5) * 16);
            v4u c0 = {pack2(acc[0], acc[1]), pack2(acc[2], acc[3]), pack2(acc[4], acc[5]), pack2(acc[6], acc[7])};
            v4u c1 = {pack2(acc[8], acc[9]), pack2(acc[10], acc[11]), pack2(acc[12], acc[13]), pack2(acc[14], acc[15])};
            const int o = o0 + (blk * 2 + tt) * 8192;
            ST16(c0, rw, o); ST16(c1, rw, o + 512);
            if (blk == 0 && tt == 0) { DRAIN(); LBAR(); if (tid == 0) ST4(0u, rw, (FL_K + cu) * 128); RST(5); }
        }
        if (blk + 1 < 32) xstore(st, lds + ((blk + 1) & 1) * (64 * XS), tid);
        const bool full = blk < 4 || blk == 31;
        if (full) DRAIN(); else asm volatile("s_waitcnt vmcnt(4)" ::: "memory");
        LBAR();
        if (tid == 0) ST4((u32)(full ? 2 * blk + 1 : 2 * blk - 1), rw, (FL_K + cu) * 128);
    }
}

constexpr int VS = 272;
__device__ __forceinline__ void role_q2p(const Args& a, lptr lds, rsrc_t rw, int cu, int par) {
    const int tid = threadIdx.x, lane = tid & 63, w = __builtin_amdgcn_readfirstlane(tid >> 6);
    v4u st[9];
    bf16x8 A[36];
    load_w2(A, a.Wq, 2 * cu, lds, tid, lane, w);
    lptr xl = lds, ql = lds + 64 * XS;
    LAS int* cnt = (LAS int*)(lds + 64 * XS + 4 * 64 * VS);
    if (tid < 2) cnt[tid] = 0;
    xload(st, a.u, par * 64, tid);
    xstore(st, xl, tid);
    __syncthreads();
    int budget = SPIN_MAX, kdone = -1;
    const int i16 = lane & 15, g = lane >> 4;
    const int sg = w >> 2, hh = (w >> 1) & 1, qt = w & 1, h = 2 * cu + hh;
    const float c2 = 0.08838834764831845f * 1.4426950408889634f;
    for (int it = -1; it < 16; ++it) {
        const int blk = 2 * it + par;
        if (it + 1 < 16) {
            if (it + 2 < 16) xload(st, a.u, (blk + 4) * 64, tid);
            lptr qb = ql + (((it + 1) & 1) * 2 + (w >> 2)) * (64 * VS) + ((w & 3) * 32 + (lane >> 5) * 16) * 2;
#pragma unroll
            for (int tt = 0; tt < 2; ++tt) {
                const f32x16 acc = proj_tile(A, xl + (32 * tt + (lane & 31)) * XS + (lane >> 5) * 16);
                v4u c0 = {pack2(acc[0], acc[1]), pack2(acc[2], acc[3]), pack2(acc[4], acc[5]), pack2(acc[6], acc[7])};
                v4u c1 = {pack2(acc[8], acc[9]), pack2(acc[10], acc[11]), pack2(acc[12], acc[13]), pack2(acc[14], acc[15])};
                lptr qp = qb + (32 * tt + (lane & 31)) * VS;
                *(LAS v4u*)qp = c0; *(LAS v4u*)(qp + 16) = c1;
            }
            __syncthreads();
            if (it + 2 < 16) xstore(st, xl, tid);
        }
        if (it >= 0) {
            const int s = 2 * blk + sg;
            if (kdone < s) kdone = wait_flags(rw, FL_K + cu, 1, 1, s, lane, budget);
            bf16x8 k0[4], k1[4], km[4], qf[4];
            const int ko = OFF_KX + (h * NSEG + s) * 8192 + g * 512 + i16 * 16;
#pragma unroll
            for (int ks = 0; ks < 4; ++ks) { k0[ks] = as8(LD16(rw, ko, ks * 2048)); k1[ks] = as8(LD16(rw, ko + 256, ks * 2048)); }
            lptr qp = ql + ((it & 1) * 2 + hh) * (64 * VS) + (32 * sg + 16 * qt + i16) * VS + g * 16;
#pragma unroll
            for (int ks = 0; ks < 4; ++ks) qf[ks] = lds8(qp + ks * 64);
            f32x4 S0 = {}, S1 = {}, Sm = {};
#pragma unroll
            for (int ks = 0; ks < 4; ++ks) { S0 = MFMA16(k0[ks], qf[ks], S0); S1 = MFMA16(k1[ks], qf[ks], S1); }
            wait_flags(rw, FL_M + h, 1, 1, s, lane, budget, s >= 62);
            const int mo = OFF_KM + (s * NH + h) * 2048 + g * 128 + (i16 & 7) * 16;
#pragma unroll
            for (int ks = 0; ks < 4; ++ks) km[ks] = as8(LD16(rw, mo + ks * 512, 0));
#pragma unroll
            for (int ks = 0; ks < 4; ++ks) Sm = MFMA16(km[ks], qf[ks], Sm);
            const int qi = 16 * qt + i16, t0 = 4 * g;
#pragma unroll
            for (int r = 0; r < 4; ++r) {
                if (t0 + r > qi) S0[r] = -INFINITY;
                if (t0 + r + 16 > qi) S1[r] = -INFINITY;
                if (g >= 2) Sm[r] = -INFINITY;
            }
            float m = fmaxf(fmaxf(fmaxf(S0[0], S0[1]), fmaxf(S0[2], S0[3])), fmaxf(fmaxf(S1[0], S1[1]), fmaxf(S1[2], S1[3])));
            m = fmaxf(m, fmaxf(fmaxf(Sm[0], Sm[1]), fmaxf(Sm[2], Sm[3])));
            m = xmax(m);
            const float nm = -m * c2;
            float sum = 0.f;
#pragma unroll
            for (int r = 0; r < 4; ++r) {
                S0[r] = __builtin_amdgcn_exp2f(fmaf(S0[r], c2, nm)); S1[r] = __builtin_amdgcn_exp2f(fmaf(S1[r], c2, nm)); Sm[r] = __builtin_amdgcn_exp2f(fmaf(Sm[r], c2, nm));
                sum += S0[r] + S1[r] + Sm[r];
            }
            sum = xsum(sum);
            const float inv = __builtin_amdgcn_rcpf(sum);
            v4u px = {pack2(S0[0] * inv, S0[1] * inv), pack2(S0[2] * inv, S0[3] * inv), pack2(S1[0] * inv, S1[1] * inv), pack2(S1[2] * inv, S1[3] * inv)};
            const int po = OFF_PX + (s * 2 + qt) * 40960;
            ST16(px, rw, po + h * 1024 + lane * 16);
            const u32 ma = pack2(Sm[0] * inv, Sm[1] * inv), mb = pack2(Sm[2] * inv, Sm[3] * inv);
            auto ra = __builtin_amdgcn_permlane16_swap(ma, ma, false, false);
            auto rb = __builtin_amdgcn_permlane16_swap(mb, mb, false, false);
            v4u pm = {ra[0], rb[0], ra[1], rb[1]};
            if (lane < 16) ST16(pm, rw, po + 32768 + h * 256 + lane * 16);
            DRAIN();
            if (lane == 0) {
                const int old = __hip_atomic_fetch_add(cnt + sg, 1, __ATOMIC_RELAXED, __HIP_MEMORY_SCOPE_WORKGROUP);
                if ((old & 3) == 3) ST4((u32)s, rw, (FL_P + 4 * cu + 2 * par + sg) * 128);
            }
        }
        __syncthreads();
    }
    if (budget < 0 && lane == 0) ST4(5u, rw, FL_ERR * 128);
}

__device__ __forceinline__ void role_vvo(const Args& a, lptr lds, rsrc_t rw, int h, int par) {
    const int tid = threadIdx.x, lane = tid & 63, w = __builtin_amdgcn_readfirstlane(tid >> 6);
    v4u st[9];
#pragma unroll
    for (int j = 0; j < 9; ++j) { const int i = tid + 512 * j; st[j] = *(const v4u*)(a.u + (size_t)(par * 32 + (i >= 2304 ? 32 : 0)) * DM + i * 8); }
    bf16x8 Av[18], Bo[5][4];
    {
        v4u v[3][6], o[2][9];
#pragma unroll
        for (int c = 0; c < 3; ++c) rows_ld<192, 16>(v[c], a.Wv + (size_t)c * 192 * 4096 + h * DH, 4096, tid);
#pragma unroll
        for (int c = 0; c < 2; ++c) rows_ld<64, 72>(o[c], a.Wo + ((size_t)h * DH + c * 64) * DM, DM, tid);
        __builtin_amdgcn_sched_barrier(0);
#pragma unroll
        for (int c = 0; c < 3; ++c) {
            lptr l = lds + (c & 1) * 77824;
            rows_st<192, 16>(v[c], l, 288, tid);
            LBAR();
#pragma unroll
            for (int ks = 0; ks < 6; ++ks) Av[c * 6 + ks] = frag16(l + ks * 32 * 288 + w * 32, 288, lane);
            __builtin_amdgcn_sched_barrier(0);
        }
#pragma unroll
        for (int c = 0; c < 2; ++c) {
            lptr l = lds + ((c + 1) & 1) * 77824;
            rows_st<64, 72>(o[c], l, 1184, tid);
            LBAR();
#pragma unroll
            for (int j = 0; j < 5; ++j) {
                const int r = (w + 8 * j < NR) ? w + 8 * j : w;
#pragma unroll
                for (int ks = 0; ks < 2; ++ks) Bo[j][c * 2 + ks] = frag16(l + ks * 32 * 1184 + r * 32, 1184, lane);
            }
            __builtin_amdgcn_sched_barrier(0);
        }
        LBAR();
    }
    RST(4);
    lptr xl = lds, vl = lds + 64 * XS;
    xstore(st, xl, tid);
    __syncthreads();
    const int rho = lane & 15, bg = lane >> 4;
    const int vo0 = OFF_VOX + ((2 * w + ((rho >> 1) & 1)) * 16 + (h >> 1)) * 1024 + ((2 * (h & 1) + (bg & 1)) * 16 + (bg >> 1) * 8 + 2 * (rho >> 2) + (rho & 1)) * 16;
    const int fo = (FL_V + 2 * h + par) * 128;
    auto stage1 = [&](int tt) {
        f32x4 acc = {};
        lptr xb = xl + (16 * tt + rho) * XS + bg * 16;
        bf16x8 f[6];
#pragma unroll
        for (int i = 0; i < 6; ++i) f[i] = lds8(xb + i * 64);
        __builtin_amdgcn_sched_barrier(0);
#pragma unroll
        for (int ks = 0; ks < 18; ++ks) {
            acc = MFMA16(Av[ks], f[ks % 6], acc);
            if (ks + 6 < 18) f[ks % 6] = lds8(xb + (ks + 6) * 64);
            __builtin_amdgcn_sched_barrier(0);
        }
        v2u o = {pack2(acc[0], acc[1]), pack2(acc[2], acc[3])};
        *(LAS v2u*)(vl + (16 * tt + rho) * VS + (16 * w + 4 * bg) * 2) = o;
    };
    auto stage2 = [&](int sg, int s) {
        bf16x8 Af[2][4];
#pragma unroll
        for (int half = 0; half < 2; ++half)
#pragma unroll
            for (int ks = 0; ks < 4; ++ks) Af[half][ks] = lds8(vl + (32 * sg + rho + 16 * half) * VS + ks * 64 + bg * 16);
#pragma unroll
        for (int j = 0; j < 5; ++j) {
            if (j < 4 || w < 4) {
                f32x4 d0 = {}, d1 = {};
#pragma unroll
                for (int ks = 0; ks < 4; ++ks) { d0 = MFMA16(Af[0][ks], Bo[j][ks], d0); d1 = MFMA16(Af[1][ks], Bo[j][ks], d1); }
                v4u o = {pack2(d0[0], d0[1]), pack2(d0[2], d0[3]), pack2(d1[0], d1[1]), pack2(d1[2], d1[3])};
                ST16(o, rw, vo0 + (s * NR + 8 * j) * (NH * 1024));
            }
        }
    };
    int prev = -1;
#pragma unroll 1
    for (int un = 0; un < 32; ++un) {
        const int sg = un & 1, blk = (un >> 1) * 2 + par;
        const int s = un < 2 ? par + 2 * un : 2 * blk + sg;
        if (sg == 0 && blk + 2 < 32) xload(st, a.u, (blk + 2) * 64, tid);
        stage1(2 * sg); stage1(2 * sg + 1);
        if (sg == 1) DRAIN(); else if (blk + 2 < 32) asm volatile("s_waitcnt vmcnt(9)" ::: "memory"); else DRAIN();
        LBAR();
        if (tid == 0 && prev >= 0) ST4((u32)prev, rw, fo);
        if (sg == 1 && blk + 2 < 32) xstore(st, xl, tid);
        stage2(sg, s);
        prev = s;
        if (un == 0 || un == 31) { DRAIN(); LBAR(); if (tid == 0) ST4((u32)s, rw, fo); if (un == 0) RST(5); }
        else if (sg == 1) LBAR();
    }
}
#define VOWN(s) ((s) < 4 ? ((s) & 1) : (((s) >> 1) & 1))

__device__ __forceinline__ u32 rt32() { return (u32)__builtin_amdgcn_s_memrealtime(); }
__device__ __forceinline__ void wait_until(u32 t) {
    if ((int)(t - rt32()) > 1000) return;
    while ((int)(rt32() - t) < -24) __builtin_amdgcn_s_sleep(4);
    while ((int)(rt32() - t) < 0) {}
}
struct TK { u32 XA, XR, EA, ER; int dA0, dA1, dA2, dR0, dR1, dR2; int okA, okR, gap; };
__device__ __forceinline__ int tk_est(int d0, int d1, int d2) { return max(min(d0, d1), min(max(d0, d1), d2)); }
template <int N>
__device__ __forceinline__ u32 beacon_max(rsrc_t rw, int fb, int slot0, int lane, int& budget) {
    const int vo = BCO(slot0 + (lane < N ? lane : 0)), v2 = BCO(64 + (lane & 7));
    u32 v, u = 0;
    for (;;) {
        v = LD4(rw, vo, fb);
        if (N > 64) u = LD4(rw, v2, fb);
        MB();
        if (__all(max(v, u) != 0xFFFFFFFFu)) break;
        if (--budget < 0) break;
        __builtin_amdgcn_s_sleep(1);
    }
    const u32 base = __builtin_amdgcn_readfirstlane(v);
    int d = (int)(v - base);
    if (N > 64) d = max(d, (int)(u - base));
    return base + (u32)wave_max(d);
}
struct BC2 { u32 v, u; };
__device__ __forceinline__ BC2 beacon_issue(rsrc_t rw, int fb, int lane) {
    BC2 b;
    b.v = LD4(rw, BCO(lane), fb);
    b.u = LD4(rw, BCO(64 + (lane & 7)), fb);
    MB();
    return b;
}
__device__ __forceinline__ u32 beacon_finish(BC2 b, rsrc_t rw, int fb, int lane, int& budget) {
    if (!__all(max(b.v, b.u) != 0xFFFFFFFFu)) return beacon_max<NR8>(rw, fb, 0, lane, budget);
    const u32 base = __builtin_amdgcn_readfirstlane(b.v);
    return base + (u32)wave_max(max((int)(b.v - base), (int)(b.u - base)));
}
__device__ __forceinline__ void tk_T0(TK& k, u32 T0, const Args& a) {
    if (k.okR) { k.dR2 = k.dR1; k.dR1 = k.dR0; k.dR0 = (int)(T0 - k.ER); }
    k.okA = (int)(k.XA - T0) >= a.pad0 - a.pha;
    if (!k.okR) k.gap = k.okA ? max(a.gap, k.gap - 6) : min(k.gap + 16, 160);
    k.EA = k.XA;
    k.XR = k.EA + tk_est(k.dA0, k.dA1, k.dA2) + a.pd;
}
__device__ __forceinline__ void tk_Tp(TK& k, u32 Tp, const Args& a) {
    const int okn = (int)(k.XR - Tp) >= a.pd - a.pha;
    if (k.okA && (k.okR || !okn)) { k.dA2 = k.dA1; k.dA1 = k.dA0; k.dA0 = (int)(Tp - k.EA); }
    k.okR = okn;
    k.ER = k.okR ? k.XR : Tp + k.gap;
    k.XA = k.ER + tk_est(k.dR0, k.dR1, k.dR2) + a.pad0;
}
__device__ __forceinline__ u32 tk_specA(const TK& k, const Args& a) { return k.XR + tk_est(k.dR0, k.dR1, k.dR2) + a.pad0; }
__device__ __forceinline__ u32 tk_specR(const TK& k, const Args& a) { return k.XA + tk_est(k.dA0, k.dA1, k.dA2) + a.pd; }
__device__ __forceinline__ void tk_init(TK& k) { const u32 n = rt32(); k.XA = k.XR = k.EA = k.ER = n - 100000u; k.dA0 = k.dA1 = k.dA2 = 130; k.dR0 = k.dR1 = k.dR2 = 70; k.okA = k.okR = 0; k.gap = 50; }

__device__ __forceinline__ void tk_post(lptr p, int tag, u32 t, int lane) { if (lane == 0) { v2u v = {(u32)tag, t}; *(volatile LAS v2u*)p = v; } }
__device__ __forceinline__ u32 tk_get(lptr p, int tag, int& budget) {
    for (;;) {
        const v2u v = *(volatile LAS v2u*)p;
        if ((int)__builtin_amdgcn_readfirstlane(v.x) >= tag || --budget < 0) return __builtin_amdgcn_readfirstlane(v.y);
        __builtin_amdgcn_s_sleep(2);
    }
}
__device__ __forceinline__ void tk_wait(lptr p, int tag, int& budget) {
    u32 X;
    for (;;) {
        const v2u v = *(volatile LAS v2u*)p;
        const u32 now = rt32();
        X = __builtin_amdgcn_readfirstlane(v.y);
        const int tg = (int)__builtin_amdgcn_readfirstlane(v.x), dt = (int)(X - now);
        if (tg >= tag) {
            if (dt <= 0 || dt > 1000 || tg > tag) return;
            if (dt <= 28) break;
        }
        if (--budget < 0) return;
        __builtin_amdgcn_s_sleep(1);
    }
    while ((int)(rt32() - X) < 0) {}
}
constexpr int OOB = 0x70000000;
__device__ __forceinline__ void stage_rows8(const u16* src, lptr ml, int tid) {
    for (int c = tid; c < 576; c += 512) {
        const u32* p = (const u32*)(src + (c & 7) * DM + 16 * (c >> 4) + 2 * ((c >> 3) & 1));
        v4u v = {p[0], p[2], p[4], p[6]};
        *(LAS v4u*)(ml + c * 16) = v;
    }
}
__device__ __forceinline__ void gather_mem(rsrc_t rw, int nb, lptr ml, int lane, int w, lptr tkp, int tag, int& budget) {
    const int gi = w == 7 ? 5 : w;
    const int l0 = gi * 1536 + lane * 16, l1 = l0 + 1024;
    const int o0 = l0 < 8192 ? l0 : l0 + (N_MEMB - 8192), o1 = lane < 32 ? (l1 < 8192 ? l1 : l1 + (N_MEMB - 8192)) : OOB;
    lptr p1 = ml + (lane < 32 ? l1 : 14336 + lane * 16);
    asm volatile("" : "+s"(nb));
    tk_wait(tkp, tag, budget);
    v4u v0 = LD16(rw, o0, nb), v1 = LD16(rw, o1, nb);
    MB();
    *(LAS v4u*)(ml + l0) = v0; *(LAS v4u*)p1 = v1;
    MB();
    if (__builtin_expect(!__all(max(bad4(v0), bad4(v1)) != 0xFFFFFFFFu), 0)) {
        do {
            v0 = LD16(rw, o0, nb); v1 = LD16(rw, o1, nb);
            MB();
        } while (!__all(max(bad4(v0), bad4(v1)) != 0xFFFFFFFFu) && --budget >= 0);
        *(LAS v4u*)(ml + l0) = v0; *(LAS v4u*)p1 = v1;
    }
}
__device__ __forceinline__ void gather_mem4(rsrc_t rw, int nb, lptr ml, int lane, int gi, lptr tkp, int tag, int& budget) {
    const int l0 = gi * 2048 + lane * 16;
    const int o2 = lane < 16 ? N_MEMB + gi * 256 + lane * 16 : OOB;
    lptr p2 = ml + (lane < 16 ? 8192 + gi * 256 + lane * 16 : 14336 + lane * 16);
    asm volatile("" : "+s"(nb));
    tk_wait(tkp, tag, budget);
    v4u v0 = LD16(rw, l0, nb), v1 = LD16(rw, l0 + 1024, nb), v2 = LD16(rw, o2, nb);
    MB();
    *(LAS v4u*)(ml + l0) = v0; *(LAS v4u*)(ml + l0 + 1024) = v1; *(LAS v4u*)p2 = v2;
    MB();
    if (__builtin_expect(!__all(max(max(bad4(v0), bad4(v1)), bad4(v2)) != 0xFFFFFFFFu), 0)) {
        do {
            v0 = LD16(rw, l0, nb); v1 = LD16(rw, l0 + 1024, nb); v2 = LD16(rw, o2, nb);
            MB();
        } while (!__all(max(max(bad4(v0), bad4(v1)), bad4(v2)) != 0xFFFFFFFFu) && --budget >= 0);
        *(LAS v4u*)(ml + l0) = v0; *(LAS v4u*)(ml + l0 + 1024) = v1; *(LAS v4u*)p2 = v2;
    }
}
__device__ __forceinline__ int get_parity(rsrc_t rw, int lane, int& budget) {
    const int vo = (FL_PAR + (lane & 7)) * 128;
    u32 v;
    for (;;) {
        v = LD4(rw, vo, 0);
        MB();
        if (__all(v != 0xFFFFFFFFu) || --budget < 0) break;
        __builtin_amdgcn_s_sleep(8);
    }
    int pm = 0;
#pragma unroll
    for (int k = 0; k < 8; ++k) pm |= (__builtin_amdgcn_readlane((int)v, k) & 3) << (2 * k);
    return __builtin_popcount(pm) > 8 ? 0xFFFF : 0;
}
__device__ __forceinline__ void role_probe(rsrc_t rw) {
    const int lane = threadIdx.x & 63, w = __builtin_amdgcn_readfirstlane(threadIdx.x >> 6);
    u32 bits = 0;
    for (int gg = 0; gg < 2; ++gg) {
        int best[2] = {1 << 30, 1 << 30};
        for (int rep = 0; rep < 3; ++rep) {
#pragma unroll
            for (int p = 0; p < 2; ++p) {
                const int o = OFF_RING + (2 * w + gg) * 65536 + N_PROBE + p * 8192 + rep * 128;
                ST4(0xFFFFFFFFu, rw, lane == 0 ? o : OOB);
                DRAIN();
                const unsigned long long t0 = __builtin_amdgcn_s_memtime();
                asm volatile("s_waitcnt lgkmcnt(0)" ::: "memory");
                u32 v = LD4(rw, o, 0);
                asm volatile("s_waitcnt vmcnt(0)" : "+v"(v) :: "memory");
                const unsigned long long t1 = __builtin_amdgcn_s_memtime();
                best[p] = min(best[p], (int)(t1 - t0));
            }
        }
        bits |= (best[1] < best[0] ? 1u : 0u) << gg;
    }
    if (lane == 0) ST4(bits, rw, (FL_PAR + w) * 128);
}
template <typename F>
__device__ __forceinline__ void qk_project_h(const bf16x16 (&Bq)[9], const bf16x16 (&Bk)[9], lptr ma, lptr xw, int idx, F hook) {
    f32x4 aq = {}, ak = {};
    bf16x8 A[9];
#pragma unroll
    for (int t = 0; t < 9; ++t) A[t] = lds8(ma + t * 1024);
    __builtin_amdgcn_sched_barrier(0);
#pragma unroll
    for (int t = 0; t < 9; ++t) {
        aq = SMF(A[t], Bq[t], aq, idx); ak = SMF(A[t], Bk[t], ak, idx);
        hook(t);
        __builtin_amdgcn_sched_barrier(0);
    }
    const float s0 = swap32_sum(aq[0], ak[0]), s1 = swap32_sum(aq[1], ak[1]), s2 = swap32_sum(aq[2], ak[2]), s3 = swap32_sum(aq[3], ak[3]);
    v2u o = {pack2(s0, s1), pack2(s2, s3)};
    *(LAS v2u*)xw = o;
}
__device__ __forceinline__ void qk_project(const bf16x16 (&Bq)[9], const bf16x16 (&Bk)[9], lptr ma, lptr xw, int idx) {
    qk_project_h(Bq, Bk, ma, xw, idx, [](int) {});
}

__device__ __forceinline__ void role_qk(const Args& a, lptr lds, rsrc_t rw, int h, int rep) {
    const int tid = threadIdx.x, lane = tid & 63, w = __builtin_amdgcn_readfirstlane(tid >> 6);
    bf16x16 Bq[9], Bk[9];
    {
        v4u v[6][6];
#pragma unroll
        for (int c = 0; c < 6; ++c) rows_ld<192, 16>(v[c], (c >= 3 ? a.Wk : a.Wq) + (size_t)(c % 3) * 192 * 4096 + h * DH, 4096, tid);
        __builtin_amdgcn_sched_barrier(0);
#pragma unroll
        for (int c = 0; c < 6; ++c) {
            lptr l = lds + (c & 1) * 77824;
            rows_st<192, 16>(v[c], l, 288, tid);
            LBAR();
#pragma unroll
            for (int t = 0; t < 3; ++t) {
                bf16x16 f = cat16(frag16(l + (64 * t) * 288 + w * 32, 288, lane), frag16(l + (64 * t + 32) * 288 + w * 32, 288, lane));
                if (c >= 3) Bk[(c % 3) * 3 + t] = f; else Bq[c * 3 + t] = f;
            }
            __builtin_amdgcn_sched_barrier(0);
        }
        LBAR();
    }
    lptr ml = lds, xl = lds + MEMB;
    const int idx = (lane & 8) ? 0xEEEE : 0x4444;
    const int i16 = lane & 15, g = lane >> 4;
    const bool hi8 = (lane & 8) != 0;
    int budget = SPIN_MAX;
    lptr pq = xl + (8 * g + (i16 >> 2)) * 32 + (i16 & 1) * 8;
    lptr kl = lds + 16384 + lane * 16;
    lptr ma = ml + lane * 16, xw = xl + (16 * w + i16) * 32 + g * 8;
    const int ox = N_PX + (h >> 4) * 16384 + (h & 15) * 512 + ((g & 1) * 16 + (g >> 1) * 8 + (i16 & 7)) * 16;
    const int om = hi8 ? OOB : N_PMM + h * 128 + (i16 & 7) * 16;
    const int ob = lane < NREP ? BCO(BC_QK + h) : OOB;
    const int pm = get_parity(rw, lane, budget);
    lptr tkp = lds + MEMB + 4096, kfp = tkp + 64;
    if (tid == 0) { tk_post(tkp, 0, rt32() - 100000u, 0); tk_post(kfp, 0, 0u, 0); }
    stage_rows8(a.mem0, ml, tid);
#pragma unroll
    for (int G = 0; G < 4; ++G) stage_rows8(a.u + G * 8 * DM, lds + 32768 + G * MEMB, tid);
    __syncthreads();
#pragma unroll 1
    for (int G = 0; G < 4; ++G) {
        qk_project(Bq, Bk, lds + 32768 + G * MEMB + lane * 16, xw, idx);
        LBAR();
        if (w < 4) {
            const bf16x8 F = as8(trd(pq + w * 1024 + 16), trd(pq + w * 1024 + 16 + 128));
            if ((i16 >> 3) == (G & 1)) *(LAS bf16x8*)(kl + ((G >> 1) * 4 + w) * 1024) = F;
        }
        LBAR();
    }
    RST(5);
    STAMP_INIT
    if (w == 0) {
        const float c2 = 0.08838834764831845f * 1.4426950408889634f;
        for (int s = 0; s < NSEG - 1; ++s) {
            tk_get(kfp, s, budget);
            bf16x8 kx[2][4];
#pragma unroll
            for (int tl = 0; tl < 2; ++tl)
#pragma unroll
                for (int ks = 0; ks < 4; ++ks) kx[tl][ks] = lds8(kl + (s & 1) * 8192 + (tl * 4 + ks) * 1024);
            int oxs = ox + NEARB(s, i16 >> 3, pm), oxt = ox + NEARB(s, 2 + (i16 >> 3), pm), oms = om + NEARB(s, g, pm), obs = ob + FARB(s, lane & 3, pm);
            const int nb = NEARB(s, rep, pm);
            asm volatile("s_waitcnt lgkmcnt(0)" : "+v"(oxs), "+v"(oxt), "+v"(oms), "+v"(obs) :: "memory");
#pragma unroll
            for (int tl = 0; tl < 2; ++tl)
#pragma unroll
                for (int ks = 0; ks < 4; ++ks) PIN4(kx[tl][ks]);
            if (s > 0) {
                gather_mem(rw, nb, ml, lane, 0, tkp, s, budget);
            }
            STAMP(1);
            LBAR();
            STAMP(2);
            qk_project(Bq, Bk, ma, xw, idx);
            STAMP(3);
            __builtin_amdgcn_s_setprio(3);
            LBAR();
            STAMP(4);
            bf16x8 Fq[4], Fk[4];
#pragma unroll
            for (int ks = 0; ks < 4; ++ks) Fq[ks] = as8(trd(pq + ks * 1024), trd(pq + ks * 1024 + 128));
            __builtin_amdgcn_sched_barrier(0);
#pragma unroll
            for (int ks = 0; ks < 4; ++ks) Fk[ks] = as8(trd(pq + ks * 1024 + 16), trd(pq + ks * 1024 + 16 + 128));
            __builtin_amdgcn_sched_barrier(0);
            f32x4 S0 = {}, S1 = {}, Sm = {};
            S0 = MFMA16(kx[0][0], Fq[0], S0); S1 = MFMA16(kx[1][0], Fq[0], S1);
            S0 = MFMA16(kx[0][1], Fq[1], S0); S1 = MFMA16(kx[1][1], Fq[1], S1);
            __builtin_amdgcn_sched_barrier(0);
            Sm = MFMA16(Fk[0], Fq[0], Sm); S0 = MFMA16(kx[0][2], Fq[2], S0);
            Sm = MFMA16(Fk[1], Fq[1], Sm); S1 = MFMA16(kx[1][2], Fq[2], S1);
            Sm = MFMA16(Fk[2], Fq[2], Sm); S0 = MFMA16(kx[0][3], Fq[3], S0);
            Sm = MFMA16(Fk[3], Fq[3], Sm); S1 = MFMA16(kx[1][3], Fq[3], S1);
            __builtin_amdgcn_sched_barrier(0);
            float y0 = hi8 ? Sm[2] : Sm[0], y1 = hi8 ? Sm[3] : Sm[1];
            float x0 = hi8 ? S1[0] : S0[0], x1 = hi8 ? S1[1] : S0[1], x2 = hi8 ? S1[2] : S0[2], x3 = hi8 ? S1[3] : S0[3];
            float m = vmax3(vmax3(y0, y1, x0), x1, vmax(x2, x3));
            m = vmax(m, ror8f(m));
            { auto r = __builtin_amdgcn_permlane16_swap(__float_as_uint(m), __float_as_uint(m), false, false); m = vmax(__uint_as_float(r[0]), __uint_as_float(r[1])); }
            { auto r = __builtin_amdgcn_permlane32_swap(__float_as_uint(m), __float_as_uint(m), false, false); m = vmax(__uint_as_float(r[0]), __uint_as_float(r[1])); }
            STAMP(5);
            const float nm = -m * c2;
            x0 = __builtin_amdgcn_exp2f(fmaf(x0, c2, nm)); x1 = __builtin_amdgcn_exp2f(fmaf(x1, c2, nm)); x2 = __builtin_amdgcn_exp2f(fmaf(x2, c2, nm)); x3 = __builtin_amdgcn_exp2f(fmaf(x3, c2, nm));
            y0 = __builtin_amdgcn_exp2f(fmaf(y0, c2, nm)); y1 = __builtin_amdgcn_exp2f(fmaf(y1, c2, nm));
            float sum = fmaf(0.5f, y0 + y1, (x0 + x1) + (x2 + x3));
            sum += ror8f(sum);
            sum = xsum(sum);
            const float inv = __builtin_amdgcn_rcpf(sum);
            const u32 pa = pack2(x0 * inv, x1 * inv) & 0x7fff7fffu, pb = pack2(x2 * inv, x3 * inv) & 0x7fff7fffu, pc = pack2(y0 * inv, y1 * inv) & 0x7fff7fffu;
            const u32 qa = ror8(pa), qb = ror8(pb), qc = ror8(pc);
            v4u px = {hi8 ? qa : pa, hi8 ? qb : pb, hi8 ? pa : qa, hi8 ? pb : qb};
            ST16(px, rw, oxs);
            ST16(px, rw, oxt);
            auto ra = __builtin_amdgcn_permlane16_swap(pc, pc, false, false);
            auto rc = __builtin_amdgcn_permlane16_swap(qc, qc, false, false);
            v4u pmv = {ra[0], rc[0], ra[1], rc[1]};
            ST16(pmv, rw, oms);
            STAMP(6);
            __builtin_amdgcn_sched_barrier(0);
            ST4(nos(rt32()), rw, obs);
            __builtin_amdgcn_s_setprio(0);
            STAMP_FLUSH;
        }
        {
            gather_mem(rw, NEARB(NSEG - 1, rep, pm), ml, lane, 0, tkp, NSEG - 1, budget);
            LBAR();
            qk_project(Bq, Bk, ma, xw, idx);
            LBAR();
        }
    } else if (w == 5) {
        int kdone = -1;
        const int kxo = OFF_KX + h * (NSEG * 8192) + g * 512 + i16 * 16;
        for (int s = 0; s < NSEG; ++s) {
            const bool need = s + 1 < NSEG - 1;
            if (need && kdone < s + 1) kdone = wait_flags(rw, FL_K + (h >> 1), 1, 1, -1, lane, budget);
            const bool go = need && kdone >= s + 1;
            int ko = (go ? kxo : OOB) + (s + 1) * 8192;
            int a0 = ox + NEARB(s + 2, i16 >> 3, pm), a1 = ox + NEARB(s + 2, 2 + (i16 >> 3), pm), a2 = om + NEARB(s + 2, g, pm), a3 = ob + FARB(s + 4, lane & 3, pm);
            v4u ff = {0xFFFFFFFFu, 0xFFFFFFFFu, 0xFFFFFFFFu, 0xFFFFFFFFu};
            asm volatile("" : "+v"(ko), "+v"(a0), "+v"(a1), "+v"(a2), "+v"(a3), "+v"(ff));
            LBAR();
            v4u kx[8];
            qk_project_h(Bq, Bk, ma, xw, idx, [&](int t) {
                if (t < 4) { kx[t] = LD16(rw, ko + (t & 1) * 2048, (t >> 1) * 4096); kx[4 + t] = LD16(rw, ko + 256 + (t & 1) * 2048, (t >> 1) * 4096); }
                if (t == 4) { ST16(ff, rw, a0); ST16(ff, rw, a1); }
                if (t == 5) { ST16(ff, rw, a2); ST4(0xFFFFFFFFu, rw, a3); }
            });
            LBAR();
            if (need) {
                if (!go) {
                    kdone = wait_flags(rw, FL_K + (h >> 1), 1, 1, s + 1, lane, budget, true);
#pragma unroll
                    for (int k = 0; k < 8; ++k) kx[k] = LD16(rw, kxo + (k >> 2) * 256 + (k & 3) * 2048, (s + 1) * 8192);
                }
#pragma unroll
                for (int k = 0; k < 8; ++k) *(LAS v4u*)(kl + ((s + 1) & 1) * 8192 + k * 1024) = kx[k];
                asm volatile("s_waitcnt lgkmcnt(0)" ::: "memory");
                tk_post(kfp, s + 1, 0u, lane);
            }
            DRAIN();
        }
    } else {
        TK tk; tk_init(tk);
        const int kmo = i16 < 8 ? OFF_KM + h * 2048 + g * 128 + i16 * 16 : OOB;
        for (int s = 0; s < NSEG; ++s) {
            if (s > 0 && w != 6) gather_mem(rw, NEARB(s, rep, pm), ml, lane, w, tkp, s, budget);
            BC2 bc = {0u, 0u};
            if (w == 3) bc = beacon_issue(rw, FARB(s, rep, pm), lane);
            LBAR();
            qk_project(Bq, Bk, ma, xw, idx);
            LBAR();
            if (w == 6) {
                bf16x8 F[4];
#pragma unroll
                for (int ks = 0; ks < 4; ++ks) F[ks] = as8(trd(pq + ks * 1024 + 16), trd(pq + ks * 1024 + 16 + 128));
#pragma unroll
                for (int ks = 0; ks < 4; ++ks) ST16(asu(F[ks]), rw, kmo + s * (NH * 2048) + ks * 512);
                DRAIN();
                if (lane == 0) ST4((u32)s, rw, (FL_M + h) * 128);
            } else if (w == 3) {
                if (s < NSEG - 1) {
                    if (s > 0) { tk_T0(tk, beacon_finish(bc, rw, FARB(s, rep, pm), lane, budget), a); if (tk.okA) tk_post(tkp, s + 1, tk_specA(tk, a), lane); }
                    wait_until(tk.XR + 8);
                    tk_Tp(tk, beacon_max<NH>(rw, FARB(s, rep, pm), BC_QK, lane, budget), a);
                    tk_post(tkp, s + 1, tk.XA, lane);
                }
            } else __builtin_amdgcn_s_sleep(14);
        }
    }
    { const int bid_ = rep + 8 * (h - 8 * rep); (void)bid_; STAMP_FIN; }
    if (budget < 0 && lane == 0) ST4(1u, rw, FL_ERR * 128);
}

__device__ __forceinline__ void role_wvo(const Args& a, lptr lds, rsrc_t rw, int idx) {
    const int tid = threadIdx.x, lane = tid & 63, w = __builtin_amdgcn_readfirstlane(tid >> 6);
    const int h = idx / 3, part = idx - 3 * h;
    bf16x8 Bo[5][4];
    v4u v[6], o[2][9];
#pragma unroll
    for (int c = 0; c < 2; ++c) rows_ld<64, 72>(o[c], a.Wo + ((size_t)h * DH + c * 64) * DM, DM, tid);
    rows_ld<192, 16>(v, a.Wv + (size_t)part * 192 * 4096 + h * DH, 4096, tid);
    __builtin_amdgcn_sched_barrier(0);
#pragma unroll
    for (int c = 0; c < 2; ++c) {
        lptr l = lds + c * 77824;
        rows_st<64, 72>(o[c], l, 1184, tid);
        LBAR();
#pragma unroll
        for (int j = 0; j < 5; ++j) {
            const int r = (w + 8 * j < NR) ? w + 8 * j : w;
#pragma unroll
            for (int ks = 0; ks < 2; ++ks) Bo[j][c * 2 + ks] = frag16(l + ks * 32 * 1184 + r * 32, 1184, lane);
        }
        __builtin_amdgcn_sched_barrier(0);
    }
    rows_st<192, 16>(v, lds, VS, tid);
    LBAR();
    RST(1);
    const int rho = lane & 15, bg = lane >> 4;
    const int wo0 = OFF_WVO + (2 * w + ((rho >> 1) & 1)) * 294912 + ((h >> 1) * 18 + part * 6) * 1024 + (bg * 16 + (h & 1) * 8 + 2 * (rho >> 2) + (rho & 1)) * 16;
#pragma unroll 1
    for (int kb = 0; kb < 6; ++kb) {
        bf16x8 Af[2][4];
#pragma unroll
        for (int mt = 0; mt < 2; ++mt)
#pragma unroll
            for (int ks = 0; ks < 4; ++ks) Af[mt][ks] = lds8(lds + (32 * kb + 8 * (rho >> 2) + 4 * mt + (rho & 3)) * VS + ks * 64 + bg * 16);
#pragma unroll
        for (int j = 0; j < 5; ++j) {
            if (j < 4 || w < 4) {
                f32x4 d0 = {}, d1 = {};
#pragma unroll
                for (int ks = 0; ks < 4; ++ks) { d0 = MFMA16(Af[0][ks], Bo[j][ks], d0); d1 = MFMA16(Af[1][ks], Bo[j][ks], d1); }
                v4u q = {pack2(d0[0], d0[1]), pack2(d0[2], d0[3]), pack2(d1[0], d1[1]), pack2(d1[2], d1[3])};
                ST16(q, rw, wo0 + j * (16 * 294912) + kb * 1024);
            }
        }
        if (kb == 2 || kb == 4) {
            asm volatile("s_waitcnt vmcnt(4)" ::: "memory");
            LBAR();
            if (tid == 0) ST4((u32)(kb >> 1), rw, (FL_W + idx) * 128);
        }
    }
    RST(2);
    DRAIN();
    LBAR();
    RST(3);
    if (tid == 0) ST4(3u, rw, (FL_W + idx) * 128);
}

__device__ __forceinline__ void role_rz(const Args& a, lptr lds, rsrc_t rw, int r, int rep) {
    const int tid = threadIdx.x, lane = tid & 63, w = __builtin_amdgcn_readfirstlane(tid >> 6);
    const int idx = (lane & 8) ? 0xEEEE : 0x4444;
    const int i16 = lane & 15, g = lane >> 4;
    int budget = SPIN_MAX;
    bf16x16 Ba[9], Bb[9];
    {
        const int wo = OFF_WVO + r * 294912 + 2 * w * 18432 + lane * 16;
#pragma unroll
        for (int p = 0; p < 3; ++p) {
            wait_flags(rw, FL_W + 12 * w, 1, 12, p + 1, lane, budget, true);
            if (p == 0) RST(4);
#pragma unroll
            for (int q = 0; q < 3; ++q) {
                const int t = 3 * q + p;
                Ba[t] = cat16(as8(LD16(rw, wo, t * 2048)), as8(LD16(rw, wo + 1024, t * 2048)));
                Bb[t] = cat16(as8(LD16(rw, wo, 18432 + t * 2048)), as8(LD16(rw, wo + 1024, 18432 + t * 2048)));
            }
            MB();
        }
    }
    lptr ml = lds, zl = lds + MEMB;
    lptr tka = lds + 13312, tkr = tka + 16, cntp = tka + 64;
    volatile LAS int* prod = (volatile LAS int*)(lds + 13312 + 512);
    lptr ring = lds + 16384;
    lptr ma = ml + lane * 16;
    lptr zw = zl + ((2 * w + (lane >> 5)) >> 2) * 1024 + ((((2 * w + (lane >> 5)) & 3) * 16 + (g & 1) * 8 + (i16 & 7)) * 16) + (i16 >> 3) * 8;
    const bool hi8r = (lane & 8) != 0;
    const int mso = i16 < 8 ? (r < 64 ? N_MEMA + r * 128 : N_MEMB + (r - 64) * 128) + i16 * 16 : OOB;
    const int ob = lane < NREP ? BCO(r) : OOB;
    const int pm = get_parity(rw, lane, budget);
    if (tid == 0) { tk_post(tka, 0, rt32() - 100000u, 0); tk_post(tkr, -1, 0u, 0); *(volatile LAS int*)cntp = 0; }
    if (tid < 4) prod[tid] = 0;
    stage_rows8(a.mem0, ml, tid);
    __syncthreads();
    RST(5);
    STAMP_INIT
    if (w < 4) {
        const int po = N_PX + (w >> 1) * 16384 + (w & 1) * 4096 + lane * 16;
        const int mo = N_PMM + w * 1024 + 2 * g * 128 + (i16 & 7) * 16 + (i16 >> 3) * 8;
        lptr cna = lane == 0 ? cntp : cntp + 4 + lane * 4;
        const u32 one = lane == 0 ? 1u : 0u;
        const bool dv = (g >> 1) == (i16 >> 3);
        const int rwo = dv ? (4 * (g & 1) + 2 * (g >> 1)) * 128 + ((i16 & 7) >> 1) * 32 + w * 8 + (i16 & 1) * 4 : 2048 + lane * 4;
        const int msq = lane < 32 ? (r < 64 ? N_MEMA + r * 128 : N_MEMB + (r - 64) * 128) + (lane >> 2) * 16 : OOB;
        lptr zrd = zl + w * 1024 + lane * 16;
        for (int s = 0; s < NSEG; ++s) {
            lptr Rw = lds + 81920 + (s & 1) * 1024 + rwo, Rr = lds + 81920 + (s & 1) * 1024 + (lane & 31) * 32;
            int mss = msq + NEARB(s + 1, lane & 3, pm), obs = ob + FARB(s + 1, lane & 3, pm);
            int sb = NEARB(s, rep, pm);
            asm volatile("" : "+s"(sb));
            lptr rg = ring + (s & 3) * 16384 + w * 4096 + lane * 16;
            bf16x8 b[5];
            const bool early = __builtin_amdgcn_readfirstlane(prod[w >> 1]) > s;
            MB();
#pragma unroll
            for (int k = 0; k < 4; ++k) b[k] = lds8(rg + k * 1024);
            const u32 X = s > 0 ? tk_get(tkr, min(s, NSEG - 2), budget) : 0u;
            asm volatile("s_waitcnt lgkmcnt(0)" : "+v"(mss), "+v"(obs), "+v"(Rw), "+v"(Rr) :: "memory");
#pragma unroll
            for (int k = 0; k < 4; ++k) PIN4(b[k]);
            LBAR();
            qk_project(Ba, Bb, ma, zw, idx);
            LBAR();
            if (s == NSEG - 1) break;
            __builtin_amdgcn_s_setprio(3);
            b[4] = lds8(zrd);
            if (!early) {
                while (__builtin_amdgcn_readfirstlane(prod[w >> 1]) <= s && --budget >= 0) __builtin_amdgcn_s_sleep(1);
                MB();
#pragma unroll
                for (int k = 0; k < 4; ++k) b[k] = lds8(rg + k * 1024);
            }
            if (s > 0 && (int)(X - rt32()) < 1000) { while ((int)(rt32() - X) < 0) {} }
            STAMP(1);
            f32x4 d;
            u32 bad;
            auto shot = [&]() {
                v4u p[4]; v2u m[2];
                m[0] = LD8(rw, mo, sb); m[1] = LD8(rw, mo + 128, sb);
                __builtin_amdgcn_sched_barrier(0);
#pragma unroll
                for (int k = 0; k < 4; ++k) { p[k] = LD16(rw, po + k * 1024, sb); __builtin_amdgcn_sched_barrier(0); }
                MB();
                d = f32x4{};
                d = MFMA16(as8(m[0], m[1]), b[4], d);
                bad = (m[0].x | m[0].y) | (m[1].x | m[1].y);
                __builtin_amdgcn_sched_barrier(0);
#pragma unroll
                for (int k = 0; k < 4; ++k) {
                    d = MFMA16(as8(p[k]), b[k], d);
                    bad |= (p[k].x | p[k].y) | (p[k].z | p[k].w);
                    __builtin_amdgcn_sched_barrier(0);
                }
            };
            shot();
            if (__builtin_expect(!__all((int)bad >= 0), 0)) {
                do shot(); while (!__all((int)bad >= 0) && --budget >= 0);
            }
            STAMP(2);
            float e0, e1;
            {
                auto r0 = __builtin_amdgcn_permlane32_swap(__float_as_uint(d[0]), __float_as_uint(d[2]), false, false);
                auto r1 = __builtin_amdgcn_permlane32_swap(__float_as_uint(d[1]), __float_as_uint(d[3]), false, false);
                const float t0 = __uint_as_float(hi8r ? r0[1] : r0[0]), t1 = __uint_as_float(hi8r ? r1[1] : r1[0]);
                e0 = t0 + ror8f(t0); e1 = t1 + ror8f(t1);
            }
            f32x4 x0, x1; u32 old;
            asm volatile("ds_write2_b32 %4, %5, %6 offset1:32\n\tds_add_rtn_u32 %0, %7, %8\n\tds_read_b128 %1, %3\n\tds_read_b128 %2, %3 offset:16\n\ts_waitcnt lgkmcnt(0)"
                         : "=&v"(old), "=&v"(x0), "=&v"(x1) : "v"(Rr), "v"(Rw), "v"(e0), "v"(e1), "v"(cna), "v"(one) : "memory");
            STAMP(4);
            {
                const u32 c = nos(pack2((x0[0] + x0[2]) + (x1[0] + x1[2]), (x0[1] + x0[3]) + (x1[1] + x1[3])));
                v4u o = {(u32)__builtin_amdgcn_update_dpp(0, (int)c, 0x00, 0xf, 0xf, true), (u32)__builtin_amdgcn_update_dpp(0, (int)c, 0x55, 0xf, 0xf, true),
                         (u32)__builtin_amdgcn_update_dpp(0, (int)c, 0xAA, 0xf, 0xf, true), (u32)__builtin_amdgcn_update_dpp(0, (int)c, 0xFF, 0xf, 0xf, true)};
                const bool last = (__builtin_amdgcn_readfirstlane(old) & 3) == 3;
                ST16(o, rw, last ? mss : OOB);
                __builtin_amdgcn_sched_barrier(0);
                if (last) ST4(nos(rt32()), rw, obs);
            }
            STAMP(5);
            __builtin_amdgcn_s_setprio(0);
            STAMP_FLUSH;
        }
    } else if (w == 5 || w == 6) {
        int vdone0 = -1, vdone1 = -1, staged = 0;
        const int hw = w - 5;
        const int vo = OFF_VOX + r * 16384 + hw * 8192 + lane * 16;
        const int zho = OFF_ZH + r * 4096 + hw * 2048 + lane * 16;
        lptr zhr = zl + hw * 2048 + (((lane >> 4) & 3) * 16 + (lane & 7)) * 16 + ((lane >> 3) & 1) * 8;
        const int aro = w == 5 ? mso : OOB, arb = w == 5 ? ob : OOB;
        v4u zv[2] = {};
        for (int s = 0; s < NSEG; ++s) {
            bool go = false;
            if (staged <= min(s + 3, NSEG - 2)) {
                const int ow = VOWN(staged);
                int& vd = ow ? vdone1 : vdone0;
                if (vd < staged) vd = wait_flags(rw, FL_V + ow, 2, NH, -1, lane, budget);
                go = vd >= staged;
            }
            const int vos = go ? vo : OOB;
            int va = vos + staged * (NR * NH * 1024), zs = s > 0 ? zho + (s - 1) * (NR * NH * 256) : OOB;
            int a0 = aro + NEARB(s + 3, g, pm), a1 = arb + FARB(s + 4, lane & 3, pm);
            v4u ff = {0xFFFFFFFFu, 0xFFFFFFFFu, 0xFFFFFFFFu, 0xFFFFFFFFu};
            asm volatile("" : "+v"(va), "+v"(zs), "+v"(a0), "+v"(a1), "+v"(ff));
            if (s > 0) gather_mem4(rw, NEARB(s, rep, pm), ml, lane, w - 4, tka, s, budget);
            LBAR();
            v4u v[8];
            qk_project_h(Ba, Bb, ma, zw, idx, [&](int t) {
                if (t < 4) { v[2 * t] = LD16(rw, va + (t & 1) * 2048, (t >> 1) * 4096); v[2 * t + 1] = LD16(rw, va + 1024 + (t & 1) * 2048, (t >> 1) * 4096); }
                if (t == 4) { ST16(zv[0], rw, zs); ST16(zv[1], rw, zs + 1024); }
                if (t == 5) { ST16(ff, rw, a0); ST4(0xFFFFFFFFu, rw, a1); }
            });
            LBAR();
#pragma unroll
            for (int k = 0; k < 2; ++k) {
                const v2u lo = *(LAS v2u*)(zhr + k * 1024), hi = *(LAS v2u*)(zhr + k * 1024 + 128);
                zv[k] = v4u{lo.x, lo.y, hi.x, hi.y};
            }
            for (;;) {
                if (go) {
#pragma unroll
                    for (int k = 0; k < 8; ++k) *(LAS v4u*)(ring + (staged & 3) * 16384 + hw * 8192 + k * 1024 + lane * 16) = v[k];
                    asm volatile("s_waitcnt lgkmcnt(0)" ::: "memory");
                    ++staged;
                    prod[hw] = staged;
                }
                if (staged > s + 1 || staged > NSEG - 2) break;
                const int ow = VOWN(staged);
                int& vd = ow ? vdone1 : vdone0;
                if (vd < staged) vd = wait_flags(rw, FL_V + ow, 2, NH, staged <= s ? staged : -1, lane, budget, true);
                if (vd < staged) break;
#pragma unroll
                for (int k = 0; k < 8; ++k) v[k] = LD16(rw, vo + (k & 3) * 1024, staged * (NR * NH * 1024) + (k >> 2) * 4096);
                go = true;
            }
            DRAIN();
        }
        {
            int l2 = (int)__builtin_amdgcn_mbcnt_hi(~0u, __builtin_amdgcn_mbcnt_lo(~0u, 0u));
            asm volatile("" : "+v"(l2));
            const int zf = OFF_ZH + r * 4096 + hw * 2048 + (NSEG - 1) * (NR * NH * 256) + l2 * 16;
            ST16(zv[0], rw, zf); ST16(zv[1], rw, zf + 1024);
        }
    } else {
        TK tk; tk_init(tk);
        for (int s = 0; s < NSEG; ++s) {
            if (s > 0) {
                gather_mem4(rw, NEARB(s, rep, pm), ml, lane, w - 4, tka, s, budget);
            }
            BC2 bc = {0u, 0u};
            if (w == 7) bc = beacon_issue(rw, FARB(s, rep, pm), lane);
            LBAR();
            qk_project(Ba, Bb, ma, zw, idx);
            LBAR();
            if (w == 7) {
                if (s < NSEG - 1) {
                    if (s > 0) { tk_T0(tk, beacon_finish(bc, rw, FARB(s, rep, pm), lane, budget), a); if (tk.okA) tk_post(tka, s + 1, tk_specA(tk, a), lane); }
                    tk_post(tkr, s, tk.XR, lane);
                    wait_until(tk.XR + 8);
                    tk_Tp(tk, beacon_max<NH>(rw, FARB(s, rep, pm), BC_QK, lane, budget), a);
                    tk_post(tka, s + 1, tk.XA, lane);
                    tk_post(tkr, s + 1, tk_specR(tk, a), lane);
                }
            } else {
                if (w == 4 && s >= 2 && lane == 0) ST4((u32)(s - 2), rw, (FL_Z + r) * 128);
                __builtin_amdgcn_s_sleep(14);
            }
        }
    }
    DRAIN();
    LBAR();
    if (tid == 0) ST4((u32)(NSEG - 1), rw, (FL_Z + r) * 128);
    { const int bid_ = rep + 8 * (8 + r - 18 * rep); (void)bid_; STAMP_FIN; }
    if (budget < 0 && lane == 0) ST4(3u, rw, FL_ERR * 128);
}

__device__ __forceinline__ void cexp_l(float lr, float li, float l, float& er, float& ei) {
    const float mag = __builtin_amdgcn_exp2f(lr * l);
    const float ph = li * l;
    const float pe = fmaf(li, l, -ph);
    const float fr = (ph - floorf(ph)) + pe;
    er = mag * __builtin_amdgcn_cosf(fr);
    ei = mag * __builtin_amdgcn_sinf(fr);
}
constexpr int CV_W = 8 * 260 * 16, CV_U = 96 * 80, CV_CH = CV_W + CV_U;
__device__ __forceinline__ void s4_unit(const Args& a, lptr lds, rsrc_t rw, int unit) {
    const int tid = threadIdx.x, lane = tid & 63, w = __builtin_amdgcn_readfirstlane(tid >> 6);
    const int d0 = unit * 4;
    v2u uv[4];
#pragma unroll
    for (int k = 0; k < 4; ++k) uv[k] = *(const v2u*)(a.u + (size_t)(2 * (tid + 512 * (k >> 1)) + (k & 1)) * DM + d0);
    {
        const int c = tid >> 7, nn = tid & 127, n = (d0 + c) * 128 + nn;
        const float dt = rbf(expf(bf2f(a.logdt[d0 + c])));
        const float lr = dt * bf2f(a.Are[n]) * 1.4426950408889634f, li = dt * bf2f(a.Aim[n]) * 0.15915494309189535f;
        const float br = dt * bf2f(a.Bre[n]), bi = dt * bf2f(a.Bim[n]);
        const float cr = bf2f(a.Cre[n]), ci = bf2f(a.Cim[n]);
        LAS f32x2* T = (LAS f32x2*)(lds + c * CV_CH) + nn;
        float er, ei, pr, pi;
        cexp_l(lr, li, 1.0f, er, ei); pr = er; pi = ei;
#pragma unroll
        for (int o = 0; o < 7; ++o) { T[o * 130] = f32x2{pr, pi}; const float t = pr * er - pi * ei; pi = pr * ei + pi * er; pr = t; }
#pragma unroll
        for (int q4 = 0; q4 < 4; ++q4) {
            if (q4 == 0) cexp_l(lr, li, 128.0f, er, ei);
            cexp_l(lr, li, (float)(128 + 512 * q4), pr, pi);
#pragma unroll
            for (int o = 0; o < 4; ++o) { T[(7 + 4 * q4 + o) * 130] = f32x2{pr, pi}; const float t = pr * er - pi * ei; pi = pr * ei + pi * er; pr = t; }
        }
        const float cbr = cr * br - ci * bi, cbi = cr * bi + ci * br;
        cexp_l(lr, li, 8.0f, er, ei);
#pragma unroll
        for (int q4 = 0; q4 < 4; ++q4) {
            float sr, si;
            cexp_l(lr, li, (float)(32 * q4), sr, si);
            pr = cbr * sr - cbi * si; pi = cbr * si + cbi * sr;
#pragma unroll
            for (int o = 0; o < 4; ++o) { T[(23 + 4 * q4 + o) * 130] = f32x2{pr, pi}; const float t = pr * er - pi * ei; pi = pr * ei + pi * er; pr = t; }
        }
    }
    __syncthreads();
    f32x4 kr[3];
    {
        const int x = lane & 15, g = lane >> 4, c = w & 3, nh = w >> 2;
        const int ah0 = x >> 3, o0 = x & 7;
        const int ah1 = x < 8 ? 2 + (x >> 2) : 4 + ((x - 8) >> 1), o1 = x < 8 ? 2 * (x & 3) : 4 * (x & 1);
        const int ah2 = x < 8 ? 8 + x : 16;
        LAS f32x2* T = (LAS f32x2*)(lds + c * CV_CH) + nh * 64 + g;
        LAS f32x2* Ta0 = T + 7 * 130; LAS f32x2* To0 = T + (o0 ? o0 - 1 : 0) * 130;
        LAS f32x2* Ta1 = T + (6 + ah1) * 130; LAS f32x2* To1 = T + (o1 ? o1 - 1 : 0) * 130;
        LAS f32x2* Ta2 = T + (6 + ah2) * 130; LAS f32x2* Tv = T + (23 + x) * 130;
        const f32x2 one = {1.0f, 0.0f};
        f32x4 ar[3] = {}, ai[3] = {};
#pragma unroll
        for (int jj = 0; jj < 16; ++jj) {
            const f32x2 v = Tv[4 * jj], u2 = Ta2[4 * jj], a1 = Ta1[4 * jj];
            f32x2 a0v = Ta0[4 * jj], e0 = To0[4 * jj], e1 = To1[4 * jj];
            a0v = ah0 ? a0v : one; e0 = o0 ? e0 : one; e1 = o1 ? e1 : one;
            const float u0r = a0v[0] * e0[0] - a0v[1] * e0[1], u0i = a0v[0] * e0[1] + a0v[1] * e0[0];
            const float u1r = a1[0] * e1[0] - a1[1] * e1[1], u1i = a1[0] * e1[1] + a1[1] * e1[0];
            ar[2] = __builtin_amdgcn_mfma_f32_16x16x4f32(u2[0], v[0], ar[2], 0, 0, 0);
            ai[2] = __builtin_amdgcn_mfma_f32_16x16x4f32(u2[1], v[1], ai[2], 0, 0, 0);
            ar[0] = __builtin_amdgcn_mfma_f32_16x16x4f32(u0r, v[0], ar[0], 0, 0, 0);
            ai[0] = __builtin_amdgcn_mfma_f32_16x16x4f32(u0i, v[1], ai[0], 0, 0, 0);
            ar[1] = __builtin_amdgcn_mfma_f32_16x16x4f32(u1r, v[0], ar[1], 0, 0, 0);
            ai[1] = __builtin_amdgcn_mfma_f32_16x16x4f32(u1i, v[1], ai[1], 0, 0, 0);
        }
#pragma unroll
        for (int t = 0; t < 3; ++t) kr[t] = ar[t] - ai[t];
    }
    __syncthreads();
    {
        LAS float* T = (LAS float*)(lds + (w & 3) * CV_CH + 4096 + (w >> 2) * 3072);
#pragma unroll
        for (int t = 0; t < 3; ++t)
#pragma unroll
            for (int r = 0; r < 4; ++r) T[(16 * t + 4 * (lane >> 4) + r) * 16 + (lane & 15)] = kr[t][r];
    }
    __syncthreads();
#pragma unroll
    for (int k = 0; k < 8; ++k) {
        const int i = tid + 512 * k, c = i >> 10, l2 = i & 1023;
        LAS float* T = (LAS float*)(lds + c * CV_CH + 4096);
        float v[2];
#pragma unroll
        for (int e = 0; e < 2; ++e) {
            const int lb = (int)rbf((float)(2 * l2 + e));
            const int hi = lb >> 7, o = lb & 7;
            const int row = lb < 256 ? 8 * hi + o : lb < 512 ? 16 + 4 * (hi - 2) + (o >> 1) : lb < 1024 ? 24 + 2 * (hi - 4) + (o >> 2) : 32 + (hi - 8);
            const int ix = row * 16 + ((lb >> 3) & 15);
            v[e] = T[ix] + T[768 + ix];
        }
        *(LAS u32*)(lds + c * CV_CH + CV_W + 2704 + l2 * 4) = pack2(v[0], v[1]);
    }
    __syncthreads();
    for (int i = tid; i < 4 * 259; i += 512) {
        const int c = i / 259, zz = i - c * 259, z = zz - 3;
        lptr ch = lds + c * CV_CH;
        v4u lo = {0, 0, 0, 0}, hi = {0, 0, 0, 0};
        if (z >= 1) lo = *(LAS v4u*)(ch + CV_W + 2704 + (z - 1) * 16);
        if (z >= 0) hi = *(LAS v4u*)(ch + CV_W + 2704 + z * 16);
        const u32 e[8] = {lo.x, lo.y, lo.z, lo.w, hi.x, hi.y, hi.z, hi.w};
#pragma unroll
        for (int cc = 0; cc < 8; ++cc) {
            v4u o;
#pragma unroll
            for (int k = 0; k < 4; ++k) {
                const int t = 8 + cc - 2 * k;
                if (t & 1) o[k] = (e[t >> 1] >> 16) | (e[t >> 1] << 16);
                else o[k] = (e[t >> 1] & 0xffffu) | (e[(t >> 1) - 1] & 0xffff0000u);
            }
            *(LAS v4u*)(ch + (cc * 260 + zz) * 16) = o;
        }
    }
    __syncthreads();
    for (int i = tid; i < 4 * 160; i += 512) *(LAS v4u*)(lds + (i / 160) * CV_CH + CV_W + (i % 160) * 16) = v4u{0, 0, 0, 0};
#pragma unroll
    for (int k2 = 0; k2 < 2; ++k2) {
        const int l = 2 * (tid + 512 * k2);
        const int o = CV_W + (32 + (l >> 5)) * 80 + (l & 31) * 2;
        const v2u p = uv[2 * k2], q = uv[2 * k2 + 1];
        *(LAS u32*)(lds + 0 * CV_CH + o) = (p.x & 0xffffu) | (q.x << 16);
        *(LAS u32*)(lds + 1 * CV_CH + o) = (p.x >> 16) | (q.x & 0xffff0000u);
        *(LAS u32*)(lds + 2 * CV_CH + o) = (p.y & 0xffffu) | (q.y << 16);
        *(LAS u32*)(lds + 3 * CV_CH + o) = (p.y >> 16) | (q.y & 0xffff0000u);
    }
    __syncthreads();
    const int c = w & 3, par = w >> 2, b = lane & 31, hh = lane >> 5;
    lptr ch = lds + c * CV_CH;
    lptr wa = ch + ((b & 7) * 260 + (b >> 3) - hh + 3) * 16;
    lptr ub = ch + CV_W + (32 + b) * 80 + hh * 16;
    f32x16 y0 = {}, y1 = {};
    wa += par * 64; ub -= par * 80;
    {
        bf16x8 fa[2][2], fb[2][2], fc[2][2];
#pragma unroll
        for (int ks = 0; ks < 2; ++ks) { fa[0][ks] = lds8(wa - ks * 32); fb[0][ks] = lds8(ub + ks * 32); fc[0][ks] = lds8(ub + 2560 + ks * 32); }
#pragma unroll
        for (int i = 0; i < 32; ++i) {
            const int cur = i & 1, nx = cur ^ 1;
            if (i + 1 < 32) {
#pragma unroll
                for (int ks = 0; ks < 2; ++ks) {
                    fa[nx][ks] = lds8(wa + (i + 1) * 128 - ks * 32);
                    if (i + 1 < 16) fb[nx][ks] = lds8(ub - (i + 1) * 160 + ks * 32);
                    fc[nx][ks] = lds8(ub + 2560 - (i + 1) * 160 + ks * 32);
                }
            }
            __builtin_amdgcn_sched_barrier(0);
#pragma unroll
            for (int ks = 0; ks < 2; ++ks) {
                if (i < 16) y0 = MFMA32(fa[cur][ks], fb[cur][ks], y0);
                y1 = MFMA32(fa[cur][ks], fc[cur][ks], y1);
            }
            __builtin_amdgcn_sched_barrier(0);
        }
    }
    __syncthreads();
    {
        LAS f32x4* X = (LAS f32x4*)(ch + par * 4096);
        const f32x16 src = par ? y0 : y1;
#pragma unroll
        for (int k = 0; k < 4; ++k) X[k * 64 + lane] = f32x4{src[4 * k], src[4 * k + 1], src[4 * k + 2], src[4 * k + 3]};
    }
    __syncthreads();
    {
        LAS f32x4* X = (LAS f32x4*)(ch + (1 - par) * 4096);
        lptr Y = ch + 8192 + (32 * par + b) * 72 + hh * 8;
        const f32x16 own = par ? y1 : y0;
#pragma unroll
        for (int k = 0; k < 4; ++k) {
            const f32x4 o = X[k * 64 + lane];
            v2u yv = {pack2(own[4 * k] + o[0], own[4 * k + 1] + o[1]), pack2(own[4 * k + 2] + o[2], own[4 * k + 3] + o[3])};
            *(LAS v2u*)(Y + k * 16) = yv;
        }
    }
    __syncthreads();
    const v2u dk = *(const v2u*)(a.dskip + d0);
#pragma unroll
    for (int k2 = 0; k2 < 2; ++k2) {
        const int i = tid + 512 * k2;
        lptr yp = lds + 8192 + (i >> 4) * 72 + (i & 15) * 4;
        const u32 ya = *(LAS u32*)(yp), yb = *(LAS u32*)(yp + CV_CH), yc = *(LAS u32*)(yp + 2 * CV_CH), yd = *(LAS u32*)(yp + 3 * CV_CH);
        const v2u p = uv[2 * k2], q = uv[2 * k2 + 1];
        v4u o;
        o.x = pack2(bflo(ya) + rbf(bflo(p.x) * bflo(dk.x)), bflo(yb) + rbf(bfhi(p.x) * bfhi(dk.x)));
        o.y = pack2(bflo(yc) + rbf(bflo(p.y) * bflo(dk.y)), bflo(yd) + rbf(bfhi(p.y) * bfhi(dk.y)));
        o.z = pack2(bfhi(ya) + rbf(bflo(q.x) * bflo(dk.x)), bfhi(yb) + rbf(bfhi(q.x) * bfhi(dk.x)));
        o.w = pack2(bfhi(yc) + rbf(bflo(q.y) * bflo(dk.y)), bfhi(yd) + rbf(bfhi(q.y) * bfhi(dk.y)));
        ST16(o, rw, OFF_A4 + unit * 16384 + i * 16);
    }
    __syncthreads();
}
__device__ __forceinline__ void role_s4(const Args& a, lptr lds, rsrc_t rw, int idx) {
    if (idx < 32) {
        const int tid = threadIdx.x, t = idx * 64 + (tid >> 3), j = tid & 7;
        float acc = 0.f;
#pragma unroll
        for (int k = 0; k < 9; ++k) {
            const v4u xv = *(const v4u*)(a.u + (size_t)t * DM + (k * 8 + j) * 8), gv = *(const v4u*)(a.gatew + (k * 8 + j) * 8);
#pragma unroll
            for (int e = 0; e < 4; ++e) acc += rbf(bflo(xv[e]) * bflo(gv[e])) + rbf(bfhi(xv[e]) * bfhi(gv[e]));
        }
        acc += __shfl_xor(acc, 1); acc += __shfl_xor(acc, 2); acc += __shfl_xor(acc, 4);
        acc = rbf(acc);
        if (j == 0) ST4(__float_as_uint(rbf(1.0f / (1.0f + __expf(-acc)))), rw, OFF_G + t * 4);
    }
#pragma unroll 1
    for (int unit = idx; unit < DM / 4; unit += NS4) s4_unit(a, lds, rw, unit);
    DRAIN();
    __syncthreads();
    if (threadIdx.x == 0) ST4(1u, rw, (FL_S + idx) * 128);
}

__device__ __forceinline__ void role_post(const Args& a, lptr lds, rsrc_t rw) {
    const int tid = threadIdx.x, lane = tid & 63, w = __builtin_amdgcn_readfirstlane(tid >> 6);
    const int i16 = lane & 15, g = lane >> 4;
    int budget = SPIN_MAX, vdone0 = -1, vdone1 = -1;
    volatile LAS int* U = (volatile LAS int*)(lds + 81920);
    wait_flags(rw, FL_S, 1, 64, 1, lane, budget);
    wait_flags(rw, FL_S + 64, 1, NS4 - 64, 1, lane, budget);
    constexpr int NFULL = 62 * 6, NUNIT = NFULL + 24;
#pragma unroll 1
    for (;;) {
        if (tid == 0) *U = 1 + (int)__hip_atomic_fetch_add((u32*)(a.ws + FL_Q * 128), 1u, __ATOMIC_RELAXED, __HIP_MEMORY_SCOPE_AGENT);
        __syncthreads();
        const int unit = __builtin_amdgcn_readfirstlane(*U);
        if (unit >= NUNIT || budget < 0) break;
        const bool half = unit >= NFULL;
        const int uu = half ? NFULL + ((unit - NFULL) >> 1) : unit, q0 = half ? (unit - NFULL) & 1 : 0;
        const int s = uu / 6, r = (uu - s * 6) * 6 + (w < 6 ? w : 0);
        bf16x8 b[32];
        v2u av[2]; float gt[2];
        if (w < 6) {
            if (VOWN(s)) { if (vdone1 < s) vdone1 = wait_flags(rw, FL_V + 1, 2, NH, s, lane, budget); }
            else { if (vdone0 < s) vdone0 = wait_flags(rw, FL_V, 2, NH, s, lane, budget); }
            const int vo = OFF_VOX + (s * NR + r) * (NH * 1024) + ((i16 >> 1) & 1) * 16384 + ((g & 1) * 16 + (g >> 1) * 8 + 2 * (i16 >> 2) + (i16 & 1)) * 16;
#pragma unroll
            for (int k = 0; k < 32; ++k) b[k] = as8(LD16(rw, vo + (k & 7) * 512, (k >> 3) * 4096));
#pragma unroll
            for (int qt = 0; qt < 2; ++qt) {
                av[qt] = LD8(rw, OFF_A4 + (4 * r + g) * 16384 + (32 * s + 16 * (qt ^ q0) + i16) * 8, 0);
                gt[qt] = __uint_as_float(LD4(rw, OFF_G + (32 * s + 16 * (qt ^ q0) + i16) * 4, 0));
            }
        }
        v4u z[8];
        const int zo = OFF_ZH + (s * NR + r) * (NH * 256) + ((i16 >> 1) & 1) * 4096 + g * 128 + (2 * (i16 >> 2) + (i16 & 1)) * 16;
        if (half) {
            wait_flags(rw, FL_Z + 2 * r, 1, 2, s, lane, budget, true);
#pragma unroll
            for (int k = 0; k < 8; ++k) z[k] = LD16(rw, zo + k * 512, 0);
            wait_flags(rw, FL_P + (s & 3), 4, 16, s, lane, budget, true);
            v4u pv[5];
#pragma unroll
            for (int k = 0; k < 5; ++k) pv[k] = LD16(rw, OFF_PX + s * 81920 + q0 * 40960 + tid * 16, k * 8192);
#pragma unroll
            for (int k = 0; k < 5; ++k) *(LAS v4u*)(lds + k * 8192 + tid * 16) = pv[k];
        } else {
            wait_flags(rw, FL_P + (s & 3), 4, 16, s, lane, budget);
            v4u pv[10];
#pragma unroll
            for (int k = 0; k < 10; ++k) pv[k] = LD16(rw, OFF_PX + s * 81920 + tid * 16, k * 8192);
            wait_flags(rw, FL_Z + 2 * r, 1, 2, s, lane, budget);
#pragma unroll
            for (int k = 0; k < 8; ++k) z[k] = LD16(rw, zo + k * 512, 0);
            MB();
#pragma unroll
            for (int k = 0; k < 10; ++k) *(LAS v4u*)(lds + k * 8192 + tid * 16) = pv[k];
        }
        __syncthreads();
        if (w < 6) {
#pragma unroll 1
            for (int qt = 0; qt < (half ? 1 : 2); ++qt) {
                f32x4 d = {}, e = {};
                lptr pl = lds + qt * 40960 + lane * 16;
                bf16x8 f[6];
#pragma unroll
                for (int i = 0; i < 6; ++i) f[i] = lds8(pl + i * 1024);
                __builtin_amdgcn_sched_barrier(0);
#pragma unroll
                for (int k = 0; k < 40; ++k) {
                    const bf16x8 v = k < 32 ? b[k < 32 ? k : 0] : as8(z[k < 32 ? 0 : k - 32]);
                    if (k & 1) e = MFMA16(v, f[k % 6], e); else d = MFMA16(v, f[k % 6], d);
                    if (k + 6 < 40) f[k % 6] = lds8(pl + (k + 6) * 1024);
                    __builtin_amdgcn_sched_barrier(0);
                }
                d += e;
                const float gq = qt ? gt[1] : gt[0], og = rbf(1.0f - gq);
                const v2u aq = qt ? av[1] : av[0];
                const float r0 = rbf(gq * bflo(aq.x)) + rbf(og * rbf(d[0])), r1 = rbf(gq * bfhi(aq.x)) + rbf(og * rbf(d[1]));
                const float r2 = rbf(gq * bflo(aq.y)) + rbf(og * rbf(d[2])), r3 = rbf(gq * bfhi(aq.y)) + rbf(og * rbf(d[3]));
                v2u ov = {pack2(r0, r1), pack2(r2, r3)};
                *(v2u*)(a.out + (size_t)(32 * s + 16 * (qt ^ q0) + i16) * DM + 16 * r + 4 * g) = ov;
            }
        }
        if (half) break;
        LBAR();
    }
    if (budget < 0 && lane == 0) ST4(6u, rw, FL_ERR * 128);
}

__global__ void __launch_bounds__(512, 2) k_all(Args a) {
    extern __shared__ __attribute__((aligned(16))) char smem[];
    lptr lds = (lptr)smem;
    const rsrc_t rw = __builtin_amdgcn_make_buffer_rsrc(a.ws, 0, WS_BYTES, 0x00020000);
    RST(0);
    if (threadIdx.x == 0) {
        const int x = (int)(__builtin_amdgcn_s_getreg(20 | (3 << 11)) & 7u);
        int role = 0;
        for (int k = 0; k < 8; ++k) {
            const int gq = (x + k) & 7;
            const int t = 1 + (int)__hip_atomic_fetch_add((u32*)(a.ws + (FL_T + gq) * 128), 1u, __ATOMIC_RELAXED, __HIP_MEMORY_SCOPE_AGENT);
            if (t < 32) { role = gq + 8 * t; break; }
        }
        *(volatile LAS int*)lds = role;
    }
    __syncthreads();
    const int b = __builtin_amdgcn_readfirstlane(*(volatile LAS int*)lds), xg = b & 7, slot = b >> 3;
    __syncthreads();
    WSTAMP(516);

    if (b == 8 * 8) role_probe(rw);
    if (slot >= 20) { role_wvo(a, lds, rw, (xg & 3) * 24 + (xg < 4 ? 12 : 0) + slot - 20); __syncthreads(); }
    if ((xg < 4 && slot >= 26) || (xg >= 4 && slot >= 20)) {
        const int idx = xg < 4 ? 48 + (slot - 26) * 4 + xg : (31 - slot) * 4 + (xg - 4);
        role_s4(a, lds, rw, idx);
        __syncthreads();
    }
    bool post = false;
    if (xg < 4) {
        if (slot < 8) role_qk(a, lds, rw, xg * 8 + slot, xg);
        else if (slot < 26) role_rz(a, lds, rw, xg * 18 + slot - 8, xg);
    } else {
        const int j = xg - 4;
        post = true;
        if (slot < 16) { role_vvo(a, lds, rw, j * 8 + (slot >> 1), slot & 1); post = slot < a.mask; }
        else if (slot < 20) role_k2(a, lds, rw, j * 4 + slot - 16);
        else if (slot < 28) { role_q2p(a, lds, rw, j * 4 + ((slot - 20) >> 1), slot & 1); post = false; }
    }
    WSTAMP(517);
    if (post) { __syncthreads(); role_post(a, lds, rw); }
    WSTAMP(518);
}

extern "C" void kernel_launch(void* const* d_in, const int* in_sizes, int n_in, void* d_out, int out_size, void* d_ws, size_t ws_size, hipStream_t stream) {
    Args a;
    a.u = (const u16*)d_in[0]; a.Are = (const u16*)d_in[1]; a.Aim = (const u16*)d_in[2]; a.Bre = (const u16*)d_in[3]; a.Bim = (const u16*)d_in[4];
    a.Cre = (const u16*)d_in[5]; a.Cim = (const u16*)d_in[6]; a.logdt = (const u16*)d_in[7]; a.dskip = (const u16*)d_in[8];
    a.mem0 = (const u16*)d_in[10]; a.Wq = (const u16*)d_in[11]; a.Wk = (const u16*)d_in[12]; a.Wv = (const u16*)d_in[13]; a.Wo = (const u16*)d_in[14];
    a.gatew = (const u16*)d_in[16]; a.out = (u16*)d_out; a.ws = (char*)d_ws;
    a.pd = 10; a.pha = 8; a.phz = 0; a.gap = 30; a.mask = 4; a.pad0 = 11;
    const int LDS_ALL = 160 * 1024;
    (void)hipFuncSetAttribute((const void*)k_all, hipFuncAttributeMaxDynamicSharedMemorySize, LDS_ALL);
    (void)hipMemsetAsync(d_ws, 0xFF, INIT_BYTES, stream);
    hipLaunchKernelGGL(k_all, dim3(256), dim3(512), LDS_ALL, stream, a);
}
```

```cpp
#include <hip/hip_runtime.h>
#include <hip/hip_bf16.h>
#include <stdint.h>

typedef unsigned short u16;
typedef unsigned int u32;
typedef u32 v4u __attribute__((ext_vector_type(4)));
typedef u32 v2u __attribute__((ext_vector_type(2)));
typedef float f32x4 __attribute__((ext_vector_type(4)));
typedef float f32x2 __attribute__((ext_vector_type(2)));
typedef float f32x16 __attribute__((ext_vector_type(16)));
typedef __bf16 bf16x2 __attribute__((ext_vector_type(2)));
typedef __bf16 bf16x8 __attribute__((ext_vector_type(8)));
typedef __bf16 bf16x16 __attribute__((ext_vector_type(16)));
typedef short s16x4 __attribute__((ext_vector_type(4)));
#define LAS __attribute__((address_space(3)))
typedef LAS char* lptr;
typedef __amdgpu_buffer_rsrc_t rsrc_t;

constexpr int SEQ = 2048, DM = 576, NH = 32, DH = 128, NSEG = 64, NR = 36, NR8 = 72, NREP = 4;
constexpr int MEMB = 9216, BC_QK = 80;
constexpr int FL_K = 0, FL_V = 16, FL_M = 80, FL_P = 112, FL_S = 176, FL_Q = 254, FL_ERR = 255, FL_Z = 256, FL_W = 328, FL_T = 424, FL_PAR = 432, NS4 = 72;
constexpr int OFF_RING = 65536;
constexpr int N_MEMA = 0, N_PX = 16384, N_PMM = 49152, N_MEMB = 49152 + 4096, N_PROBE = 49152 + 6144;
constexpr int INIT_BYTES = OFF_RING + 16 * 65536;
constexpr int OFF_ZH = INIT_BYTES;
constexpr int OFF_KX = OFF_ZH + NSEG * NR * NH * 256;
constexpr int OFF_VOX = OFF_KX + NH * SEQ * DH * 2;
constexpr int OFF_KM = OFF_VOX + NSEG * NR * NH * 1024;
constexpr int OFF_PX = OFF_KM + NSEG * NH * 2048;
constexpr int OFF_A4 = OFF_PX + NSEG * 81920;
constexpr int OFF_G = OFF_A4 + SEQ * DM * 2;
constexpr int OFF_WVO = OFF_G + SEQ * 4;
constexpr int WS_BYTES = OFF_WVO + NR8 * 294912;
constexpr int TK_LA = 11, TK_LR = 10, TK_TOL = 8, TK_GAP = 30;
constexpr int NJOIN = 4;

struct Args {
    const u16* u; const u16* Are; const u16* Aim; const u16* Bre; const u16* Bim; const u16* Cre; const u16* Cim;
    const u16* logdt; const u16* dskip; const u16* mem0; const u16* Wq; const u16* Wk; const u16* Wv; const u16* Wo;
    const u16* gatew; u16* out; char* ws;
};

__device__ __forceinline__ float bf2f(u16 v) { return __uint_as_float((u32)v << 16); }
__device__ __forceinline__ float bflo(u32 v) { return __uint_as_float(v << 16); }
__device__ __forceinline__ float bfhi(u32 v) { return __uint_as_float(v & 0xffff0000u); }
__device__ __forceinline__ u32 pack2(float lo, float hi) { f32x2 f = {lo, hi}; bf16x2 b = __builtin_convertvector(f, bf16x2); return __builtin_bit_cast(u32, b); }
__device__ __forceinline__ float rbf(float x) { return bflo(pack2(x, x)); }
__device__ __forceinline__ u32 nos(u32 v) { return min(v, 0xFFFFFFFEu); }
__device__ __forceinline__ v2u trd(lptr p) { return __builtin_bit_cast(v2u, __builtin_amdgcn_ds_read_tr16_b64_v4i16((LAS s16x4*)p)); }
__device__ __forceinline__ bf16x8 as8(v4u v) { return __builtin_bit_cast(bf16x8, v); }
__device__ __forceinline__ bf16x8 as8(v2u a, v2u b) { v4u v = {a.x, a.y, b.x, b.y}; return __builtin_bit_cast(bf16x8, v); }
__device__ __forceinline__ v4u asu(bf16x8 v) { return __builtin_bit_cast(v4u, v); }
__device__ __forceinline__ bf16x16 cat16(bf16x8 a, bf16x8 b) { return __builtin_shufflevector(a, b, 0, 1, 2, 3, 4, 5, 6, 7, 8, 9, 10, 11, 12, 13, 14, 15); }
__device__ __forceinline__ bf16x8 lds8(lptr p) { return *(LAS bf16x8*)p; }
__device__ __forceinline__ u32 bad4(v4u v) { return max(max(v.x, v.y), max(v.z, v.w)); }
#define NEARB(s, rep, pm) (OFF_RING + ((((s) & 3) * 4 + (rep)) << 16) + ((((pm) >> (((s) & 3) * 4 + (rep))) & 1) << 13))
#define FARB(s, rep, pm) ((NEARB(s, rep, pm) ^ 8192) + (((s) & 4) << 13))
#define BCO(slot) (((slot) & 63) * 128 + ((slot) >> 6) * 16384)
#define LBAR() do { asm volatile("s_waitcnt lgkmcnt(0)" ::: "memory"); __builtin_amdgcn_s_barrier(); asm volatile("" ::: "memory"); } while (0)
#define MB() asm volatile("" ::: "memory")
#define DRAIN() asm volatile("s_waitcnt vmcnt(0)" ::: "memory")
#define PIN4(x) asm volatile("" : "+v"(x))
#define MFMA16(a, b, c) __builtin_amdgcn_mfma_f32_16x16x32_bf16((a), (b), (c), 0, 0, 0)
#define MFMA32(a, b, c) __builtin_amdgcn_mfma_f32_32x32x16_bf16((a), (b), (c), 0, 0, 0)
#define SMF(a, b, c, idx) __builtin_amdgcn_smfmac_f32_16x16x64_bf16((a), (b), (c), (idx), 0, 0)
#define LD16(r, vo, so) __builtin_amdgcn_raw_buffer_load_b128((r), (vo), (so), 16)
#define LD8(r, vo, so) __builtin_amdgcn_raw_buffer_load_b64((r), (vo), (so), 16)
#define LD4(r, vo, so) __builtin_amdgcn_raw_buffer_load_b32((r), (vo), (so), 16)
#define ST16(v, r, vo) __builtin_amdgcn_raw_buffer_store_b128((v), (r), (vo), 0, 16)
#define ST8(v, r, vo) __builtin_amdgcn_raw_buffer_store_b64((v), (r), (vo), 0, 16)
#define ST4(v, r, vo) __builtin_amdgcn_raw_buffer_store_b32((v), (r), (vo), 0, 16)

__device__ __forceinline__ float swap32_sum(float a, float b) {
    auto r = __builtin_amdgcn_permlane32_swap(__float_as_uint(a), __float_as_uint(b), false, false);
    return __uint_as_float(r[0]) + __uint_as_float(r[1]); }
__device__ __forceinline__ float xmax(float m) {
    auto r = __builtin_amdgcn_permlane16_swap(__float_as_uint(m), __float_as_uint(m), false, false);
    m = fmaxf(__uint_as_float(r[0]), __uint_as_float(r[1]));
    auto q = __builtin_amdgcn_permlane32_swap(__float_as_uint(m), __float_as_uint(m), false, false);
    return fmaxf(__uint_as_float(q[0]), __uint_as_float(q[1])); }
__device__ __forceinline__ float xsum(float m) {
    auto r = __builtin_amdgcn_permlane16_swap(__float_as_uint(m), __float_as_uint(m), false, false);
    m = __uint_as_float(r[0]) + __uint_as_float(r[1]);
    auto q = __builtin_amdgcn_permlane32_swap(__float_as_uint(m), __float_as_uint(m), false, false);
    return __uint_as_float(q[0]) + __uint_as_float(q[1]); }
__device__ __forceinline__ float vmax(float a, float b) { float r; asm("v_max_f32 %0, %1, %2" : "=v"(r) : "v"(a), "v"(b)); return r; }
__device__ __forceinline__ float vmax3(float a, float b, float c) { float r; asm("v_max3_f32 %0, %1, %2, %3" : "=v"(r) : "v"(a), "v"(b), "v"(c)); return r; }
__device__ __forceinline__ u32 ror8(u32 v) { return (u32)__builtin_amdgcn_update_dpp(0, (int)v, 0x128, 0xf, 0xf, true); }
__device__ __forceinline__ float ror8f(float v) { return __uint_as_float(ror8(__float_as_uint(v))); }
__device__ __forceinline__ int wave_max(int d) {
    d = max(d, __builtin_amdgcn_update_dpp(d, d, 0x128, 0xf, 0xf, false));
    d = max(d, __builtin_amdgcn_update_dpp(d, d, 0x124, 0xf, 0xf, false));
    d = max(d, __builtin_amdgcn_update_dpp(d, d, 0x122, 0xf, 0xf, false));
    d = max(d, __builtin_amdgcn_update_dpp(d, d, 0x121, 0xf, 0xf, false));
    return max(max(__builtin_amdgcn_readlane(d, 0), __builtin_amdgcn_readlane(d, 16)), max(__builtin_amdgcn_readlane(d, 32), __builtin_amdgcn_readlane(d, 48)));
}

constexpr int SPIN_MAX = 2000000;
__device__ __forceinline__ int wait_flags(rsrc_t rw, int line0, int stride, int n, int need, int lane, int& budget, bool fast = false) {
    const int vo = (line0 + (lane < n ? lane : 0) * stride) * 128;
    int mn;
    for (;;) {
        const int v = (int)LD4(rw, vo, 0);
        MB();
        mn = -wave_max(-v);
        if (mn >= need) break;
        if (--budget < 0) break;
        if (fast) __builtin_amdgcn_s_sleep(2); else __builtin_amdgcn_s_sleep(12);
    }
    return mn;
}

template <int NRW, int CW>
__device__ __forceinline__ void stage_rows(const u16* g, int gs, lptr lds, int ls, int tid) {
    constexpr int N = NRW * CW / 512;
    static_assert(NRW * CW % 512 == 0, "");
    v4u v[N];
#pragma unroll
    for (int j = 0; j < N; ++j) { const int i = tid + 512 * j, row = i / CW, c = i % CW; v[j] = *(const v4u*)(g + (size_t)row * gs + c * 8); }
#pragma unroll
    for (int j = 0; j < N; ++j) { const int i = tid + 512 * j, row = i / CW, c = i % CW; *(LAS v4u*)(lds + row * ls + c * 16) = v[j]; }
}
template <int NRW, int CW>
__device__ __forceinline__ void rows_ld(v4u (&v)[NRW * CW / 512], const u16* g, int gs, int tid) {
#pragma unroll
    for (int j = 0; j < NRW * CW / 512; ++j) { const int i = tid + 512 * j, row = i / CW, c = i % CW; v[j] = *(const v4u*)(g + (size_t)row * gs + c * 8); }
}
template <int NRW, int CW>
__device__ __forceinline__ void rows_st(const v4u (&v)[NRW * CW / 512], lptr lds, int ls, int tid) {
#pragma unroll
    for (int j = 0; j < NRW * CW / 512; ++j) { const int i = tid + 512 * j, row = i / CW, c = i % CW; *(LAS v4u*)(lds + row * ls + c * 16) = v[j]; }
}
__device__ __forceinline__ bf16x8 frag16(lptr base, int ls, int lane) {
    const int i16 = lane & 15, bg = lane >> 4, q = i16 >> 2, p = i16 & 3;
    lptr a = base + (8 * bg + q) * ls + p * 8;
    return as8(trd(a), trd(a + 4 * ls));
}
__device__ __forceinline__ bf16x8 frag32p(lptr base, int ls, int lane) {
    const int i16 = lane & 15, G = lane >> 4, q = i16 >> 2, p = i16 & 3;
    lptr a = base + (8 * (G >> 1) + q) * ls + (16 * (p & 1) + 8 * (G & 1) + 4 * (p >> 1)) * 2;
    return as8(trd(a), trd(a + 4 * ls));
}

constexpr int XS = 1168;
__device__ __forceinline__ void xload(v4u (&st)[9], const u16* x, int tok0, int tid) {
#pragma unroll
    for (int j = 0; j < 9; ++j) st[j] = *(const v4u*)(x + (size_t)tok0 * DM + (tid + 512 * j) * 8);
}
__device__ __forceinline__ void xstore(const v4u (&st)[9], lptr xl, int tid) {
#pragma unroll
    for (int j = 0; j < 9; ++j) { const int i = tid + 512 * j; *(LAS v4u*)(xl + i * 16 + (i / 72) * 16) = st[j]; }
}
__device__ __forceinline__ void load_w2(bf16x8 (&A)[36], const u16* W, int h0, lptr lds, int tid, int lane, int w) {
    v4u v[6][6];
#pragma unroll
    for (int c = 0; c < 6; ++c) rows_ld<96, 32>(v[c], W + (size_t)c * 96 * 4096 + h0 * DH, 4096, tid);
    __builtin_amdgcn_sched_barrier(0);
#pragma unroll
    for (int c = 0; c < 6; ++c) {
        lptr l = lds + (c & 1) * (96 * 544);
        rows_st<96, 32>(v[c], l, 544, tid);
        LBAR();
#pragma unroll
        for (int ks = 0; ks < 6; ++ks) A[c * 6 + ks] = frag32p(l + ks * 16 * 544 + w * 64, 544, lane);
        __builtin_amdgcn_sched_barrier(0);
    }
    LBAR();
}

__device__ __forceinline__ f32x16 proj_tile(const bf16x8 (&A)[36], lptr xb) {
    f32x16 acc = {};
    bf16x8 f[6];
#pragma unroll
    for (int i = 0; i < 6; ++i) f[i] = lds8(xb + i * 32);
    __builtin_amdgcn_sched_barrier(0);
#pragma unroll
    for (int ks = 0; ks < 36; ++ks) {
        acc = MFMA32(A[ks], f[ks % 6], acc);
        if (ks + 6 < 36) f[ks % 6] = lds8(xb + (ks + 6) * 32);
        __builtin_amdgcn_sched_barrier(0);
    }
    return acc;
}
__device__ __forceinline__ void role_k2(const Args& a, lptr lds, rsrc_t rw, int cu) {
    const int tid = threadIdx.x, lane = tid & 63, w = __builtin_amdgcn_readfirstlane(tid >> 6);
    v4u st[9];
    bf16x8 A[36];
    xload(st, a.u, 0, tid);
    load_w2(A, a.Wk, 2 * cu, lds, tid, lane, w);
    xstore(st, lds, tid);
    __syncthreads();
    const int o0 = OFF_KX + (2 * cu + (w >> 2)) * (NSEG * 8192) + ((w & 3) * 4 + (lane >> 5) * 2) * 512 + (lane & 31) * 16;
    for (int blk = 0; blk < 32; ++blk) {
        lptr xl = lds + (blk & 1) * (64 * XS);
        if (blk + 1 < 32) xload(st, a.u, (blk + 1) * 64, tid);
#pragma unroll
        for (int tt = 0; tt < 2; ++tt) {
            const f32x16 acc = proj_tile(A, xl + (32 * tt + (lane & 31)) * XS + (lane >> 5) * 16);
            v4u c0 = {pack2(acc[0], acc[1]), pack2(acc[2], acc[3]), pack2(acc[4], acc[5]), pack2(acc[6], acc[7])};
            v4u c1 = {pack2(acc[8], acc[9]), pack2(acc[10], acc[11]), pack2(acc[12], acc[13]), pack2(acc[14], acc[15])};
            const int o = o0 + (blk * 2 + tt) * 8192;
            ST16(c0, rw, o); ST16(c1, rw, o + 512);
            if (blk == 0 && tt == 0) { DRAIN(); LBAR(); if (tid == 0) ST4(0u, rw, (FL_K + cu) * 128); }
        }
        if (blk + 1 < 32) xstore(st, lds + ((blk + 1) & 1) * (64 * XS), tid);
        const bool full = blk < 4 || blk == 31;
        if (full) DRAIN(); else asm volatile("s_waitcnt vmcnt(4)" ::: "memory");
        LBAR();
        if (tid == 0) ST4((u32)(full ? 2 * blk + 1 : 2 * blk - 1), rw, (FL_K + cu) * 128);
    }
}

constexpr int VS = 272;
__device__ __forceinline__ void role_q2p(const Args& a, lptr lds, rsrc_t rw, int cu, int par) {
    const int tid = threadIdx.x, lane = tid & 63, w = __builtin_amdgcn_readfirstlane(tid >> 6);
    v4u st[9];
    bf16x8 A[36];
    load_w2(A, a.Wq, 2 * cu, lds, tid, lane, w);
    lptr xl = lds, ql = lds + 64 * XS;
    LAS int* cnt = (LAS int*)(lds + 64 * XS + 4 * 64 * VS);
    if (tid < 2) cnt[tid] = 0;
    xload(st, a.u, par * 64, tid);
    xstore(st, xl, tid);
    __syncthreads();
    int budget = SPIN_MAX, kdone = -1;
    const int i16 = lane & 15, g = lane >> 4;
    const int sg = w >> 2, hh = (w >> 1) & 1, qt = w & 1, h = 2 * cu + hh;
    const float c2 = 0.08838834764831845f * 1.4426950408889634f;
    for (int it = -1; it < 16; ++it) {
        const int blk = 2 * it + par;
        if (it + 1 < 16) {
            if (it + 2 < 16) xload(st, a.u, (blk + 4) * 64, tid);
            lptr qb = ql + (((it + 1) & 1) * 2 + (w >> 2)) * (64 * VS) + ((w & 3) * 32 + (lane >> 5) * 16) * 2;
#pragma unroll
            for (int tt = 0; tt < 2; ++tt) {
                const f32x16 acc = proj_tile(A, xl + (32 * tt + (lane & 31)) * XS + (lane >> 5) * 16);
                v4u c0 = {pack2(acc[0], acc[1]), pack2(acc[2], acc[3]), pack2(acc[4], acc[5]), pack2(acc[6], acc[7])};
                v4u c1 = {pack2(acc[8], acc[9]), pack2(acc[10], acc[11]), pack2(acc[12], acc[13]), pack2(acc[14], acc[15])};
                lptr qp = qb + (32 * tt + (lane & 31)) * VS;
                *(LAS v4u*)qp = c0; *(LAS v4u*)(qp + 16) = c1;
            }
            __syncthreads();
            if (it + 2 < 16) xstore(st, xl, tid);
        }
        if (it >= 0) {
            const int s = 2 * blk + sg;
            if (kdone < s) kdone = wait_flags(rw, FL_K + cu, 1, 1, s, lane, budget);
            bf16x8 k0[4], k1[4], km[4], qf[4];
            const int ko = OFF_KX + (h * NSEG + s) * 8192 + g * 512 + i16 * 16;
#pragma unroll
            for (int ks = 0; ks < 4; ++ks) { k0[ks] = as8(LD16(rw, ko, ks * 2048)); k1[ks] = as8(LD16(rw, ko + 256, ks * 2048)); }
            lptr qp = ql + ((it & 1) * 2 + hh) * (64 * VS) + (32 * sg + 16 * qt + i16) * VS + g * 16;
#pragma unroll
            for (int ks = 0; ks < 4; ++ks) qf[ks] = lds8(qp + ks * 64);
            f32x4 S0 = {}, S1 = {}, Sm = {};
#pragma unroll
            for (int ks = 0; ks < 4; ++ks) { S0 = MFMA16(k0[ks], qf[ks], S0); S1 = MFMA16(k1[ks], qf[ks], S1); }
            wait_flags(rw, FL_M + h, 1, 1, s, lane, budget, s >= 62);
            const int mo = OFF_KM + (s * NH + h) * 2048 + g * 128 + (i16 & 7) * 16;
#pragma unroll
            for (int ks = 0; ks < 4; ++ks) km[ks] = as8(LD16(rw, mo + ks * 512, 0));
#pragma unroll
            for (int ks = 0; ks < 4; ++ks) Sm = MFMA16(km[ks], qf[ks], Sm);
            const int qi = 16 * qt + i16, t0 = 4 * g;
#pragma unroll
            for (int r = 0; r < 4; ++r) {
                if (t0 + r > qi) S0[r] = -INFINITY;
                if (t0 + r + 16 > qi) S1[r] = -INFINITY;
                if (g >= 2) Sm[r] = -INFINITY;
            }
            float m = fmaxf(fmaxf(fmaxf(S0[0], S0[1]), fmaxf(S0[2], S0[3])), fmaxf(fmaxf(S1[0], S1[1]), fmaxf(S1[2], S1[3])));
            m = fmaxf(m, fmaxf(fmaxf(Sm[0], Sm[1]), fmaxf(Sm[2], Sm[3])));
            m = xmax(m);
            const float nm = -m * c2;
            float sum = 0.f;
#pragma unroll
            for (int r = 0; r < 4; ++r) {
                S0[r] = __builtin_amdgcn_exp2f(fmaf(S0[r], c2, nm)); S1[r] = __builtin_amdgcn_exp2f(fmaf(S1[r], c2, nm)); Sm[r] = __builtin_amdgcn_exp2f(fmaf(Sm[r], c2, nm));
                sum += S0[r] + S1[r] + Sm[r];
            }
            sum = xsum(sum);
            const float inv = __builtin_amdgcn_rcpf(sum);
            v4u px = {pack2(S0[0] * inv, S0[1] * inv), pack2(S0[2] * inv, S0[3] * inv), pack2(S1[0] * inv, S1[1] * inv), pack2(S1[2] * inv, S1[3] * inv)};
            const int po = OFF_PX + (s * 2 + qt) * 40960;
            ST16(px, rw, po + h * 1024 + lane * 16);
            const u32 ma = pack2(Sm[0] * inv, Sm[1] * inv), mb = pack2(Sm[2] * inv, Sm[3] * inv);
            auto ra = __builtin_amdgcn_permlane16_swap(ma, ma, false, false);
            auto rb = __builtin_amdgcn_permlane16_swap(mb, mb, false, false);
            v4u pm = {ra[0], rb[0], ra[1], rb[1]};
            if (lane < 16) ST16(pm, rw, po + 32768 + h * 256 + lane * 16);
            DRAIN();
            if (lane == 0) {
                const int old = __hip_atomic_fetch_add(cnt + sg, 1, __ATOMIC_RELAXED, __HIP_MEMORY_SCOPE_WORKGROUP);
                if ((old & 3) == 3) ST4((u32)s, rw, (FL_P + 4 * cu + 2 * par + sg) * 128);
            }
        }
        __syncthreads();
    }
    if (budget < 0 && lane == 0) ST4(5u, rw, FL_ERR * 128);
}

__device__ __forceinline__ void role_vvo(const Args& a, lptr lds, rsrc_t rw, int h, int par) {
    const int tid = threadIdx.x, lane = tid & 63, w = __builtin_amdgcn_readfirstlane(tid >> 6);
    v4u st[9];
#pragma unroll
    for (int j = 0; j < 9; ++j) { const int i = tid + 512 * j; st[j] = *(const v4u*)(a.u + (size_t)(par * 32 + (i >= 2304 ? 32 : 0)) * DM + i * 8); }
    bf16x8 Av[18], Bo[5][4];
    {
        v4u v[3][6], o[2][9];
#pragma unroll
        for (int c = 0; c < 3; ++c) rows_ld<192, 16>(v[c], a.Wv + (size_t)c * 192 * 4096 + h * DH, 4096, tid);
#pragma unroll
        for (int c = 0; c < 2; ++c) rows_ld<64, 72>(o[c], a.Wo + ((size_t)h * DH + c * 64) * DM, DM, tid);
        __builtin_amdgcn_sched_barrier(0);
#pragma unroll
        for (int c = 0; c < 3; ++c) {
            lptr l = lds + (c & 1) * 77824;
            rows_st<192, 16>(v[c], l, 288, tid);
            LBAR();
#pragma unroll
            for (int ks = 0; ks < 6; ++ks) Av[c * 6 + ks] = frag16(l + ks * 32 * 288 + w * 32, 288, lane);
            __builtin_amdgcn_sched_barrier(0);
        }
#pragma unroll
        for (int c = 0; c < 2; ++c) {
            lptr l = lds + ((c + 1) & 1) * 77824;
            rows_st<64, 72>(o[c], l, 1184, tid);
            LBAR();
#pragma unroll
            for (int j = 0; j < 5; ++j) {
                const int r = (w + 8 * j < NR) ? w + 8 * j : w;
#pragma unroll
                for (int ks = 0; ks < 2; ++ks) Bo[j][c * 2 + ks] = frag16(l + ks * 32 * 1184 + r * 32, 1184, lane);
            }
            __builtin_amdgcn_sched_barrier(0);
        }
        LBAR();
    }
    lptr xl = lds, vl = lds + 64 * XS;
    xstore(st, xl, tid);
    __syncthreads();
    const int rho = lane & 15, bg = lane >> 4;
    const int vo0 = OFF_VOX + ((2 * w + ((rho >> 1) & 1)) * 16 + (h >> 1)) * 1024 + ((2 * (h & 1) + (bg & 1)) * 16 + (bg >> 1) * 8 + 2 * (rho >> 2) + (rho & 1)) * 16;
    const int fo = (FL_V + 2 * h + par) * 128;
    auto stage1 = [&](int tt) {
        f32x4 acc = {};
        lptr xb = xl + (16 * tt + rho) * XS + bg * 16;
        bf16x8 f[6];
#pragma unroll
        for (int i = 0; i < 6; ++i) f[i] = lds8(xb + i * 64);
        __builtin_amdgcn_sched_barrier(0);
#pragma unroll
        for (int ks = 0; ks < 18; ++ks) {
            acc = MFMA16(Av[ks], f[ks % 6], acc);
            if (ks + 6 < 18) f[ks % 6] = lds8(xb + (ks + 6) * 64);
            __builtin_amdgcn_sched_barrier(0);
        }
        v2u o = {pack2(acc[0], acc[1]), pack2(acc[2], acc[3])};
        *(LAS v2u*)(vl + (16 * tt + rho) * VS + (16 * w + 4 * bg) * 2) = o;
    };
    auto stage2 = [&](int sg, int s) {
        bf16x8 Af[2][4];
#pragma unroll
        for (int half = 0; half < 2; ++half)
#pragma unroll
            for (int ks = 0; ks < 4; ++ks) Af[half][ks] = lds8(vl + (32 * sg + rho + 16 * half) * VS + ks * 64 + bg * 16);
#pragma unroll
        for (int j = 0; j < 5; ++j) {
            if (j < 4 || w < 4) {
                f32x4 d0 = {}, d1 = {};
#pragma unroll
                for (int ks = 0; ks < 4; ++ks) { d0 = MFMA16(Af[0][ks], Bo[j][ks], d0); d1 = MFMA16(Af[1][ks], Bo[j][ks], d1); }
                v4u o = {pack2(d0[0], d0[1]), pack2(d0[2], d0[3]), pack2(d1[0], d1[1]), pack2(d1[2], d1[3])};
                ST16(o, rw, vo0 + (s * NR + 8 * j) * (NH * 1024));
            }
        }
    };
    int prev = -1;
#pragma unroll 1
    for (int un = 0; un < 32; ++un) {
        const int sg = un & 1, blk = (un >> 1) * 2 + par;
        const int s = un < 2 ? par + 2 * un : 2 * blk + sg;
        if (sg == 0 && blk + 2 < 32) xload(st, a.u, (blk + 2) * 64, tid);
        stage1(2 * sg); stage1(2 * sg + 1);
        if (sg == 1) DRAIN(); else if (blk + 2 < 32) asm volatile("s_waitcnt vmcnt(9)" ::: "memory"); else DRAIN();
        LBAR();
        if (tid == 0 && prev >= 0) ST4((u32)prev, rw, fo);
        if (sg == 1 && blk + 2 < 32) xstore(st, xl, tid);
        stage2(sg, s);
        prev = s;
        if (un == 0 || un == 31) { DRAIN(); LBAR(); if (tid == 0) ST4((u32)s, rw, fo); }
        else if (sg == 1) LBAR();
    }
}
#define VOWN(s) ((s) < 4 ? ((s) & 1) : (((s) >> 1) & 1))

__device__ __forceinline__ u32 rt32() { return (u32)__builtin_amdgcn_s_memrealtime(); }
__device__ __forceinline__ void wait_until(u32 t) {
    if ((int)(t - rt32()) > 1000) return;
    while ((int)(rt32() - t) < -24) __builtin_amdgcn_s_sleep(4);
    while ((int)(rt32() - t) < 0) {}
}
struct TK { u32 XA, XR, EA, ER; int dA0, dA1, dA2, dR0, dR1, dR2; int okA, okR, gap; };
__device__ __forceinline__ int tk_est(int d0, int d1, int d2) { return max(min(d0, d1), min(max(d0, d1), d2)); }
template <int N>
__device__ __forceinline__ u32 beacon_max(rsrc_t rw, int fb, int slot0, int lane, int& budget) {
    const int vo = BCO(slot0 + (lane < N ? lane : 0)), v2 = BCO(64 + (lane & 7));
    u32 v, u = 0;
    for (;;) {
        v = LD4(rw, vo, fb);
        if (N > 64) u = LD4(rw, v2, fb);
        MB();
        if (__all(max(v, u) != 0xFFFFFFFFu)) break;
        if (--budget < 0) break;
        __builtin_amdgcn_s_sleep(1);
    }
    const u32 base = __builtin_amdgcn_readfirstlane(v);
    int d = (int)(v - base);
    if (N > 64) d = max(d, (int)(u - base));
    return base + (u32)wave_max(d);
}
struct BC2 { u32 v, u; };
__device__ __forceinline__ BC2 beacon_issue(rsrc_t rw, int fb, int lane) {
    BC2 b;
    b.v = LD4(rw, BCO(lane), fb);
    b.u = LD4(rw, BCO(64 + (lane & 7)), fb);
    MB();
    return b;
}
__device__ __forceinline__ u32 beacon_finish(BC2 b, rsrc_t rw, int fb, int lane, int& budget) {
    if (!__all(max(b.v, b.u) != 0xFFFFFFFFu)) return beacon_max<NR8>(rw, fb, 0, lane, budget);
    const u32 base = __builtin_amdgcn_readfirstlane(b.v);
    return base + (u32)wave_max(max((int)(b.v - base), (int)(b.u - base)));
}
__device__ __forceinline__ void tk_T0(TK& k, u32 T0, const Args& a) {
    if (k.okR) { k.dR2 = k.dR1; k.dR1 = k.dR0; k.dR0 = (int)(T0 - k.ER); }
    k.okA = (int)(k.XA - T0) >= TK_LA - TK_TOL;
    if (!k.okR) k.gap = k.okA ? max(TK_GAP, k.gap - 6) : min(k.gap + 16, 160);
    k.EA = k.XA;
    k.XR = k.EA + tk_est(k.dA0, k.dA1, k.dA2) + TK_LR;
}
__device__ __forceinline__ void tk_Tp(TK& k, u32 Tp, const Args& a) {
    const int okn = (int)(k.XR - Tp) >= TK_LR - TK_TOL;
    if (k.okA && (k.okR || !okn)) { k.dA2 = k.dA1; k.dA1 = k.dA0; k.dA0 = (int)(Tp - k.EA); }
    k.okR = okn;
    k.ER = k.okR ? k.XR : Tp + k.gap;
    k.XA = k.ER + tk_est(k.dR0, k.dR1, k.dR2) + TK_LA;
}
__device__ __forceinline__ u32 tk_specA(const TK& k, const Args& a) { return k.XR + tk_est(k.dR0, k.dR1, k.dR2) + TK_LA; }
__device__ __forceinline__ u32 tk_specR(const TK& k, const Args& a) { return k.XA + tk_est(k.dA0, k.dA1, k.dA2) + TK_LR; }
__device__ __forceinline__ void tk_init(TK& k) { const u32 n = rt32(); k.XA = k.XR = k.EA = k.ER = n - 100000u; k.dA0 = k.dA1 = k.dA2 = 128; k.dR0 = k.dR1 = k.dR2 = 66; k.okA = k.okR = 0; k.gap = 50; }

__device__ __forceinline__ void tk_post(lptr p, int tag, u32 t, int lane) { if (lane == 0) { v2u v = {(u32)tag, t}; *(volatile LAS v2u*)p = v; } }
__device__ __forceinline__ u32 tk_get(lptr p, int tag, int& budget) {
    for (;;) {
        const v2u v = *(volatile LAS v2u*)p;
        if ((int)__builtin_amdgcn_readfirstlane(v.x) >= tag || --budget < 0) return __builtin_amdgcn_readfirstlane(v.y);
        __builtin_amdgcn_s_sleep(2);
    }
}
__device__ __forceinline__ void tk_wait(lptr p, int tag, int& budget) {
    u32 X;
    for (;;) {
        const v2u v = *(volatile LAS v2u*)p;
        const u32 now = rt32();
        X = __builtin_amdgcn_readfirstlane(v.y);
        const int tg = (int)__builtin_amdgcn_readfirstlane(v.x), dt = (int)(X - now);
        if (tg >= tag) {
            if (dt <= 0 || dt > 1000 || tg > tag) return;
            if (dt <= 28) break;
        }
        if (--budget < 0) return;
        __builtin_amdgcn_s_sleep(1);
    }
    while ((int)(rt32() - X) < 0) {}
}
constexpr int OOB = 0x70000000;
__device__ __forceinline__ void stage_rows8(const u16* src, lptr ml, int tid) {
    for (int c = tid; c < 576; c += 512) {
        const u32* p = (const u32*)(src + (c & 7) * DM + 16 * (c >> 4) + 2 * ((c >> 3) & 1));
        v4u v = {p[0], p[2], p[4], p[6]};
        *(LAS v4u*)(ml + c * 16) = v;
    }
}
__device__ __forceinline__ void gather_mem(rsrc_t rw, int nb, lptr ml, int lane, int w, lptr tkp, int tag, int& budget) {
    const int gi = w == 7 ? 5 : w;
    const int l0 = gi * 1536 + lane * 16, l1 = l0 + 1024;
    const int o0 = l0 < 8192 ? l0 : l0 + (N_MEMB - 8192), o1 = lane < 32 ? (l1 < 8192 ? l1 : l1 + (N_MEMB - 8192)) : OOB;
    lptr p1 = ml + (lane < 32 ? l1 : 14336 + lane * 16);
    asm volatile("" : "+s"(nb));
    tk_wait(tkp, tag, budget);
    v4u v0 = LD16(rw, o0, nb), v1 = LD16(rw, o1, nb);
    MB();
    *(LAS v4u*)(ml + l0) = v0; *(LAS v4u*)p1 = v1;
    MB();
    if (__builtin_expect(!__all(max(bad4(v0), bad4(v1)) != 0xFFFFFFFFu), 0)) {
        do {
            v0 = LD16(rw, o0, nb); v1 = LD16(rw, o1, nb);
            MB();
        } while (!__all(max(bad4(v0), bad4(v1)) != 0xFFFFFFFFu) && --budget >= 0);
        *(LAS v4u*)(ml + l0) = v0; *(LAS v4u*)p1 = v1;
    }
}
__device__ __forceinline__ void gather_mem4(rsrc_t rw, int nb, lptr ml, int lane, int gi, lptr tkp, int tag, int& budget) {
    const int l0 = gi * 2048 + lane * 16;
    const int o2 = lane < 16 ? N_MEMB + gi * 256 + lane * 16 : OOB;
    lptr p2 = ml + (lane < 16 ? 8192 + gi * 256 + lane * 16 : 14336 + lane * 16);
    asm volatile("" : "+s"(nb));
    tk_wait(tkp, tag, budget);
    v4u v0 = LD16(rw, l0, nb), v1 = LD16(rw, l0 + 1024, nb), v2 = LD16(rw, o2, nb);
    MB();
    *(LAS v4u*)(ml + l0) = v0; *(LAS v4u*)(ml + l0 + 1024) = v1; *(LAS v4u*)p2 = v2;
    MB();
    if (__builtin_expect(!__all(max(max(bad4(v0), bad4(v1)), bad4(v2)) != 0xFFFFFFFFu), 0)) {
        do {
            v0 = LD16(rw, l0, nb); v1 = LD16(rw, l0 + 1024, nb); v2 = LD16(rw, o2, nb);
            MB();
        } while (!__all(max(max(bad4(v0), bad4(v1)), bad4(v2)) != 0xFFFFFFFFu) && --budget >= 0);
        *(LAS v4u*)(ml + l0) = v0; *(LAS v4u*)(ml + l0 + 1024) = v1; *(LAS v4u*)p2 = v2;
    }
}
__device__ __forceinline__ int get_parity(rsrc_t rw, int lane, int& budget) {
    const int vo = (FL_PAR + (lane & 7)) * 128;
    u32 v;
    for (;;) {
        v = LD4(rw, vo, 0);
        MB();
        if (__all(v != 0xFFFFFFFFu) || --budget < 0) break;
        __builtin_amdgcn_s_sleep(8);
    }
    int pm = 0;
#pragma unroll
    for (int k = 0; k < 8; ++k) pm |= (__builtin_amdgcn_readlane((int)v, k) & 3) << (2 * k);
    return __builtin_popcount(pm) > 8 ? 0xFFFF : 0;
}
__device__ __forceinline__ void role_probe(rsrc_t rw) {
    const int lane = threadIdx.x & 63, w = __builtin_amdgcn_readfirstlane(threadIdx.x >> 6);
    const int o = OFF_RING + 2 * w * 65536 + N_PROBE;
    ST4(0xFFFFFFFFu, rw, lane < 6 ? o + (lane & 1) * 8192 + (lane >> 1) * 128 : OOB);
    DRAIN();
    int best[2] = {1 << 30, 1 << 30};
#pragma unroll
    for (int k = 0; k < 6; ++k) {
        const unsigned long long t0 = __builtin_amdgcn_s_memtime();
        asm volatile("s_waitcnt lgkmcnt(0)" ::: "memory");
        u32 v = LD4(rw, o + (k & 1) * 8192 + (k >> 1) * 128, 0);
        asm volatile("s_waitcnt vmcnt(0)" : "+v"(v) :: "memory");
        const unsigned long long t1 = __builtin_amdgcn_s_memtime();
        best[k & 1] = min(best[k & 1], (int)(t1 - t0));
    }
    if (lane == 0) ST4(best[1] < best[0] ? 3u : 0u, rw, (FL_PAR + w) * 128);
}
template <typename F>
__device__ __forceinline__ void qk_project_h(const bf16x16 (&Bq)[9], const bf16x16 (&Bk)[9], lptr ma, lptr xw, int idx, F hook) {
    f32x4 aq = {}, ak = {};
    bf16x8 A[9];
#pragma unroll
    for (int t = 0; t < 9; ++t) A[t] = lds8(ma + t * 1024);
    __builtin_amdgcn_sched_barrier(0);
#pragma unroll
    for (int t = 0; t < 9; ++t) {
        aq = SMF(A[t], Bq[t], aq, idx); ak = SMF(A[t], Bk[t], ak, idx);
        hook(t);
        __builtin_amdgcn_sched_barrier(0);
    }
    const float s0 = swap32_sum(aq[0], ak[0]), s1 = swap32_sum(aq[1], ak[1]), s2 = swap32_sum(aq[2], ak[2]), s3 = swap32_sum(aq[3], ak[3]);
    v2u o = {pack2(s0, s1), pack2(s2, s3)};
    *(LAS v2u*)xw = o;
}
__device__ __forceinline__ void qk_project(const bf16x16 (&Bq)[9], const bf16x16 (&Bk)[9], lptr ma, lptr xw, int idx) {
    qk_project_h(Bq, Bk, ma, xw, idx, [](int) {});
}

__device__ __forceinline__ void role_qk(const Args& a, lptr lds, rsrc_t rw, int h, int rep) {
    const int tid = threadIdx.x, lane = tid & 63, w = __builtin_amdgcn_readfirstlane(tid >> 6);
    bf16x16 Bq[9], Bk[9];
    {
        v4u xr[6];
#pragma unroll
        for (int j = 0; j < 6; ++j) {
            const int i = min(tid + 512 * j, 2879), row = i / 72, c = i - row * 72;
            xr[j] = *(const v4u*)((row < 8 ? a.mem0 + row * DM : a.u + (row - 8) * DM) + c * 8);
        }
        v4u v[6][6];
#pragma unroll
        for (int c = 0; c < 6; ++c) rows_ld<192, 16>(v[c], (c >= 3 ? a.Wk : a.Wq) + (size_t)(c % 3) * 192 * 4096 + h * DH, 4096, tid);
        __builtin_amdgcn_sched_barrier(0);
#pragma unroll
        for (int j = 0; j < 6; ++j) {
            const int i = min(tid + 512 * j, 2879), row = i / 72, c = i - row * 72, G = row >> 3;
            lptr d = lds + (G < 2 ? 57344 + G * MEMB : 135168 + (G - 2) * MEMB) + (c >> 1) * 256 + (row & 7) * 16 + (c & 1) * 8;
            *(LAS v2u*)d = v2u{xr[j].x, xr[j].z}; *(LAS v2u*)(d + 128) = v2u{xr[j].y, xr[j].w};
        }
#pragma unroll
        for (int c = 0; c < 6; ++c) {
            lptr l = lds + (c & 1) * 77824;
            rows_st<192, 16>(v[c], l, 288, tid);
            LBAR();
#pragma unroll
            for (int t = 0; t < 3; ++t) {
                bf16x16 f = cat16(frag16(l + (64 * t) * 288 + w * 32, 288, lane), frag16(l + (64 * t + 32) * 288 + w * 32, 288, lane));
                if (c >= 3) Bk[(c % 3) * 3 + t] = f; else Bq[c * 3 + t] = f;
            }
            __builtin_amdgcn_sched_barrier(0);
        }
        LBAR();
    }
    lptr ml = lds, xl = lds + MEMB;
    const int idx = (lane & 8) ? 0xEEEE : 0x4444;
    const int i16 = lane & 15, g = lane >> 4;
    const bool hi8 = (lane & 8) != 0;
    int budget = SPIN_MAX;
    lptr pq = xl + (8 * g + (i16 >> 2)) * 32 + (i16 & 1) * 8;
    lptr kl = lds + 16384 + lane * 16;
    lptr ma = ml + lane * 16, xw = xl + (16 * w + i16) * 32 + g * 8;
    const int ox = N_PX + (h >> 4) * 16384 + (h & 15) * 512 + ((g & 1) * 16 + (g >> 1) * 8 + (i16 & 7)) * 16;
    const int om = hi8 ? OOB : N_PMM + h * 128 + (i16 & 7) * 16;
    const int ob = lane < NREP ? BCO(BC_QK + h) : OOB;
    lptr tkp = lds + MEMB + 4096, kfp = tkp + 64;
    if (tid == 0) { tk_post(tkp, 0, rt32() - 100000u, 0); tk_post(kfp, 0, 0u, 0); }
    for (int c = tid; c < 576; c += 512) *(LAS v4u*)(ml + c * 16) = *(LAS v4u*)(lds + 57344 + c * 16);
    __syncthreads();
#pragma unroll 1
    for (int G = 0; G < 4; ++G) {
        qk_project(Bq, Bk, lds + (G ? 135168 + (G - 1) * MEMB : 57344 + MEMB) + lane * 16, xw, idx);
        LBAR();
        if (w < 4) {
            const bf16x8 F = as8(trd(pq + w * 1024 + 16), trd(pq + w * 1024 + 16 + 128));
            if ((i16 >> 3) == (G & 1)) *(LAS bf16x8*)(kl + ((G >> 1) * 4 + w) * 1024) = F;
        }
        LBAR();
    }
    const int pm = get_parity(rw, lane, budget);
    LBAR();
    if (w == 0) {
        const float c2 = 0.08838834764831845f * 1.4426950408889634f;
        for (int s = 0; s < NSEG - 1; ++s) {
            tk_get(kfp, s, budget);
            bf16x8 kx[2][4];
#pragma unroll
            for (int tl = 0; tl < 2; ++tl)
#pragma unroll
                for (int ks = 0; ks < 4; ++ks) kx[tl][ks] = lds8(kl + (s & 1) * 8192 + (tl * 4 + ks) * 1024);
            int oxs = ox + NEARB(s, i16 >> 3, pm), oxt = ox + NEARB(s, 2 + (i16 >> 3), pm), oms = om + NEARB(s, g, pm), obs = ob + FARB(s, lane & 3, pm);
            const int nb = NEARB(s, rep, pm);
            asm volatile("s_waitcnt lgkmcnt(0)" : "+v"(oxs), "+v"(oxt), "+v"(oms), "+v"(obs) :: "memory");
#pragma unroll
            for (int tl = 0; tl < 2; ++tl)
#pragma unroll
                for (int ks = 0; ks < 4; ++ks) PIN4(kx[tl][ks]);
            if (s > 0) {
                gather_mem(rw, nb, ml, lane, 0, tkp, s, budget);
            }
            LBAR();
            qk_project(Bq, Bk, ma, xw, idx);
            __builtin_amdgcn_s_setprio(3);
            LBAR();
            bf16x8 Fq[4], Fk[4];
#pragma unroll
            for (int ks = 0; ks < 4; ++ks) Fq[ks] = as8(trd(pq + ks * 1024), trd(pq + ks * 1024 + 128));
            __builtin_amdgcn_sched_barrier(0);
#pragma unroll
            for (int ks = 0; ks < 4; ++ks) Fk[ks] = as8(trd(pq + ks * 1024 + 16), trd(pq + ks * 1024 + 16 + 128));
            __builtin_amdgcn_sched_barrier(0);
            f32x4 S0 = {}, S1 = {}, Sm = {};
            S0 = MFMA16(kx[0][0], Fq[0], S0); S1 = MFMA16(kx[1][0], Fq[0], S1);
            S0 = MFMA16(kx[0][1], Fq[1], S0); S1 = MFMA16(kx[1][1], Fq[1], S1);
            __builtin_amdgcn_sched_barrier(0);
            Sm = MFMA16(Fk[0], Fq[0], Sm); S0 = MFMA16(kx[0][2], Fq[2], S0);
            Sm = MFMA16(Fk[1], Fq[1], Sm); S1 = MFMA16(kx[1][2], Fq[2], S1);
            Sm = MFMA16(Fk[2], Fq[2], Sm); S0 = MFMA16(kx[0][3], Fq[3], S0);
            Sm = MFMA16(Fk[3], Fq[3], Sm); S1 = MFMA16(kx[1][3], Fq[3], S1);
            __builtin_amdgcn_sched_barrier(0);
            float y0 = hi8 ? Sm[2] : Sm[0], y1 = hi8 ? Sm[3] : Sm[1];
            float x0 = hi8 ? S1[0] : S0[0], x1 = hi8 ? S1[1] : S0[1], x2 = hi8 ? S1[2] : S0[2], x3 = hi8 ? S1[3] : S0[3];
            float m = vmax3(vmax3(y0, y1, x0), x1, vmax(x2, x3));
            m = vmax(m, ror8f(m));
            { auto r = __builtin_amdgcn_permlane16_swap(__float_as_uint(m), __float_as_uint(m), false, false); m = vmax(__uint_as_float(r[0]), __uint_as_float(r[1])); }
            { auto r = __builtin_amdgcn_permlane32_swap(__float_as_uint(m), __float_as_uint(m), false, false); m = vmax(__uint_as_float(r[0]), __uint_as_float(r[1])); }
            const float nm = -m * c2;
            x0 = __builtin_amdgcn_exp2f(fmaf(x0, c2, nm)); x1 = __builtin_amdgcn_exp2f(fmaf(x1, c2, nm)); x2 = __builtin_amdgcn_exp2f(fmaf(x2, c2, nm)); x3 = __builtin_amdgcn_exp2f(fmaf(x3, c2, nm));
            y0 = __builtin_amdgcn_exp2f(fmaf(y0, c2, nm)); y1 = __builtin_amdgcn_exp2f(fmaf(y1, c2, nm));
            float sum = fmaf(0.5f, y0 + y1, (x0 + x1) + (x2 + x3));
            sum += ror8f(sum);
            sum = xsum(sum);
            const float inv = __builtin_amdgcn_rcpf(sum);
            const u32 pa = pack2(x0 * inv, x1 * inv) & 0x7fff7fffu, pb = pack2(x2 * inv, x3 * inv) & 0x7fff7fffu, pc = pack2(y0 * inv, y1 * inv) & 0x7fff7fffu;
            const u32 qa = ror8(pa), qb = ror8(pb), qc = ror8(pc);
            v4u px = {hi8 ? qa : pa, hi8 ? qb : pb, hi8 ? pa : qa, hi8 ? pb : qb};
            ST16(px, rw, oxs);
            ST16(px, rw, oxt);
            auto ra = __builtin_amdgcn_permlane16_swap(pc, pc, false, false);
            auto rc = __builtin_amdgcn_permlane16_swap(qc, qc, false, false);
            v4u pmv = {ra[0], rc[0], ra[1], rc[1]};
            ST16(pmv, rw, oms);
            __builtin_amdgcn_sched_barrier(0);
            ST4(nos(rt32()), rw, obs);
            __builtin_amdgcn_s_setprio(0);
        }
        {
            gather_mem(rw, NEARB(NSEG - 1, rep, pm), ml, lane, 0, tkp, NSEG - 1, budget);
            LBAR();
            qk_project(Bq, Bk, ma, xw, idx);
            LBAR();
        }
    } else if (w == 5) {
        int kdone = -1;
        const int kxo = OFF_KX + h * (NSEG * 8192) + g * 512 + i16 * 16;
        for (int s = 0; s < NSEG; ++s) {
            const bool need = s + 1 < NSEG - 1;
            if (need && kdone < s + 1) kdone = wait_flags(rw, FL_K + (h >> 1), 1, 1, -1, lane, budget);
            const bool go = need && kdone >= s + 1;
            int ko = (go ? kxo : OOB) + (s + 1) * 8192;
            int a0 = ox + NEARB(s + 2, i16 >> 3, pm), a1 = ox + NEARB(s + 2, 2 + (i16 >> 3), pm), a2 = om + NEARB(s + 2, g, pm), a3 = ob + FARB(s + 4, lane & 3, pm);
            v4u ff = {0xFFFFFFFFu, 0xFFFFFFFFu, 0xFFFFFFFFu, 0xFFFFFFFFu};
            asm volatile("" : "+v"(ko), "+v"(a0), "+v"(a1), "+v"(a2), "+v"(a3), "+v"(ff));
            LBAR();
            v4u kx[8];
            qk_project_h(Bq, Bk, ma, xw, idx, [&](int t) {
                if (t < 4) { kx[t] = LD16(rw, ko + (t & 1) * 2048, (t >> 1) * 4096); kx[4 + t] = LD16(rw, ko + 256 + (t & 1) * 2048, (t >> 1) * 4096); }
                if (t == 4) { ST16(ff, rw, a0); ST16(ff, rw, a1); }
                if (t == 5) { ST16(ff, rw, a2); ST4(0xFFFFFFFFu, rw, a3); }
            });
            LBAR();
            if (need) {
                if (!go) {
                    kdone = wait_flags(rw, FL_K + (h >> 1), 1, 1, s + 1, lane, budget, true);
#pragma unroll
                    for (int k = 0; k < 8; ++k) kx[k] = LD16(rw, kxo + (k >> 2) * 256 + (k & 3) * 2048, (s + 1) * 8192);
                }
#pragma unroll
                for (int k = 0; k < 8; ++k) *(LAS v4u*)(kl + ((s + 1) & 1) * 8192 + k * 1024) = kx[k];
                asm volatile("s_waitcnt lgkmcnt(0)" ::: "memory");
                tk_post(kfp, s + 1, 0u, lane);
            }
            DRAIN();
        }
    } else {
        TK tk; tk_init(tk);
        const int kmo = i16 < 8 ? OFF_KM + h * 2048 + g * 128 + i16 * 16 : OOB;
        for (int s = 0; s < NSEG; ++s) {
            if (s > 0 && w != 6) gather_mem(rw, NEARB(s, rep, pm), ml, lane, w, tkp, s, budget);
            BC2 bc = {0u, 0u};
            if (w == 3) bc = beacon_issue(rw, FARB(s, rep, pm), lane);
            LBAR();
            qk_project(Bq, Bk, ma, xw, idx);
            LBAR();
            if (w == 6) {
                bf16x8 F[4];
#pragma unroll
                for (int ks = 0; ks < 4; ++ks) F[ks] = as8(trd(pq + ks * 1024 + 16), trd(pq + ks * 1024 + 16 + 128));
#pragma unroll
                for (int ks = 0; ks < 4; ++ks) ST16(asu(F[ks]), rw, kmo + s * (NH * 2048) + ks * 512);
                DRAIN();
                if (lane == 0) ST4((u32)s, rw, (FL_M + h) * 128);
            } else if (w == 3) {
                if (s < NSEG - 1) {
                    if (s > 0) { tk_T0(tk, beacon_finish(bc, rw, FARB(s, rep, pm), lane, budget), a); if (tk.okA) tk_post(tkp, s + 1, tk_specA(tk, a), lane); }
                    wait_until(tk.XR + 8);
                    tk_Tp(tk, beacon_max<NH>(rw, FARB(s, rep, pm), BC_QK, lane, budget), a);
                    tk_post(tkp, s + 1, tk.XA, lane);
                }
            } else __builtin_amdgcn_s_sleep(14);
        }
    }
    if (budget < 0 && lane == 0) ST4(1u, rw, FL_ERR * 128);
}

__device__ __forceinline__ void role_wvo(const Args& a, lptr lds, rsrc_t rw, int idx) {
    const int tid = threadIdx.x, lane = tid & 63, w = __builtin_amdgcn_readfirstlane(tid >> 6);
    const int h = idx / 3, part = idx - 3 * h;
    bf16x8 Bo[5][4];
    v4u v[6], o[2][9];
#pragma unroll
    for (int c = 0; c < 2; ++c) rows_ld<64, 72>(o[c], a.Wo + ((size_t)h * DH + c * 64) * DM, DM, tid);
    rows_ld<192, 16>(v, a.Wv + (size_t)part * 192 * 4096 + h * DH, 4096, tid);
    __builtin_amdgcn_sched_barrier(0);
#pragma unroll
    for (int c = 0; c < 2; ++c) {
        lptr l = lds + c * 77824;
        rows_st<64, 72>(o[c], l, 1184, tid);
        LBAR();
#pragma unroll
        for (int j = 0; j < 5; ++j) {
            const int r = (w + 8 * j < NR) ? w + 8 * j : w;
#pragma unroll
            for (int ks = 0; ks < 2; ++ks) Bo[j][c * 2 + ks] = frag16(l + ks * 32 * 1184 + r * 32, 1184, lane);
        }
        __builtin_amdgcn_sched_barrier(0);
    }
    rows_st<192, 16>(v, lds, VS, tid);
    LBAR();
    const int rho = lane & 15, bg = lane >> 4;
    const int wo0 = OFF_WVO + (2 * w + ((rho >> 1) & 1)) * 294912 + ((h >> 1) * 18 + part * 6) * 1024 + (bg * 16 + (h & 1) * 8 + 2 * (rho >> 2) + (rho & 1)) * 16;
#pragma unroll 1
    for (int kb = 0; kb < 6; ++kb) {
        bf16x8 Af[2][4];
#pragma unroll
        for (int mt = 0; mt < 2; ++mt)
#pragma unroll
            for (int ks = 0; ks < 4; ++ks) Af[mt][ks] = lds8(lds + (32 * kb + 8 * (rho >> 2) + 4 * mt + (rho & 3)) * VS + ks * 64 + bg * 16);
#pragma unroll
        for (int j = 0; j < 5; ++j) {
            if (j < 4 || w < 4) {
                f32x4 d0 = {}, d1 = {};
#pragma unroll
                for (int ks = 0; ks < 4; ++ks) { d0 = MFMA16(Af[0][ks], Bo[j][ks], d0); d1 = MFMA16(Af[1][ks], Bo[j][ks], d1); }
                v4u q = {pack2(d0[0], d0[1]), pack2(d0[2], d0[3]), pack2(d1[0], d1[1]), pack2(d1[2], d1[3])};
                ST16(q, rw, wo0 + j * (16 * 294912) + kb * 1024);
            }
        }
        if (kb == 2 || kb == 4) {
            asm volatile("s_waitcnt vmcnt(4)" ::: "memory");
            LBAR();
            if (tid == 0) ST4((u32)(kb >> 1), rw, (FL_W + idx) * 128);
        }
    }
    DRAIN();
    LBAR();
    if (tid == 0) ST4(3u, rw, (FL_W + idx) * 128);
}

__device__ __forceinline__ void role_rz(const Args& a, lptr lds, rsrc_t rw, int r, int rep) {
    const int tid = threadIdx.x, lane = tid & 63, w = __builtin_amdgcn_readfirstlane(tid >> 6);
    const int idx = (lane & 8) ? 0xEEEE : 0x4444;
    const int i16 = lane & 15, g = lane >> 4;
    int budget = SPIN_MAX;
    v4u m0[2];
#pragma unroll
    for (int j = 0; j < 2; ++j) { const int i = min(tid + 512 * j, 575), row = i / 72, c = i - row * 72; m0[j] = *(const v4u*)(a.mem0 + row * DM + c * 8); }
    const int pm = get_parity(rw, lane, budget);
#pragma unroll
    for (int j = 0; j < 2; ++j) {
        const int i = min(tid + 512 * j, 575), row = i / 72, c = i - row * 72;
        lptr d = lds + (c >> 1) * 256 + row * 16 + (c & 1) * 8;
        *(LAS v2u*)d = v2u{m0[j].x, m0[j].z}; *(LAS v2u*)(d + 128) = v2u{m0[j].y, m0[j].w};
    }
    bf16x16 Ba[9], Bb[9];
    v4u t0[4];
    {
        const int wo = OFF_WVO + r * 294912 + 2 * w * 18432 + lane * 16;
        int have = 0;
#pragma unroll
        for (int p = 0; p < 3; ++p) {
            if (have < p + 1) have = wait_flags(rw, FL_W + 12 * w, 1, 12, p + 1, lane, budget, true);
            if (p == 2) {
                wait_flags(rw, FL_V, 1, 64, 0, lane, budget, true);
                const int vq = OFF_VOX + r * 16384 + w * 2048 + lane * 16;
#pragma unroll
                for (int k = 0; k < 4; ++k) t0[k] = LD16(rw, vq + (k & 1) * 1024, (k >> 1) * (NR * NH * 1024));
            }
#pragma unroll
            for (int q = 0; q < 3; ++q) {
                const int t = 3 * q + p;
                Ba[t] = cat16(as8(LD16(rw, wo, t * 2048)), as8(LD16(rw, wo + 1024, t * 2048)));
                Bb[t] = cat16(as8(LD16(rw, wo, 18432 + t * 2048)), as8(LD16(rw, wo + 1024, 18432 + t * 2048)));
            }
            MB();
        }
    }
    lptr ml = lds, zl = lds + MEMB;
    lptr tka = lds + 13312, tkr = tka + 16, cntp = tka + 64;
    volatile LAS int* prod = (volatile LAS int*)(lds + 13312 + 512);
    lptr ring = lds + 16384;
    lptr ma = ml + lane * 16;
    lptr zw = zl + ((2 * w + (lane >> 5)) >> 2) * 1024 + ((((2 * w + (lane >> 5)) & 3) * 16 + (g & 1) * 8 + (i16 & 7)) * 16) + (i16 >> 3) * 8;
    const bool hi8r = (lane & 8) != 0;
    const int mso = i16 < 8 ? (r < 64 ? N_MEMA + r * 128 : N_MEMB + (r - 64) * 128) + i16 * 16 : OOB;
    const int ob = lane < NREP ? BCO(r) : OOB;
    if (tid == 0) { tk_post(tka, 0, rt32() - 100000u, 0); tk_post(tkr, -1, 0u, 0); *(volatile LAS int*)cntp = 0; }
#pragma unroll
    for (int k = 0; k < 4; ++k) *(LAS v4u*)(ring + (k >> 1) * 16384 + w * 2048 + (k & 1) * 1024 + lane * 16) = t0[k];
    if (tid < 4) prod[tid] = 2;
    LBAR();
    if (w < 4) {
        const int po = N_PX + (w >> 1) * 16384 + (w & 1) * 4096 + lane * 16;
        const int mo = N_PMM + w * 1024 + 2 * g * 128 + (i16 & 7) * 16 + (i16 >> 3) * 8;
        lptr cna = lane == 0 ? cntp : cntp + 4 + lane * 4;
        const u32 one = lane == 0 ? 1u : 0u;
        const bool dv = (g >> 1) == (i16 >> 3);
        const int rwo = dv ? (4 * (g & 1) + 2 * (g >> 1)) * 128 + ((i16 & 7) >> 1) * 32 + w * 8 + (i16 & 1) * 4 : 2048 + lane * 4;
        const int msq = lane < 32 ? (r < 64 ? N_MEMA + r * 128 : N_MEMB + (r - 64) * 128) + (lane >> 2) * 16 : OOB;
        lptr zrd = zl + w * 1024 + lane * 16;
        for (int s = 0; s < NSEG; ++s) {
            lptr Rw = lds + 81920 + (s & 1) * 1024 + rwo, Rr = lds + 81920 + (s & 1) * 1024 + (lane & 31) * 32;
            int mss = msq + NEARB(s + 1, lane & 3, pm), obs = ob + FARB(s + 1, lane & 3, pm);
            int sb = NEARB(s, rep, pm);
            asm volatile("" : "+s"(sb));
            lptr rg = ring + (s & 3) * 16384 + w * 4096 + lane * 16;
            bf16x8 b[5];
            const bool early = __builtin_amdgcn_readfirstlane(prod[w >> 1]) > s;
            MB();
#pragma unroll
            for (int k = 0; k < 4; ++k) b[k] = lds8(rg + k * 1024);
            const u32 X = s > 0 ? tk_get(tkr, min(s, NSEG - 2), budget) : 0u;
            asm volatile("s_waitcnt lgkmcnt(0)" : "+v"(mss), "+v"(obs), "+v"(Rw), "+v"(Rr) :: "memory");
#pragma unroll
            for (int k = 0; k < 4; ++k) PIN4(b[k]);
            LBAR();
            qk_project(Ba, Bb, ma, zw, idx);
            LBAR();
            if (s == NSEG - 1) break;
            __builtin_amdgcn_s_setprio(3);
            b[4] = lds8(zrd);
            if (!early) {
                while (__builtin_amdgcn_readfirstlane(prod[w >> 1]) <= s && --budget >= 0) __builtin_amdgcn_s_sleep(1);
                MB();
#pragma unroll
                for (int k = 0; k < 4; ++k) b[k] = lds8(rg + k * 1024);
            }
            if (s > 0 && (int)(X - rt32()) < 1000) { while ((int)(rt32() - X) < 0) {} }
            f32x4 d;
            u32 bad;
            auto shot = [&]() {
                v4u p[4]; v2u m[2];
                m[0] = LD8(rw, mo, sb); m[1] = LD8(rw, mo + 128, sb);
                __builtin_amdgcn_sched_barrier(0);
#pragma unroll
                for (int k = 0; k < 4; ++k) { p[k] = LD16(rw, po + k * 1024, sb); __builtin_amdgcn_sched_barrier(0); }
                MB();
                d = f32x4{};
                d = MFMA16(as8(m[0], m[1]), b[4], d);
                bad = (m[0].x | m[0].y) | (m[1].x | m[1].y);
                __builtin_amdgcn_sched_barrier(0);
#pragma unroll
                for (int k = 0; k < 4; ++k) {
                    d = MFMA16(as8(p[k]), b[k], d);
                    bad |= (p[k].x | p[k].y) | (p[k].z | p[k].w);
                    __builtin_amdgcn_sched_barrier(0);
                }
            };
            shot();
            if (__builtin_expect(!__all((int)bad >= 0), 0)) {
                do shot(); while (!__all((int)bad >= 0) && --budget >= 0);
            }
            float e0, e1;
            {
                auto r0 = __builtin_amdgcn_permlane32_swap(__float_as_uint(d[0]), __float_as_uint(d[2]), false, false);
                auto r1 = __builtin_amdgcn_permlane32_swap(__float_as_uint(d[1]), __float_as_uint(d[3]), false, false);
                const float t0 = __uint_as_float(hi8r ? r0[1] : r0[0]), t1 = __uint_as_float(hi8r ? r1[1] : r1[0]);
                e0 = t0 + ror8f(t0); e1 = t1 + ror8f(t1);
            }
            f32x4 x0, x1; u32 old;
            asm volatile("ds_write2_b32 %4, %5, %6 offset1:32\n\tds_add_rtn_u32 %0, %7, %8\n\tds_read_b128 %1, %3\n\tds_read_b128 %2, %3 offset:16\n\ts_waitcnt lgkmcnt(0)"
                         : "=&v"(old), "=&v"(x0), "=&v"(x1) : "v"(Rr), "v"(Rw), "v"(e0), "v"(e1), "v"(cna), "v"(one) : "memory");
            {
                const u32 c = nos(pack2((x0[0] + x0[2]) + (x1[0] + x1[2]), (x0[1] + x0[3]) + (x1[1] + x1[3])));
                v4u o = {(u32)__builtin_amdgcn_update_dpp(0, (int)c, 0x00, 0xf, 0xf, true), (u32)__builtin_amdgcn_update_dpp(0, (int)c, 0x55, 0xf, 0xf, true),
                         (u32)__builtin_amdgcn_update_dpp(0, (int)c, 0xAA, 0xf, 0xf, true), (u32)__builtin_amdgcn_update_dpp(0, (int)c, 0xFF, 0xf, 0xf, true)};
                const bool last = (__builtin_amdgcn_readfirstlane(old) & 3) == 3;
                ST16(o, rw, last ? mss : OOB);
                __builtin_amdgcn_sched_barrier(0);
                if (last) ST4(nos(rt32()), rw, obs);
            }
            __builtin_amdgcn_s_setprio(0);
        }
    } else if (w == 5 || w == 6) {
        int vdone0 = -1, vdone1 = -1, staged = 2;
        const int hw = w - 5;
        const int vo = OFF_VOX + r * 16384 + hw * 8192 + lane * 16;
        const int zho = OFF_ZH + r * 4096 + hw * 2048 + lane * 16;
        lptr zhr = zl + hw * 2048 + (((lane >> 4) & 3) * 16 + (lane & 7)) * 16 + ((lane >> 3) & 1) * 8;
        const int aro = w == 5 ? mso : OOB, arb = w == 5 ? ob : OOB;
        v4u zv[2] = {};
        for (int s = 0; s < NSEG; ++s) {
            bool go = false;
            if (staged <= min(s + 3, NSEG - 2)) {
                const int ow = VOWN(staged);
                int& vd = ow ? vdone1 : vdone0;
                if (vd < staged) vd = wait_flags(rw, FL_V + ow, 2, NH, -1, lane, budget);
                go = vd >= staged;
            }
            const int vos = go ? vo : OOB;
            int va = vos + staged * (NR * NH * 1024), zs = s > 0 ? zho + (s - 1) * (NR * NH * 256) : OOB;
            int a0 = aro + NEARB(s + 3, g, pm), a1 = arb + FARB(s + 4, lane & 3, pm);
            v4u ff = {0xFFFFFFFFu, 0xFFFFFFFFu, 0xFFFFFFFFu, 0xFFFFFFFFu};
            asm volatile("" : "+v"(va), "+v"(zs), "+v"(a0), "+v"(a1), "+v"(ff));
            if (s > 0) gather_mem4(rw, NEARB(s, rep, pm), ml, lane, w - 4, tka, s, budget);
            LBAR();
            v4u v[8];
            qk_project_h(Ba, Bb, ma, zw, idx, [&](int t) {
                if (t < 4) { v[2 * t] = LD16(rw, va + (t & 1) * 2048, (t >> 1) * 4096); v[2 * t + 1] = LD16(rw, va + 1024 + (t & 1) * 2048, (t >> 1) * 4096); }
                if (t == 4) { ST16(zv[0], rw, zs); ST16(zv[1], rw, zs + 1024); }
                if (t == 5) { ST16(ff, rw, a0); ST4(0xFFFFFFFFu, rw, a1); }
            });
            LBAR();
#pragma unroll
            for (int k = 0; k < 2; ++k) {
                const v2u lo = *(LAS v2u*)(zhr + k * 1024), hi = *(LAS v2u*)(zhr + k * 1024 + 128);
                zv[k] = v4u{lo.x, lo.y, hi.x, hi.y};
            }
            for (;;) {
                if (go) {
#pragma unroll
                    for (int k = 0; k < 8; ++k) *(LAS v4u*)(ring + (staged & 3) * 16384 + hw * 8192 + k * 1024 + lane * 16) = v[k];
                    asm volatile("s_waitcnt lgkmcnt(0)" ::: "memory");
                    ++staged;
                    prod[hw] = staged;
                }
                if (staged > s + 1 || staged > NSEG - 2) break;
                const int ow = VOWN(staged);
                int& vd = ow ? vdone1 : vdone0;
                if (vd < staged) vd = wait_flags(rw, FL_V + ow, 2, NH, staged <= s ? staged : -1, lane, budget, true);
                if (vd < staged) break;
#pragma unroll
                for (int k = 0; k < 8; ++k) v[k] = LD16(rw, vo + (k & 3) * 1024, staged * (NR * NH * 1024) + (k >> 2) * 4096);
                go = true;
            }
            DRAIN();
        }
        {
            int l2 = (int)__builtin_amdgcn_mbcnt_hi(~0u, __builtin_amdgcn_mbcnt_lo(~0u, 0u));
            asm volatile("" : "+v"(l2));
            const int zf = OFF_ZH + r * 4096 + hw * 2048 + (NSEG - 1) * (NR * NH * 256) + l2 * 16;
            ST16(zv[0], rw, zf); ST16(zv[1], rw, zf + 1024);
        }
    } else {
        TK tk; tk_init(tk);
        for (int s = 0; s < NSEG; ++s) {
            if (s > 0) {
                gather_mem4(rw, NEARB(s, rep, pm), ml, lane, w - 4, tka, s, budget);
            }
            BC2 bc = {0u, 0u};
            if (w == 7) bc = beacon_issue(rw, FARB(s, rep, pm), lane);
            LBAR();
            qk_project(Ba, Bb, ma, zw, idx);
            LBAR();
            if (w == 7) {
                if (s < NSEG - 1) {
                    if (s > 0) { tk_T0(tk, beacon_finish(bc, rw, FARB(s, rep, pm), lane, budget), a); if (tk.okA) tk_post(tka, s + 1, tk_specA(tk, a), lane); }
                    tk_post(tkr, s, tk.XR, lane);
                    wait_until(tk.XR + 8);
                    tk_Tp(tk, beacon_max<NH>(rw, FARB(s, rep, pm), BC_QK, lane, budget), a);
                    tk_post(tka, s + 1, tk.XA, lane);
                    tk_post(tkr, s + 1, tk_specR(tk, a), lane);
                }
            } else {
                if (w == 4 && s >= 2 && lane == 0) ST4((u32)(s - 2), rw, (FL_Z + r) * 128);
                __builtin_amdgcn_s_sleep(14);
            }
        }
    }
    DRAIN();
    LBAR();
    if (tid == 0) ST4((u32)(NSEG - 1), rw, (FL_Z + r) * 128);
    if (budget < 0 && lane == 0) ST4(3u, rw, FL_ERR * 128);
}

__device__ __forceinline__ void cexp_l(float lr, float li, float l, float& er, float& ei) {
    const float mag = __builtin_amdgcn_exp2f(lr * l);
    const float ph = li * l;
    const float pe = fmaf(li, l, -ph);
    const float fr = (ph - floorf(ph)) + pe;
    er = mag * __builtin_amdgcn_cosf(fr);
    ei = mag * __builtin_amdgcn_sinf(fr);
}
constexpr int CV_W = 8 * 260 * 16, CV_U = 96 * 80, CV_CH = CV_W + CV_U;
__device__ __forceinline__ void s4_unit(const Args& a, lptr lds, rsrc_t rw, int unit) {
    const int tid = threadIdx.x, lane = tid & 63, w = __builtin_amdgcn_readfirstlane(tid >> 6);
    const int d0 = unit * 4;
    v2u uv[4];
#pragma unroll
    for (int k = 0; k < 4; ++k) uv[k] = *(const v2u*)(a.u + (size_t)(2 * (tid + 512 * (k >> 1)) + (k & 1)) * DM + d0);
    {
        const int c = tid >> 7, nn = tid & 127, n = (d0 + c) * 128 + nn;
        const float dt = rbf(expf(bf2f(a.logdt[d0 + c])));
        const float lr = dt * bf2f(a.Are[n]) * 1.4426950408889634f, li = dt * bf2f(a.Aim[n]) * 0.15915494309189535f;
        const float br = dt * bf2f(a.Bre[n]), bi = dt * bf2f(a.Bim[n]);
        const float cr = bf2f(a.Cre[n]), ci = bf2f(a.Cim[n]);
        LAS f32x2* T = (LAS f32x2*)(lds + c * CV_CH) + nn;
        float er, ei, pr, pi;
        cexp_l(lr, li, 1.0f, er, ei); pr = er; pi = ei;
#pragma unroll
        for (int o = 0; o < 7; ++o) { T[o * 130] = f32x2{pr, pi}; const float t = pr * er - pi * ei; pi = pr * ei + pi * er; pr = t; }
#pragma unroll
        for (int q4 = 0; q4 < 4; ++q4) {
            if (q4 == 0) cexp_l(lr, li, 128.0f, er, ei);
            cexp_l(lr, li, (float)(128 + 512 * q4), pr, pi);
#pragma unroll
            for (int o = 0; o < 4; ++o) { T[(7 + 4 * q4 + o) * 130] = f32x2{pr, pi}; const float t = pr * er - pi * ei; pi = pr * ei + pi * er; pr = t; }
        }
        const float cbr = cr * br - ci * bi, cbi = cr * bi + ci * br;
        cexp_l(lr, li, 8.0f, er, ei);
#pragma unroll
        for (int q4 = 0; q4 < 4; ++q4) {
            float sr, si;
            cexp_l(lr, li, (float)(32 * q4), sr, si);
            pr = cbr * sr - cbi * si; pi = cbr * si + cbi * sr;
#pragma unroll
            for (int o = 0; o < 4; ++o) { T[(23 + 4 * q4 + o) * 130] = f32x2{pr, pi}; const float t = pr * er - pi * ei; pi = pr * ei + pi * er; pr = t; }
        }
    }
    __syncthreads();
    f32x4 kr[3];
    {
        const int x = lane & 15, g = lane >> 4, c = w & 3, nh = w >> 2;
        const int ah0 = x >> 3, o0 = x & 7;
        const int ah1 = x < 8 ? 2 + (x >> 2) : 4 + ((x - 8) >> 1), o1 = x < 8 ? 2 * (x & 3) : 4 * (x & 1);
        const int ah2 = x < 8 ? 8 + x : 16;
        LAS f32x2* T = (LAS f32x2*)(lds + c * CV_CH) + nh * 64 + g;
        LAS f32x2* Ta0 = T + 7 * 130; LAS f32x2* To0 = T + (o0 ? o0 - 1 : 0) * 130;
        LAS f32x2* Ta1 = T + (6 + ah1) * 130; LAS f32x2* To1 = T + (o1 ? o1 - 1 : 0) * 130;
        LAS f32x2* Ta2 = T + (6 + ah2) * 130; LAS f32x2* Tv = T + (23 + x) * 130;
        const f32x2 one = {1.0f, 0.0f};
        f32x4 ar[3] = {}, ai[3] = {};
#pragma unroll
        for (int jj = 0; jj < 16; ++jj) {
            const f32x2 v = Tv[4 * jj], u2 = Ta2[4 * jj], a1 = Ta1[4 * jj];
            f32x2 a0v = Ta0[4 * jj], e0 = To0[4 * jj], e1 = To1[4 * jj];
            a0v = ah0 ? a0v : one; e0 = o0 ? e0 : one; e1 = o1 ? e1 : one;
            const float u0r = a0v[0] * e0[0] - a0v[1] * e0[1], u0i = a0v[0] * e0[1] + a0v[1] * e0[0];
            const float u1r = a1[0] * e1[0] - a1[1] * e1[1], u1i = a1[0] * e1[1] + a1[1] * e1[0];
            ar[2] = __builtin_amdgcn_mfma_f32_16x16x4f32(u2[0], v[0], ar[2], 0, 0, 0);
            ai[2] = __builtin_amdgcn_mfma_f32_16x16x4f32(u2[1], v[1], ai[2], 0, 0, 0);
            ar[0] = __builtin_amdgcn_mfma_f32_16x16x4f32(u0r, v[0], ar[0], 0, 0, 0);
            ai[0] = __builtin_amdgcn_mfma_f32_16x16x4f32(u0i, v[1], ai[0], 0, 0, 0);
            ar[1] = __builtin_amdgcn_mfma_f32_16x16x4f32(u1r, v[0], ar[1], 0, 0, 0);
            ai[1] = __builtin_amdgcn_mfma_f32_16x16x4f32(u1i, v[1], ai[1], 0, 0, 0);
        }
#pragma unroll
        for (int t = 0; t < 3; ++t) kr[t] = ar[t] - ai[t];
    }
    __syncthreads();
    {
        LAS float* T = (LAS float*)(lds + (w & 3) * CV_CH + 4096 + (w >> 2) * 3072);
#pragma unroll
        for (int t = 0; t < 3; ++t)
#pragma unroll
            for (int r = 0; r < 4; ++r) T[(16 * t + 4 * (lane >> 4) + r) * 16 + (lane & 15)] = kr[t][r];
    }
    __syncthreads();
#pragma unroll
    for (int k = 0; k < 8; ++k) {
        const int i = tid + 512 * k, c = i >> 10, l2 = i & 1023;
        LAS float* T = (LAS float*)(lds + c * CV_CH + 4096);
        float v[2];
#pragma unroll
        for (int e = 0; e < 2; ++e) {
            const int lb = (int)rbf((float)(2 * l2 + e));
            const int hi = lb >> 7, o = lb & 7;
            const int row = lb < 256 ? 8 * hi + o : lb < 512 ? 16 + 4 * (hi - 2) + (o >> 1) : lb < 1024 ? 24 + 2 * (hi - 4) + (o >> 2) : 32 + (hi - 8);
            const int ix = row * 16 + ((lb >> 3) & 15);
            v[e] = T[ix] + T[768 + ix];
        }
        *(LAS u32*)(lds + c * CV_CH + CV_W + 2704 + l2 * 4) = pack2(v[0], v[1]);
    }
    __syncthreads();
    for (int i = tid; i < 4 * 259; i += 512) {
        const int c = i / 259, zz = i - c * 259, z = zz - 3;
        lptr ch = lds + c * CV_CH;
        v4u lo = {0, 0, 0, 0}, hi = {0, 0, 0, 0};
        if (z >= 1) lo = *(LAS v4u*)(ch + CV_W + 2704 + (z - 1) * 16);
        if (z >= 0) hi = *(LAS v4u*)(ch + CV_W + 2704 + z * 16);
        const u32 e[8] = {lo.x, lo.y, lo.z, lo.w, hi.x, hi.y, hi.z, hi.w};
#pragma unroll
        for (int cc = 0; cc < 8; ++cc) {
            v4u o;
#pragma unroll
            for (int k = 0; k < 4; ++k) {
                const int t = 8 + cc - 2 * k;
                if (t & 1) o[k] = (e[t >> 1] >> 16) | (e[t >> 1] << 16);
                else o[k] = (e[t >> 1] & 0xffffu) | (e[(t >> 1) - 1] & 0xffff0000u);
            }
            *(LAS v4u*)(ch + (cc * 260 + zz) * 16) = o;
        }
    }
    __syncthreads();
    for (int i = tid; i < 4 * 160; i += 512) *(LAS v4u*)(lds + (i / 160) * CV_CH + CV_W + (i % 160) * 16) = v4u{0, 0, 0, 0};
#pragma unroll
    for (int k2 = 0; k2 < 2; ++k2) {
        const int l = 2 * (tid + 512 * k2);
        const int o = CV_W + (32 + (l >> 5)) * 80 + (l & 31) * 2;
        const v2u p = uv[2 * k2], q = uv[2 * k2 + 1];
        *(LAS u32*)(lds + 0 * CV_CH + o) = (p.x & 0xffffu) | (q.x << 16);
        *(LAS u32*)(lds + 1 * CV_CH + o) = (p.x >> 16) | (q.x & 0xffff0000u);
        *(LAS u32*)(lds + 2 * CV_CH + o) = (p.y & 0xffffu) | (q.y << 16);
        *(LAS u32*)(lds + 3 * CV_CH + o) = (p.y >> 16) | (q.y & 0xffff0000u);
    }
    __syncthreads();
    const int c = w & 3, par = w >> 2, b = lane & 31, hh = lane >> 5;
    lptr ch = lds + c * CV_CH;
    lptr wa = ch + ((b & 7) * 260 + (b >> 3) - hh + 3) * 16;
    lptr ub = ch + CV_W + (32 + b) * 80 + hh * 16;
    f32x16 y0 = {}, y1 = {};
    wa += par * 64; ub -= par * 80;
    {
        bf16x8 fa[2][2], fb[2][2], fc[2][2];
#pragma unroll
        for (int ks = 0; ks < 2; ++ks) { fa[0][ks] = lds8(wa - ks * 32); fb[0][ks] = lds8(ub + ks * 32); fc[0][ks] = lds8(ub + 2560 + ks * 32); }
#pragma unroll
        for (int i = 0; i < 32; ++i) {
            const int cur = i & 1, nx = cur ^ 1;
            if (i + 1 < 32) {
#pragma unroll
                for (int ks = 0; ks < 2; ++ks) {
                    fa[nx][ks] = lds8(wa + (i + 1) * 128 - ks * 32);
                    if (i + 1 < 16) fb[nx][ks] = lds8(ub - (i + 1) * 160 + ks * 32);
                    fc[nx][ks] = lds8(ub + 2560 - (i + 1) * 160 + ks * 32);
                }
            }
            __builtin_amdgcn_sched_barrier(0);
#pragma unroll
            for (int ks = 0; ks < 2; ++ks) {
                if (i < 16) y0 = MFMA32(fa[cur][ks], fb[cur][ks], y0);
                y1 = MFMA32(fa[cur][ks], fc[cur][ks], y1);
            }
            __builtin_amdgcn_sched_barrier(0);
        }
    }
    __syncthreads();
    {
        LAS f32x4* X = (LAS f32x4*)(ch + par * 4096);
        const f32x16 src = par ? y0 : y1;
#pragma unroll
        for (int k = 0; k < 4; ++k) X[k * 64 + lane] = f32x4{src[4 * k], src[4 * k + 1], src[4 * k + 2], src[4 * k + 3]};
    }
    __syncthreads();
    {
        LAS f32x4* X = (LAS f32x4*)(ch + (1 - par) * 4096);
        lptr Y = ch + 8192 + (32 * par + b) * 72 + hh * 8;
        const f32x16 own = par ? y1 : y0;
#pragma unroll
        for (int k = 0; k < 4; ++k) {
            const f32x4 o = X[k * 64 + lane];
            v2u yv = {pack2(own[4 * k] + o[0], own[4 * k + 1] + o[1]), pack2(own[4 * k + 2] + o[2], own[4 * k + 3] + o[3])};
            *(LAS v2u*)(Y + k * 16) = yv;
        }
    }
    __syncthreads();
    const v2u dk = *(const v2u*)(a.dskip + d0);
#pragma unroll
    for (int k2 = 0; k2 < 2; ++k2) {
        const int i = tid + 512 * k2;
        lptr yp = lds + 8192 + (i >> 4) * 72 + (i & 15) * 4;
        const u32 ya = *(LAS u32*)(yp), yb = *(LAS u32*)(yp + CV_CH), yc = *(LAS u32*)(yp + 2 * CV_CH), yd = *(LAS u32*)(yp + 3 * CV_CH);
        const v2u p = uv[2 * k2], q = uv[2 * k2 + 1];
        v4u o;
        o.x = pack2(bflo(ya) + rbf(bflo(p.x) * bflo(dk.x)), bflo(yb) + rbf(bfhi(p.x) * bfhi(dk.x)));
        o.y = pack2(bflo(yc) + rbf(bflo(p.y) * bflo(dk.y)), bflo(yd) + rbf(bfhi(p.y) * bfhi(dk.y)));
        o.z = pack2(bfhi(ya) + rbf(bflo(q.x) * bflo(dk.x)), bfhi(yb) + rbf(bfhi(q.x) * bfhi(dk.x)));
        o.w = pack2(bfhi(yc) + rbf(bflo(q.y) * bflo(dk.y)), bfhi(yd) + rbf(bfhi(q.y) * bfhi(dk.y)));
        ST16(o, rw, OFF_A4 + unit * 16384 + i * 16);
    }
    __syncthreads();
}
__device__ __forceinline__ void role_s4(const Args& a, lptr lds, rsrc_t rw, int idx) {
    if (idx < 32) {
        const int tid = threadIdx.x, t = idx * 64 + (tid >> 3), j = tid & 7;
        float acc = 0.f;
#pragma unroll
        for (int k = 0; k < 9; ++k) {
            const v4u xv = *(const v4u*)(a.u + (size_t)t * DM + (k * 8 + j) * 8), gv = *(const v4u*)(a.gatew + (k * 8 + j) * 8);
#pragma unroll
            for (int e = 0; e < 4; ++e) acc += rbf(bflo(xv[e]) * bflo(gv[e])) + rbf(bfhi(xv[e]) * bfhi(gv[e]));
        }
        acc += __shfl_xor(acc, 1); acc += __shfl_xor(acc, 2); acc += __shfl_xor(acc, 4);
        acc = rbf(acc);
        if (j == 0) ST4(__float_as_uint(rbf(1.0f / (1.0f + __expf(-acc)))), rw, OFF_G + t * 4);
    }
#pragma unroll 1
    for (int unit = idx; unit < DM / 4; unit += NS4) s4_unit(a, lds, rw, unit);
    DRAIN();
    __syncthreads();
    if (threadIdx.x == 0) ST4(1u, rw, (FL_S + idx) * 128);
}

__device__ __forceinline__ void role_post(const Args& a, lptr lds, rsrc_t rw) {
    const int tid = threadIdx.x, lane = tid & 63, w = __builtin_amdgcn_readfirstlane(tid >> 6);
    const int i16 = lane & 15, g = lane >> 4;
    int budget = SPIN_MAX, vdone0 = -1, vdone1 = -1;
    volatile LAS int* U = (volatile LAS int*)(lds + 81920);
    wait_flags(rw, FL_S, 1, 64, 1, lane, budget);
    wait_flags(rw, FL_S + 64, 1, NS4 - 64, 1, lane, budget);
    constexpr int NFULL = 62 * 6, NUNIT = NFULL + 24;
#pragma unroll 1
    for (;;) {
        if (tid == 0) *U = 1 + (int)__hip_atomic_fetch_add((u32*)(a.ws + FL_Q * 128), 1u, __ATOMIC_RELAXED, __HIP_MEMORY_SCOPE_AGENT);
        __syncthreads();
        const int unit = __builtin_amdgcn_readfirstlane(*U);
        if (unit >= NUNIT || budget < 0) break;
        const bool half = unit >= NFULL;
        const int uu = half ? NFULL + ((unit - NFULL) >> 1) : unit, q0 = half ? (unit - NFULL) & 1 : 0;
        const int s = uu / 6, r = (uu - s * 6) * 6 + (w < 6 ? w : 0);
        bf16x8 b[32];
        v2u av[2]; float gt[2];
        if (w < 6) {
            if (VOWN(s)) { if (vdone1 < s) vdone1 = wait_flags(rw, FL_V + 1, 2, NH, s, lane, budget); }
            else { if (vdone0 < s) vdone0 = wait_flags(rw, FL_V, 2, NH, s, lane, budget); }
            const int vo = OFF_VOX + (s * NR + r) * (NH * 1024) + ((i16 >> 1) & 1) * 16384 + ((g & 1) * 16 + (g >> 1) * 8 + 2 * (i16 >> 2) + (i16 & 1)) * 16;
#pragma unroll
            for (int k = 0; k < 32; ++k) b[k] = as8(LD16(rw, vo + (k & 7) * 512, (k >> 3) * 4096));
#pragma unroll
            for (int qt = 0; qt < 2; ++qt) {
                av[qt] = LD8(rw, OFF_A4 + (4 * r + g) * 16384 + (32 * s + 16 * (qt ^ q0) + i16) * 8, 0);
                gt[qt] = __uint_as_float(LD4(rw, OFF_G + (32 * s + 16 * (qt ^ q0) + i16) * 4, 0));
            }
        }
        v4u z[8];
        const int zo = OFF_ZH + (s * NR + r) * (NH * 256) + ((i16 >> 1) & 1) * 4096 + g * 128 + (2 * (i16 >> 2) + (i16 & 1)) * 16;
        if (half) {
            wait_flags(rw, FL_Z + 2 * r, 1, 2, s, lane, budget, true);
#pragma unroll
            for (int k = 0; k < 8; ++k) z[k] = LD16(rw, zo + k * 512, 0);
            wait_flags(rw, FL_P + (s & 3), 4, 16, s, lane, budget, true);
            v4u pv[5];
#pragma unroll
            for (int k = 0; k < 5; ++k) pv[k] = LD16(rw, OFF_PX + s * 81920 + q0 * 40960 + tid * 16, k * 8192);
#pragma unroll
            for (int k = 0; k < 5; ++k) *(LAS v4u*)(lds + k * 8192 + tid * 16) = pv[k];
        } else {
            wait_flags(rw, FL_P + (s & 3), 4, 16, s, lane, budget);
            v4u pv[10];
#pragma unroll
            for (int k = 0; k < 10; ++k) pv[k] = LD16(rw, OFF_PX + s * 81920 + tid * 16, k * 8192);
            wait_flags(rw, FL_Z + 2 * r, 1, 2, s, lane, budget);
#pragma unroll
            for (int k = 0; k < 8; ++k) z[k] = LD16(rw, zo + k * 512, 0);
            MB();
#pragma unroll
            for (int k = 0; k < 10; ++k) *(LAS v4u*)(lds + k * 8192 + tid * 16) = pv[k];
        }
        __syncthreads();
        if (w < 6) {
#pragma unroll 1
            for (int qt = 0; qt < (half ? 1 : 2); ++qt) {
                f32x4 d = {}, e = {};
                lptr pl = lds + qt * 40960 + lane * 16;
                bf16x8 f[6];
#pragma unroll
                for (int i = 0; i < 6; ++i) f[i] = lds8(pl + i * 1024);
                __builtin_amdgcn_sched_barrier(0);
#pragma unroll
                for (int k = 0; k < 40; ++k) {
                    const bf16x8 v = k < 32 ? b[k < 32 ? k : 0] : as8(z[k < 32 ? 0 : k - 32]);
                    if (k & 1) e = MFMA16(v, f[k % 6], e); else d = MFMA16(v, f[k % 6], d);
                    if (k + 6 < 40) f[k % 6] = lds8(pl + (k + 6) * 1024);
                    __builtin_amdgcn_sched_barrier(0);
                }
                d += e;
                const float gq = qt ? gt[1] : gt[0], og = rbf(1.0f - gq);
                const v2u aq = qt ? av[1] : av[0];
                const float r0 = rbf(gq * bflo(aq.x)) + rbf(og * rbf(d[0])), r1 = rbf(gq * bfhi(aq.x)) + rbf(og * rbf(d[1]));
                const float r2 = rbf(gq * bflo(aq.y)) + rbf(og * rbf(d[2])), r3 = rbf(gq * bfhi(aq.y)) + rbf(og * rbf(d[3]));
                v2u ov = {pack2(r0, r1), pack2(r2, r3)};
                *(v2u*)(a.out + (size_t)(32 * s + 16 * (qt ^ q0) + i16) * DM + 16 * r + 4 * g) = ov;
            }
        }
        if (half) break;
        LBAR();
    }
    if (budget < 0 && lane == 0) ST4(6u, rw, FL_ERR * 128);
}

__global__ void __launch_bounds__(512, 2) k_all(Args a) {
    extern __shared__ __attribute__((aligned(16))) char smem[];
    lptr lds = (lptr)smem;
    const rsrc_t rw = __builtin_amdgcn_make_buffer_rsrc(a.ws, 0, WS_BYTES, 0x00020000);
    if (threadIdx.x == 0) {
        const int x = (int)(__builtin_amdgcn_s_getreg(20 | (3 << 11)) & 7u);
        int role = 0;
        for (int k = 0; k < 8; ++k) {
            const int gq = (x + k) & 7;
            const int t = 1 + (int)__hip_atomic_fetch_add((u32*)(a.ws + (FL_T + gq) * 128), 1u, __ATOMIC_RELAXED, __HIP_MEMORY_SCOPE_AGENT);
            if (t < 32) { role = gq + 8 * t; break; }
        }
        *(volatile LAS int*)lds = role;
    }
    __syncthreads();
    const int b = __builtin_amdgcn_readfirstlane(*(volatile LAS int*)lds), xg = b & 7, slot = b >> 3;
    __syncthreads();

    if (b == 8 * 8) role_probe(rw);
    if (slot >= 20) { role_wvo(a, lds, rw, (xg & 3) * 24 + (xg < 4 ? 12 : 0) + slot - 20); __syncthreads(); }
    if ((xg < 4 && slot >= 26) || (xg >= 4 && slot >= 20)) {
        const int idx = xg < 4 ? 48 + (slot - 26) * 4 + xg : (31 - slot) * 4 + (xg - 4);
        role_s4(a, lds, rw, idx);
        __syncthreads();
    }
    bool post = false;
    if (xg < 4) {
        if (slot < 8) role_qk(a, lds, rw, xg * 8 + slot, xg);
        else if (slot < 26) role_rz(a, lds, rw, xg * 18 + slot - 8, xg);
    } else {
        const int j = xg - 4;
        post = true;
        if (slot < 16) { role_vvo(a, lds, rw, j * 8 + (slot >> 1), slot & 1); post = slot < NJOIN; }
        else if (slot < 20) role_k2(a, lds, rw, j * 4 + slot - 16);
        else if (slot < 28) { role_q2p(a, lds, rw, j * 4 + ((slot - 20) >> 1), slot & 1); post = false; }
    }
    if (post) { __syncthreads(); role_post(a, lds, rw); }
}

extern "C" void kernel_launch(void* const* d_in, const int* in_sizes, int n_in, void* d_out, int out_size, void* d_ws, size_t ws_size, hipStream_t stream) {
    Args a;
    a.u = (const u16*)d_in[0]; a.Are = (const u16*)d_in[1]; a.Aim = (const u16*)d_in[2]; a.Bre = (const u16*)d_in[3]; a.Bim = (const u16*)d_in[4];
    a.Cre = (const u16*)d_in[5]; a.Cim = (const u16*)d_in[6]; a.logdt = (const u16*)d_in[7]; a.dskip = (const u16*)d_in[8];
    a.mem0 = (const u16*)d_in[10]; a.Wq = (const u16*)d_in[11]; a.Wk = (const u16*)d_in[12]; a.Wv = (const u16*)d_in[13]; a.Wo = (const u16*)d_in[14];
    a.gatew = (const u16*)d_in[16]; a.out = (u16*)d_out; a.ws = (char*)d_ws;
    const int LDS_ALL = 160 * 1024;
    (void)hipFuncSetAttribute((const void*)k_all, hipFuncAttributeMaxDynamicSharedMemorySize, LDS_ALL);
    (void)hipMemsetAsync(d_ws, 0xFF, INIT_BYTES, stream);
    hipLaunchKernelGGL(k_all, dim3(256), dim3(512), LDS_ALL, stream, a);
}
```

```cpp
#include <hip/hip_runtime.h>
#include <hip/hip_bf16.h>
#include <stdint.h>

typedef unsigned short u16;
typedef unsigned int u32;
typedef u32 v4u __attribute__((ext_vector_type(4)));
typedef u32 v2u __attribute__((ext_vector_type(2)));
typedef float f32x4 __attribute__((ext_vector_type(4)));
typedef float f32x2 __attribute__((ext_vector_type(2)));
typedef float f32x16 __attribute__((ext_vector_type(16)));
typedef __bf16 bf16x2 __attribute__((ext_vector_type(2)));
typedef __bf16 bf16x8 __attribute__((ext_vector_type(8)));
typedef __bf16 bf16x16 __attribute__((ext_vector_type(16)));
typedef short s16x4 __attribute__((ext_vector_type(4)));
#define LAS __attribute__((address_space(3)))
typedef LAS char* lptr;
typedef __amdgpu_buffer_rsrc_t rsrc_t;

constexpr int SEQ = 2048, DM = 576, NH = 32, DH = 128, NSEG = 64, NR = 36, NR8 = 72, NREP = 4;
constexpr int MEMB = 9216, BC_QK = 80;
constexpr int FL_K = 0, FL_V = 16, FL_M = 80, FL_P = 112, FL_S = 176, FL_Q = 254, FL_ERR = 255, FL_Z = 256, FL_W = 328, FL_T = 424, FL_PAR = 432, NS4 = 72;
constexpr int OFF_RING = 65536;
constexpr int N_MEMA = 0, N_PX = 16384, N_PMM = 49152, N_MEMB = 49152 + 4096, N_PROBE = 49152 + 6144;
constexpr int INIT_BYTES = OFF_RING + 16 * 65536;
constexpr int OFF_ZH = INIT_BYTES;
constexpr int OFF_KX = OFF_ZH + NSEG * NR * NH * 256;
constexpr int OFF_VOX = OFF_KX + NH * SEQ * DH * 2;
constexpr int OFF_KM = OFF_VOX + NSEG * NR * NH * 1024;
constexpr int OFF_PX = OFF_KM + NSEG * NH * 2048;
constexpr int OFF_A4 = OFF_PX + NSEG * 81920;
constexpr int OFF_G = OFF_A4 + SEQ * DM * 2;
constexpr int OFF_WVO = OFF_G + SEQ * 4;
constexpr int WS_BYTES = OFF_WVO + NR8 * 294912;
constexpr int TK_LA = 10, TK_LR = 10, TK_TOL = 8, TK_GAP = 30;
constexpr int NJOIN = 4;

struct Args {
    const u16* u; const u16* Are; const u16* Aim; const u16* Bre; const u16* Bim; const u16* Cre; const u16* Cim;
    const u16* logdt; const u16* dskip; const u16* mem0; const u16* Wq; const u16* Wk; const u16* Wv; const u16* Wo;
    const u16* gatew; u16* out; char* ws;
};

__device__ __forceinline__ float bf2f(u16 v) { return __uint_as_float((u32)v << 16); }
__device__ __forceinline__ float bflo(u32 v) { return __uint_as_float(v << 16); }
__device__ __forceinline__ float bfhi(u32 v) { return __uint_as_float(v & 0xffff0000u); }
__device__ __forceinline__ u32 pack2(float lo, float hi) { f32x2 f = {lo, hi}; bf16x2 b = __builtin_convertvector(f, bf16x2); return __builtin_bit_cast(u32, b); }
__device__ __forceinline__ float rbf(float x) { return bflo(pack2(x, x)); }
__device__ __forceinline__ u32 nos(u32 v) { return min(v, 0xFFFFFFFEu); }
__device__ __forceinline__ v2u trd(lptr p) { return __builtin_bit_cast(v2u, __builtin_amdgcn_ds_read_tr16_b64_v4i16((LAS s16x4*)p)); }
__device__ __forceinline__ bf16x8 as8(v4u v) { return __builtin_bit_cast(bf16x8, v); }
__device__ __forceinline__ bf16x8 as8(v2u a, v2u b) { v4u v = {a.x, a.y, b.x, b.y}; return __builtin_bit_cast(bf16x8, v); }
__device__ __forceinline__ v4u asu(bf16x8 v) { return __builtin_bit_cast(v4u, v); }
__device__ __forceinline__ bf16x16 cat16(bf16x8 a, bf16x8 b) { return __builtin_shufflevector(a, b, 0, 1, 2, 3, 4, 5, 6, 7, 8, 9, 10, 11, 12, 13, 14, 15); }
__device__ __forceinline__ bf16x8 lds8(lptr p) { return *(LAS bf16x8*)p; }
__device__ __forceinline__ u32 bad4(v4u v) { return max(max(v.x, v.y), max(v.z, v.w)); }
#define NEARB(s, rep, pm) (OFF_RING + ((((s) & 3) * 4 + (rep)) << 16) + ((((pm) >> (((s) & 3) * 4 + (rep))) & 1) << 13))
#define FARB(s, rep, pm) ((NEARB(s, rep, pm) ^ 8192) + (((s) & 4) << 13))
#define BCO(slot) (((slot) & 63) * 128 + ((slot) >> 6) * 16384)
#define LBAR() do { asm volatile("s_waitcnt lgkmcnt(0)" ::: "memory"); __builtin_amdgcn_s_barrier(); asm volatile("" ::: "memory"); } while (0)
#define MB() asm volatile("" ::: "memory")
#define DRAIN() asm volatile("s_waitcnt vmcnt(0)" ::: "memory")
#define PIN4(x) asm volatile("" : "+v"(x))
#define MFMA16(a, b, c) __builtin_amdgcn_mfma_f32_16x16x32_bf16((a), (b), (c), 0, 0, 0)
#define MFMA32(a, b, c) __builtin_amdgcn_mfma_f32_32x32x16_bf16((a), (b), (c), 0, 0, 0)
#define SMF(a, b, c, idx) __builtin_amdgcn_smfmac_f32_16x16x64_bf16((a), (b), (c), (idx), 0, 0)
#define LD16(r, vo, so) __builtin_amdgcn_raw_buffer_load_b128((r), (vo), (so), 16)
#define LD8(r, vo, so) __builtin_amdgcn_raw_buffer_load_b64((r), (vo), (so), 16)
#define LD4(r, vo, so) __builtin_amdgcn_raw_buffer_load_b32((r), (vo), (so), 16)
#define ST16(v, r, vo) __builtin_amdgcn_raw_buffer_store_b128((v), (r), (vo), 0, 16)
#define ST8(v, r, vo) __builtin_amdgcn_raw_buffer_store_b64((v), (r), (vo), 0, 16)
#define ST4(v, r, vo) __builtin_amdgcn_raw_buffer_store_b32((v), (r), (vo), 0, 16)

__device__ __forceinline__ float swap32_sum(float a, float b) {
    auto r = __builtin_amdgcn_permlane32_swap(__float_as_uint(a), __float_as_uint(b), false, false);
    return __uint_as_float(r[0]) + __uint_as_float(r[1]); }
__device__ __forceinline__ float xmax(float m) {
    auto r = __builtin_amdgcn_permlane16_swap(__float_as_uint(m), __float_as_uint(m), false, false);
    m = fmaxf(__uint_as_float(r[0]), __uint_as_float(r[1]));
    auto q = __builtin_amdgcn_permlane32_swap(__float_as_uint(m), __float_as_uint(m), false, false);
    return fmaxf(__uint_as_float(q[0]), __uint_as_float(q[1])); }
__device__ __forceinline__ float xsum(float m) {
    auto r = __builtin_amdgcn_permlane16_swap(__float_as_uint(m), __float_as_uint(m), false, false);
    m = __uint_as_float(r[0]) + __uint_as_float(r[1]);
    auto q = __builtin_amdgcn_permlane32_swap(__float_as_uint(m), __float_as_uint(m), false, false);
    return __uint_as_float(q[0]) + __uint_as_float(q[1]); }
__device__ __forceinline__ float vmax(float a, float b) { float r; asm("v_max_f32 %0, %1, %2" : "=v"(r) : "v"(a), "v"(b)); return r; }
__device__ __forceinline__ float vmax3(float a, float b, float c) { float r; asm("v_max3_f32 %0, %1, %2, %3" : "=v"(r) : "v"(a), "v"(b), "v"(c)); return r; }
__device__ __forceinline__ u32 ror8(u32 v) { return (u32)__builtin_amdgcn_update_dpp(0, (int)v, 0x128, 0xf, 0xf, true); }
__device__ __forceinline__ float ror8f(float v) { return __uint_as_float(ror8(__float_as_uint(v))); }
__device__ __forceinline__ int wave_max(int d) {
    d = max(d, __builtin_amdgcn_update_dpp(d, d, 0x128, 0xf, 0xf, false));
    d = max(d, __builtin_amdgcn_update_dpp(d, d, 0x124, 0xf, 0xf, false));
    d = max(d, __builtin_amdgcn_update_dpp(d, d, 0x122, 0xf, 0xf, false));
    d = max(d, __builtin_amdgcn_update_dpp(d, d, 0x121, 0xf, 0xf, false));
    return max(max(__builtin_amdgcn_readlane(d, 0), __builtin_amdgcn_readlane(d, 16)), max(__builtin_amdgcn_readlane(d, 32), __builtin_amdgcn_readlane(d, 48)));
}

constexpr int SPIN_MAX = 2000000;
__device__ __forceinline__ int wait_flags(rsrc_t rw, int line0, int stride, int n, int need, int lane, int& budget, bool fast = false) {
    const int vo = (line0 + (lane < n ? lane : 0) * stride) * 128;
    int mn;
    for (;;) {
        const int v = (int)LD4(rw, vo, 0);
        MB();
        mn = -wave_max(-v);
        if (mn >= need) break;
        if (--budget < 0) break;
        if (fast) __builtin_amdgcn_s_sleep(2); else __builtin_amdgcn_s_sleep(12);
    }
    return mn;
}

template <int NRW, int CW>
__device__ __forceinline__ void stage_rows(const u16* g, int gs, lptr lds, int ls, int tid) {
    constexpr int N = NRW * CW / 512;
    static_assert(NRW * CW % 512 == 0, "");
    v4u v[N];
#pragma unroll
    for (int j = 0; j < N; ++j) { const int i = tid + 512 * j, row = i / CW, c = i % CW; v[j] = *(const v4u*)(g + (size_t)row * gs + c * 8); }
#pragma unroll
    for (int j = 0; j < N; ++j) { const int i = tid + 512 * j, row = i / CW, c = i % CW; *(LAS v4u*)(lds + row * ls + c * 16) = v[j]; }
}
template <int NRW, int CW>
__device__ __forceinline__ void rows_ld(v4u (&v)[NRW * CW / 512], const u16* g, int gs, int tid) {
#pragma unroll
    for (int j = 0; j < NRW * CW / 512; ++j) { const int i = tid + 512 * j, row = i / CW, c = i % CW; v[j] = *(const v4u*)(g + (size_t)row * gs + c * 8); }
}
template <int NRW, int CW>
__device__ __forceinline__ void rows_st(const v4u (&v)[NRW * CW / 512], lptr lds, int ls, int tid) {
#pragma unroll
    for (int j = 0; j < NRW * CW / 512; ++j) { const int i = tid + 512 * j, row = i / CW, c = i % CW; *(LAS v4u*)(lds + row * ls + c * 16) = v[j]; }
}
__device__ __forceinline__ bf16x8 frag16(lptr base, int ls, int lane) {
    const int i16 = lane & 15, bg = lane >> 4, q = i16 >> 2, p = i16 & 3;
    lptr a = base + (8 * bg + q) * ls + p * 8;
    return as8(trd(a), trd(a + 4 * ls));
}
__device__ __forceinline__ bf16x8 frag32p(lptr base, int ls, int lane) {
    const int i16 = lane & 15, G = lane >> 4, q = i16 >> 2, p = i16 & 3;
    lptr a = base + (8 * (G >> 1) + q) * ls + (16 * (p & 1) + 8 * (G & 1) + 4 * (p >> 1)) * 2;
    return as8(trd(a), trd(a + 4 * ls));
}

constexpr int XS = 1168;
__device__ __forceinline__ void xload(v4u (&st)[9], const u16* x, int tok0, int tid) {
#pragma unroll
    for (int j = 0; j < 9; ++j) st[j] = *(const v4u*)(x + (size_t)tok0 * DM + (tid + 512 * j) * 8);
}
__device__ __forceinline__ void xstore(const v4u (&st)[9], lptr xl, int tid) {
#pragma unroll
    for (int j = 0; j < 9; ++j) { const int i = tid + 512 * j; *(LAS v4u*)(xl + i * 16 + (i / 72) * 16) = st[j]; }
}
__device__ __forceinline__ void load_w2(bf16x8 (&A)[36], const u16* W, int h0, lptr lds, int tid, int lane, int w) {
    v4u v[6][6];
#pragma unroll
    for (int c = 0; c < 6; ++c) rows_ld<96, 32>(v[c], W + (size_t)c * 96 * 4096 + h0 * DH, 4096, tid);
    __builtin_amdgcn_sched_barrier(0);
#pragma unroll
    for (int c = 0; c < 6; ++c) {
        lptr l = lds + (c & 1) * (96 * 544);
        rows_st<96, 32>(v[c], l, 544, tid);
        LBAR();
#pragma unroll
        for (int ks = 0; ks < 6; ++ks) A[c * 6 + ks] = frag32p(l + ks * 16 * 544 + w * 64, 544, lane);
        __builtin_amdgcn_sched_barrier(0);
    }
    LBAR();
}

__device__ __forceinline__ f32x16 proj_tile(const bf16x8 (&A)[36], lptr xb) {
    f32x16 acc = {};
    bf16x8 f[6];
#pragma unroll
    for (int i = 0; i < 6; ++i) f[i] = lds8(xb + i * 32);
    __builtin_amdgcn_sched_barrier(0);
#pragma unroll
    for (int ks = 0; ks < 36; ++ks) {
        acc = MFMA32(A[ks], f[ks % 6], acc);
        if (ks + 6 < 36) f[ks % 6] = lds8(xb + (ks + 6) * 32);
        __builtin_amdgcn_sched_barrier(0);
    }
    return acc;
}
__device__ __forceinline__ void role_k2(const Args& a, lptr lds, rsrc_t rw, int cu) {
    const int tid = threadIdx.x, lane = tid & 63, w = __builtin_amdgcn_readfirstlane(tid >> 6);
    v4u st[9];
    bf16x8 A[36];
    xload(st, a.u, 0, tid);
    load_w2(A, a.Wk, 2 * cu, lds, tid, lane, w);
    xstore(st, lds, tid);
    __syncthreads();
    const int o0 = OFF_KX + (2 * cu + (w >> 2)) * (NSEG * 8192) + ((w & 3) * 4 + (lane >> 5) * 2) * 512 + (lane & 31) * 16;
    for (int blk = 0; blk < 32; ++blk) {
        lptr xl = lds + (blk & 1) * (64 * XS);
        if (blk + 1 < 32) xload(st, a.u, (blk + 1) * 64, tid);
#pragma unroll
        for (int tt = 0; tt < 2; ++tt) {
            const f32x16 acc = proj_tile(A, xl + (32 * tt + (lane & 31)) * XS + (lane >> 5) * 16);
            v4u c0 = {pack2(acc[0], acc[1]), pack2(acc[2], acc[3]), pack2(acc[4], acc[5]), pack2(acc[6], acc[7])};
            v4u c1 = {pack2(acc[8], acc[9]), pack2(acc[10], acc[11]), pack2(acc[12], acc[13]), pack2(acc[14], acc[15])};
            const int o = o0 + (blk * 2 + tt) * 8192;
            ST16(c0, rw, o); ST16(c1, rw, o + 512);
            if (blk == 0 && tt == 0) { DRAIN(); LBAR(); if (tid == 0) ST4(0u, rw, (FL_K + cu) * 128); }
        }
        if (blk + 1 < 32) xstore(st, lds + ((blk + 1) & 1) * (64 * XS), tid);
        const bool full = blk < 4 || blk == 31;
        if (full) DRAIN(); else asm volatile("s_waitcnt vmcnt(4)" ::: "memory");
        LBAR();
        if (tid == 0) ST4((u32)(full ? 2 * blk + 1 : 2 * blk - 1), rw, (FL_K + cu) * 128);
    }
}

constexpr int VS = 272;
__device__ __forceinline__ void role_q2p(const Args& a, lptr lds, rsrc_t rw, int cu, int par) {
    const int tid = threadIdx.x, lane = tid & 63, w = __builtin_amdgcn_readfirstlane(tid >> 6);
    v4u st[9];
    bf16x8 A[36];
    load_w2(A, a.Wq, 2 * cu, lds, tid, lane, w);
    lptr xl = lds, ql = lds + 64 * XS;
    LAS int* cnt = (LAS int*)(lds + 64 * XS + 4 * 64 * VS);
    if (tid < 2) cnt[tid] = 0;
    xload(st, a.u, par * 64, tid);
    xstore(st, xl, tid);
    __syncthreads();
    int budget = SPIN_MAX, kdone = -1;
    const int i16 = lane & 15, g = lane >> 4;
    const int sg = w >> 2, hh = (w >> 1) & 1, qt = w & 1, h = 2 * cu + hh;
    const float c2 = 0.08838834764831845f * 1.4426950408889634f;
    for (int it = -1; it < 16; ++it) {
        const int blk = 2 * it + par;
        if (it + 1 < 16) {
            if (it + 2 < 16) xload(st, a.u, (blk + 4) * 64, tid);
            lptr qb = ql + (((it + 1) & 1) * 2 + (w >> 2)) * (64 * VS) + ((w & 3) * 32 + (lane >> 5) * 16) * 2;
#pragma unroll
            for (int tt = 0; tt < 2; ++tt) {
                const f32x16 acc = proj_tile(A, xl + (32 * tt + (lane & 31)) * XS + (lane >> 5) * 16);
                v4u c0 = {pack2(acc[0], acc[1]), pack2(acc[2], acc[3]), pack2(acc[4], acc[5]), pack2(acc[6], acc[7])};
                v4u c1 = {pack2(acc[8], acc[9]), pack2(acc[10], acc[11]), pack2(acc[12], acc[13]), pack2(acc[14], acc[15])};
                lptr qp = qb + (32 * tt + (lane & 31)) * VS;
                *(LAS v4u*)qp = c0; *(LAS v4u*)(qp + 16) = c1;
            }
            __syncthreads();
            if (it + 2 < 16) xstore(st, xl, tid);
        }
        if (it >= 0) {
            const int s = 2 * blk + sg;
            if (kdone < s) kdone = wait_flags(rw, FL_K + cu, 1, 1, s, lane, budget);
            bf16x8 k0[4], k1[4], km[4], qf[4];
            const int ko = OFF_KX + (h * NSEG + s) * 8192 + g * 512 + i16 * 16;
#pragma unroll
            for (int ks = 0; ks < 4; ++ks) { k0[ks] = as8(LD16(rw, ko, ks * 2048)); k1[ks] = as8(LD16(rw, ko + 256, ks * 2048)); }
            lptr qp = ql + ((it & 1) * 2 + hh) * (64 * VS) + (32 * sg + 16 * qt + i16) * VS + g * 16;
#pragma unroll
            for (int ks = 0; ks < 4; ++ks) qf[ks] = lds8(qp + ks * 64);
            f32x4 S0 = {}, S1 = {}, Sm = {};
#pragma unroll
            for (int ks = 0; ks < 4; ++ks) { S0 = MFMA16(k0[ks], qf[ks], S0); S1 = MFMA16(k1[ks], qf[ks], S1); }
            wait_flags(rw, FL_M + h, 1, 1, s, lane, budget, s >= 62);
            const int mo = OFF_KM + (s * NH + h) * 2048 + g * 128 + (i16 & 7) * 16;
#pragma unroll
            for (int ks = 0; ks < 4; ++ks) km[ks] = as8(LD16(rw, mo + ks * 512, 0));
#pragma unroll
            for (int ks = 0; ks < 4; ++ks) Sm = MFMA16(km[ks], qf[ks], Sm);
            const int qi = 16 * qt + i16, t0 = 4 * g;
#pragma unroll
            for (int r = 0; r < 4; ++r) {
                if (t0 + r > qi) S0[r] = -INFINITY;
                if (t0 + r + 16 > qi) S1[r] = -INFINITY;
                if (g >= 2) Sm[r] = -INFINITY;
            }
            float m = fmaxf(fmaxf(fmaxf(S0[0], S0[1]), fmaxf(S0[2], S0[3])), fmaxf(fmaxf(S1[0], S1[1]), fmaxf(S1[2], S1[3])));
            m = fmaxf(m, fmaxf(fmaxf(Sm[0], Sm[1]), fmaxf(Sm[2], Sm[3])));
            m = xmax(m);
            const float nm = -m * c2;
            float sum = 0.f;
#pragma unroll
            for (int r = 0; r < 4; ++r) {
                S0[r] = __builtin_amdgcn_exp2f(fmaf(S0[r], c2, nm)); S1[r] = __builtin_amdgcn_exp2f(fmaf(S1[r], c2, nm)); Sm[r] = __builtin_amdgcn_exp2f(fmaf(Sm[r], c2, nm));
                sum += S0[r] + S1[r] + Sm[r];
            }
            sum = xsum(sum);
            const float inv = __builtin_amdgcn_rcpf(sum);
            v4u px = {pack2(S0[0] * inv, S0[1] * inv), pack2(S0[2] * inv, S0[3] * inv), pack2(S1[0] * inv, S1[1] * inv), pack2(S1[2] * inv, S1[3] * inv)};
            const int po = OFF_PX + (s * 2 + qt) * 40960;
            ST16(px, rw, po + h * 1024 + lane * 16);
            const u32 ma = pack2(Sm[0] * inv, Sm[1] * inv), mb = pack2(Sm[2] * inv, Sm[3] * inv);
            auto ra = __builtin_amdgcn_permlane16_swap(ma, ma, false, false);
            auto rb = __builtin_amdgcn_permlane16_swap(mb, mb, false, false);
            v4u pm = {ra[0], rb[0], ra[1], rb[1]};
            if (lane < 16) ST16(pm, rw, po + 32768 + h * 256 + lane * 16);
            DRAIN();
            if (lane == 0) {
                const int old = __hip_atomic_fetch_add(cnt + sg, 1, __ATOMIC_RELAXED, __HIP_MEMORY_SCOPE_WORKGROUP);
                if ((old & 3) == 3) ST4((u32)s, rw, (FL_P + 4 * cu + 2 * par + sg) * 128);
            }
        }
        __syncthreads();
    }
    if (budget < 0 && lane == 0) ST4(5u, rw, FL_ERR * 128);
}

__device__ __forceinline__ void role_vvo(const Args& a, lptr lds, rsrc_t rw, int h, int par) {
    const int tid = threadIdx.x, lane = tid & 63, w = __builtin_amdgcn_readfirstlane(tid >> 6);
    v4u st[9];
#pragma unroll
    for (int j = 0; j < 9; ++j) { const int i = tid + 512 * j; st[j] = *(const v4u*)(a.u + (size_t)(par * 32 + (i >= 2304 ? 32 : 0)) * DM + i * 8); }
    bf16x8 Av[18], Bo[5][4];
    {
        v4u v[3][6], o[2][9];
#pragma unroll
        for (int c = 0; c < 3; ++c) rows_ld<192, 16>(v[c], a.Wv + (size_t)c * 192 * 4096 + h * DH, 4096, tid);
#pragma unroll
        for (int c = 0; c < 2; ++c) rows_ld<64, 72>(o[c], a.Wo + ((size_t)h * DH + c * 64) * DM, DM, tid);
        __builtin_amdgcn_sched_barrier(0);
#pragma unroll
        for (int c = 0; c < 3; ++c) {
            lptr l = lds + (c & 1) * 77824;
            rows_st<192, 16>(v[c], l, 288, tid);
            LBAR();
#pragma unroll
            for (int ks = 0; ks < 6; ++ks) Av[c * 6 + ks] = frag16(l + ks * 32 * 288 + w * 32, 288, lane);
            __builtin_amdgcn_sched_barrier(0);
        }
#pragma unroll
        for (int c = 0; c < 2; ++c) {
            lptr l = lds + ((c + 1) & 1) * 77824;
            rows_st<64, 72>(o[c], l, 1184, tid);
            LBAR();
#pragma unroll
            for (int j = 0; j < 5; ++j) {
                const int r = (w + 8 * j < NR) ? w + 8 * j : w;
#pragma unroll
                for (int ks = 0; ks < 2; ++ks) Bo[j][c * 2 + ks] = frag16(l + ks * 32 * 1184 + r * 32, 1184, lane);
            }
            __builtin_amdgcn_sched_barrier(0);
        }
        LBAR();
    }
    lptr xl = lds, vl = lds + 64 * XS;
    xstore(st, xl, tid);
    __syncthreads();
    const int rho = lane & 15, bg = lane >> 4;
    const int vo0 = OFF_VOX + ((2 * w + ((rho >> 1) & 1)) * 16 + (h >> 1)) * 1024 + ((2 * (h & 1) + (bg & 1)) * 16 + (bg >> 1) * 8 + 2 * (rho >> 2) + (rho & 1)) * 16;
    const int fo = (FL_V + 2 * h + par) * 128;
    auto stage1 = [&](int tt) {
        f32x4 acc = {};
        lptr xb = xl + (16 * tt + rho) * XS + bg * 16;
        bf16x8 f[6];
#pragma unroll
        for (int i = 0; i < 6; ++i) f[i] = lds8(xb + i * 64);
        __builtin_amdgcn_sched_barrier(0);
#pragma unroll
        for (int ks = 0; ks < 18; ++ks) {
            acc = MFMA16(Av[ks], f[ks % 6], acc);
            if (ks + 6 < 18) f[ks % 6] = lds8(xb + (ks + 6) * 64);
            __builtin_amdgcn_sched_barrier(0);
        }
        v2u o = {pack2(acc[0], acc[1]), pack2(acc[2], acc[3])};
        *(LAS v2u*)(vl + (16 * tt + rho) * VS + (16 * w + 4 * bg) * 2) = o;
    };
    auto stage2 = [&](int sg, int s) {
        bf16x8 Af[2][4];
#pragma unroll
        for (int half = 0; half < 2; ++half)
#pragma unroll
            for (int ks = 0; ks < 4; ++ks) Af[half][ks] = lds8(vl + (32 * sg + rho + 16 * half) * VS + ks * 64 + bg * 16);
#pragma unroll
        for (int j = 0; j < 5; ++j) {
            if (j < 4 || w < 4) {
                f32x4 d0 = {}, d1 = {};
#pragma unroll
                for (int ks = 0; ks < 4; ++ks) { d0 = MFMA16(Af[0][ks], Bo[j][ks], d0); d1 = MFMA16(Af[1][ks], Bo[j][ks], d1); }
                v4u o = {pack2(d0[0], d0[1]), pack2(d0[2], d0[3]), pack2(d1[0], d1[1]), pack2(d1[2], d1[3])};
                ST16(o, rw, vo0 + (s * NR + 8 * j) * (NH * 1024));
            }
        }
    };
    int prev = -1;
#pragma unroll 1
    for (int un = 0; un < 32; ++un) {
        const int sg = un & 1, blk = (un >> 1) * 2 + par;
        const int s = un < 2 ? par + 2 * un : 2 * blk + sg;
        if (sg == 0 && blk + 2 < 32) xload(st, a.u, (blk + 2) * 64, tid);
        stage1(2 * sg); stage1(2 * sg + 1);
        if (sg == 1) DRAIN(); else if (blk + 2 < 32) asm volatile("s_waitcnt vmcnt(9)" ::: "memory"); else DRAIN();
        LBAR();
        if (tid == 0 && prev >= 0) ST4((u32)prev, rw, fo);
        if (sg == 1 && blk + 2 < 32) xstore(st, xl, tid);
        stage2(sg, s);
        prev = s;
        if (un == 0 || un == 31) { DRAIN(); LBAR(); if (tid == 0) ST4((u32)s, rw, fo); }
        else if (sg == 1) LBAR();
    }
}
#define VOWN(s) ((s) < 4 ? ((s) & 1) : (((s) >> 1) & 1))

__device__ __forceinline__ u32 rt32() { return (u32)__builtin_amdgcn_s_memrealtime(); }
__device__ __forceinline__ void wait_until(u32 t) {
    if ((int)(t - rt32()) > 1000) return;
    while ((int)(rt32() - t) < -24) __builtin_amdgcn_s_sleep(4);
    while ((int)(rt32() - t) < 0) {}
}
struct TK { u32 XA, XR, EA, ER; int dA0, dA1, dA2, dR0, dR1, dR2; int okA, okR, gap; };
__device__ __forceinline__ int tk_est(int d0, int d1, int d2) { return max(min(d0, d1), min(max(d0, d1), d2)); }
template <int N>
__device__ __forceinline__ u32 beacon_max(rsrc_t rw, int fb, int slot0, int lane, int& budget) {
    const int vo = BCO(slot0 + (lane < N ? lane : 0)), v2 = BCO(64 + (lane & 7));
    u32 v, u = 0;
    for (;;) {
        v = LD4(rw, vo, fb);
        if (N > 64) u = LD4(rw, v2, fb);
        MB();
        if (__all(max(v, u) != 0xFFFFFFFFu)) break;
        if (--budget < 0) break;
        __builtin_amdgcn_s_sleep(1);
    }
    const u32 base = __builtin_amdgcn_readfirstlane(v);
    int d = (int)(v - base);
    if (N > 64) d = max(d, (int)(u - base));
    return base + (u32)wave_max(d);
}
struct BC2 { u32 v, u; };
__device__ __forceinline__ BC2 beacon_issue(rsrc_t rw, int fb, int lane) {
    BC2 b;
    b.v = LD4(rw, BCO(lane), fb);
    b.u = LD4(rw, BCO(64 + (lane & 7)), fb);
    MB();
    return b;
}
__device__ __forceinline__ u32 beacon_finish(BC2 b, rsrc_t rw, int fb, int lane, int& budget) {
    if (!__all(max(b.v, b.u) != 0xFFFFFFFFu)) return beacon_max<NR8>(rw, fb, 0, lane, budget);
    const u32 base = __builtin_amdgcn_readfirstlane(b.v);
    return base + (u32)wave_max(max((int)(b.v - base), (int)(b.u - base)));
}
__device__ __forceinline__ void tk_T0(TK& k, u32 T0, const Args& a) {
    if (k.okR) { k.dR2 = k.dR1; k.dR1 = k.dR0; k.dR0 = (int)(T0 - k.ER); }
    k.okA = (int)(k.XA - T0) >= TK_LA - TK_TOL;
    if (!k.okR) k.gap = k.okA ? max(TK_GAP, k.gap - 6) : min(k.gap + 16, 160);
    k.EA = k.XA;
    k.XR = k.EA + tk_est(k.dA0, k.dA1, k.dA2) + TK_LR;
}
__device__ __forceinline__ void tk_Tp(TK& k, u32 Tp, const Args& a) {
    const int okn = (int)(k.XR - Tp) >= TK_LR - TK_TOL;
    if (k.okA && (k.okR || !okn)) { k.dA2 = k.dA1; k.dA1 = k.dA0; k.dA0 = (int)(Tp - k.EA); }
    k.okR = okn;
    k.ER = k.okR ? k.XR : Tp + k.gap;
    k.XA = k.ER + tk_est(k.dR0, k.dR1, k.dR2) + TK_LA;
}
__device__ __forceinline__ u32 tk_specA(const TK& k, const Args& a) { return k.XR + tk_est(k.dR0, k.dR1, k.dR2) + TK_LA; }
__device__ __forceinline__ u32 tk_specR(const TK& k, const Args& a) { return k.XA + tk_est(k.dA0, k.dA1, k.dA2) + TK_LR; }
__device__ __forceinline__ void tk_init(TK& k) { const u32 n = rt32(); k.XA = k.XR = k.EA = k.ER = n - 100000u; k.dA0 = k.dA1 = k.dA2 = 125; k.dR0 = k.dR1 = k.dR2 = 65; k.okA = k.okR = 0; k.gap = 50; }

__device__ __forceinline__ void tk_post(lptr p, int tag, u32 t, int lane) { if (lane == 0) { v2u v = {(u32)tag, t}; *(volatile LAS v2u*)p = v; } }
__device__ __forceinline__ u32 tk_get(lptr p, int tag, int& budget) {
    for (;;) {
        const v2u v = *(volatile LAS v2u*)p;
        if ((int)__builtin_amdgcn_readfirstlane(v.x) >= tag || --budget < 0) return __builtin_amdgcn_readfirstlane(v.y);
        __builtin_amdgcn_s_sleep(2);
    }
}
__device__ __forceinline__ void spin_to(u32 X) {
    int rem;
    asm volatile("1:\n\ts_memrealtime vcc\n\ts_waitcnt lgkmcnt(0)\n\ts_sub_i32 %0, %1, vcc_lo\n\ts_cmp_gt_i32 %0, 3\n\ts_cbranch_scc1 1b\n\t"
                 "s_cmp_lt_i32 %0, 1\n\ts_cbranch_scc1 2f\n\ts_nop 3\n\ts_cmp_lt_i32 %0, 2\n\ts_cbranch_scc1 2f\n\ts_nop 3\n\t"
                 "s_cmp_lt_i32 %0, 3\n\ts_cbranch_scc1 2f\n\ts_nop 7\n2:"
                 : "=&s"(rem) : "s"(X) : "memory", "scc", "vcc");
}
__device__ __forceinline__ void tk_wait(lptr p, int tag, int& budget) {
    u32 X;
    for (;;) {
        const v2u v = *(volatile LAS v2u*)p;
        const u32 now = rt32();
        X = __builtin_amdgcn_readfirstlane(v.y);
        const int tg = (int)__builtin_amdgcn_readfirstlane(v.x), dt = (int)(X - now);
        if (tg >= tag) {
            if (dt <= 0 || dt > 1000 || tg > tag) return;
            if (dt <= 28) break;
        }
        if (--budget < 0) return;
        __builtin_amdgcn_s_sleep(1);
    }
    spin_to(X);
}
constexpr int OOB = 0x70000000;
__device__ __forceinline__ void stage_rows8(const u16* src, lptr ml, int tid) {
    for (int c = tid; c < 576; c += 512) {
        const u32* p = (const u32*)(src + (c & 7) * DM + 16 * (c >> 4) + 2 * ((c >> 3) & 1));
        v4u v = {p[0], p[2], p[4], p[6]};
        *(LAS v4u*)(ml + c * 16) = v;
    }
}
__device__ __forceinline__ void gather_mem(rsrc_t rw, int nb, lptr ml, int lane, int w, lptr tkp, int tag, int& budget) {
    const int gi = w == 7 ? 5 : w;
    const int l0 = gi * 1536 + lane * 16, l1 = l0 + 1024;
    const int o0 = l0 < 8192 ? l0 : l0 + (N_MEMB - 8192), o1 = lane < 32 ? (l1 < 8192 ? l1 : l1 + (N_MEMB - 8192)) : OOB;
    lptr p1 = ml + (lane < 32 ? l1 : 14336 + lane * 16);
    asm volatile("" : "+s"(nb));
    tk_wait(tkp, tag, budget);
    v4u v0 = LD16(rw, o0, nb), v1 = LD16(rw, o1, nb);
    MB();
    *(LAS v4u*)(ml + l0) = v0; *(LAS v4u*)p1 = v1;
    MB();
    if (__builtin_expect(!__all(max(bad4(v0), bad4(v1)) != 0xFFFFFFFFu), 0)) {
        do {
            v0 = LD16(rw, o0, nb); v1 = LD16(rw, o1, nb);
            MB();
        } while (!__all(max(bad4(v0), bad4(v1)) != 0xFFFFFFFFu) && --budget >= 0);
        *(LAS v4u*)(ml + l0) = v0; *(LAS v4u*)p1 = v1;
    }
}
__device__ __forceinline__ void gather_mem4(rsrc_t rw, int nb, lptr ml, int lane, int gi, lptr tkp, int tag, int& budget) {
    const int l0 = gi * 2048 + lane * 16;
    const int o2 = lane < 16 ? N_MEMB + gi * 256 + lane * 16 : OOB;
    lptr p2 = ml + (lane < 16 ? 8192 + gi * 256 + lane * 16 : 14336 + lane * 16);
    asm volatile("" : "+s"(nb));
    tk_wait(tkp, tag, budget);
    v4u v0 = LD16(rw, l0, nb), v1 = LD16(rw, l0 + 1024, nb), v2 = LD16(rw, o2, nb);
    MB();
    *(LAS v4u*)(ml + l0) = v0; *(LAS v4u*)(ml + l0 + 1024) = v1; *(LAS v4u*)p2 = v2;
    MB();
    if (__builtin_expect(!__all(max(max(bad4(v0), bad4(v1)), bad4(v2)) != 0xFFFFFFFFu), 0)) {
        do {
            v0 = LD16(rw, l0, nb); v1 = LD16(rw, l0 + 1024, nb); v2 = LD16(rw, o2, nb);
            MB();
        } while (!__all(max(max(bad4(v0), bad4(v1)), bad4(v2)) != 0xFFFFFFFFu) && --budget >= 0);
        *(LAS v4u*)(ml + l0) = v0; *(LAS v4u*)(ml + l0 + 1024) = v1; *(LAS v4u*)p2 = v2;
    }
}
__device__ __forceinline__ int get_parity(rsrc_t rw, int lane, int& budget) {
    const int vo = (FL_PAR + (lane & 7)) * 128;
    u32 v;
    for (;;) {
        v = LD4(rw, vo, 0);
        MB();
        if (__all(v != 0xFFFFFFFFu) || --budget < 0) break;
        __builtin_amdgcn_s_sleep(8);
    }
    int pm = 0;
#pragma unroll
    for (int k = 0; k < 8; ++k) pm |= (__builtin_amdgcn_readlane((int)v, k) & 3) << (2 * k);
    return __builtin_popcount(pm) > 8 ? 0xFFFF : 0;
}
__device__ __forceinline__ void role_probe(rsrc_t rw) {
    const int lane = threadIdx.x & 63, w = __builtin_amdgcn_readfirstlane(threadIdx.x >> 6);
    const int o = OFF_RING + 2 * w * 65536 + N_PROBE;
    ST4(0xFFFFFFFFu, rw, lane < 6 ? o + (lane & 1) * 8192 + (lane >> 1) * 128 : OOB);
    DRAIN();
    int best[2] = {1 << 30, 1 << 30};
#pragma unroll
    for (int k = 0; k < 6; ++k) {
        const unsigned long long t0 = __builtin_amdgcn_s_memtime();
        asm volatile("s_waitcnt lgkmcnt(0)" ::: "memory");
        u32 v = LD4(rw, o + (k & 1) * 8192 + (k >> 1) * 128, 0);
        asm volatile("s_waitcnt vmcnt(0)" : "+v"(v) :: "memory");
        const unsigned long long t1 = __builtin_amdgcn_s_memtime();
        best[k & 1] = min(best[k & 1], (int)(t1 - t0));
    }
    if (lane == 0) ST4(best[1] < best[0] ? 3u : 0u, rw, (FL_PAR + w) * 128);
}
template <typename F>
__device__ __forceinline__ void qk_project_h(const bf16x16 (&Bq)[9], const bf16x16 (&Bk)[9], lptr ma, lptr xw, int idx, F hook) {
    f32x4 aq = {}, ak = {};
    bf16x8 A[9];
#pragma unroll
    for (int t = 0; t < 9; ++t) A[t] = lds8(ma + t * 1024);
    __builtin_amdgcn_sched_barrier(0);
#pragma unroll
    for (int t = 0; t < 9; ++t) {
        aq = SMF(A[t], Bq[t], aq, idx); ak = SMF(A[t], Bk[t], ak, idx);
        hook(t);
        __builtin_amdgcn_sched_barrier(0);
    }
    const float s0 = swap32_sum(aq[0], ak[0]), s1 = swap32_sum(aq[1], ak[1]), s2 = swap32_sum(aq[2], ak[2]), s3 = swap32_sum(aq[3], ak[3]);
    v2u o = {pack2(s0, s1), pack2(s2, s3)};
    *(LAS v2u*)xw = o;
}
__device__ __forceinline__ void qk_project(const bf16x16 (&Bq)[9], const bf16x16 (&Bk)[9], lptr ma, lptr xw, int idx) {
    qk_project_h(Bq, Bk, ma, xw, idx, [](int) {});
}

__device__ __forceinline__ void role_qk(const Args& a, lptr lds, rsrc_t rw, int h, int rep) {
    const int tid = threadIdx.x, lane = tid & 63, w = __builtin_amdgcn_readfirstlane(tid >> 6);
    bf16x16 Bq[9], Bk[9];
    {
        v4u xr[6];
#pragma unroll
        for (int j = 0; j < 6; ++j) {
            const int i = min(tid + 512 * j, 2879), row = i / 72, c = i - row * 72;
            xr[j] = *(const v4u*)((row < 8 ? a.mem0 + row * DM : a.u + (row - 8) * DM) + c * 8);
        }
        v4u v[6][6];
#pragma unroll
        for (int c = 0; c < 6; ++c) rows_ld<192, 16>(v[c], (c >= 3 ? a.Wk : a.Wq) + (size_t)(c % 3) * 192 * 4096 + h * DH, 4096, tid);
        __builtin_amdgcn_sched_barrier(0);
#pragma unroll
        for (int j = 0; j < 6; ++j) {
            const int i = min(tid + 512 * j, 2879), row = i / 72, c = i - row * 72, G = row >> 3;
            lptr d = lds + (G < 2 ? 57344 + G * MEMB : 135168 + (G - 2) * MEMB) + (c >> 1) * 256 + (row & 7) * 16 + (c & 1) * 8;
            *(LAS v2u*)d = v2u{xr[j].x, xr[j].z}; *(LAS v2u*)(d + 128) = v2u{xr[j].y, xr[j].w};
        }
#pragma unroll
        for (int c = 0; c < 6; ++c) {
            lptr l = lds + (c & 1) * 77824;
            rows_st<192, 16>(v[c], l, 288, tid);
            LBAR();
#pragma unroll
            for (int t = 0; t < 3; ++t) {
                bf16x16 f = cat16(frag16(l + (64 * t) * 288 + w * 32, 288, lane), frag16(l + (64 * t + 32) * 288 + w * 32, 288, lane));
                if (c >= 3) Bk[(c % 3) * 3 + t] = f; else Bq[c * 3 + t] = f;
            }
            __builtin_amdgcn_sched_barrier(0);
        }
        LBAR();
    }
    lptr ml = lds, xl = lds + MEMB;
    const int idx = (lane & 8) ? 0xEEEE : 0x4444;
    const int i16 = lane & 15, g = lane >> 4;
    const bool hi8 = (lane & 8) != 0;
    int budget = SPIN_MAX;
    lptr pq = xl + (8 * g + (i16 >> 2)) * 32 + (i16 & 1) * 8;
    lptr kl = lds + 16384 + lane * 16;
    lptr ma = ml + lane * 16, xw = xl + (16 * w + i16) * 32 + g * 8;
    const int ox = N_PX + (h >> 4) * 16384 + (h & 15) * 512 + ((g & 1) * 16 + (g >> 1) * 8 + (i16 & 7)) * 16;
    const int om = hi8 ? OOB : N_PMM + h * 128 + (i16 & 7) * 16;
    const int ob = lane < NREP ? BCO(BC_QK + h) : OOB;
    lptr tkp = lds + MEMB + 4096, kfp = tkp + 64;
    if (tid == 0) { tk_post(tkp, 0, rt32() - 100000u, 0); tk_post(kfp, 0, 0u, 0); }
    for (int c = tid; c < 576; c += 512) *(LAS v4u*)(ml + c * 16) = *(LAS v4u*)(lds + 57344 + c * 16);
    __syncthreads();
#pragma unroll 1
    for (int G = 0; G < 4; ++G) {
        qk_project(Bq, Bk, lds + (G ? 135168 + (G - 1) * MEMB : 57344 + MEMB) + lane * 16, xw, idx);
        LBAR();
        if (w < 4) {
            const bf16x8 F = as8(trd(pq + w * 1024 + 16), trd(pq + w * 1024 + 16 + 128));
            if ((i16 >> 3) == (G & 1)) *(LAS bf16x8*)(kl + ((G >> 1) * 4 + w) * 1024) = F;
        }
        LBAR();
    }
    const int pm = get_parity(rw, lane, budget);
    LBAR();
    if (w == 0) {
        const float c2 = 0.08838834764831845f * 1.4426950408889634f;
        for (int s = 0; s < NSEG - 1; ++s) {
            tk_get(kfp, s, budget);
            bf16x8 kx[2][4];
#pragma unroll
            for (int tl = 0; tl < 2; ++tl)
#pragma unroll
                for (int ks = 0; ks < 4; ++ks) kx[tl][ks] = lds8(kl + (s & 1) * 8192 + (tl * 4 + ks) * 1024);
            int oxs = ox + NEARB(s, i16 >> 3, pm), oxt = ox + NEARB(s, 2 + (i16 >> 3), pm), oms = om + NEARB(s, g, pm), obs = ob + FARB(s, lane & 3, pm);
            const int nb = NEARB(s, rep, pm);
            asm volatile("s_waitcnt lgkmcnt(0)" : "+v"(oxs), "+v"(oxt), "+v"(oms), "+v"(obs) :: "memory");
#pragma unroll
            for (int tl = 0; tl < 2; ++tl)
#pragma unroll
                for (int ks = 0; ks < 4; ++ks) PIN4(kx[tl][ks]);
            if (s > 0) {
                gather_mem(rw, nb, ml, lane, 0, tkp, s, budget);
            }
            LBAR();
            qk_project(Bq, Bk, ma, xw, idx);
            __builtin_amdgcn_s_setprio(3);
            LBAR();
            bf16x8 Fq[4], Fk[4];
#pragma unroll
            for (int ks = 0; ks < 4; ++ks) Fq[ks] = as8(trd(pq + ks * 1024), trd(pq + ks * 1024 + 128));
            __builtin_amdgcn_sched_barrier(0);
#pragma unroll
            for (int ks = 0; ks < 4; ++ks) Fk[ks] = as8(trd(pq + ks * 1024 + 16), trd(pq + ks * 1024 + 16 + 128));
            __builtin_amdgcn_sched_barrier(0);
            f32x4 S0 = {}, S1 = {}, Sm = {};
            S0 = MFMA16(kx[0][0], Fq[0], S0); S1 = MFMA16(kx[1][0], Fq[0], S1);
            S0 = MFMA16(kx[0][1], Fq[1], S0); S1 = MFMA16(kx[1][1], Fq[1], S1);
            __builtin_amdgcn_sched_barrier(0);
            Sm = MFMA16(Fk[0], Fq[0], Sm); S0 = MFMA16(kx[0][2], Fq[2], S0);
            Sm = MFMA16(Fk[1], Fq[1], Sm); S1 = MFMA16(kx[1][2], Fq[2], S1);
            Sm = MFMA16(Fk[2], Fq[2], Sm); S0 = MFMA16(kx[0][3], Fq[3], S0);
            Sm = MFMA16(Fk[3], Fq[3], Sm); S1 = MFMA16(kx[1][3], Fq[3], S1);
            __builtin_amdgcn_sched_barrier(0);
            float y0 = hi8 ? Sm[2] : Sm[0], y1 = hi8 ? Sm[3] : Sm[1];
            float x0 = hi8 ? S1[0] : S0[0], x1 = hi8 ? S1[1] : S0[1], x2 = hi8 ? S1[2] : S0[2], x3 = hi8 ? S1[3] : S0[3];
            const float ml_ = vmax3(vmax3(y0, y1, x0), x1, vmax(x2, x3));
            const float nm = -ml_ * c2;
            float m = vmax(ml_, ror8f(ml_));
            x0 = __builtin_amdgcn_exp2f(fmaf(x0, c2, nm)); x1 = __builtin_amdgcn_exp2f(fmaf(x1, c2, nm));
            { auto r = __builtin_amdgcn_permlane16_swap(__float_as_uint(m), __float_as_uint(m), false, false); m = vmax(__uint_as_float(r[0]), __uint_as_float(r[1])); }
            x2 = __builtin_amdgcn_exp2f(fmaf(x2, c2, nm)); x3 = __builtin_amdgcn_exp2f(fmaf(x3, c2, nm));
            { auto r = __builtin_amdgcn_permlane32_swap(__float_as_uint(m), __float_as_uint(m), false, false); m = vmax(__uint_as_float(r[0]), __uint_as_float(r[1])); }
            y0 = __builtin_amdgcn_exp2f(fmaf(y0, c2, nm)); y1 = __builtin_amdgcn_exp2f(fmaf(y1, c2, nm));
            const float f = __builtin_amdgcn_exp2f((ml_ - m) * c2);
            float sum = f * fmaf(0.5f, y0 + y1, (x0 + x1) + (x2 + x3));
            sum += ror8f(sum);
            sum = xsum(sum);
            const float inv = f * __builtin_amdgcn_rcpf(sum);
            const u32 pa = pack2(x0 * inv, x1 * inv) & 0x7fff7fffu, pb = pack2(x2 * inv, x3 * inv) & 0x7fff7fffu, pc = pack2(y0 * inv, y1 * inv) & 0x7fff7fffu;
            const u32 qc = ror8(pc);
            v4u px = {(u32)__builtin_amdgcn_update_dpp((int)pa, (int)pa, 0x128, 0xf, 0xc, false), (u32)__builtin_amdgcn_update_dpp((int)pb, (int)pb, 0x128, 0xf, 0xc, false),
                      (u32)__builtin_amdgcn_update_dpp((int)pa, (int)pa, 0x128, 0xf, 0x3, false), (u32)__builtin_amdgcn_update_dpp((int)pb, (int)pb, 0x128, 0xf, 0x3, false)};
            ST16(px, rw, oxs);
            ST16(px, rw, oxt);
            auto ra = __builtin_amdgcn_permlane16_swap(pc, pc, false, false);
            auto rc = __builtin_amdgcn_permlane16_swap(qc, qc, false, false);
            v4u pmv = {ra[0], rc[0], ra[1], rc[1]};
            ST16(pmv, rw, oms);
            __builtin_amdgcn_sched_barrier(0);
            ST4(nos(rt32()), rw, obs);
            __builtin_amdgcn_s_setprio(0);
        }
        {
            gather_mem(rw, NEARB(NSEG - 1, rep, pm), ml, lane, 0, tkp, NSEG - 1, budget);
            LBAR();
            qk_project(Bq, Bk, ma, xw, idx);
            LBAR();
        }
    } else if (w == 5) {
        int kdone = -1;
        const int kxo = OFF_KX + h * (NSEG * 8192) + g * 512 + i16 * 16;
        for (int s = 0; s < NSEG; ++s) {
            const bool need = s + 1 < NSEG - 1;
            if (need && kdone < s + 1) kdone = wait_flags(rw, FL_K + (h >> 1), 1, 1, -1, lane, budget);
            const bool go = need && kdone >= s + 1;
            int ko = (go ? kxo : OOB) + (s + 1) * 8192;
            int a0 = ox + NEARB(s + 2, i16 >> 3, pm), a1 = ox + NEARB(s + 2, 2 + (i16 >> 3), pm), a2 = om + NEARB(s + 2, g, pm), a3 = ob + FARB(s + 4, lane & 3, pm);
            v4u ff = {0xFFFFFFFFu, 0xFFFFFFFFu, 0xFFFFFFFFu, 0xFFFFFFFFu};
            asm volatile("" : "+v"(ko), "+v"(a0), "+v"(a1), "+v"(a2), "+v"(a3), "+v"(ff));
            LBAR();
            v4u kx[8];
            qk_project_h(Bq, Bk, ma, xw, idx, [&](int t) {
                if (t < 4) { kx[t] = LD16(rw, ko + (t & 1) * 2048, (t >> 1) * 4096); kx[4 + t] = LD16(rw, ko + 256 + (t & 1) * 2048, (t >> 1) * 4096); }
                if (t == 4) { ST16(ff, rw, a0); ST16(ff, rw, a1); }
                if (t == 5) { ST16(ff, rw, a2); ST4(0xFFFFFFFFu, rw, a3); }
            });
            LBAR();
            if (need) {
                if (!go) {
                    kdone = wait_flags(rw, FL_K + (h >> 1), 1, 1, s + 1, lane, budget, true);
#pragma unroll
                    for (int k = 0; k < 8; ++k) kx[k] = LD16(rw, kxo + (k >> 2) * 256 + (k & 3) * 2048, (s + 1) * 8192);
                }
#pragma unroll
                for (int k = 0; k < 8; ++k) *(LAS v4u*)(kl + ((s + 1) & 1) * 8192 + k * 1024) = kx[k];
                asm volatile("s_waitcnt lgkmcnt(0)" ::: "memory");
                tk_post(kfp, s + 1, 0u, lane);
            }
            DRAIN();
        }
    } else {
        TK tk; tk_init(tk);
        const int kmo = i16 < 8 ? OFF_KM + h * 2048 + g * 128 + i16 * 16 : OOB;
        for (int s = 0; s < NSEG; ++s) {
            if (s > 0 && w != 6) gather_mem(rw, NEARB(s, rep, pm), ml, lane, w, tkp, s, budget);
            BC2 bc = {0u, 0u};
            if (w == 3) bc = beacon_issue(rw, FARB(s, rep, pm), lane);
            LBAR();
            qk_project(Bq, Bk, ma, xw, idx);
            LBAR();
            if (w == 6) {
                bf16x8 F[4];
#pragma unroll
                for (int ks = 0; ks < 4; ++ks) F[ks] = as8(trd(pq + ks * 1024 + 16), trd(pq + ks * 1024 + 16 + 128));
#pragma unroll
                for (int ks = 0; ks < 4; ++ks) ST16(asu(F[ks]), rw, kmo + s * (NH * 2048) + ks * 512);
                DRAIN();
                if (lane == 0) ST4((u32)s, rw, (FL_M + h) * 128);
            } else if (w == 3) {
                if (s < NSEG - 1) {
                    if (s > 0) { tk_T0(tk, beacon_finish(bc, rw, FARB(s, rep, pm), lane, budget), a); if (tk.okA) tk_post(tkp, s + 1, tk_specA(tk, a), lane); }
                    wait_until(tk.XR + 8);
                    tk_Tp(tk, beacon_max<NH>(rw, FARB(s, rep, pm), BC_QK, lane, budget), a);
                    tk_post(tkp, s + 1, tk.XA, lane);
                }
            } else __builtin_amdgcn_s_sleep(14);
        }
    }
    if (budget < 0 && lane == 0) ST4(1u, rw, FL_ERR * 128);
}

__device__ __forceinline__ void role_wvo(const Args& a, lptr lds, rsrc_t rw, int idx) {
    const int tid = threadIdx.x, lane = tid & 63, w = __builtin_amdgcn_readfirstlane(tid >> 6);
    const int h = idx / 3, part = idx - 3 * h;
    bf16x8 Bo[5][4];
    v4u v[6], o[2][9];
#pragma unroll
    for (int c = 0; c < 2; ++c) rows_ld<64, 72>(o[c], a.Wo + ((size_t)h * DH + c * 64) * DM, DM, tid);
    rows_ld<192, 16>(v, a.Wv + (size_t)part * 192 * 4096 + h * DH, 4096, tid);
    __builtin_amdgcn_sched_barrier(0);
#pragma unroll
    for (int c = 0; c < 2; ++c) {
        lptr l = lds + c * 77824;
        rows_st<64, 72>(o[c], l, 1184, tid);
        LBAR();
#pragma unroll
        for (int j = 0; j < 5; ++j) {
            const int r = (w + 8 * j < NR) ? w + 8 * j : w;
#pragma unroll
            for (int ks = 0; ks < 2; ++ks) Bo[j][c * 2 + ks] = frag16(l + ks * 32 * 1184 + r * 32, 1184, lane);
        }
        __builtin_amdgcn_sched_barrier(0);
    }
    rows_st<192, 16>(v, lds, VS, tid);
    LBAR();
    const int rho = lane & 15, bg = lane >> 4;
    const int wo0 = OFF_WVO + (2 * w + ((rho >> 1) & 1)) * 294912 + ((h >> 1) * 18 + part * 6) * 1024 + (bg * 16 + (h & 1) * 8 + 2 * (rho >> 2) + (rho & 1)) * 16;
#pragma unroll 1
    for (int kb = 0; kb < 6; ++kb) {
        bf16x8 Af[2][4];
#pragma unroll
        for (int mt = 0; mt < 2; ++mt)
#pragma unroll
            for (int ks = 0; ks < 4; ++ks) Af[mt][ks] = lds8(lds + (32 * kb + 8 * (rho >> 2) + 4 * mt + (rho & 3)) * VS + ks * 64 + bg * 16);
#pragma unroll
        for (int j = 0; j < 5; ++j) {
            if (j < 4 || w < 4) {
                f32x4 d0 = {}, d1 = {};
#pragma unroll
                for (int ks = 0; ks < 4; ++ks) { d0 = MFMA16(Af[0][ks], Bo[j][ks], d0); d1 = MFMA16(Af[1][ks], Bo[j][ks], d1); }
                v4u q = {pack2(d0[0], d0[1]), pack2(d0[2], d0[3]), pack2(d1[0], d1[1]), pack2(d1[2], d1[3])};
                ST16(q, rw, wo0 + j * (16 * 294912) + kb * 1024);
            }
        }
        if (kb == 2 || kb == 4) {
            asm volatile("s_waitcnt vmcnt(4)" ::: "memory");
            LBAR();
            if (tid == 0) ST4((u32)(kb >> 1), rw, (FL_W + idx) * 128);
        }
    }
    DRAIN();
    LBAR();
    if (tid == 0) ST4(3u, rw, (FL_W + idx) * 128);
}

__device__ __forceinline__ void role_rz(const Args& a, lptr lds, rsrc_t rw, int r, int rep) {
    const int tid = threadIdx.x, lane = tid & 63, w = __builtin_amdgcn_readfirstlane(tid >> 6);
    const int idx = (lane & 8) ? 0xEEEE : 0x4444;
    const int i16 = lane & 15, g = lane >> 4;
    int budget = SPIN_MAX;
    v4u m0[2];
#pragma unroll
    for (int j = 0; j < 2; ++j) { const int i = min(tid + 512 * j, 575), row = i / 72, c = i - row * 72; m0[j] = *(const v4u*)(a.mem0 + row * DM + c * 8); }
    const int pm = get_parity(rw, lane, budget);
#pragma unroll
    for (int j = 0; j < 2; ++j) {
        const int i = min(tid + 512 * j, 575), row = i / 72, c = i - row * 72;
        lptr d = lds + (c >> 1) * 256 + row * 16 + (c & 1) * 8;
        *(LAS v2u*)d = v2u{m0[j].x, m0[j].z}; *(LAS v2u*)(d + 128) = v2u{m0[j].y, m0[j].w};
    }
    bf16x16 Ba[9], Bb[9];
    v4u t0[4];
    {
        const int wo = OFF_WVO + r * 294912 + 2 * w * 18432 + lane * 16;
        int have = 0;
#pragma unroll
        for (int p = 0; p < 3; ++p) {
            if (have < p + 1) have = wait_flags(rw, FL_W + 12 * w, 1, 12, p + 1, lane, budget, true);
            if (p == 2) {
                wait_flags(rw, FL_V, 1, 64, 0, lane, budget, true);
                const int vq = OFF_VOX + r * 16384 + w * 2048 + lane * 16;
#pragma unroll
                for (int k = 0; k < 4; ++k) t0[k] = LD16(rw, vq + (k & 1) * 1024, (k >> 1) * (NR * NH * 1024));
            }
#pragma unroll
            for (int q = 0; q < 3; ++q) {
                const int t = 3 * q + p;
                Ba[t] = cat16(as8(LD16(rw, wo, t * 2048)), as8(LD16(rw, wo + 1024, t * 2048)));
                Bb[t] = cat16(as8(LD16(rw, wo, 18432 + t * 2048)), as8(LD16(rw, wo + 1024, 18432 + t * 2048)));
            }
            MB();
        }
    }
    lptr ml = lds, zl = lds + MEMB;
    lptr tka = lds + 13312, tkr = tka + 16, cntp = tka + 64;
    volatile LAS int* prod = (volatile LAS int*)(lds + 13312 + 512);
    lptr ring = lds + 16384;
    lptr ma = ml + lane * 16;
    lptr zw = zl + ((2 * w + (lane >> 5)) >> 2) * 1024 + ((((2 * w + (lane >> 5)) & 3) * 16 + (g & 1) * 8 + (i16 & 7)) * 16) + (i16 >> 3) * 8;
    const bool hi8r = (lane & 8) != 0;
    const int mso = i16 < 8 ? (r < 64 ? N_MEMA + r * 128 : N_MEMB + (r - 64) * 128) + i16 * 16 : OOB;
    const int ob = lane < NREP ? BCO(r) : OOB;
    if (tid == 0) { tk_post(tka, 0, rt32() - 100000u, 0); tk_post(tkr, -1, 0u, 0); *(volatile LAS int*)cntp = 0; }
#pragma unroll
    for (int k = 0; k < 4; ++k) *(LAS v4u*)(ring + (k >> 1) * 16384 + w * 2048 + (k & 1) * 1024 + lane * 16) = t0[k];
    if (tid < 4) prod[tid] = 2;
    LBAR();
    if (w < 4) {
        const int po = N_PX + (w >> 1) * 16384 + (w & 1) * 4096 + lane * 16;
        const int mo = N_PMM + w * 1024 + 2 * g * 128 + (i16 & 7) * 16 + (i16 >> 3) * 8;
        lptr cna = lane == 0 ? cntp : cntp + 4 + lane * 4;
        const u32 one = lane == 0 ? 1u : 0u;
        const bool dv = (g >> 1) == (i16 >> 3);
        const int rwo = dv ? (4 * (g & 1) + 2 * (g >> 1)) * 128 + ((i16 & 7) >> 1) * 32 + w * 8 + (i16 & 1) * 4 : 2048 + lane * 4;
        const int msq = lane < 32 ? (r < 64 ? N_MEMA + r * 128 : N_MEMB + (r - 64) * 128) + (lane >> 2) * 16 : OOB;
        lptr zrd = zl + w * 1024 + lane * 16;
        for (int s = 0; s < NSEG; ++s) {
            lptr Rw = lds + 81920 + (s & 1) * 1024 + rwo, Rr = lds + 81920 + (s & 1) * 1024 + (lane & 31) * 32;
            int mss = msq + NEARB(s + 1, lane & 3, pm), obs = ob + FARB(s + 1, lane & 3, pm);
            int sb = NEARB(s, rep, pm);
            asm volatile("" : "+s"(sb));
            lptr rg = ring + (s & 3) * 16384 + w * 4096 + lane * 16;
            bf16x8 b[5];
            const bool early = __builtin_amdgcn_readfirstlane(prod[w >> 1]) > s;
            MB();
#pragma unroll
            for (int k = 0; k < 4; ++k) b[k] = lds8(rg + k * 1024);
            const u32 X = s > 0 ? tk_get(tkr, min(s, NSEG - 2), budget) : 0u;
            asm volatile("s_waitcnt lgkmcnt(0)" : "+v"(mss), "+v"(obs), "+v"(Rw), "+v"(Rr) :: "memory");
#pragma unroll
            for (int k = 0; k < 4; ++k) PIN4(b[k]);
            LBAR();
            qk_project(Ba, Bb, ma, zw, idx);
            LBAR();
            if (s == NSEG - 1) break;
            __builtin_amdgcn_s_setprio(3);
            b[4] = lds8(zrd);
            if (!early) {
                while (__builtin_amdgcn_readfirstlane(prod[w >> 1]) <= s && --budget >= 0) __builtin_amdgcn_s_sleep(1);
                MB();
#pragma unroll
                for (int k = 0; k < 4; ++k) b[k] = lds8(rg + k * 1024);
            }
            if (s > 0 && (int)(X - rt32()) < 1000) spin_to(X);
            f32x4 d;
            u32 bad;
            auto shot = [&]() {
                v4u p[4]; v2u m[2];
                m[0] = LD8(rw, mo, sb); m[1] = LD8(rw, mo + 128, sb);
                __builtin_amdgcn_sched_barrier(0);
#pragma unroll
                for (int k = 0; k < 4; ++k) { p[k] = LD16(rw, po + k * 1024, sb); __builtin_amdgcn_sched_barrier(0); }
                MB();
                d = f32x4{};
                d = MFMA16(as8(m[0], m[1]), b[4], d);
                bad = (m[0].x | m[0].y) | (m[1].x | m[1].y);
                __builtin_amdgcn_sched_barrier(0);
#pragma unroll
                for (int k = 0; k < 4; ++k) {
                    d = MFMA16(as8(p[k]), b[k], d);
                    bad |= (p[k].x | p[k].y) | (p[k].z | p[k].w);
                    __builtin_amdgcn_sched_barrier(0);
                }
            };
            shot();
            if (__builtin_expect(!__all((int)bad >= 0), 0)) {
                do shot(); while (!__all((int)bad >= 0) && --budget >= 0);
            }
            float e0, e1;
            {
                auto r0 = __builtin_amdgcn_permlane32_swap(__float_as_uint(d[0]), __float_as_uint(d[2]), false, false);
                auto r1 = __builtin_amdgcn_permlane32_swap(__float_as_uint(d[1]), __float_as_uint(d[3]), false, false);
                const float t0 = __uint_as_float(hi8r ? r0[1] : r0[0]), t1 = __uint_as_float(hi8r ? r1[1] : r1[0]);
                e0 = t0 + ror8f(t0); e1 = t1 + ror8f(t1);
            }
            f32x4 x0, x1; u32 old;
            asm volatile("ds_write2_b32 %4, %5, %6 offset1:32\n\tds_add_rtn_u32 %0, %7, %8\n\tds_read_b128 %1, %3\n\tds_read_b128 %2, %3 offset:16\n\ts_waitcnt lgkmcnt(0)"
                         : "=&v"(old), "=&v"(x0), "=&v"(x1) : "v"(Rr), "v"(Rw), "v"(e0), "v"(e1), "v"(cna), "v"(one) : "memory");
            {
                const u32 c = nos(pack2((x0[0] + x0[2]) + (x1[0] + x1[2]), (x0[1] + x0[3]) + (x1[1] + x1[3])));
                v4u o = {(u32)__builtin_amdgcn_update_dpp(0, (int)c, 0x00, 0xf, 0xf, true), (u32)__builtin_amdgcn_update_dpp(0, (int)c, 0x55, 0xf, 0xf, true),
                         (u32)__builtin_amdgcn_update_dpp(0, (int)c, 0xAA, 0xf, 0xf, true), (u32)__builtin_amdgcn_update_dpp(0, (int)c, 0xFF, 0xf, 0xf, true)};
                const bool last = (__builtin_amdgcn_readfirstlane(old) & 3) == 3;
                ST16(o, rw, last ? mss : OOB);
                __builtin_amdgcn_sched_barrier(0);
                if (last) ST4(nos(rt32()), rw, obs);
            }
            __builtin_amdgcn_s_setprio(0);
        }
    } else if (w == 5 || w == 6) {
        int vdone0 = -1, vdone1 = -1, staged = 2;
        const int hw = w - 5;
        const int vo = OFF_VOX + r * 16384 + hw * 8192 + lane * 16;
        const int zho = OFF_ZH + r * 4096 + hw * 2048 + lane * 16;
        lptr zhr = zl + hw * 2048 + (((lane >> 4) & 3) * 16 + (lane & 7)) * 16 + ((lane >> 3) & 1) * 8;
        const int aro = w == 5 ? mso : OOB, arb = w == 5 ? ob : OOB;
        v4u zv[2] = {};
        for (int s = 0; s < NSEG; ++s) {
            bool go = false;
            if (staged <= min(s + 3, NSEG - 2)) {
                const int ow = VOWN(staged);
                int& vd = ow ? vdone1 : vdone0;
                if (vd < staged) vd = wait_flags(rw, FL_V + ow, 2, NH, -1, lane, budget);
                go = vd >= staged;
            }
            const int vos = go ? vo : OOB;
            int va = vos + staged * (NR * NH * 1024), zs = s > 0 ? zho + (s - 1) * (NR * NH * 256) : OOB;
            int a0 = aro + NEARB(s + 3, g, pm), a1 = arb + FARB(s + 4, lane & 3, pm);
            v4u ff = {0xFFFFFFFFu, 0xFFFFFFFFu, 0xFFFFFFFFu, 0xFFFFFFFFu};
            asm volatile("" : "+v"(va), "+v"(zs), "+v"(a0), "+v"(a1), "+v"(ff));
            if (s > 0) gather_mem4(rw, NEARB(s, rep, pm), ml, lane, w - 4, tka, s, budget);
            LBAR();
            v4u v[8];
            qk_project_h(Ba, Bb, ma, zw, idx, [&](int t) {
                if (t < 4) { v[2 * t] = LD16(rw, va + (t & 1) * 2048, (t >> 1) * 4096); v[2 * t + 1] = LD16(rw, va + 1024 + (t & 1) * 2048, (t >> 1) * 4096); }
                if (t == 4) { ST16(zv[0], rw, zs); ST16(zv[1], rw, zs + 1024); }
                if (t == 5) { ST16(ff, rw, a0); ST4(0xFFFFFFFFu, rw, a1); }
            });
            LBAR();
#pragma unroll
            for (int k = 0; k < 2; ++k) {
                const v2u lo = *(LAS v2u*)(zhr + k * 1024), hi = *(LAS v2u*)(zhr + k * 1024 + 128);
                zv[k] = v4u{lo.x, lo.y, hi.x, hi.y};
            }
            for (;;) {
                if (go) {
#pragma unroll
                    for (int k = 0; k < 8; ++k) *(LAS v4u*)(ring + (staged & 3) * 16384 + hw * 8192 + k * 1024 + lane * 16) = v[k];
                    asm volatile("s_waitcnt lgkmcnt(0)" ::: "memory");
                    ++staged;
                    prod[hw] = staged;
                }
                if (staged > s + 1 || staged > NSEG - 2) break;
                const int ow = VOWN(staged);
                int& vd = ow ? vdone1 : vdone0;
                if (vd < staged) vd = wait_flags(rw, FL_V + ow, 2, NH, staged <= s ? staged : -1, lane, budget, true);
                if (vd < staged) break;
#pragma unroll
                for (int k = 0; k < 8; ++k) v[k] = LD16(rw, vo + (k & 3) * 1024, staged * (NR * NH * 1024) + (k >> 2) * 4096);
                go = true;
            }
            DRAIN();
        }
        {
            int l2 = (int)__builtin_amdgcn_mbcnt_hi(~0u, __builtin_amdgcn_mbcnt_lo(~0u, 0u));
            asm volatile("" : "+v"(l2));
            const int zf = OFF_ZH + r * 4096 + hw * 2048 + (NSEG - 1) * (NR * NH * 256) + l2 * 16;
            ST16(zv[0], rw, zf); ST16(zv[1], rw, zf + 1024);
        }
    } else {
        TK tk; tk_init(tk);
        for (int s = 0; s < NSEG; ++s) {
            if (s > 0) {
                gather_mem4(rw, NEARB(s, rep, pm), ml, lane, w - 4, tka, s, budget);
            }
            BC2 bc = {0u, 0u};
            if (w == 7) bc = beacon_issue(rw, FARB(s, rep, pm), lane);
            LBAR();
            qk_project(Ba, Bb, ma, zw, idx);
            LBAR();
            if (w == 7) {
                if (s < NSEG - 1) {
                    if (s > 0) { tk_T0(tk, beacon_finish(bc, rw, FARB(s, rep, pm), lane, budget), a); if (tk.okA) tk_post(tka, s + 1, tk_specA(tk, a), lane); }
                    tk_post(tkr, s, tk.XR, lane);
                    wait_until(tk.XR + 8);
                    tk_Tp(tk, beacon_max<NH>(rw, FARB(s, rep, pm), BC_QK, lane, budget), a);
                    tk_post(tka, s + 1, tk.XA, lane);
                    tk_post(tkr, s + 1, tk_specR(tk, a), lane);
                }
            } else {
                if (w == 4 && s >= 2 && lane == 0) ST4((u32)(s - 2), rw, (FL_Z + r) * 128);
                __builtin_amdgcn_s_sleep(14);
            }
        }
    }
    DRAIN();
    LBAR();
    if (tid == 0) ST4((u32)(NSEG - 1), rw, (FL_Z + r) * 128);
    if (budget < 0 && lane == 0) ST4(3u, rw, FL_ERR * 128);
}

__device__ __forceinline__ void cexp_l(float lr, float li, float l, float& er, float& ei) {
    const float mag = __builtin_amdgcn_exp2f(lr * l);
    const float ph = li * l;
    const float pe = fmaf(li, l, -ph);
    const float fr = (ph - floorf(ph)) + pe;
    er = mag * __builtin_amdgcn_cosf(fr);
    ei = mag * __builtin_amdgcn_sinf(fr);
}
constexpr int CV_W = 8 * 260 * 16, CV_U = 96 * 80, CV_CH = CV_W + CV_U;
__device__ __forceinline__ void s4_unit(const Args& a, lptr lds, rsrc_t rw, int unit) {
    const int tid = threadIdx.x, lane = tid & 63, w = __builtin_amdgcn_readfirstlane(tid >> 6);
    const int d0 = unit * 4;
    v2u uv[4];
#pragma unroll
    for (int k = 0; k < 4; ++k) uv[k] = *(const v2u*)(a.u + (size_t)(2 * (tid + 512 * (k >> 1)) + (k & 1)) * DM + d0);
    {
        const int c = tid >> 7, nn = tid & 127, n = (d0 + c) * 128 + nn;
        const float dt = rbf(expf(bf2f(a.logdt[d0 + c])));
        const float lr = dt * bf2f(a.Are[n]) * 1.4426950408889634f, li = dt * bf2f(a.Aim[n]) * 0.15915494309189535f;
        const float br = dt * bf2f(a.Bre[n]), bi = dt * bf2f(a.Bim[n]);
        const float cr = bf2f(a.Cre[n]), ci = bf2f(a.Cim[n]);
        LAS f32x2* T = (LAS f32x2*)(lds + c * CV_CH) + nn;
        float er, ei, pr, pi;
        cexp_l(lr, li, 1.0f, er, ei); pr = er; pi = ei;
#pragma unroll
        for (int o = 0; o < 7; ++o) { T[o * 130] = f32x2{pr, pi}; const float t = pr * er - pi * ei; pi = pr * ei + pi * er; pr = t; }
#pragma unroll
        for (int q4 = 0; q4 < 4; ++q4) {
            if (q4 == 0) cexp_l(lr, li, 128.0f, er, ei);
            cexp_l(lr, li, (float)(128 + 512 * q4), pr, pi);
#pragma unroll
            for (int o = 0; o < 4; ++o) { T[(7 + 4 * q4 + o) * 130] = f32x2{pr, pi}; const float t = pr * er - pi * ei; pi = pr * ei + pi * er; pr = t; }
        }
        const float cbr = cr * br - ci * bi, cbi = cr * bi + ci * br;
        cexp_l(lr, li, 8.0f, er, ei);
#pragma unroll
        for (int q4 = 0; q4 < 4; ++q4) {
            float sr, si;
            cexp_l(lr, li, (float)(32 * q4), sr, si);
            pr = cbr * sr - cbi * si; pi = cbr * si + cbi * sr;
#pragma unroll
            for (int o = 0; o < 4; ++o) { T[(23 + 4 * q4 + o) * 130] = f32x2{pr, pi}; const float t = pr * er - pi * ei; pi = pr * ei + pi * er; pr = t; }
        }
    }
    __syncthreads();
    f32x4 kr[3];
    {
        const int x = lane & 15, g = lane >> 4, c = w & 3, nh = w >> 2;
        const int ah0 = x >> 3, o0 = x & 7;
        const int ah1 = x < 8 ? 2 + (x >> 2) : 4 + ((x - 8) >> 1), o1 = x < 8 ? 2 * (x & 3) : 4 * (x & 1);
        const int ah2 = x < 8 ? 8 + x : 16;
        LAS f32x2* T = (LAS f32x2*)(lds + c * CV_CH) + nh * 64 + g;
        LAS f32x2* Ta0 = T + 7 * 130; LAS f32x2* To0 = T + (o0 ? o0 - 1 : 0) * 130;
        LAS f32x2* Ta1 = T + (6 + ah1) * 130; LAS f32x2* To1 = T + (o1 ? o1 - 1 : 0) * 130;
        LAS f32x2* Ta2 = T + (6 + ah2) * 130; LAS f32x2* Tv = T + (23 + x) * 130;
        const f32x2 one = {1.0f, 0.0f};
        f32x4 ar[3] = {}, ai[3] = {};
#pragma unroll
        for (int jj = 0; jj < 16; ++jj) {
            const f32x2 v = Tv[4 * jj], u2 = Ta2[4 * jj], a1 = Ta1[4 * jj];
            f32x2 a0v = Ta0[4 * jj], e0 = To0[4 * jj], e1 = To1[4 * jj];
            a0v = ah0 ? a0v : one; e0 = o0 ? e0 : one; e1 = o1 ? e1 : one;
            const float u0r = a0v[0] * e0[0] - a0v[1] * e0[1], u0i = a0v[0] * e0[1] + a0v[1] * e0[0];
            const float u1r = a1[0] * e1[0] - a1[1] * e1[1], u1i = a1[0] * e1[1] + a1[1] * e1[0];
            ar[2] = __builtin_amdgcn_mfma_f32_16x16x4f32(u2[0], v[0], ar[2], 0, 0, 0);
            ai[2] = __builtin_amdgcn_mfma_f32_16x16x4f32(u2[1], v[1], ai[2], 0, 0, 0);
            ar[0] = __builtin_amdgcn_mfma_f32_16x16x4f32(u0r, v[0], ar[0], 0, 0, 0);
            ai[0] = __builtin_amdgcn_mfma_f32_16x16x4f32(u0i, v[1], ai[0], 0, 0, 0);
            ar[1] = __builtin_amdgcn_mfma_f32_16x16x4f32(u1r, v[0], ar[1], 0, 0, 0);
            ai[1] = __builtin_amdgcn_mfma_f32_16x16x4f32(u1i, v[1], ai[1], 0, 0, 0);
        }
#pragma unroll
        for (int t = 0; t < 3; ++t) kr[t] = ar[t] - ai[t];
    }
    __syncthreads();
    {
        LAS float* T = (LAS float*)(lds + (w & 3) * CV_CH + 4096 + (w >> 2) * 3072);
#pragma unroll
        for (int t = 0; t < 3; ++t)
#pragma unroll
            for (int r = 0; r < 4; ++r) T[(16 * t + 4 * (lane >> 4) + r) * 16 + (lane & 15)] = kr[t][r];
    }
    __syncthreads();
#pragma unroll
    for (int k = 0; k < 8; ++k) {
        const int i = tid + 512 * k, c = i >> 10, l2 = i & 1023;
        LAS float* T = (LAS float*)(lds + c * CV_CH + 4096);
        float v[2];
#pragma unroll
        for (int e = 0; e < 2; ++e) {
            const int lb = (int)rbf((float)(2 * l2 + e));
            const int hi = lb >> 7, o = lb & 7;
            const int row = lb < 256 ? 8 * hi + o : lb < 512 ? 16 + 4 * (hi - 2) + (o >> 1) : lb < 1024 ? 24 + 2 * (hi - 4) + (o >> 2) : 32 + (hi - 8);
            const int ix = row * 16 + ((lb >> 3) & 15);
            v[e] = T[ix] + T[768 + ix];
        }
        *(LAS u32*)(lds + c * CV_CH + CV_W + 2704 + l2 * 4) = pack2(v[0], v[1]);
    }
    __syncthreads();
    for (int i = tid; i < 4 * 259; i += 512) {
        const int c = i / 259, zz = i - c * 259, z = zz - 3;
        lptr ch = lds + c * CV_CH;
        v4u lo = {0, 0, 0, 0}, hi = {0, 0, 0, 0};
        if (z >= 1) lo = *(LAS v4u*)(ch + CV_W + 2704 + (z - 1) * 16);
        if (z >= 0) hi = *(LAS v4u*)(ch + CV_W + 2704 + z * 16);
        const u32 e[8] = {lo.x, lo.y, lo.z, lo.w, hi.x, hi.y, hi.z, hi.w};
#pragma unroll
        for (int cc = 0; cc < 8; ++cc) {
            v4u o;
#pragma unroll
            for (int k = 0; k < 4; ++k) {
                const int t = 8 + cc - 2 * k;
                if (t & 1) o[k] = (e[t >> 1] >> 16) | (e[t >> 1] << 16);
                else o[k] = (e[t >> 1] & 0xffffu) | (e[(t >> 1) - 1] & 0xffff0000u);
            }
            *(LAS v4u*)(ch + (cc * 260 + zz) * 16) = o;
        }
    }
    __syncthreads();
    for (int i = tid; i < 4 * 160; i += 512) *(LAS v4u*)(lds + (i / 160) * CV_CH + CV_W + (i % 160) * 16) = v4u{0, 0, 0, 0};
#pragma unroll
    for (int k2 = 0; k2 < 2; ++k2) {
        const int l = 2 * (tid + 512 * k2);
        const int o = CV_W + (32 + (l >> 5)) * 80 + (l & 31) * 2;
        const v2u p = uv[2 * k2], q = uv[2 * k2 + 1];
        *(LAS u32*)(lds + 0 * CV_CH + o) = (p.x & 0xffffu) | (q.x << 16);
        *(LAS u32*)(lds + 1 * CV_CH + o) = (p.x >> 16) | (q.x & 0xffff0000u);
        *(LAS u32*)(lds + 2 * CV_CH + o) = (p.y & 0xffffu) | (q.y << 16);
        *(LAS u32*)(lds + 3 * CV_CH + o) = (p.y >> 16) | (q.y & 0xffff0000u);
    }
    __syncthreads();
    const int c = w & 3, par = w >> 2, b = lane & 31, hh = lane >> 5;
    lptr ch = lds + c * CV_CH;
    lptr wa = ch + ((b & 7) * 260 + (b >> 3) - hh + 3) * 16;
    lptr ub = ch + CV_W + (32 + b) * 80 + hh * 16;
    f32x16 y0 = {}, y1 = {};
    wa += par * 64; ub -= par * 80;
    {
        bf16x8 fa[2][2], fb[2][2], fc[2][2];
#pragma unroll
        for (int ks = 0; ks < 2; ++ks) { fa[0][ks] = lds8(wa - ks * 32); fb[0][ks] = lds8(ub + ks * 32); fc[0][ks] = lds8(ub + 2560 + ks * 32); }
#pragma unroll
        for (int i = 0; i < 32; ++i) {
            const int cur = i & 1, nx = cur ^ 1;
            if (i + 1 < 32) {
#pragma unroll
                for (int ks = 0; ks < 2; ++ks) {
                    fa[nx][ks] = lds8(wa + (i + 1) * 128 - ks * 32);
                    if (i + 1 < 16) fb[nx][ks] = lds8(ub - (i + 1) * 160 + ks * 32);
                    fc[nx][ks] = lds8(ub + 2560 - (i + 1) * 160 + ks * 32);
                }
            }
            __builtin_amdgcn_sched_barrier(0);
#pragma unroll
            for (int ks = 0; ks < 2; ++ks) {
                if (i < 16) y0 = MFMA32(fa[cur][ks], fb[cur][ks], y0);
                y1 = MFMA32(fa[cur][ks], fc[cur][ks], y1);
            }
            __builtin_amdgcn_sched_barrier(0);
        }
    }
    __syncthreads();
    {
        LAS f32x4* X = (LAS f32x4*)(ch + par * 4096);
        const f32x16 src = par ? y0 : y1;
#pragma unroll
        for (int k = 0; k < 4; ++k) X[k * 64 + lane] = f32x4{src[4 * k], src[4 * k + 1], src[4 * k + 2], src[4 * k + 3]};
    }
    __syncthreads();
    {
        LAS f32x4* X = (LAS f32x4*)(ch + (1 - par) * 4096);
        lptr Y = ch + 8192 + (32 * par + b) * 72 + hh * 8;
        const f32x16 own = par ? y1 : y0;
#pragma unroll
        for (int k = 0; k < 4; ++k) {
            const f32x4 o = X[k * 64 + lane];
            v2u yv = {pack2(own[4 * k] + o[0], own[4 * k + 1] + o[1]), pack2(own[4 * k + 2] + o[2], own[4 * k + 3] + o[3])};
            *(LAS v2u*)(Y + k * 16) = yv;
        }
    }
    __syncthreads();
    const v2u dk = *(const v2u*)(a.dskip + d0);
#pragma unroll
    for (int k2 = 0; k2 < 2; ++k2) {
        const int i = tid + 512 * k2;
        lptr yp = lds + 8192 + (i >> 4) * 72 + (i & 15) * 4;
        const u32 ya = *(LAS u32*)(yp), yb = *(LAS u32*)(yp + CV_CH), yc = *(LAS u32*)(yp + 2 * CV_CH), yd = *(LAS u32*)(yp + 3 * CV_CH);
        const v2u p = uv[2 * k2], q = uv[2 * k2 + 1];
        v4u o;
        o.x = pack2(bflo(ya) + rbf(bflo(p.x) * bflo(dk.x)), bflo(yb) + rbf(bfhi(p.x) * bfhi(dk.x)));
        o.y = pack2(bflo(yc) + rbf(bflo(p.y) * bflo(dk.y)), bflo(yd) + rbf(bfhi(p.y) * bfhi(dk.y)));
        o.z = pack2(bfhi(ya) + rbf(bflo(q.x) * bflo(dk.x)), bfhi(yb) + rbf(bfhi(q.x) * bfhi(dk.x)));
        o.w = pack2(bfhi(yc) + rbf(bflo(q.y) * bflo(dk.y)), bfhi(yd) + rbf(bfhi(q.y) * bfhi(dk.y)));
        ST16(o, rw, OFF_A4 + unit * 16384 + i * 16);
    }
    __syncthreads();
}
__device__ __forceinline__ void role_s4(const Args& a, lptr lds, rsrc_t rw, int idx) {
    if (idx < 32) {
        const int tid = threadIdx.x, t = idx * 64 + (tid >> 3), j = tid & 7;
        float acc = 0.f;
#pragma unroll
        for (int k = 0; k < 9; ++k) {
            const v4u xv = *(const v4u*)(a.u + (size_t)t * DM + (k * 8 + j) * 8), gv = *(const v4u*)(a.gatew + (k * 8 + j) * 8);
#pragma unroll
            for (int e = 0; e < 4; ++e) acc += rbf(bflo(xv[e]) * bflo(gv[e])) + rbf(bfhi(xv[e]) * bfhi(gv[e]));
        }
        acc += __shfl_xor(acc, 1); acc += __shfl_xor(acc, 2); acc += __shfl_xor(acc, 4);
        acc = rbf(acc);
        if (j == 0) ST4(__float_as_uint(rbf(1.0f / (1.0f + __expf(-acc)))), rw, OFF_G + t * 4);
    }
#pragma unroll 1
    for (int unit = idx; unit < DM / 4; unit += NS4) s4_unit(a, lds, rw, unit);
    DRAIN();
    __syncthreads();
    if (threadIdx.x == 0) ST4(1u, rw, (FL_S + idx) * 128);
}

__device__ __forceinline__ void role_post(const Args& a, lptr lds, rsrc_t rw) {
    const int tid = threadIdx.x, lane = tid & 63, w = __builtin_amdgcn_readfirstlane(tid >> 6);
    const int i16 = lane & 15, g = lane >> 4;
    int budget = SPIN_MAX, vdone0 = -1, vdone1 = -1;
    volatile LAS int* U = (volatile LAS int*)(lds + 81920);
    wait_flags(rw, FL_S, 1, 64, 1, lane, budget);
    wait_flags(rw, FL_S + 64, 1, NS4 - 64, 1, lane, budget);
    constexpr int NFULL = 62 * 6, NUNIT = NFULL + 24;
#pragma unroll 1
    for (;;) {
        if (tid == 0) *U = 1 + (int)__hip_atomic_fetch_add((u32*)(a.ws + FL_Q * 128), 1u, __ATOMIC_RELAXED, __HIP_MEMORY_SCOPE_AGENT);
        __syncthreads();
        const int unit = __builtin_amdgcn_readfirstlane(*U);
        if (unit >= NUNIT || budget < 0) break;
        const bool half = unit >= NFULL;
        const int uu = half ? NFULL + ((unit - NFULL) >> 1) : unit, q0 = half ? (unit - NFULL) & 1 : 0;
        const int s = uu / 6, r = (uu - s * 6) * 6 + (w < 6 ? w : 0);
        bf16x8 b[32];
        v2u av[2]; float gt[2];
        if (w < 6) {
            if (VOWN(s)) { if (vdone1 < s) vdone1 = wait_flags(rw, FL_V + 1, 2, NH, s, lane, budget); }
            else { if (vdone0 < s) vdone0 = wait_flags(rw, FL_V, 2, NH, s, lane, budget); }
            const int vo = OFF_VOX + (s * NR + r) * (NH * 1024) + ((i16 >> 1) & 1) * 16384 + ((g & 1) * 16 + (g >> 1) * 8 + 2 * (i16 >> 2) + (i16 & 1)) * 16;
#pragma unroll
            for (int k = 0; k < 32; ++k) b[k] = as8(LD16(rw, vo + (k & 7) * 512, (k >> 3) * 4096));
#pragma unroll
            for (int qt = 0; qt < 2; ++qt) {
                av[qt] = LD8(rw, OFF_A4 + (4 * r + g) * 16384 + (32 * s + 16 * (qt ^ q0) + i16) * 8, 0);
                gt[qt] = __uint_as_float(LD4(rw, OFF_G + (32 * s + 16 * (qt ^ q0) + i16) * 4, 0));
            }
        }
        v4u z[8];
        const int zo = OFF_ZH + (s * NR + r) * (NH * 256) + ((i16 >> 1) & 1) * 4096 + g * 128 + (2 * (i16 >> 2) + (i16 & 1)) * 16;
        if (half) {
            wait_flags(rw, FL_Z + 2 * r, 1, 2, s, lane, budget, true);
#pragma unroll
            for (int k = 0; k < 8; ++k) z[k] = LD16(rw, zo + k * 512, 0);
            wait_flags(rw, FL_P + (s & 3), 4, 16, s, lane, budget, true);
            v4u pv[5];
#pragma unroll
            for (int k = 0; k < 5; ++k) pv[k] = LD16(rw, OFF_PX + s * 81920 + q0 * 40960 + tid * 16, k * 8192);
#pragma unroll
            for (int k = 0; k < 5; ++k) *(LAS v4u*)(lds + k * 8192 + tid * 16) = pv[k];
        } else {
            wait_flags(rw, FL_P + (s & 3), 4, 16, s, lane, budget);
            v4u pv[10];
#pragma unroll
            for (int k = 0; k < 10; ++k) pv[k] = LD16(rw, OFF_PX + s * 81920 + tid * 16, k * 8192);
            wait_flags(rw, FL_Z + 2 * r, 1, 2, s, lane, budget);
#pragma unroll
            for (int k = 0; k < 8; ++k) z[k] = LD16(rw, zo + k * 512, 0);
            MB();
#pragma unroll
            for (int k = 0; k < 10; ++k) *(LAS v4u*)(lds + k * 8192 + tid * 16) = pv[k];
        }
        __syncthreads();
        if (w < 6) {
#pragma unroll 1
            for (int qt = 0; qt < (half ? 1 : 2); ++qt) {
                f32x4 d = {}, e = {};
                lptr pl = lds + qt * 40960 + lane * 16;
                bf16x8 f[6];
#pragma unroll
                for (int i = 0; i < 6; ++i) f[i] = lds8(pl + i * 1024);
                __builtin_amdgcn_sched_barrier(0);
#pragma unroll
                for (int k = 0; k < 40; ++k) {
                    const bf16x8 v = k < 32 ? b[k < 32 ? k : 0] : as8(z[k < 32 ? 0 : k - 32]);
                    if (k & 1) e = MFMA16(v, f[k % 6], e); else d = MFMA16(v, f[k % 6], d);
                    if (k + 6 < 40) f[k % 6] = lds8(pl + (k + 6) * 1024);
                    __builtin_amdgcn_sched_barrier(0);
                }
                d += e;
                const float gq = qt ? gt[1] : gt[0], og = rbf(1.0f - gq);
                const v2u aq = qt ? av[1] : av[0];
                const float r0 = rbf(gq * bflo(aq.x)) + rbf(og * rbf(d[0])), r1 = rbf(gq * bfhi(aq.x)) + rbf(og * rbf(d[1]));
                const float r2 = rbf(gq * bflo(aq.y)) + rbf(og * rbf(d[2])), r3 = rbf(gq * bfhi(aq.y)) + rbf(og * rbf(d[3]));
                v2u ov = {pack2(r0, r1), pack2(r2, r3)};
                *(v2u*)(a.out + (size_t)(32 * s + 16 * (qt ^ q0) + i16) * DM + 16 * r + 4 * g) = ov;
            }
        }
        if (half) break;
        LBAR();
    }
    if (budget < 0 && lane == 0) ST4(6u, rw, FL_ERR * 128);
}

__global__ void __launch_bounds__(512, 2) k_all(Args a) {
    extern __shared__ __attribute__((aligned(16))) char smem[];
    lptr lds = (lptr)smem;
    const rsrc_t rw = __builtin_amdgcn_make_buffer_rsrc(a.ws, 0, WS_BYTES, 0x00020000);
    if (threadIdx.x == 0) {
        const int x = (int)(__builtin_amdgcn_s_getreg(20 | (3 << 11)) & 7u);
        int role = 0;
        for (int k = 0; k < 8; ++k) {
            const int gq = (x + k) & 7;
            const int t = 1 + (int)__hip_atomic_fetch_add((u32*)(a.ws + (FL_T + gq) * 128), 1u, __ATOMIC_RELAXED, __HIP_MEMORY_SCOPE_AGENT);
            if (t < 32) { role = gq + 8 * t; break; }
        }
        *(volatile LAS int*)lds = role;
    }
    __syncthreads();
    const int b = __builtin_amdgcn_readfirstlane(*(volatile LAS int*)lds), xg = b & 7, slot = b >> 3;
    __syncthreads();

    if (b == 8 * 8) role_probe(rw);
    if (slot >= 20) { role_wvo(a, lds, rw, (xg & 3) * 24 + (xg < 4 ? 12 : 0) + slot - 20); __syncthreads(); }
    if ((xg < 4 && slot >= 26) || (xg >= 4 && slot >= 20)) {
        const int idx = xg < 4 ? 48 + (slot - 26) * 4 + xg : (31 - slot) * 4 + (xg - 4);
        role_s4(a, lds, rw, idx);
        __syncthreads();
    }
    bool post = false;
    if (xg < 4) {
        if (slot < 8) role_qk(a, lds, rw, xg * 8 + slot, xg);
        else if (slot < 26) role_rz(a, lds, rw, xg * 18 + slot - 8, xg);
    } else {
        const int j = xg - 4;
        post = true;
        if (slot < 16) { role_vvo(a, lds, rw, j * 8 + (slot >> 1), slot & 1); post = slot < NJOIN; }
        else if (slot < 20) role_k2(a, lds, rw, j * 4 + slot - 16);
        else if (slot < 28) { role_q2p(a, lds, rw, j * 4 + ((slot - 20) >> 1), slot & 1); post = false; }
    }
    if (post) { __syncthreads(); role_post(a, lds, rw); }
}

extern "C" void kernel_launch(void* const* d_in, const int* in_sizes, int n_in, void* d_out, int out_size, void* d_ws, size_t ws_size, hipStream_t stream) {
    Args a;
    a.u = (const u16*)d_in[0]; a.Are = (const u16*)d_in[1]; a.Aim = (const u16*)d_in[2]; a.Bre = (const u16*)d_in[3]; a.Bim = (const u16*)d_in[4];
    a.Cre = (const u16*)d_in[5]; a.Cim = (const u16*)d_in[6]; a.logdt = (const u16*)d_in[7]; a.dskip = (const u16*)d_in[8];
    a.mem0 = (const u16*)d_in[10]; a.Wq = (const u16*)d_in[11]; a.Wk = (const u16*)d_in[12]; a.Wv = (const u16*)d_in[13]; a.Wo = (const u16*)d_in[14];
    a.gatew = (const u16*)d_in[16]; a.out = (u16*)d_out; a.ws = (char*)d_ws;
    const int LDS_ALL = 160 * 1024;
    (void)hipFuncSetAttribute((const void*)k_all, hipFuncAttributeMaxDynamicSharedMemorySize, LDS_ALL);
    (void)hipMemsetAsync(d_ws, 0xFF, INIT_BYTES, stream);
    hipLaunchKernelGGL(k_all, dim3(256), dim3(512), LDS_ALL, stream, a);
}
```

```cpp
#include <hip/hip_runtime.h>
#include <hip/hip_bf16.h>
#include <stdint.h>

typedef unsigned short u16;
typedef unsigned int u32;
typedef u32 v4u __attribute__((ext_vector_type(4)));
typedef u32 v2u __attribute__((ext_vector_type(2)));
typedef float f32x4 __attribute__((ext_vector_type(4)));
typedef float f32x2 __attribute__((ext_vector_type(2)));
typedef float f32x16 __attribute__((ext_vector_type(16)));
typedef __bf16 bf16x2 __attribute__((ext_vector_type(2)));
typedef __bf16 bf16x8 __attribute__((ext_vector_type(8)));
typedef __bf16 bf16x16 __attribute__((ext_vector_type(16)));
typedef short s16x4 __attribute__((ext_vector_type(4)));
#define LAS __attribute__((address_space(3)))
typedef LAS char* lptr;
typedef __amdgpu_buffer_rsrc_t rsrc_t;

constexpr int SEQ = 2048, DM = 576, NH = 32, DH = 128, NSEG = 64, NR = 36, NR8 = 72, NREP = 4;
constexpr int MEMB = 9216, BC_QK = 80;
constexpr int FL_K = 0, FL_V = 16, FL_M = 80, FL_P = 112, FL_S = 176, FL_Q = 254, FL_ERR = 255, FL_Z = 256, FL_W = 328, FL_T = 424, FL_PAR = 432, NS4 = 72;
constexpr int OFF_RING = 65536;
constexpr int N_MEMA = 0, N_PX = 16384, N_PMM = 49152, N_MEMB = 49152 + 4096, N_PROBE = 49152 + 6144;
constexpr int INIT_BYTES = OFF_RING + 16 * 65536;
constexpr int OFF_ZH = INIT_BYTES;
constexpr int OFF_KX = OFF_ZH + NSEG * NR * NH * 256;
constexpr int OFF_VOX = OFF_KX + NH * SEQ * DH * 2;
constexpr int OFF_KM = OFF_VOX + NSEG * NR * NH * 1024;
constexpr int OFF_PX = OFF_KM + NSEG * NH * 2048;
constexpr int OFF_A4 = OFF_PX + NSEG * 81920;
constexpr int OFF_G = OFF_A4 + SEQ * DM * 2;
constexpr int OFF_WVO = OFF_G + SEQ * 4;
constexpr int WS_BYTES = OFF_WVO + NR8 * 294912;
constexpr int TK_LA = 10, TK_LR = 10, TK_TOL = 8, TK_GAP = 30;
constexpr int NJOIN = 4;

struct Args {
    const u16* u; const u16* Are; const u16* Aim; const u16* Bre; const u16* Bim; const u16* Cre; const u16* Cim;
    const u16* logdt; const u16* dskip; const u16* mem0; const u16* Wq; const u16* Wk; const u16* Wv; const u16* Wo;
    const u16* gatew; u16* out; char* ws;
};

__device__ __forceinline__ float bf2f(u16 v) { return __uint_as_float((u32)v << 16); }
__device__ __forceinline__ float bflo(u32 v) { return __uint_as_float(v << 16); }
__device__ __forceinline__ float bfhi(u32 v) { return __uint_as_float(v & 0xffff0000u); }
__device__ __forceinline__ u32 pack2(float lo, float hi) { f32x2 f = {lo, hi}; bf16x2 b = __builtin_convertvector(f, bf16x2); return __builtin_bit_cast(u32, b); }
__device__ __forceinline__ float rbf(float x) { return bflo(pack2(x, x)); }
__device__ __forceinline__ u32 nos(u32 v) { return min(v, 0xFFFFFFFEu); }
__device__ __forceinline__ v2u trd(lptr p) { return __builtin_bit_cast(v2u, __builtin_amdgcn_ds_read_tr16_b64_v4i16((LAS s16x4*)p)); }
__device__ __forceinline__ bf16x8 as8(v4u v) { return __builtin_bit_cast(bf16x8, v); }
__device__ __forceinline__ bf16x8 as8(v2u a, v2u b) { v4u v = {a.x, a.y, b.x, b.y}; return __builtin_bit_cast(bf16x8, v); }
__device__ __forceinline__ v4u asu(bf16x8 v) { return __builtin_bit_cast(v4u, v); }
__device__ __forceinline__ bf16x16 cat16(bf16x8 a, bf16x8 b) { return __builtin_shufflevector(a, b, 0, 1, 2, 3, 4, 5, 6, 7, 8, 9, 10, 11, 12, 13, 14, 15); }
__device__ __forceinline__ bf16x8 lds8(lptr p) { return *(LAS bf16x8*)p; }
__device__ __forceinline__ u32 bad4(v4u v) { return max(max(v.x, v.y), max(v.z, v.w)); }
#define NEARB(s, rep, pm) (OFF_RING + ((((s) & 3) * 4 + (rep)) << 16) + ((((pm) >> (((s) & 3) * 4 + (rep))) & 1) << 13))
#define FARB(s, rep, pm) ((NEARB(s, rep, pm) ^ 8192) + (((s) & 4) << 13))
#define BCO(slot) (((slot) & 63) * 128 + ((slot) >> 6) * 16384)
#define LBAR() do { asm volatile("s_waitcnt lgkmcnt(0)" ::: "memory"); __builtin_amdgcn_s_barrier(); asm volatile("" ::: "memory"); } while (0)
#define MB() asm volatile("" ::: "memory")
#define DRAIN() asm volatile("s_waitcnt vmcnt(0)" ::: "memory")
#define PIN4(x) asm volatile("" : "+v"(x))
#define MFMA16(a, b, c) __builtin_amdgcn_mfma_f32_16x16x32_bf16((a), (b), (c), 0, 0, 0)
#define MFMA32(a, b, c) __builtin_amdgcn_mfma_f32_32x32x16_bf16((a), (b), (c), 0, 0, 0)
#define SMF(a, b, c, idx) __builtin_amdgcn_smfmac_f32_16x16x64_bf16((a), (b), (c), (idx), 0, 0)
#define LD16(r, vo, so) __builtin_amdgcn_raw_buffer_load_b128((r), (vo), (so), 16)
#define LD8(r, vo, so) __builtin_amdgcn_raw_buffer_load_b64((r), (vo), (so), 16)
#define LD4(r, vo, so) __builtin_amdgcn_raw_buffer_load_b32((r), (vo), (so), 16)
#define ST16(v, r, vo) __builtin_amdgcn_raw_buffer_store_b128((v), (r), (vo), 0, 16)
#define ST8(v, r, vo) __builtin_amdgcn_raw_buffer_store_b64((v), (r), (vo), 0, 16)
#define ST4(v, r, vo) __builtin_amdgcn_raw_buffer_store_b32((v), (r), (vo), 0, 16)

__device__ __forceinline__ float swap32_sum(float a, float b) {
    auto r = __builtin_amdgcn_permlane32_swap(__float_as_uint(a), __float_as_uint(b), false, false);
    return __uint_as_float(r[0]) + __uint_as_float(r[1]); }
__device__ __forceinline__ float xmax(float m) {
    auto r = __builtin_amdgcn_permlane16_swap(__float_as_uint(m), __float_as_uint(m), false, false);
    m = fmaxf(__uint_as_float(r[0]), __uint_as_float(r[1]));
    auto q = __builtin_amdgcn_permlane32_swap(__float_as_uint(m), __float_as_uint(m), false, false);
    return fmaxf(__uint_as_float(q[0]), __uint_as_float(q[1])); }
__device__ __forceinline__ float xsum(float m) {
    auto r = __builtin_amdgcn_permlane16_swap(__float_as_uint(m), __float_as_uint(m), false, false);
    m = __uint_as_float(r[0]) + __uint_as_float(r[1]);
    auto q = __builtin_amdgcn_permlane32_swap(__float_as_uint(m), __float_as_uint(m), false, false);
    return __uint_as_float(q[0]) + __uint_as_float(q[1]); }
__device__ __forceinline__ float vmax(float a, float b) { float r; asm("v_max_f32 %0, %1, %2" : "=v"(r) : "v"(a), "v"(b)); return r; }
__device__ __forceinline__ float vmax3(float a, float b, float c) { float r; asm("v_max3_f32 %0, %1, %2, %3" : "=v"(r) : "v"(a), "v"(b), "v"(c)); return r; }
__device__ __forceinline__ u32 ror8(u32 v) { return (u32)__builtin_amdgcn_update_dpp(0, (int)v, 0x128, 0xf, 0xf, true); }
__device__ __forceinline__ float ror8f(float v) { return __uint_as_float(ror8(__float_as_uint(v))); }
__device__ __forceinline__ int wave_max(int d) {
    d = max(d, __builtin_amdgcn_update_dpp(d, d, 0x128, 0xf, 0xf, false));
    d = max(d, __builtin_amdgcn_update_dpp(d, d, 0x124, 0xf, 0xf, false));
    d = max(d, __builtin_amdgcn_update_dpp(d, d, 0x122, 0xf, 0xf, false));
    d = max(d, __builtin_amdgcn_update_dpp(d, d, 0x121, 0xf, 0xf, false));
    return max(max(__builtin_amdgcn_readlane(d, 0), __builtin_amdgcn_readlane(d, 16)), max(__builtin_amdgcn_readlane(d, 32), __builtin_amdgcn_readlane(d, 48)));
}

constexpr int SPIN_MAX = 2000000;
__device__ __forceinline__ int wait_flags(rsrc_t rw, int line0, int stride, int n, int need, int lane, int& budget, bool fast = false) {
    const int vo = (line0 + (lane < n ? lane : 0) * stride) * 128;
    int mn;
    for (;;) {
        const int v = (int)LD4(rw, vo, 0);
        MB();
        mn = -wave_max(-v);
        if (mn >= need) break;
        if (--budget < 0) break;
        if (fast) __builtin_amdgcn_s_sleep(2); else __builtin_amdgcn_s_sleep(12);
    }
    return mn;
}

template <int NRW, int CW>
__device__ __forceinline__ void stage_rows(const u16* g, int gs, lptr lds, int ls, int tid) {
    constexpr int N = NRW * CW / 512;
    static_assert(NRW * CW % 512 == 0, "");
    v4u v[N];
#pragma unroll
    for (int j = 0; j < N; ++j) { const int i = tid + 512 * j, row = i / CW, c = i % CW; v[j] = *(const v4u*)(g + (size_t)row * gs + c * 8); }
#pragma unroll
    for (int j = 0; j < N; ++j) { const int i = tid + 512 * j, row = i / CW, c = i % CW; *(LAS v4u*)(lds + row * ls + c * 16) = v[j]; }
}
template <int NRW, int CW>
__device__ __forceinline__ void rows_ld(v4u (&v)[NRW * CW / 512], const u16* g, int gs, int tid) {
#pragma unroll
    for (int j = 0; j < NRW * CW / 512; ++j) { const int i = tid + 512 * j, row = i / CW, c = i % CW; v[j] = *(const v4u*)(g + (size_t)row * gs + c * 8); }
}
template <int NRW, int CW>
__device__ __forceinline__ void rows_st(const v4u (&v)[NRW * CW / 512], lptr lds, int ls, int tid) {
#pragma unroll
    for (int j = 0; j < NRW * CW / 512; ++j) { const int i = tid + 512 * j, row = i / CW, c = i % CW; *(LAS v4u*)(lds + row * ls + c * 16) = v[j]; }
}
__device__ __forceinline__ bf16x8 frag16(lptr base, int ls, int lane) {
    const int i16 = lane & 15, bg = lane >> 4, q = i16 >> 2, p = i16 & 3;
    lptr a = base + (8 * bg + q) * ls + p * 8;
    return as8(trd(a), trd(a + 4 * ls));
}
__device__ __forceinline__ bf16x8 frag32p(lptr base, int ls, int lane) {
    const int i16 = lane & 15, G = lane >> 4, q = i16 >> 2, p = i16 & 3;
    lptr a = base + (8 * (G >> 1) + q) * ls + (16 * (p & 1) + 8 * (G & 1) + 4 * (p >> 1)) * 2;
    return as8(trd(a), trd(a + 4 * ls));
}

constexpr int XS = 1168;
__device__ __forceinline__ void xload(v4u (&st)[9], const u16* x, int tok0, int tid) {
#pragma unroll
    for (int j = 0; j < 9; ++j) st[j] = *(const v4u*)(x + (size_t)tok0 * DM + (tid + 512 * j) * 8);
}
__device__ __forceinline__ void xstore(const v4u (&st)[9], lptr xl, int tid) {
#pragma unroll
    for (int j = 0; j < 9; ++j) { const int i = tid + 512 * j; *(LAS v4u*)(xl + i * 16 + (i / 72) * 16) = st[j]; }
}
__device__ __forceinline__ void load_w2(bf16x8 (&A)[36], const u16* W, int h0, lptr lds, int tid, int lane, int w) {
    v4u v[6][6];
#pragma unroll
    for (int c = 0; c < 6; ++c) rows_ld<96, 32>(v[c], W + (size_t)c * 96 * 4096 + h0 * DH, 4096, tid);
    __builtin_amdgcn_sched_barrier(0);
#pragma unroll
    for (int c = 0; c < 6; ++c) {
        lptr l = lds + (c & 1) * (96 * 544);
        rows_st<96, 32>(v[c], l, 544, tid);
        LBAR();
#pragma unroll
        for (int ks = 0; ks < 6; ++ks) A[c * 6 + ks] = frag32p(l + ks * 16 * 544 + w * 64, 544, lane);
        __builtin_amdgcn_sched_barrier(0);
    }
    LBAR();
}

__device__ __forceinline__ f32x16 proj_tile(const bf16x8 (&A)[36], lptr xb) {
    f32x16 acc = {};
    bf16x8 f[6];
#pragma unroll
    for (int i = 0; i < 6; ++i) f[i] = lds8(xb + i * 32);
    __builtin_amdgcn_sched_barrier(0);
#pragma unroll
    for (int ks = 0; ks < 36; ++ks) {
        acc = MFMA32(A[ks], f[ks % 6], acc);
        if (ks + 6 < 36) f[ks % 6] = lds8(xb + (ks + 6) * 32);
        __builtin_amdgcn_sched_barrier(0);
    }
    return acc;
}
__device__ __forceinline__ void role_k2(const Args& a, lptr lds, rsrc_t rw, int cu) {
    const int tid = threadIdx.x, lane = tid & 63, w = __builtin_amdgcn_readfirstlane(tid >> 6);
    v4u st[9];
    bf16x8 A[36];
    xload(st, a.u, 0, tid);
    load_w2(A, a.Wk, 2 * cu, lds, tid, lane, w);
    xstore(st, lds, tid);
    __syncthreads();
    const int o0 = OFF_KX + (2 * cu + (w >> 2)) * (NSEG * 8192) + ((w & 3) * 4 + (lane >> 5) * 2) * 512 + (lane & 31) * 16;
    for (int blk = 0; blk < 32; ++blk) {
        lptr xl = lds + (blk & 1) * (64 * XS);
        if (blk + 1 < 32) xload(st, a.u, (blk + 1) * 64, tid);
#pragma unroll
        for (int tt = 0; tt < 2; ++tt) {
            const f32x16 acc = proj_tile(A, xl + (32 * tt + (lane & 31)) * XS + (lane >> 5) * 16);
            v4u c0 = {pack2(acc[0], acc[1]), pack2(acc[2], acc[3]), pack2(acc[4], acc[5]), pack2(acc[6], acc[7])};
            v4u c1 = {pack2(acc[8], acc[9]), pack2(acc[10], acc[11]), pack2(acc[12], acc[13]), pack2(acc[14], acc[15])};
            const int o = o0 + (blk * 2 + tt) * 8192;
            ST16(c0, rw, o); ST16(c1, rw, o + 512);
            if (blk == 0 && tt == 0) { DRAIN(); LBAR(); if (tid == 0) ST4(0u, rw, (FL_K + cu) * 128); }
        }
        if (blk + 1 < 32) xstore(st, lds + ((blk + 1) & 1) * (64 * XS), tid);
        const bool full = blk < 4 || blk == 31;
        if (full) DRAIN(); else asm volatile("s_waitcnt vmcnt(4)" ::: "memory");
        LBAR();
        if (tid == 0) ST4((u32)(full ? 2 * blk + 1 : 2 * blk - 1), rw, (FL_K + cu) * 128);
    }
}

constexpr int VS = 272;
__device__ __forceinline__ void role_q2p(const Args& a, lptr lds, rsrc_t rw, int cu, int par) {
    const int tid = threadIdx.x, lane = tid & 63, w = __builtin_amdgcn_readfirstlane(tid >> 6);
    v4u st[9];
    bf16x8 A[36];
    load_w2(A, a.Wq, 2 * cu, lds, tid, lane, w);
    lptr xl = lds, ql = lds + 64 * XS;
    LAS int* cnt = (LAS int*)(lds + 64 * XS + 4 * 64 * VS);
    if (tid < 2) cnt[tid] = 0;
    xload(st, a.u, par * 64, tid);
    xstore(st, xl, tid);
    __syncthreads();
    int budget = SPIN_MAX, kdone = -1;
    const int i16 = lane & 15, g = lane >> 4;
    const int sg = w >> 2, hh = (w >> 1) & 1, qt = w & 1, h = 2 * cu + hh;
    const float c2 = 0.08838834764831845f * 1.4426950408889634f;
    for (int it = -1; it < 16; ++it) {
        const int blk = 2 * it + par;
        if (it + 1 < 16) {
            if (it + 2 < 16) xload(st, a.u, (blk + 4) * 64, tid);
            lptr qb = ql + (((it + 1) & 1) * 2 + (w >> 2)) * (64 * VS) + ((w & 3) * 32 + (lane >> 5) * 16) * 2;
#pragma unroll
            for (int tt = 0; tt < 2; ++tt) {
                const f32x16 acc = proj_tile(A, xl + (32 * tt + (lane & 31)) * XS + (lane >> 5) * 16);
                v4u c0 = {pack2(acc[0], acc[1]), pack2(acc[2], acc[3]), pack2(acc[4], acc[5]), pack2(acc[6], acc[7])};
                v4u c1 = {pack2(acc[8], acc[9]), pack2(acc[10], acc[11]), pack2(acc[12], acc[13]), pack2(acc[14], acc[15])};
                lptr qp = qb + (32 * tt + (lane & 31)) * VS;
                *(LAS v4u*)qp = c0; *(LAS v4u*)(qp + 16) = c1;
            }
            __syncthreads();
            if (it + 2 < 16) xstore(st, xl, tid);
        }
        if (it >= 0) {
            const int s = 2 * blk + sg;
            if (kdone < s) kdone = wait_flags(rw, FL_K + cu, 1, 1, s, lane, budget);
            bf16x8 k0[4], k1[4], km[4], qf[4];
            const int ko = OFF_KX + (h * NSEG + s) * 8192 + g * 512 + i16 * 16;
#pragma unroll
            for (int ks = 0; ks < 4; ++ks) { k0[ks] = as8(LD16(rw, ko, ks * 2048)); k1[ks] = as8(LD16(rw, ko + 256, ks * 2048)); }
            lptr qp = ql + ((it & 1) * 2 + hh) * (64 * VS) + (32 * sg + 16 * qt + i16) * VS + g * 16;
#pragma unroll
            for (int ks = 0; ks < 4; ++ks) qf[ks] = lds8(qp + ks * 64);
            f32x4 S0 = {}, S1 = {}, Sm = {};
#pragma unroll
            for (int ks = 0; ks < 4; ++ks) { S0 = MFMA16(k0[ks], qf[ks], S0); S1 = MFMA16(k1[ks], qf[ks], S1); }
            wait_flags(rw, FL_M + h, 1, 1, s, lane, budget, s >= 62);
            const int mo = OFF_KM + (s * NH + h) * 2048 + g * 128 + (i16 & 7) * 16;
#pragma unroll
            for (int ks = 0; ks < 4; ++ks) km[ks] = as8(LD16(rw, mo + ks * 512, 0));
#pragma unroll
            for (int ks = 0; ks < 4; ++ks) Sm = MFMA16(km[ks], qf[ks], Sm);
            const int qi = 16 * qt + i16, t0 = 4 * g;
#pragma unroll
            for (int r = 0; r < 4; ++r) {
                if (t0 + r > qi) S0[r] = -INFINITY;
                if (t0 + r + 16 > qi) S1[r] = -INFINITY;
                if (g >= 2) Sm[r] = -INFINITY;
            }
            float m = fmaxf(fmaxf(fmaxf(S0[0], S0[1]), fmaxf(S0[2], S0[3])), fmaxf(fmaxf(S1[0], S1[1]), fmaxf(S1[2], S1[3])));
            m = fmaxf(m, fmaxf(fmaxf(Sm[0], Sm[1]), fmaxf(Sm[2], Sm[3])));
            m = xmax(m);
            const float nm = -m * c2;
            float sum = 0.f;
#pragma unroll
            for (int r = 0; r < 4; ++r) {
                S0[r] = __builtin_amdgcn_exp2f(fmaf(S0[r], c2, nm)); S1[r] = __builtin_amdgcn_exp2f(fmaf(S1[r], c2, nm)); Sm[r] = __builtin_amdgcn_exp2f(fmaf(Sm[r], c2, nm));
                sum += S0[r] + S1[r] + Sm[r];
            }
            sum = xsum(sum);
            const float inv = __builtin_amdgcn_rcpf(sum);
            v4u px = {pack2(S0[0] * inv, S0[1] * inv), pack2(S0[2] * inv, S0[3] * inv), pack2(S1[0] * inv, S1[1] * inv), pack2(S1[2] * inv, S1[3] * inv)};
            const int po = OFF_PX + (s * 2 + qt) * 40960;
            ST16(px, rw, po + h * 1024 + lane * 16);
            const u32 ma = pack2(Sm[0] * inv, Sm[1] * inv), mb = pack2(Sm[2] * inv, Sm[3] * inv);
            auto ra = __builtin_amdgcn_permlane16_swap(ma, ma, false, false);
            auto rb = __builtin_amdgcn_permlane16_swap(mb, mb, false, false);
            v4u pm = {ra[0], rb[0], ra[1], rb[1]};
            if (lane < 16) ST16(pm, rw, po + 32768 + h * 256 + lane * 16);
            DRAIN();
            if (lane == 0) {
                const int old = __hip_atomic_fetch_add(cnt + sg, 1, __ATOMIC_RELAXED, __HIP_MEMORY_SCOPE_WORKGROUP);
                if ((old & 3) == 3) ST4((u32)s, rw, (FL_P + 4 * cu + 2 * par + sg) * 128);
            }
        }
        __syncthreads();
    }
    if (budget < 0 && lane == 0) ST4(5u, rw, FL_ERR * 128);
}

__device__ __forceinline__ void role_vvo(const Args& a, lptr lds, rsrc_t rw, int h, int par) {
    const int tid = threadIdx.x, lane = tid & 63, w = __builtin_amdgcn_readfirstlane(tid >> 6);
    v4u st[9];
#pragma unroll
    for (int j = 0; j < 9; ++j) { const int i = tid + 512 * j; st[j] = *(const v4u*)(a.u + (size_t)(par * 32 + (i >= 2304 ? 32 : 0)) * DM + i * 8); }
    bf16x8 Av[18], Bo[5][4];
    {
        v4u v[3][6], o[2][9];
#pragma unroll
        for (int c = 0; c < 3; ++c) rows_ld<192, 16>(v[c], a.Wv + (size_t)c * 192 * 4096 + h * DH, 4096, tid);
#pragma unroll
        for (int c = 0; c < 2; ++c) rows_ld<64, 72>(o[c], a.Wo + ((size_t)h * DH + c * 64) * DM, DM, tid);
        __builtin_amdgcn_sched_barrier(0);
#pragma unroll
        for (int c = 0; c < 3; ++c) {
            lptr l = lds + (c & 1) * 77824;
            rows_st<192, 16>(v[c], l, 288, tid);
            LBAR();
#pragma unroll
            for (int ks = 0; ks < 6; ++ks) Av[c * 6 + ks] = frag16(l + ks * 32 * 288 + w * 32, 288, lane);
            __builtin_amdgcn_sched_barrier(0);
        }
#pragma unroll
        for (int c = 0; c < 2; ++c) {
            lptr l = lds + ((c + 1) & 1) * 77824;
            rows_st<64, 72>(o[c], l, 1184, tid);
            LBAR();
#pragma unroll
            for (int j = 0; j < 5; ++j) {
                const int r = (w + 8 * j < NR) ? w + 8 * j : w;
#pragma unroll
                for (int ks = 0; ks < 2; ++ks) Bo[j][c * 2 + ks] = frag16(l + ks * 32 * 1184 + r * 32, 1184, lane);
            }
            __builtin_amdgcn_sched_barrier(0);
        }
        LBAR();
    }
    lptr xl = lds, vl = lds + 64 * XS;
    xstore(st, xl, tid);
    __syncthreads();
    const int rho = lane & 15, bg = lane >> 4;
    const int vo0 = OFF_VOX + ((2 * w + ((rho >> 1) & 1)) * 16 + (h >> 1)) * 1024 + ((2 * (h & 1) + (bg & 1)) * 16 + (bg >> 1) * 8 + 2 * (rho >> 2) + (rho & 1)) * 16;
    const int fo = (FL_V + 2 * h + par) * 128;
    auto stage1 = [&](int tt) {
        f32x4 acc = {};
        lptr xb = xl + (16 * tt + rho) * XS + bg * 16;
        bf16x8 f[6];
#pragma unroll
        for (int i = 0; i < 6; ++i) f[i] = lds8(xb + i * 64);
        __builtin_amdgcn_sched_barrier(0);
#pragma unroll
        for (int ks = 0; ks < 18; ++ks) {
            acc = MFMA16(Av[ks], f[ks % 6], acc);
            if (ks + 6 < 18) f[ks % 6] = lds8(xb + (ks + 6) * 64);
            __builtin_amdgcn_sched_barrier(0);
        }
        v2u o = {pack2(acc[0], acc[1]), pack2(acc[2], acc[3])};
        *(LAS v2u*)(vl + (16 * tt + rho) * VS + (16 * w + 4 * bg) * 2) = o;
    };
    auto stage2 = [&](int sg, int s) {
        bf16x8 Af[2][4];
#pragma unroll
        for (int half = 0; half < 2; ++half)
#pragma unroll
            for (int ks = 0; ks < 4; ++ks) Af[half][ks] = lds8(vl + (32 * sg + rho + 16 * half) * VS + ks * 64 + bg * 16);
#pragma unroll
        for (int j = 0; j < 5; ++j) {
            if (j < 4 || w < 4) {
                f32x4 d0 = {}, d1 = {};
#pragma unroll
                for (int ks = 0; ks < 4; ++ks) { d0 = MFMA16(Af[0][ks], Bo[j][ks], d0); d1 = MFMA16(Af[1][ks], Bo[j][ks], d1); }
                v4u o = {pack2(d0[0], d0[1]), pack2(d0[2], d0[3]), pack2(d1[0], d1[1]), pack2(d1[2], d1[3])};
                ST16(o, rw, vo0 + (s * NR + 8 * j) * (NH * 1024));
            }
        }
    };
    int prev = -1;
#pragma unroll 1
    for (int un = 0; un < 32; ++un) {
        const int sg = un & 1, blk = (un >> 1) * 2 + par;
        const int s = un < 2 ? par + 2 * un : 2 * blk + sg;
        if (sg == 0 && blk + 2 < 32) xload(st, a.u, (blk + 2) * 64, tid);
        stage1(2 * sg); stage1(2 * sg + 1);
        if (sg == 1) DRAIN(); else if (blk + 2 < 32) asm volatile("s_waitcnt vmcnt(9)" ::: "memory"); else DRAIN();
        LBAR();
        if (tid == 0 && prev >= 0) ST4((u32)prev, rw, fo);
        if (sg == 1 && blk + 2 < 32) xstore(st, xl, tid);
        stage2(sg, s);
        prev = s;
        if (un == 0 || un == 31) { DRAIN(); LBAR(); if (tid == 0) ST4((u32)s, rw, fo); }
        else if (sg == 1) LBAR();
    }
}
#define VOWN(s) ((s) < 4 ? ((s) & 1) : (((s) >> 1) & 1))

__device__ __forceinline__ u32 rt32() { return (u32)__builtin_amdgcn_s_memrealtime(); }
__device__ __forceinline__ void wait_until(u32 t) {
    if ((int)(t - rt32()) > 1000) return;
    while ((int)(rt32() - t) < -24) __builtin_amdgcn_s_sleep(4);
    while ((int)(rt32() - t) < 0) {}
}
struct TK { u32 XA, XR, EA, ER; int dA0, dA1, dA2, dR0, dR1, dR2; int okA, okR, gap; };
__device__ __forceinline__ int tk_est(int d0, int d1, int d2) { return max(min(d0, d1), min(max(d0, d1), d2)); }
template <int N>
__device__ __forceinline__ u32 beacon_max(rsrc_t rw, int fb, int slot0, int lane, int& budget) {
    const int vo = BCO(slot0 + (lane < N ? lane : 0)), v2 = BCO(64 + (lane & 7));
    u32 v, u = 0;
    for (;;) {
        v = LD4(rw, vo, fb);
        if (N > 64) u = LD4(rw, v2, fb);
        MB();
        if (__all(max(v, u) != 0xFFFFFFFFu)) break;
        if (--budget < 0) break;
        __builtin_amdgcn_s_sleep(1);
    }
    const u32 base = __builtin_amdgcn_readfirstlane(v);
    int d = (int)(v - base);
    if (N > 64) d = max(d, (int)(u - base));
    return base + (u32)wave_max(d);
}
struct BC2 { u32 v, u; };
__device__ __forceinline__ BC2 beacon_issue(rsrc_t rw, int fb, int lane) {
    BC2 b;
    b.v = LD4(rw, BCO(lane), fb);
    b.u = LD4(rw, BCO(64 + (lane & 7)), fb);
    MB();
    return b;
}
__device__ __forceinline__ u32 beacon_finish(BC2 b, rsrc_t rw, int fb, int lane, int& budget) {
    if (!__all(max(b.v, b.u) != 0xFFFFFFFFu)) return beacon_max<NR8>(rw, fb, 0, lane, budget);
    const u32 base = __builtin_amdgcn_readfirstlane(b.v);
    return base + (u32)wave_max(max((int)(b.v - base), (int)(b.u - base)));
}
__device__ __forceinline__ void tk_T0(TK& k, u32 T0, const Args& a) {
    if (k.okR) { k.dR2 = k.dR1; k.dR1 = k.dR0; k.dR0 = (int)(T0 - k.ER); }
    k.okA = (int)(k.XA - T0) >= TK_LA - TK_TOL;
    if (!k.okR) k.gap = k.okA ? max(TK_GAP, k.gap - 6) : min(k.gap + 16, 160);
    k.EA = k.XA;
    k.XR = k.EA + tk_est(k.dA0, k.dA1, k.dA2) + TK_LR;
}
__device__ __forceinline__ void tk_Tp(TK& k, u32 Tp, const Args& a) {
    const int okn = (int)(k.XR - Tp) >= TK_LR - TK_TOL;
    if (k.okA && (k.okR || !okn)) { k.dA2 = k.dA1; k.dA1 = k.dA0; k.dA0 = (int)(Tp - k.EA); }
    k.okR = okn;
    k.ER = k.okR ? k.XR : Tp + k.gap;
    k.XA = k.ER + tk_est(k.dR0, k.dR1, k.dR2) + TK_LA;
}
__device__ __forceinline__ u32 tk_specA(const TK& k, const Args& a) { return k.XR + tk_est(k.dR0, k.dR1, k.dR2) + TK_LA; }
__device__ __forceinline__ u32 tk_specR(const TK& k, const Args& a) { return k.XA + tk_est(k.dA0, k.dA1, k.dA2) + TK_LR; }
__device__ __forceinline__ void tk_init(TK& k) { const u32 n = rt32(); k.XA = k.XR = k.EA = k.ER = n - 100000u; k.dA0 = k.dA1 = k.dA2 = 125; k.dR0 = k.dR1 = k.dR2 = 65; k.okA = k.okR = 0; k.gap = 50; }

__device__ __forceinline__ void tk_post(lptr p, int tag, u32 t, int lane) { if (lane == 0) { v2u v = {(u32)tag, t}; *(volatile LAS v2u*)p = v; } }
__device__ __forceinline__ u32 tk_get(lptr p, int tag, int& budget) {
    for (;;) {
        const v2u v = *(volatile LAS v2u*)p;
        if ((int)__builtin_amdgcn_readfirstlane(v.x) >= tag || --budget < 0) return __builtin_amdgcn_readfirstlane(v.y);
        __builtin_amdgcn_s_sleep(2);
    }
}
__device__ __forceinline__ void spin_to(u32 X) {
    int rem;
    asm volatile("1:\n\ts_memrealtime vcc\n\ts_waitcnt lgkmcnt(0)\n\ts_sub_i32 %0, %1, vcc_lo\n\ts_cmp_gt_i32 %0, 3\n\ts_cbranch_scc1 1b\n\t"
                 "s_cmp_lt_i32 %0, 1\n\ts_cbranch_scc1 2f\n\ts_nop 3\n\ts_cmp_lt_i32 %0, 2\n\ts_cbranch_scc1 2f\n\ts_nop 3\n\t"
                 "s_cmp_lt_i32 %0, 3\n\ts_cbranch_scc1 2f\n\ts_nop 7\n2:"
                 : "=&s"(rem) : "s"(X) : "memory", "scc", "vcc");
}
__device__ __forceinline__ void tk_wait(lptr p, int tag, int& budget) {
    u32 X;
    for (;;) {
        const v2u v = *(volatile LAS v2u*)p;
        const u32 now = rt32();
        X = __builtin_amdgcn_readfirstlane(v.y);
        const int tg = (int)__builtin_amdgcn_readfirstlane(v.x), dt = (int)(X - now);
        if (tg >= tag) {
            if (dt <= 0 || dt > 1000 || tg > tag) return;
            if (dt <= 28) break;
        }
        if (--budget < 0) return;
        __builtin_amdgcn_s_sleep(1);
    }
    spin_to(X);
}
constexpr int OOB = 0x70000000;
__device__ __forceinline__ void stage_rows8(const u16* src, lptr ml, int tid) {
    for (int c = tid; c < 576; c += 512) {
        const u32* p = (const u32*)(src + (c & 7) * DM + 16 * (c >> 4) + 2 * ((c >> 3) & 1));
        v4u v = {p[0], p[2], p[4], p[6]};
        *(LAS v4u*)(ml + c * 16) = v;
    }
}
__device__ __forceinline__ void gather_mem(rsrc_t rw, int nb, lptr ml, int lane, int w, lptr tkp, int tag, int& budget) {
    const int gi = w == 7 ? 5 : w;
    const int l0 = gi * 1536 + lane * 16, l1 = l0 + 1024;
    const int o0 = l0 < 8192 ? l0 : l0 + (N_MEMB - 8192), o1 = lane < 32 ? (l1 < 8192 ? l1 : l1 + (N_MEMB - 8192)) : OOB;
    lptr p1 = ml + (lane < 32 ? l1 : 14336 + lane * 16);
    asm volatile("" : "+s"(nb));
    tk_wait(tkp, tag, budget);
    v4u v0 = LD16(rw, o0, nb), v1 = LD16(rw, o1, nb);
    MB();
    *(LAS v4u*)(ml + l0) = v0; *(LAS v4u*)p1 = v1;
    MB();
    if (__builtin_expect(!__all(max(bad4(v0), bad4(v1)) != 0xFFFFFFFFu), 0)) {
        do {
            v0 = LD16(rw, o0, nb); v1 = LD16(rw, o1, nb);
            MB();
        } while (!__all(max(bad4(v0), bad4(v1)) != 0xFFFFFFFFu) && --budget >= 0);
        *(LAS v4u*)(ml + l0) = v0; *(LAS v4u*)p1 = v1;
    }
}
__device__ __forceinline__ void gather_mem4(rsrc_t rw, int nb, lptr ml, int lane, int gi, lptr tkp, int tag, int& budget) {
    const int l0 = gi * 2048 + lane * 16;
    const int o2 = lane < 16 ? N_MEMB + gi * 256 + lane * 16 : OOB;
    lptr p2 = ml + (lane < 16 ? 8192 + gi * 256 + lane * 16 : 14336 + lane * 16);
    asm volatile("" : "+s"(nb));
    tk_wait(tkp, tag, budget);
    v4u v0 = LD16(rw, l0, nb), v1 = LD16(rw, l0 + 1024, nb), v2 = LD16(rw, o2, nb);
    MB();
    *(LAS v4u*)(ml + l0) = v0; *(LAS v4u*)(ml + l0 + 1024) = v1; *(LAS v4u*)p2 = v2;
    MB();
    if (__builtin_expect(!__all(max(max(bad4(v0), bad4(v1)), bad4(v2)) != 0xFFFFFFFFu), 0)) {
        do {
            v0 = LD16(rw, l0, nb); v1 = LD16(rw, l0 + 1024, nb); v2 = LD16(rw, o2, nb);
            MB();
        } while (!__all(max(max(bad4(v0), bad4(v1)), bad4(v2)) != 0xFFFFFFFFu) && --budget >= 0);
        *(LAS v4u*)(ml + l0) = v0; *(LAS v4u*)(ml + l0 + 1024) = v1; *(LAS v4u*)p2 = v2;
    }
}
__device__ __forceinline__ int get_parity(rsrc_t rw, int lane, int& budget) {
    const int vo = (FL_PAR + (lane & 7)) * 128;
    u32 v;
    for (;;) {
        v = LD4(rw, vo, 0);
        MB();
        if (__all(v != 0xFFFFFFFFu) || --budget < 0) break;
        __builtin_amdgcn_s_sleep(8);
    }
    int pm = 0;
#pragma unroll
    for (int k = 0; k < 8; ++k) pm |= (__builtin_amdgcn_readlane((int)v, k) & 3) << (2 * k);
    return __builtin_popcount(pm) > 8 ? 0xFFFF : 0;
}
__device__ __forceinline__ void role_probe(rsrc_t rw) {
    const int lane = threadIdx.x & 63, w = __builtin_amdgcn_readfirstlane(threadIdx.x >> 6);
    const int o = OFF_RING + 2 * w * 65536 + N_PROBE;
    ST4(0xFFFFFFFFu, rw, lane < 6 ? o + (lane & 1) * 8192 + (lane >> 1) * 128 : OOB);
    DRAIN();
    int best[2] = {1 << 30, 1 << 30};
#pragma unroll
    for (int k = 0; k < 6; ++k) {
        const unsigned long long t0 = __builtin_amdgcn_s_memtime();
        asm volatile("s_waitcnt lgkmcnt(0)" ::: "memory");
        u32 v = LD4(rw, o + (k & 1) * 8192 + (k >> 1) * 128, 0);
        asm volatile("s_waitcnt vmcnt(0)" : "+v"(v) :: "memory");
        const unsigned long long t1 = __builtin_amdgcn_s_memtime();
        best[k & 1] = min(best[k & 1], (int)(t1 - t0));
    }
    if (lane == 0) ST4(best[1] < best[0] ? 3u : 0u, rw, (FL_PAR + w) * 128);
}
template <typename F>
__device__ __forceinline__ void qk_project_h(const bf16x16 (&Bq)[9], const bf16x16 (&Bk)[9], lptr ma, lptr xw, int idx, F hook) {
    f32x4 aq = {}, ak = {};
    bf16x8 A[9];
#pragma unroll
    for (int t = 0; t < 9; ++t) A[t] = lds8(ma + t * 1024);
    __builtin_amdgcn_sched_barrier(0);
#pragma unroll
    for (int t = 0; t < 9; ++t) {
        aq = SMF(A[t], Bq[t], aq, idx); ak = SMF(A[t], Bk[t], ak, idx);
        hook(t);
        __builtin_amdgcn_sched_barrier(0);
    }
    const float s0 = swap32_sum(aq[0], ak[0]), s1 = swap32_sum(aq[1], ak[1]), s2 = swap32_sum(aq[2], ak[2]), s3 = swap32_sum(aq[3], ak[3]);
    v2u o = {pack2(s0, s1), pack2(s2, s3)};
    *(LAS v2u*)xw = o;
}
__device__ __forceinline__ void qk_project(const bf16x16 (&Bq)[9], const bf16x16 (&Bk)[9], lptr ma, lptr xw, int idx) {
    qk_project_h(Bq, Bk, ma, xw, idx, [](int) {});
}

__device__ __forceinline__ void role_qk(const Args& a, lptr lds, rsrc_t rw, int h, int rep) {
    const int tid = threadIdx.x, lane = tid & 63, w = __builtin_amdgcn_readfirstlane(tid >> 6);
    bf16x16 Bq[9], Bk[9];
    {
        v4u xr[6];
#pragma unroll
        for (int j = 0; j < 6; ++j) {
            const int i = min(tid + 512 * j, 2879), row = i / 72, c = i - row * 72;
            xr[j] = *(const v4u*)((row < 8 ? a.mem0 + row * DM : a.u + (row - 8) * DM) + c * 8);
        }
        v4u v[6][6];
#pragma unroll
        for (int c = 0; c < 6; ++c) rows_ld<192, 16>(v[c], (c >= 3 ? a.Wk : a.Wq) + (size_t)(c % 3) * 192 * 4096 + h * DH, 4096, tid);
        __builtin_amdgcn_sched_barrier(0);
#pragma unroll
        for (int j = 0; j < 6; ++j) {
            const int i = min(tid + 512 * j, 2879), row = i / 72, c = i - row * 72, G = row >> 3;
            lptr d = lds + (G < 2 ? 57344 + G * MEMB : 135168 + (G - 2) * MEMB) + (c >> 1) * 256 + (row & 7) * 16 + (c & 1) * 8;
            *(LAS v2u*)d = v2u{xr[j].x, xr[j].z}; *(LAS v2u*)(d + 128) = v2u{xr[j].y, xr[j].w};
        }
#pragma unroll
        for (int c = 0; c < 6; ++c) {
            lptr l = lds + (c & 1) * 77824;
            rows_st<192, 16>(v[c], l, 288, tid);
            LBAR();
#pragma unroll
            for (int t = 0; t < 3; ++t) {
                bf16x16 f = cat16(frag16(l + (64 * t) * 288 + w * 32, 288, lane), frag16(l + (64 * t + 32) * 288 + w * 32, 288, lane));
                if (c >= 3) Bk[(c % 3) * 3 + t] = f; else Bq[c * 3 + t] = f;
            }
            __builtin_amdgcn_sched_barrier(0);
        }
        LBAR();
    }
    lptr ml = lds, xl = lds + MEMB;
    const int idx = (lane & 8) ? 0xEEEE : 0x4444;
    const int i16 = lane & 15, g = lane >> 4;
    const bool hi8 = (lane & 8) != 0;
    int budget = SPIN_MAX;
    lptr pq = xl + (8 * g + (i16 >> 2)) * 32 + (i16 & 1) * 8;
    lptr kl = lds + 16384 + lane * 16;
    lptr ma = ml + lane * 16, xw = xl + (16 * w + i16) * 32 + g * 8;
    const int ox = N_PX + (h >> 4) * 16384 + (h & 15) * 512 + ((g & 1) * 16 + (g >> 1) * 8 + (i16 & 7)) * 16;
    const int om = hi8 ? OOB : N_PMM + h * 128 + (i16 & 7) * 16;
    const int ob = lane < NREP ? BCO(BC_QK + h) : OOB;
    lptr tkp = lds + MEMB + 4096, kfp = tkp + 64;
    if (tid == 0) { tk_post(tkp, 0, rt32() - 100000u, 0); tk_post(kfp, 0, 0u, 0); }
    for (int c = tid; c < 576; c += 512) *(LAS v4u*)(ml + c * 16) = *(LAS v4u*)(lds + 57344 + c * 16);
    __syncthreads();
#pragma unroll 1
    for (int G = 0; G < 4; ++G) {
        qk_project(Bq, Bk, lds + (G ? 135168 + (G - 1) * MEMB : 57344 + MEMB) + lane * 16, xw, idx);
        LBAR();
        if (w < 4) {
            const bf16x8 F = as8(trd(pq + w * 1024 + 16), trd(pq + w * 1024 + 16 + 128));
            if ((i16 >> 3) == (G & 1)) *(LAS bf16x8*)(kl + ((G >> 1) * 4 + w) * 1024) = F;
        }
        LBAR();
    }
    const int pm = get_parity(rw, lane, budget);
    LBAR();
    if (w == 0) {
        const float c2 = 0.08838834764831845f * 1.4426950408889634f;
        for (int s = 0; s < NSEG - 1; ++s) {
            tk_get(kfp, s, budget);
            bf16x8 kx[2][4];
#pragma unroll
            for (int tl = 0; tl < 2; ++tl)
#pragma unroll
                for (int ks = 0; ks < 4; ++ks) kx[tl][ks] = lds8(kl + (s & 1) * 8192 + (tl * 4 + ks) * 1024);
            int oxs = ox + NEARB(s, i16 >> 3, pm), oxt = ox + NEARB(s, 2 + (i16 >> 3), pm), oms = om + NEARB(s, g, pm), obs = ob + FARB(s, lane & 3, pm);
            const int nb = NEARB(s, rep, pm);
            asm volatile("s_waitcnt lgkmcnt(0)" : "+v"(oxs), "+v"(oxt), "+v"(oms), "+v"(obs) :: "memory");
#pragma unroll
            for (int tl = 0; tl < 2; ++tl)
#pragma unroll
                for (int ks = 0; ks < 4; ++ks) PIN4(kx[tl][ks]);
            if (s > 0) {
                gather_mem(rw, nb, ml, lane, 0, tkp, s, budget);
            }
            LBAR();
            qk_project(Bq, Bk, ma, xw, idx);
            __builtin_amdgcn_s_setprio(3);
            LBAR();
            bf16x8 Fq[4], Fk[4];
#pragma unroll
            for (int ks = 0; ks < 4; ++ks) Fq[ks] = as8(trd(pq + ks * 1024), trd(pq + ks * 1024 + 128));
            __builtin_amdgcn_sched_barrier(0);
#pragma unroll
            for (int ks = 0; ks < 4; ++ks) Fk[ks] = as8(trd(pq + ks * 1024 + 16), trd(pq + ks * 1024 + 16 + 128));
            __builtin_amdgcn_sched_barrier(0);
            f32x4 S0 = {}, S1 = {}, Sm = {};
            S0 = MFMA16(kx[0][0], Fq[0], S0); S1 = MFMA16(kx[1][0], Fq[0], S1);
            S0 = MFMA16(kx[0][1], Fq[1], S0); S1 = MFMA16(kx[1][1], Fq[1], S1);
            __builtin_amdgcn_sched_barrier(0);
            Sm = MFMA16(Fk[0], Fq[0], Sm); S0 = MFMA16(kx[0][2], Fq[2], S0);
            Sm = MFMA16(Fk[1], Fq[1], Sm); S1 = MFMA16(kx[1][2], Fq[2], S1);
            Sm = MFMA16(Fk[2], Fq[2], Sm); S0 = MFMA16(kx[0][3], Fq[3], S0);
            Sm = MFMA16(Fk[3], Fq[3], Sm); S1 = MFMA16(kx[1][3], Fq[3], S1);
            __builtin_amdgcn_sched_barrier(0);
            float y0 = hi8 ? Sm[2] : Sm[0], y1 = hi8 ? Sm[3] : Sm[1];
            float x0 = hi8 ? S1[0] : S0[0], x1 = hi8 ? S1[1] : S0[1], x2 = hi8 ? S1[2] : S0[2], x3 = hi8 ? S1[3] : S0[3];
            const float ml_ = vmax3(vmax3(y0, y1, x0), x1, vmax(x2, x3));
            const float nm = -ml_ * c2;
            float m = vmax(ml_, ror8f(ml_));
            x0 = __builtin_amdgcn_exp2f(fmaf(x0, c2, nm)); x1 = __builtin_amdgcn_exp2f(fmaf(x1, c2, nm));
            { auto r = __builtin_amdgcn_permlane16_swap(__float_as_uint(m), __float_as_uint(m), false, false); m = vmax(__uint_as_float(r[0]), __uint_as_float(r[1])); }
            x2 = __builtin_amdgcn_exp2f(fmaf(x2, c2, nm)); x3 = __builtin_amdgcn_exp2f(fmaf(x3, c2, nm));
            { auto r = __builtin_amdgcn_permlane32_swap(__float_as_uint(m), __float_as_uint(m), false, false); m = vmax(__uint_as_float(r[0]), __uint_as_float(r[1])); }
            y0 = __builtin_amdgcn_exp2f(fmaf(y0, c2, nm)); y1 = __builtin_amdgcn_exp2f(fmaf(y1, c2, nm));
            const float f = __builtin_amdgcn_exp2f((ml_ - m) * c2);
            float sum = f * fmaf(0.5f, y0 + y1, (x0 + x1) + (x2 + x3));
            sum += ror8f(sum);
            sum = xsum(sum);
            const float inv = f * __builtin_amdgcn_rcpf(sum);
            const u32 pa = pack2(x0 * inv, x1 * inv) & 0x7fff7fffu, pb = pack2(x2 * inv, x3 * inv) & 0x7fff7fffu, pc = pack2(y0 * inv, y1 * inv) & 0x7fff7fffu;
            const u32 qc = ror8(pc);
            v4u px = {(u32)__builtin_amdgcn_update_dpp((int)pa, (int)pa, 0x128, 0xf, 0xc, false), (u32)__builtin_amdgcn_update_dpp((int)pb, (int)pb, 0x128, 0xf, 0xc, false),
                      (u32)__builtin_amdgcn_update_dpp((int)pa, (int)pa, 0x128, 0xf, 0x3, false), (u32)__builtin_amdgcn_update_dpp((int)pb, (int)pb, 0x128, 0xf, 0x3, false)};
            ST16(px, rw, oxs);
            ST16(px, rw, oxt);
            auto ra = __builtin_amdgcn_permlane16_swap(pc, pc, false, false);
            auto rc = __builtin_amdgcn_permlane16_swap(qc, qc, false, false);
            v4u pmv = {ra[0], rc[0], ra[1], rc[1]};
            ST16(pmv, rw, oms);
            __builtin_amdgcn_sched_barrier(0);
            ST4(nos(rt32()), rw, obs);
            __builtin_amdgcn_s_setprio(0);
        }
        {
            gather_mem(rw, NEARB(NSEG - 1, rep, pm), ml, lane, 0, tkp, NSEG - 1, budget);
            LBAR();
            qk_project(Bq, Bk, ma, xw, idx);
            LBAR();
        }
    } else if (w == 5) {
        int kdone = -1;
        const int kxo = OFF_KX + h * (NSEG * 8192) + g * 512 + i16 * 16;
        for (int s = 0; s < NSEG; ++s) {
            const bool need = s + 1 < NSEG - 1;
            if (need && kdone < s + 1) kdone = wait_flags(rw, FL_K + (h >> 1), 1, 1, -1, lane, budget);
            const bool go = need && kdone >= s + 1;
            int ko = (go ? kxo : OOB) + (s + 1) * 8192;
            int a0 = ox + NEARB(s + 2, i16 >> 3, pm), a1 = ox + NEARB(s + 2, 2 + (i16 >> 3), pm), a2 = om + NEARB(s + 2, g, pm), a3 = ob + FARB(s + 4, lane & 3, pm);
            v4u ff = {0xFFFFFFFFu, 0xFFFFFFFFu, 0xFFFFFFFFu, 0xFFFFFFFFu};
            asm volatile("" : "+v"(ko), "+v"(a0), "+v"(a1), "+v"(a2), "+v"(a3), "+v"(ff));
            LBAR();
            v4u kx[8];
            qk_project_h(Bq, Bk, ma, xw, idx, [&](int t) {
                if (t < 4) { kx[t] = LD16(rw, ko + (t & 1) * 2048, (t >> 1) * 4096); kx[4 + t] = LD16(rw, ko + 256 + (t & 1) * 2048, (t >> 1) * 4096); }
                if (t == 4) { ST16(ff, rw, a0); ST16(ff, rw, a1); }
                if (t == 5) { ST16(ff, rw, a2); ST4(0xFFFFFFFFu, rw, a3); }
            });
            LBAR();
            if (need) {
                if (!go) {
                    kdone = wait_flags(rw, FL_K + (h >> 1), 1, 1, s + 1, lane, budget, true);
#pragma unroll
                    for (int k = 0; k < 8; ++k) kx[k] = LD16(rw, kxo + (k >> 2) * 256 + (k & 3) * 2048, (s + 1) * 8192);
                }
#pragma unroll
                for (int k = 0; k < 8; ++k) *(LAS v4u*)(kl + ((s + 1) & 1) * 8192 + k * 1024) = kx[k];
                asm volatile("s_waitcnt lgkmcnt(0)" ::: "memory");
                tk_post(kfp, s + 1, 0u, lane);
            }
            DRAIN();
        }
    } else {
        TK tk; tk_init(tk);
        const int kmo = i16 < 8 ? OFF_KM + h * 2048 + g * 128 + i16 * 16 : OOB;
        for (int s = 0; s < NSEG; ++s) {
            if (s > 0 && w != 6) gather_mem(rw, NEARB(s, rep, pm), ml, lane, w, tkp, s, budget);
            BC2 bc = {0u, 0u};
            if (w == 3) bc = beacon_issue(rw, FARB(s, rep, pm), lane);
            LBAR();
            qk_project(Bq, Bk, ma, xw, idx);
            LBAR();
            if (w == 6) {
                bf16x8 F[4];
#pragma unroll
                for (int ks = 0; ks < 4; ++ks) F[ks] = as8(trd(pq + ks * 1024 + 16), trd(pq + ks * 1024 + 16 + 128));
#pragma unroll
                for (int ks = 0; ks < 4; ++ks) ST16(asu(F[ks]), rw, kmo + s * (NH * 2048) + ks * 512);
                DRAIN();
                if (lane == 0) ST4((u32)s, rw, (FL_M + h) * 128);
            } else if (w == 3) {
                if (s < NSEG - 1) {
                    if (s > 0) { tk_T0(tk, beacon_finish(bc, rw, FARB(s, rep, pm), lane, budget), a); if (tk.okA) tk_post(tkp, s + 1, tk_specA(tk, a), lane); }
                    wait_until(tk.XR + 8);
                    tk_Tp(tk, beacon_max<NH>(rw, FARB(s, rep, pm), BC_QK, lane, budget), a);
                    tk_post(tkp, s + 1, tk.XA, lane);
                }
            } else __builtin_amdgcn_s_sleep(14);
        }
    }
    if (budget < 0 && lane == 0) ST4(1u, rw, FL_ERR * 128);
}

__device__ __forceinline__ void role_wvo(const Args& a, lptr lds, rsrc_t rw, int idx) {
    const int tid = threadIdx.x, lane = tid & 63, w = __builtin_amdgcn_readfirstlane(tid >> 6);
    const int h = idx / 3, part = idx - 3 * h;
    bf16x8 Bo[5][4];
    v4u v[6], o[2][9];
#pragma unroll
    for (int c = 0; c < 2; ++c) rows_ld<64, 72>(o[c], a.Wo + ((size_t)h * DH + c * 64) * DM, DM, tid);
    rows_ld<192, 16>(v, a.Wv + (size_t)part * 192 * 4096 + h * DH, 4096, tid);
    __builtin_amdgcn_sched_barrier(0);
#pragma unroll
    for (int c = 0; c < 2; ++c) {
        lptr l = lds + c * 77824;
        rows_st<64, 72>(o[c], l, 1184, tid);
        LBAR();
#pragma unroll
        for (int j = 0; j < 5; ++j) {
            const int r = (w + 8 * j < NR) ? w + 8 * j : w;
#pragma unroll
            for (int ks = 0; ks < 2; ++ks) Bo[j][c * 2 + ks] = frag16(l + ks * 32 * 1184 + r * 32, 1184, lane);
        }
        __builtin_amdgcn_sched_barrier(0);
    }
    rows_st<192, 16>(v, lds, VS, tid);
    LBAR();
    const int rho = lane & 15, bg = lane >> 4;
    const int wo0 = OFF_WVO + (2 * w + ((rho >> 1) & 1)) * 294912 + ((h >> 1) * 18 + part * 6) * 1024 + (bg * 16 + (h & 1) * 8 + 2 * (rho >> 2) + (rho & 1)) * 16;
#pragma unroll 1
    for (int kb = 0; kb < 6; ++kb) {
        bf16x8 Af[2][4];
#pragma unroll
        for (int mt = 0; mt < 2; ++mt)
#pragma unroll
            for (int ks = 0; ks < 4; ++ks) Af[mt][ks] = lds8(lds + (32 * kb + 8 * (rho >> 2) + 4 * mt + (rho & 3)) * VS + ks * 64 + bg * 16);
#pragma unroll
        for (int j = 0; j < 5; ++j) {
            if (j < 4 || w < 4) {
                f32x4 d0 = {}, d1 = {};
#pragma unroll
                for (int ks = 0; ks < 4; ++ks) { d0 = MFMA16(Af[0][ks], Bo[j][ks], d0); d1 = MFMA16(Af[1][ks], Bo[j][ks], d1); }
                v4u q = {pack2(d0[0], d0[1]), pack2(d0[2], d0[3]), pack2(d1[0], d1[1]), pack2(d1[2], d1[3])};
                ST16(q, rw, wo0 + j * (16 * 294912) + kb * 1024);
            }
        }
        if (kb == 2 || kb == 4) {
            asm volatile("s_waitcnt vmcnt(4)" ::: "memory");
            LBAR();
            if (tid == 0) ST4((u32)(kb >> 1), rw, (FL_W + idx) * 128);
        }
    }
    DRAIN();
    LBAR();
    if (tid == 0) ST4(3u, rw, (FL_W + idx) * 128);
}

__device__ __forceinline__ void role_rz(const Args& a, lptr lds, rsrc_t rw, int r, int rep) {
    const int tid = threadIdx.x, lane = tid & 63, w = __builtin_amdgcn_readfirstlane(tid >> 6);
    const int idx = (lane & 8) ? 0xEEEE : 0x4444;
    const int i16 = lane & 15, g = lane >> 4;
    int budget = SPIN_MAX;
    v4u m0[2];
#pragma unroll
    for (int j = 0; j < 2; ++j) { const int i = min(tid + 512 * j, 575), row = i / 72, c = i - row * 72; m0[j] = *(const v4u*)(a.mem0 + row * DM + c * 8); }
    const int pm = get_parity(rw, lane, budget);
#pragma unroll
    for (int j = 0; j < 2; ++j) {
        const int i = min(tid + 512 * j, 575), row = i / 72, c = i - row * 72;
        lptr d = lds + (c >> 1) * 256 + row * 16 + (c & 1) * 8;
        *(LAS v2u*)d = v2u{m0[j].x, m0[j].z}; *(LAS v2u*)(d + 128) = v2u{m0[j].y, m0[j].w};
    }
    bf16x16 Ba[9], Bb[9];
    v4u t0[4];
    {
        const int wo = OFF_WVO + r * 294912 + 2 * w * 18432 + lane * 16;
        int have = 0;
#pragma unroll
        for (int p = 0; p < 3; ++p) {
            if (have < p + 1) have = wait_flags(rw, FL_W + 12 * w, 1, 12, p + 1, lane, budget, true);
            if (p == 2) {
                wait_flags(rw, FL_V, 1, 64, 0, lane, budget, true);
                const int vq = OFF_VOX + r * 16384 + w * 2048 + lane * 16;
#pragma unroll
                for (int k = 0; k < 4; ++k) t0[k] = LD16(rw, vq + (k & 1) * 1024, (k >> 1) * (NR * NH * 1024));
            }
#pragma unroll
            for (int q = 0; q < 3; ++q) {
                const int t = 3 * q + p;
                Ba[t] = cat16(as8(LD16(rw, wo, t * 2048)), as8(LD16(rw, wo + 1024, t * 2048)));
                Bb[t] = cat16(as8(LD16(rw, wo, 18432 + t * 2048)), as8(LD16(rw, wo + 1024, 18432 + t * 2048)));
            }
            MB();
        }
    }
    lptr ml = lds, zl = lds + MEMB;
    lptr tka = lds + 13312, tkr = tka + 16, cntp = tka + 64;
    volatile LAS int* prod = (volatile LAS int*)(lds + 13312 + 512);
    lptr ring = lds + 16384;
    lptr ma = ml + lane * 16;
    lptr zw = zl + ((2 * w + (lane >> 5)) >> 2) * 1024 + ((((2 * w + (lane >> 5)) & 3) * 16 + (g & 1) * 8 + (i16 & 7)) * 16) + (i16 >> 3) * 8;
    const bool hi8r = (lane & 8) != 0;
    const int mso = i16 < 8 ? (r < 64 ? N_MEMA + r * 128 : N_MEMB + (r - 64) * 128) + i16 * 16 : OOB;
    const int ob = lane < NREP ? BCO(r) : OOB;
    if (tid == 0) { tk_post(tka, 0, rt32() - 100000u, 0); tk_post(tkr, -1, 0u, 0); *(volatile LAS int*)cntp = 0; }
#pragma unroll
    for (int k = 0; k < 4; ++k) *(LAS v4u*)(ring + (k >> 1) * 16384 + w * 2048 + (k & 1) * 1024 + lane * 16) = t0[k];
    if (tid < 4) prod[tid] = 2;
    LBAR();
    if (w < 4) {
        const int po = N_PX + (w >> 1) * 16384 + (w & 1) * 4096 + lane * 16;
        const int mo = N_PMM + w * 1024 + 2 * g * 128 + (i16 & 7) * 16 + (i16 >> 3) * 8;
        lptr cna = lane == 0 ? cntp : cntp + 4 + lane * 4;
        const u32 one = lane == 0 ? 1u : 0u;
        const bool dv = (g >> 1) == (i16 >> 3);
        const int rwo = dv ? (4 * (g & 1) + 2 * (g >> 1)) * 128 + ((i16 & 7) >> 1) * 32 + w * 8 + (i16 & 1) * 4 : 2048 + lane * 4;
        const int msq = lane < 32 ? (r < 64 ? N_MEMA + r * 128 : N_MEMB + (r - 64) * 128) + (lane >> 2) * 16 : OOB;
        lptr zrd = zl + w * 1024 + lane * 16;
        for (int s = 0; s < NSEG; ++s) {
            lptr Rw = lds + 81920 + (s & 1) * 1024 + rwo, Rr = lds + 81920 + (s & 1) * 1024 + (lane & 31) * 32;
            int mss = msq + NEARB(s + 1, lane & 3, pm), obs = ob + FARB(s + 1, lane & 3, pm);
            int sb = NEARB(s, rep, pm);
            asm volatile("" : "+s"(sb));
            lptr rg = ring + (s & 3) * 16384 + w * 4096 + lane * 16;
            bf16x8 b[5];
            const bool early = __builtin_amdgcn_readfirstlane(prod[w >> 1]) > s;
            MB();
#pragma unroll
            for (int k = 0; k < 4; ++k) b[k] = lds8(rg + k * 1024);
            const u32 X = s > 0 ? tk_get(tkr, min(s, NSEG - 2), budget) : 0u;
            asm volatile("s_waitcnt lgkmcnt(0)" : "+v"(mss), "+v"(obs), "+v"(Rw), "+v"(Rr) :: "memory");
#pragma unroll
            for (int k = 0; k < 4; ++k) PIN4(b[k]);
            LBAR();
            qk_project(Ba, Bb, ma, zw, idx);
            LBAR();
            if (s == NSEG - 1) break;
            __builtin_amdgcn_s_setprio(3);
            b[4] = lds8(zrd);
            if (!early) {
                while (__builtin_amdgcn_readfirstlane(prod[w >> 1]) <= s && --budget >= 0) __builtin_amdgcn_s_sleep(1);
                MB();
#pragma unroll
                for (int k = 0; k < 4; ++k) b[k] = lds8(rg + k * 1024);
            }
            if (s > 0 && (int)(X - rt32()) < 1000) spin_to(X);
            f32x4 d;
            u32 bad;
            auto shot = [&]() {
                v4u p[4]; v2u m[2];
                m[0] = LD8(rw, mo, sb); m[1] = LD8(rw, mo + 128, sb);
                __builtin_amdgcn_sched_barrier(0);
#pragma unroll
                for (int k = 0; k < 4; ++k) { p[k] = LD16(rw, po + k * 1024, sb); __builtin_amdgcn_sched_barrier(0); }
                MB();
                d = f32x4{};
                d = MFMA16(as8(m[0], m[1]), b[4], d);
                bad = (m[0].x | m[0].y) | (m[1].x | m[1].y);
                __builtin_amdgcn_sched_barrier(0);
#pragma unroll
                for (int k = 0; k < 4; ++k) {
                    d = MFMA16(as8(p[k]), b[k], d);
                    bad |= (p[k].x | p[k].y) | (p[k].z | p[k].w);
                    __builtin_amdgcn_sched_barrier(0);
                }
            };
            shot();
            if (__builtin_expect(!__all((int)bad >= 0), 0)) {
                do shot(); while (!__all((int)bad >= 0) && --budget >= 0);
            }
            float e0, e1;
            {
                auto r0 = __builtin_amdgcn_permlane32_swap(__float_as_uint(d[0]), __float_as_uint(d[2]), false, false);
                auto r1 = __builtin_amdgcn_permlane32_swap(__float_as_uint(d[1]), __float_as_uint(d[3]), false, false);
                const float t0 = __uint_as_float(hi8r ? r0[1] : r0[0]), t1 = __uint_as_float(hi8r ? r1[1] : r1[0]);
                e0 = t0 + ror8f(t0); e1 = t1 + ror8f(t1);
            }
            f32x4 x0, x1; u32 old;
            asm volatile("ds_write2_b32 %4, %5, %6 offset1:32\n\tds_add_rtn_u32 %0, %7, %8\n\tds_read_b128 %1, %3\n\tds_read_b128 %2, %3 offset:16\n\ts_waitcnt lgkmcnt(0)"
                         : "=&v"(old), "=&v"(x0), "=&v"(x1) : "v"(Rr), "v"(Rw), "v"(e0), "v"(e1), "v"(cna), "v"(one) : "memory");
            {
                const u32 c = nos(pack2((x0[0] + x0[2]) + (x1[0] + x1[2]), (x0[1] + x0[3]) + (x1[1] + x1[3])));
                v4u o = {(u32)__builtin_amdgcn_update_dpp(0, (int)c, 0x00, 0xf, 0xf, true), (u32)__builtin_amdgcn_update_dpp(0, (int)c, 0x55, 0xf, 0xf, true),
                         (u32)__builtin_amdgcn_update_dpp(0, (int)c, 0xAA, 0xf, 0xf, true), (u32)__builtin_amdgcn_update_dpp(0, (int)c, 0xFF, 0xf, 0xf, true)};
                const bool last = (__builtin_amdgcn_readfirstlane(old) & 3) == 3;
                ST16(o, rw, last ? mss : OOB);
                __builtin_amdgcn_sched_barrier(0);
                if (last) ST4(nos(rt32()), rw, obs);
            }
            __builtin_amdgcn_s_setprio(0);
        }
    } else if (w == 5 || w == 6) {
        int vdone0 = -1, vdone1 = -1, staged = 2;
        const int hw = w - 5;
        const int vo = OFF_VOX + r * 16384 + hw * 8192 + lane * 16;
        const int zho = OFF_ZH + r * 4096 + hw * 2048 + lane * 16;
        lptr zhr = zl + hw * 2048 + (((lane >> 4) & 3) * 16 + (lane & 7)) * 16 + ((lane >> 3) & 1) * 8;
        const int aro = w == 5 ? mso : OOB, arb = w == 5 ? ob : OOB;
        v4u zv[2] = {};
        for (int s = 0; s < NSEG; ++s) {
            bool go = false;
            if (staged <= min(s + 3, NSEG - 2)) {
                const int ow = VOWN(staged);
                int& vd = ow ? vdone1 : vdone0;
                if (vd < staged) vd = wait_flags(rw, FL_V + ow, 2, NH, -1, lane, budget);
                go = vd >= staged;
            }
            const int vos = go ? vo : OOB;
            int va = vos + staged * (NR * NH * 1024), zs = s > 0 ? zho + (s - 1) * (NR * NH * 256) : OOB;
            int a0 = aro + NEARB(s + 3, g, pm), a1 = arb + FARB(s + 4, lane & 3, pm);
            v4u ff = {0xFFFFFFFFu, 0xFFFFFFFFu, 0xFFFFFFFFu, 0xFFFFFFFFu};
            asm volatile("" : "+v"(va), "+v"(zs), "+v"(a0), "+v"(a1), "+v"(ff));
            if (s > 0) gather_mem4(rw, NEARB(s, rep, pm), ml, lane, w - 4, tka, s, budget);
            LBAR();
            v4u v[8];
            qk_project_h(Ba, Bb, ma, zw, idx, [&](int t) {
                if (t < 4) { v[2 * t] = LD16(rw, va + (t & 1) * 2048, (t >> 1) * 4096); v[2 * t + 1] = LD16(rw, va + 1024 + (t & 1) * 2048, (t >> 1) * 4096); }
                if (t == 4) { ST16(zv[0], rw, zs); ST16(zv[1], rw, zs + 1024); }
                if (t == 5) { ST16(ff, rw, a0); ST4(0xFFFFFFFFu, rw, a1); }
            });
            LBAR();
#pragma unroll
            for (int k = 0; k < 2; ++k) {
                const v2u lo = *(LAS v2u*)(zhr + k * 1024), hi = *(LAS v2u*)(zhr + k * 1024 + 128);
                zv[k] = v4u{lo.x, lo.y, hi.x, hi.y};
            }
            for (;;) {
                if (go) {
#pragma unroll
                    for (int k = 0; k < 8; ++k) *(LAS v4u*)(ring + (staged & 3) * 16384 + hw * 8192 + k * 1024 + lane * 16) = v[k];
                    asm volatile("s_waitcnt lgkmcnt(0)" ::: "memory");
                    ++staged;
                    prod[hw] = staged;
                }
                if (staged > s + 1 || staged > NSEG - 2) break;
                const int ow = VOWN(staged);
                int& vd = ow ? vdone1 : vdone0;
                if (vd < staged) vd = wait_flags(rw, FL_V + ow, 2, NH, staged <= s ? staged : -1, lane, budget, true);
                if (vd < staged) break;
#pragma unroll
                for (int k = 0; k < 8; ++k) v[k] = LD16(rw, vo + (k & 3) * 1024, staged * (NR * NH * 1024) + (k >> 2) * 4096);
                go = true;
            }
            DRAIN();
        }
        {
            int l2 = (int)__builtin_amdgcn_mbcnt_hi(~0u, __builtin_amdgcn_mbcnt_lo(~0u, 0u));
            asm volatile("" : "+v"(l2));
            const int zf = OFF_ZH + r * 4096 + hw * 2048 + (NSEG - 1) * (NR * NH * 256) + l2 * 16;
            ST16(zv[0], rw, zf); ST16(zv[1], rw, zf + 1024);
        }
    } else {
        TK tk; tk_init(tk);
        for (int s = 0; s < NSEG; ++s) {
            if (s > 0) {
                gather_mem4(rw, NEARB(s, rep, pm), ml, lane, w - 4, tka, s, budget);
            }
            BC2 bc = {0u, 0u};
            if (w == 7) bc = beacon_issue(rw, FARB(s, rep, pm), lane);
            LBAR();
            qk_project(Ba, Bb, ma, zw, idx);
            LBAR();
            if (w == 7) {
                if (s < NSEG - 1) {
                    if (s > 0) { tk_T0(tk, beacon_finish(bc, rw, FARB(s, rep, pm), lane, budget), a); if (tk.okA) tk_post(tka, s + 1, tk_specA(tk, a), lane); }
                    tk_post(tkr, s, tk.XR, lane);
                    wait_until(tk.XR + 8);
                    tk_Tp(tk, beacon_max<NH>(rw, FARB(s, rep, pm), BC_QK, lane, budget), a);
                    tk_post(tka, s + 1, tk.XA, lane);
                    tk_post(tkr, s + 1, tk_specR(tk, a), lane);
                }
            } else {
                if (w == 4 && s >= 2 && lane == 0) ST4((u32)(s - 2), rw, (FL_Z + r) * 128);
                __builtin_amdgcn_s_sleep(14);
            }
        }
    }
    DRAIN();
    LBAR();
    if (tid == 0) ST4((u32)(NSEG - 1), rw, (FL_Z + r) * 128);
    if (budget < 0 && lane == 0) ST4(3u, rw, FL_ERR * 128);
}

__device__ __forceinline__ void cexp_l(float lr, float li, float l, float& er, float& ei) {
    const float mag = __builtin_amdgcn_exp2f(lr * l);
    const float ph = li * l;
    const float pe = fmaf(li, l, -ph);
    const float fr = (ph - floorf(ph)) + pe;
    er = mag * __builtin_amdgcn_cosf(fr);
    ei = mag * __builtin_amdgcn_sinf(fr);
}
constexpr int CV_W = 8 * 260 * 16, CV_U = 96 * 80, CV_CH = CV_W + CV_U;
__device__ __forceinline__ void s4_unit(const Args& a, lptr lds, rsrc_t rw, int unit) {
    const int tid = threadIdx.x, lane = tid & 63, w = __builtin_amdgcn_readfirstlane(tid >> 6);
    const int d0 = unit * 4;
    v2u uv[4];
#pragma unroll
    for (int k = 0; k < 4; ++k) uv[k] = *(const v2u*)(a.u + (size_t)(2 * (tid + 512 * (k >> 1)) + (k & 1)) * DM + d0);
    {
        const int c = tid >> 7, nn = tid & 127, n = (d0 + c) * 128 + nn;
        const float dt = rbf(expf(bf2f(a.logdt[d0 + c])));
        const float lr = dt * bf2f(a.Are[n]) * 1.4426950408889634f, li = dt * bf2f(a.Aim[n]) * 0.15915494309189535f;
        const float br = dt * bf2f(a.Bre[n]), bi = dt * bf2f(a.Bim[n]);
        const float cr = bf2f(a.Cre[n]), ci = bf2f(a.Cim[n]);
        LAS f32x2* T = (LAS f32x2*)(lds + c * CV_CH) + nn;
        float er, ei, pr, pi;
        cexp_l(lr, li, 1.0f, er, ei); pr = er; pi = ei;
#pragma unroll
        for (int o = 0; o < 7; ++o) { T[o * 130] = f32x2{pr, pi}; const float t = pr * er - pi * ei; pi = pr * ei + pi * er; pr = t; }
#pragma unroll
        for (int q4 = 0; q4 < 4; ++q4) {
            if (q4 == 0) cexp_l(lr, li, 128.0f, er, ei);
            cexp_l(lr, li, (float)(128 + 512 * q4), pr, pi);
#pragma unroll
            for (int o = 0; o < 4; ++o) { T[(7 + 4 * q4 + o) * 130] = f32x2{pr, pi}; const float t = pr * er - pi * ei; pi = pr * ei + pi * er; pr = t; }
        }
        const float cbr = cr * br - ci * bi, cbi = cr * bi + ci * br;
        cexp_l(lr, li, 8.0f, er, ei);
#pragma unroll
        for (int q4 = 0; q4 < 4; ++q4) {
            float sr, si;
            cexp_l(lr, li, (float)(32 * q4), sr, si);
            pr = cbr * sr - cbi * si; pi = cbr * si + cbi * sr;
#pragma unroll
            for (int o = 0; o < 4; ++o) { T[(23 + 4 * q4 + o) * 130] = f32x2{pr, pi}; const float t = pr * er - pi * ei; pi = pr * ei + pi * er; pr = t; }
        }
    }
    __syncthreads();
    f32x4 kr[3];
    {
        const int x = lane & 15, g = lane >> 4, c = w & 3, nh = w >> 2;
        const int ah0 = x >> 3, o0 = x & 7;
        const int ah1 = x < 8 ? 2 + (x >> 2) : 4 + ((x - 8) >> 1), o1 = x < 8 ? 2 * (x & 3) : 4 * (x & 1);
        const int ah2 = x < 8 ? 8 + x : 16;
        LAS f32x2* T = (LAS f32x2*)(lds + c * CV_CH) + nh * 64 + g;
        LAS f32x2* Ta0 = T + 7 * 130; LAS f32x2* To0 = T + (o0 ? o0 - 1 : 0) * 130;
        LAS f32x2* Ta1 = T + (6 + ah1) * 130; LAS f32x2* To1 = T + (o1 ? o1 - 1 : 0) * 130;
        LAS f32x2* Ta2 = T + (6 + ah2) * 130; LAS f32x2* Tv = T + (23 + x) * 130;
        const f32x2 one = {1.0f, 0.0f};
        f32x4 ar[3] = {}, ai[3] = {};
#pragma unroll
        for (int jj = 0; jj < 16; ++jj) {
            const f32x2 v = Tv[4 * jj], u2 = Ta2[4 * jj], a1 = Ta1[4 * jj];
            f32x2 a0v = Ta0[4 * jj], e0 = To0[4 * jj], e1 = To1[4 * jj];
            a0v = ah0 ? a0v : one; e0 = o0 ? e0 : one; e1 = o1 ? e1 : one;
            const float u0r = a0v[0] * e0[0] - a0v[1] * e0[1], u0i = a0v[0] * e0[1] + a0v[1] * e0[0];
            const float u1r = a1[0] * e1[0] - a1[1] * e1[1], u1i = a1[0] * e1[1] + a1[1] * e1[0];
            ar[2] = __builtin_amdgcn_mfma_f32_16x16x4f32(u2[0], v[0], ar[2], 0, 0, 0);
            ai[2] = __builtin_amdgcn_mfma_f32_16x16x4f32(u2[1], v[1], ai[2], 0, 0, 0);
            ar[0] = __builtin_amdgcn_mfma_f32_16x16x4f32(u0r, v[0], ar[0], 0, 0, 0);
            ai[0] = __builtin_amdgcn_mfma_f32_16x16x4f32(u0i, v[1], ai[0], 0, 0, 0);
            ar[1] = __builtin_amdgcn_mfma_f32_16x16x4f32(u1r, v[0], ar[1], 0, 0, 0);
            ai[1] = __builtin_amdgcn_mfma_f32_16x16x4f32(u1i, v[1], ai[1], 0, 0, 0);
        }
#pragma unroll
        for (int t = 0; t < 3; ++t) kr[t] = ar[t] - ai[t];
    }
    __syncthreads();
    {
        LAS float* T = (LAS float*)(lds + (w & 3) * CV_CH + 4096 + (w >> 2) * 3072);
#pragma unroll
        for (int t = 0; t < 3; ++t)
#pragma unroll
            for (int r = 0; r < 4; ++r) T[(16 * t + 4 * (lane >> 4) + r) * 16 + (lane & 15)] = kr[t][r];
    }
    __syncthreads();
#pragma unroll
    for (int k = 0; k < 8; ++k) {
        const int i = tid + 512 * k, c = i >> 10, l2 = i & 1023;
        LAS float* T = (LAS float*)(lds + c * CV_CH + 4096);
        float v[2];
#pragma unroll
        for (int e = 0; e < 2; ++e) {
            const int lb = (int)rbf((float)(2 * l2 + e));
            const int hi = lb >> 7, o = lb & 7;
            const int row = lb < 256 ? 8 * hi + o : lb < 512 ? 16 + 4 * (hi - 2) + (o >> 1) : lb < 1024 ? 24 + 2 * (hi - 4) + (o >> 2) : 32 + (hi - 8);
            const int ix = row * 16 + ((lb >> 3) & 15);
            v[e] = T[ix] + T[768 + ix];
        }
        *(LAS u32*)(lds + c * CV_CH + CV_W + 2704 + l2 * 4) = pack2(v[0], v[1]);
    }
    __syncthreads();
    for (int i = tid; i < 4 * 259; i += 512) {
        const int c = i / 259, zz = i - c * 259, z = zz - 3;
        lptr ch = lds + c * CV_CH;
        v4u lo = {0, 0, 0, 0}, hi = {0, 0, 0, 0};
        if (z >= 1) lo = *(LAS v4u*)(ch + CV_W + 2704 + (z - 1) * 16);
        if (z >= 0) hi = *(LAS v4u*)(ch + CV_W + 2704 + z * 16);
        const u32 e[8] = {lo.x, lo.y, lo.z, lo.w, hi.x, hi.y, hi.z, hi.w};
#pragma unroll
        for (int cc = 0; cc < 8; ++cc) {
            v4u o;
#pragma unroll
            for (int k = 0; k < 4; ++k) {
                const int t = 8 + cc - 2 * k;
                if (t & 1) o[k] = (e[t >> 1] >> 16) | (e[t >> 1] << 16);
                else o[k] = (e[t >> 1] & 0xffffu) | (e[(t >> 1) - 1] & 0xffff0000u);
            }
            *(LAS v4u*)(ch + (cc * 260 + zz) * 16) = o;
        }
    }
    __syncthreads();
    for (int i = tid; i < 4 * 160; i += 512) *(LAS v4u*)(lds + (i / 160) * CV_CH + CV_W + (i % 160) * 16) = v4u{0, 0, 0, 0};
#pragma unroll
    for (int k2 = 0; k2 < 2; ++k2) {
        const int l = 2 * (tid + 512 * k2);
        const int o = CV_W + (32 + (l >> 5)) * 80 + (l & 31) * 2;
        const v2u p = uv[2 * k2], q = uv[2 * k2 + 1];
        *(LAS u32*)(lds + 0 * CV_CH + o) = (p.x & 0xffffu) | (q.x << 16);
        *(LAS u32*)(lds + 1 * CV_CH + o) = (p.x >> 16) | (q.x & 0xffff0000u);
        *(LAS u32*)(lds + 2 * CV_CH + o) = (p.y & 0xffffu) | (q.y << 16);
        *(LAS u32*)(lds + 3 * CV_CH + o) = (p.y >> 16) | (q.y & 0xffff0000u);
    }
    __syncthreads();
    const int c = w & 3, par = w >> 2, b = lane & 31, hh = lane >> 5;
    lptr ch = lds + c * CV_CH;
    lptr wa = ch + ((b & 7) * 260 + (b >> 3) - hh + 3) * 16;
    lptr ub = ch + CV_W + (32 + b) * 80 + hh * 16;
    f32x16 y0 = {}, y1 = {};
    wa += par * 64; ub -= par * 80;
    {
        bf16x8 fa[2][2], fb[2][2], fc[2][2];
#pragma unroll
        for (int ks = 0; ks < 2; ++ks) { fa[0][ks] = lds8(wa - ks * 32); fb[0][ks] = lds8(ub + ks * 32); fc[0][ks] = lds8(ub + 2560 + ks * 32); }
#pragma unroll
        for (int i = 0; i < 32; ++i) {
            const int cur = i & 1, nx = cur ^ 1;
            if (i + 1 < 32) {
#pragma unroll
                for (int ks = 0; ks < 2; ++ks) {
                    fa[nx][ks] = lds8(wa + (i + 1) * 128 - ks * 32);
                    if (i + 1 < 16) fb[nx][ks] = lds8(ub - (i + 1) * 160 + ks * 32);
                    fc[nx][ks] = lds8(ub + 2560 - (i + 1) * 160 + ks * 32);
                }
            }
            __builtin_amdgcn_sched_barrier(0);
#pragma unroll
            for (int ks = 0; ks < 2; ++ks) {
                if (i < 16) y0 = MFMA32(fa[cur][ks], fb[cur][ks], y0);
                y1 = MFMA32(fa[cur][ks], fc[cur][ks], y1);
            }
            __builtin_amdgcn_sched_barrier(0);
        }
    }
    __syncthreads();
    {
        LAS f32x4* X = (LAS f32x4*)(ch + par * 4096);
        const f32x16 src = par ? y0 : y1;
#pragma unroll
        for (int k = 0; k < 4; ++k) X[k * 64 + lane] = f32x4{src[4 * k], src[4 * k + 1], src[4 * k + 2], src[4 * k + 3]};
    }
    __syncthreads();
    {
        LAS f32x4* X = (LAS f32x4*)(ch + (1 - par) * 4096);
        lptr Y = ch + 8192 + (32 * par + b) * 72 + hh * 8;
        const f32x16 own = par ? y1 : y0;
#pragma unroll
        for (int k = 0; k < 4; ++k) {
            const f32x4 o = X[k * 64 + lane];
            v2u yv = {pack2(own[4 * k] + o[0], own[4 * k + 1] + o[1]), pack2(own[4 * k + 2] + o[2], own[4 * k + 3] + o[3])};
            *(LAS v2u*)(Y + k * 16) = yv;
        }
    }
    __syncthreads();
    const v2u dk = *(const v2u*)(a.dskip + d0);
#pragma unroll
    for (int k2 = 0; k2 < 2; ++k2) {
        const int i = tid + 512 * k2;
        lptr yp = lds + 8192 + (i >> 4) * 72 + (i & 15) * 4;
        const u32 ya = *(LAS u32*)(yp), yb = *(LAS u32*)(yp + CV_CH), yc = *(LAS u32*)(yp + 2 * CV_CH), yd = *(LAS u32*)(yp + 3 * CV_CH);
        const v2u p = uv[2 * k2], q = uv[2 * k2 + 1];
        v4u o;
        o.x = pack2(bflo(ya) + rbf(bflo(p.x) * bflo(dk.x)), bflo(yb) + rbf(bfhi(p.x) * bfhi(dk.x)));
        o.y = pack2(bflo(yc) + rbf(bflo(p.y) * bflo(dk.y)), bflo(yd) + rbf(bfhi(p.y) * bfhi(dk.y)));
        o.z = pack2(bfhi(ya) + rbf(bflo(q.x) * bflo(dk.x)), bfhi(yb) + rbf(bfhi(q.x) * bfhi(dk.x)));
        o.w = pack2(bfhi(yc) + rbf(bflo(q.y) * bflo(dk.y)), bfhi(yd) + rbf(bfhi(q.y) * bfhi(dk.y)));
        ST16(o, rw, OFF_A4 + unit * 16384 + i * 16);
    }
    __syncthreads();
}
__device__ __forceinline__ void role_s4(const Args& a, lptr lds, rsrc_t rw, int idx) {
    if (idx < 32) {
        const int tid = threadIdx.x, t = idx * 64 + (tid >> 3), j = tid & 7;
        float acc = 0.f;
#pragma unroll
        for (int k = 0; k < 9; ++k) {
            const v4u xv = *(const v4u*)(a.u + (size_t)t * DM + (k * 8 + j) * 8), gv = *(const v4u*)(a.gatew + (k * 8 + j) * 8);
#pragma unroll
            for (int e = 0; e < 4; ++e) acc += rbf(bflo(xv[e]) * bflo(gv[e])) + rbf(bfhi(xv[e]) * bfhi(gv[e]));
        }
        acc += __shfl_xor(acc, 1); acc += __shfl_xor(acc, 2); acc += __shfl_xor(acc, 4);
        acc = rbf(acc);
        if (j == 0) ST4(__float_as_uint(rbf(1.0f / (1.0f + __expf(-acc)))), rw, OFF_G + t * 4);
    }
#pragma unroll 1
    for (int unit = idx; unit < DM / 4; unit += NS4) s4_unit(a, lds, rw, unit);
    DRAIN();
    __syncthreads();
    if (threadIdx.x == 0) ST4(1u, rw, (FL_S + idx) * 128);
}

__device__ __forceinline__ void role_post(const Args& a, lptr lds, rsrc_t rw) {
    const int tid = threadIdx.x, lane = tid & 63, w = __builtin_amdgcn_readfirstlane(tid >> 6);
    const int i16 = lane & 15, g = lane >> 4;
    int budget = SPIN_MAX, vdone0 = -1, vdone1 = -1;
    volatile LAS int* U = (volatile LAS int*)(lds + 81920);
    wait_flags(rw, FL_S, 1, 64, 1, lane, budget);
    wait_flags(rw, FL_S + 64, 1, NS4 - 64, 1, lane, budget);
    constexpr int NFULL = 62 * 6, NUNIT = NFULL + 24;
#pragma unroll 1
    for (;;) {
        if (tid == 0) *U = 1 + (int)__hip_atomic_fetch_add((u32*)(a.ws + FL_Q * 128), 1u, __ATOMIC_RELAXED, __HIP_MEMORY_SCOPE_AGENT);
        __syncthreads();
        const int unit = __builtin_amdgcn_readfirstlane(*U);
        if (unit >= NUNIT || budget < 0) break;
        const bool half = unit >= NFULL;
        const int uu = half ? NFULL + ((unit - NFULL) >> 1) : unit, q0 = half ? (unit - NFULL) & 1 : 0;
        const int s = uu / 6, r = (uu - s * 6) * 6 + (w < 6 ? w : 0);
        bf16x8 b[32];
        v2u av[2]; float gt[2];
        if (w < 6) {
            if (VOWN(s)) { if (vdone1 < s) vdone1 = wait_flags(rw, FL_V + 1, 2, NH, s, lane, budget); }
            else { if (vdone0 < s) vdone0 = wait_flags(rw, FL_V, 2, NH, s, lane, budget); }
            const int vo = OFF_VOX + (s * NR + r) * (NH * 1024) + ((i16 >> 1) & 1) * 16384 + ((g & 1) * 16 + (g >> 1) * 8 + 2 * (i16 >> 2) + (i16 & 1)) * 16;
#pragma unroll
            for (int k = 0; k < 32; ++k) b[k] = as8(LD16(rw, vo + (k & 7) * 512, (k >> 3) * 4096));
#pragma unroll
            for (int qt = 0; qt < 2; ++qt) {
                av[qt] = LD8(rw, OFF_A4 + (4 * r + g) * 16384 + (32 * s + 16 * (qt ^ q0) + i16) * 8, 0);
                gt[qt] = __uint_as_float(LD4(rw, OFF_G + (32 * s + 16 * (qt ^ q0) + i16) * 4, 0));
            }
        }
        v4u z[8];
        const int zo = OFF_ZH + (s * NR + r) * (NH * 256) + ((i16 >> 1) & 1) * 4096 + g * 128 + (2 * (i16 >> 2) + (i16 & 1)) * 16;
        if (half) {
            wait_flags(rw, FL_Z + 2 * r, 1, 2, s, lane, budget, true);
#pragma unroll
            for (int k = 0; k < 8; ++k) z[k] = LD16(rw, zo + k * 512, 0);
            wait_flags(rw, FL_P + (s & 3), 4, 16, s, lane, budget, true);
            v4u pv[5];
#pragma unroll
            for (int k = 0; k < 5; ++k) pv[k] = LD16(rw, OFF_PX + s * 81920 + q0 * 40960 + tid * 16, k * 8192);
#pragma unroll
            for (int k = 0; k < 5; ++k) *(LAS v4u*)(lds + k * 8192 + tid * 16) = pv[k];
        } else {
            wait_flags(rw, FL_P + (s & 3), 4, 16, s, lane, budget);
            v4u pv[10];
#pragma unroll
            for (int k = 0; k < 10; ++k) pv[k] = LD16(rw, OFF_PX + s * 81920 + tid * 16, k * 8192);
            wait_flags(rw, FL_Z + 2 * r, 1, 2, s, lane, budget);
#pragma unroll
            for (int k = 0; k < 8; ++k) z[k] = LD16(rw, zo + k * 512, 0);
            MB();
#pragma unroll
            for (int k = 0; k < 10; ++k) *(LAS v4u*)(lds + k * 8192 + tid * 16) = pv[k];
        }
        __syncthreads();
        if (w < 6) {
#pragma unroll 1
            for (int qt = 0; qt < (half ? 1 : 2); ++qt) {
                f32x4 d = {}, e = {};
                lptr pl = lds + qt * 40960 + lane * 16;
                bf16x8 f[6];
#pragma unroll
                for (int i = 0; i < 6; ++i) f[i] = lds8(pl + i * 1024);
                __builtin_amdgcn_sched_barrier(0);
#pragma unroll
                for (int k = 0; k < 40; ++k) {
                    const bf16x8 v = k < 32 ? b[k < 32 ? k : 0] : as8(z[k < 32 ? 0 : k - 32]);
                    if (k & 1) e = MFMA16(v, f[k % 6], e); else d = MFMA16(v, f[k % 6], d);
                    if (k + 6 < 40) f[k % 6] = lds8(pl + (k + 6) * 1024);
                    __builtin_amdgcn_sched_barrier(0);
                }
                d += e;
                const float gq = qt ? gt[1] : gt[0], og = rbf(1.0f - gq);
                const v2u aq = qt ? av[1] : av[0];
                const float r0 = rbf(gq * bflo(aq.x)) + rbf(og * rbf(d[0])), r1 = rbf(gq * bfhi(aq.x)) + rbf(og * rbf(d[1]));
                const float r2 = rbf(gq * bflo(aq.y)) + rbf(og * rbf(d[2])), r3 = rbf(gq * bfhi(aq.y)) + rbf(og * rbf(d[3]));
                v2u ov = {pack2(r0, r1), pack2(r2, r3)};
                *(v2u*)(a.out + (size_t)(32 * s + 16 * (qt ^ q0) + i16) * DM + 16 * r + 4 * g) = ov;
            }
        }
        if (half) break;
        LBAR();
    }
    if (budget < 0 && lane == 0) ST4(6u, rw, FL_ERR * 128);
}

__global__ void __launch_bounds__(512, 2) k_all(Args a) {
    extern __shared__ __attribute__((aligned(16))) char smem[];
    lptr lds = (lptr)smem;
    const rsrc_t rw = __builtin_amdgcn_make_buffer_rsrc(a.ws, 0, WS_BYTES, 0x00020000);
    if (threadIdx.x == 0) {
        const int x = (int)(__builtin_amdgcn_s_getreg(20 | (3 << 11)) & 7u);
        int role = 0;
        for (int k = 0; k < 8; ++k) {
            const int gq = (x + k) & 7;
            const int t = 1 + (int)__hip_atomic_fetch_add((u32*)(a.ws + (FL_T + gq) * 128), 1u, __ATOMIC_RELAXED, __HIP_MEMORY_SCOPE_AGENT);
            if (t < 32) { role = gq + 8 * t; break; }
        }
        *(volatile LAS int*)lds = role;
    }
    __syncthreads();
    const int b = __builtin_amdgcn_readfirstlane(*(volatile LAS int*)lds), xg = b & 7, slot = b >> 3;
    __syncthreads();

    if (b == 8 * 8) role_probe(rw);
    if (slot >= 20) { role_wvo(a, lds, rw, (xg & 3) * 24 + (xg < 4 ? 12 : 0) + slot - 20); __syncthreads(); }
    if ((xg < 4 && slot >= 26) || (xg >= 4 && slot >= 20)) {
        const int idx = xg < 4 ? 48 + (slot - 26) * 4 + xg : (31 - slot) * 4 + (xg - 4);
        role_s4(a, lds, rw, idx);
        __syncthreads();
    }
    bool post = false;
    if (xg < 4) {
        if (slot < 8) role_qk(a, lds, rw, xg * 8 + slot, xg);
        else if (slot < 26) role_rz(a, lds, rw, xg * 18 + slot - 8, xg);
    } else {
        const int j = xg - 4;
        post = true;
        if (slot < 16) { role_vvo(a, lds, rw, j * 8 + (slot >> 1), slot & 1); post = slot < NJOIN; }
        else if (slot < 20) role_k2(a, lds, rw, j * 4 + slot - 16);
        else if (slot < 28) { role_q2p(a, lds, rw, j * 4 + ((slot - 20) >> 1), slot & 1); post = false; }
    }
    if (post) { __syncthreads(); role_post(a, lds, rw); }
}

__global__ void __launch_bounds__(512) k_init(Args a) {
    const int t = threadIdx.x, b = blockIdx.x;
    const v4u ff = {0xFFFFFFFFu, 0xFFFFFFFFu, 0xFFFFFFFFu, 0xFFFFFFFFu};
    if (b < INIT_BYTES / 8192) *(v4u*)(a.ws + ((size_t)b * 512 + t) * 16) = ff;
    const int q = t / 144, l = t - q * 144;
    u32 v0 = 0, v1 = 0;
    if (q < 3) v0 = *(const u32*)((q == 0 ? a.Wo : q == 1 ? a.Wv : a.Wk) + (size_t)b * 9216 + l * 64);
    if (q == 0) v1 = *(const u32*)(a.Wq + (size_t)b * 9216 + l * 64);
    else if (q == 1 && l < 72) v1 = *(const u32*)(a.u + (size_t)b * 4608 + l * 64);
    asm volatile("" :: "v"(v0), "v"(v1));
}
extern "C" void kernel_launch(void* const* d_in, const int* in_sizes, int n_in, void* d_out, int out_size, void* d_ws, size_t ws_size, hipStream_t stream) {
    Args a;
    a.u = (const u16*)d_in[0]; a.Are = (const u16*)d_in[1]; a.Aim = (const u16*)d_in[2]; a.Bre = (const u16*)d_in[3]; a.Bim = (const u16*)d_in[4];
    a.Cre = (const u16*)d_in[5]; a.Cim = (const u16*)d_in[6]; a.logdt = (const u16*)d_in[7]; a.dskip = (const u16*)d_in[8];
    a.mem0 = (const u16*)d_in[10]; a.Wq = (const u16*)d_in[11]; a.Wk = (const u16*)d_in[12]; a.Wv = (const u16*)d_in[13]; a.Wo = (const u16*)d_in[14];
    a.gatew = (const u16*)d_in[16]; a.out = (u16*)d_out; a.ws = (char*)d_ws;
    const int LDS_ALL = 160 * 1024;
    (void)hipFuncSetAttribute((const void*)k_all, hipFuncAttributeMaxDynamicSharedMemorySize, LDS_ALL);
    hipLaunchKernelGGL(k_init, dim3(256), dim3(512), 0, stream, a);
    hipLaunchKernelGGL(k_all, dim3(256), dim3(512), LDS_ALL, stream, a);
}
```

```cpp
#include <hip/hip_runtime.h>
#include <hip/hip_bf16.h>
#include <stdint.h>

typedef unsigned short u16;
typedef unsigned int u32;
typedef u32 v4u __attribute__((ext_vector_type(4)));
typedef u32 v2u __attribute__((ext_vector_type(2)));
typedef float f32x4 __attribute__((ext_vector_type(4)));
typedef float f32x2 __attribute__((ext_vector_type(2)));
typedef float f32x16 __attribute__((ext_vector_type(16)));
typedef __bf16 bf16x2 __attribute__((ext_vector_type(2)));
typedef __bf16 bf16x8 __attribute__((ext_vector_type(8)));
typedef __bf16 bf16x16 __attribute__((ext_vector_type(16)));
typedef short s16x4 __attribute__((ext_vector_type(4)));
#define LAS __attribute__((address_space(3)))
typedef LAS char* lptr;
typedef __amdgpu_buffer_rsrc_t rsrc_t;

constexpr int SEQ = 2048, DM = 576, NH = 32, DH = 128, NSEG = 64, NR = 36, NR8 = 72, NREP = 4;
constexpr int MEMB = 9216, BC_QK = 80;
constexpr int FL_K = 0, FL_V = 16, FL_M = 80, FL_P = 112, FL_S = 176, FL_Q = 254, FL_ERR = 255, FL_Z = 256, FL_W = 328, FL_T = 424, FL_PAR = 432, NS4 = 72;
constexpr int OFF_RING = 65536;
constexpr int N_MEMA = 0, N_PX = 16384, N_PMM = 49152, N_MEMB = 49152 + 4096, N_PROBE = 49152 + 6144;
constexpr int INIT_BYTES = OFF_RING + 16 * 65536;
constexpr int OFF_ZH = INIT_BYTES;
constexpr int OFF_KX = OFF_ZH + NSEG * NR * NH * 256;
constexpr int OFF_VOX = OFF_KX + NH * SEQ * DH * 2;
constexpr int OFF_KM = OFF_VOX + NSEG * NR * NH * 1024;
constexpr int OFF_PX = OFF_KM + NSEG * NH * 2048;
constexpr int OFF_A4 = OFF_PX + NSEG * 81920;
constexpr int OFF_G = OFF_A4 + SEQ * DM * 2;
constexpr int OFF_WVO = OFF_G + SEQ * 4;
constexpr int WS_BYTES = OFF_WVO + NR8 * 294912;
constexpr int TK_LA = 10, TK_LR = 10, TK_TOL = 8, TK_GAP = 30;
constexpr int NJOIN = 4;

struct Args {
    const u16* u; const u16* Are; const u16* Aim; const u16* Bre; const u16* Bim; const u16* Cre; const u16* Cim;
    const u16* logdt; const u16* dskip; const u16* mem0; const u16* Wq; const u16* Wk; const u16* Wv; const u16* Wo;
    const u16* gatew; u16* out; char* ws;
};

__device__ __forceinline__ float bf2f(u16 v) { return __uint_as_float((u32)v << 16); }
__device__ __forceinline__ float bflo(u32 v) { return __uint_as_float(v << 16); }
__device__ __forceinline__ float bfhi(u32 v) { return __uint_as_float(v & 0xffff0000u); }
__device__ __forceinline__ u32 pack2(float lo, float hi) { f32x2 f = {lo, hi}; bf16x2 b = __builtin_convertvector(f, bf16x2); return __builtin_bit_cast(u32, b); }
__device__ __forceinline__ float rbf(float x) { return bflo(pack2(x, x)); }
__device__ __forceinline__ u32 nos(u32 v) { return min(v, 0xFFFFFFFEu); }
__device__ __forceinline__ v2u trd(lptr p) { return __builtin_bit_cast(v2u, __builtin_amdgcn_ds_read_tr16_b64_v4i16((LAS s16x4*)p)); }
__device__ __forceinline__ bf16x8 as8(v4u v) { return __builtin_bit_cast(bf16x8, v); }
__device__ __forceinline__ bf16x8 as8(v2u a, v2u b) { v4u v = {a.x, a.y, b.x, b.y}; return __builtin_bit_cast(bf16x8, v); }
__device__ __forceinline__ v4u asu(bf16x8 v) { return __builtin_bit_cast(v4u, v); }
__device__ __forceinline__ bf16x16 cat16(bf16x8 a, bf16x8 b) { return __builtin_shufflevector(a, b, 0, 1, 2, 3, 4, 5, 6, 7, 8, 9, 10, 11, 12, 13, 14, 15); }
__device__ __forceinline__ bf16x8 lds8(lptr p) { return *(LAS bf16x8*)p; }
__device__ __forceinline__ u32 bad4(v4u v) { return max(max(v.x, v.y), max(v.z, v.w)); }
#define NEARB(s, rep, pm) (OFF_RING + ((((s) & 3) * 4 + (rep)) << 16) + ((((pm) >> (((s) & 3) * 4 + (rep))) & 1) << 13))
#define FARB(s, rep, pm) ((NEARB(s, rep, pm) ^ 8192) + (((s) & 4) << 13))
#define BCO(slot) (((slot) & 63) * 128 + ((slot) >> 6) * 16384)
#define LBAR() do { asm volatile("s_waitcnt lgkmcnt(0)" ::: "memory"); __builtin_amdgcn_s_barrier(); asm volatile("" ::: "memory"); } while (0)
#define MB() asm volatile("" ::: "memory")
#define DRAIN() asm volatile("s_waitcnt vmcnt(0)" ::: "memory")
#define PIN4(x) asm volatile("" : "+v"(x))
#define MFMA16(a, b, c) __builtin_amdgcn_mfma_f32_16x16x32_bf16((a), (b), (c), 0, 0, 0)
#define MFMA32(a, b, c) __builtin_amdgcn_mfma_f32_32x32x16_bf16((a), (b), (c), 0, 0, 0)
#define SMF(a, b, c, idx) __builtin_amdgcn_smfmac_f32_16x16x64_bf16((a), (b), (c), (idx), 0, 0)
#define LD16(r, vo, so) __builtin_amdgcn_raw_buffer_load_b128((r), (vo), (so), 16)
#define LD8(r, vo, so) __builtin_amdgcn_raw_buffer_load_b64((r), (vo), (so), 16)
#define LD4(r, vo, so) __builtin_amdgcn_raw_buffer_load_b32((r), (vo), (so), 16)
#define ST16(v, r, vo) __builtin_amdgcn_raw_buffer_store_b128((v), (r), (vo), 0, 16)
#define ST8(v, r, vo) __builtin_amdgcn_raw_buffer_store_b64((v), (r), (vo), 0, 16)
#define ST4(v, r, vo) __builtin_amdgcn_raw_buffer_store_b32((v), (r), (vo), 0, 16)

__device__ __forceinline__ float swap32_sum(float a, float b) {
    auto r = __builtin_amdgcn_permlane32_swap(__float_as_uint(a), __float_as_uint(b), false, false);
    return __uint_as_float(r[0]) + __uint_as_float(r[1]); }
__device__ __forceinline__ float xmax(float m) {
    auto r = __builtin_amdgcn_permlane16_swap(__float_as_uint(m), __float_as_uint(m), false, false);
    m = fmaxf(__uint_as_float(r[0]), __uint_as_float(r[1]));
    auto q = __builtin_amdgcn_permlane32_swap(__float_as_uint(m), __float_as_uint(m), false, false);
    return fmaxf(__uint_as_float(q[0]), __uint_as_float(q[1])); }
__device__ __forceinline__ float xsum(float m) {
    auto r = __builtin_amdgcn_permlane16_swap(__float_as_uint(m), __float_as_uint(m), false, false);
    m = __uint_as_float(r[0]) + __uint_as_float(r[1]);
    auto q = __builtin_amdgcn_permlane32_swap(__float_as_uint(m), __float_as_uint(m), false, false);
    return __uint_as_float(q[0]) + __uint_as_float(q[1]); }
__device__ __forceinline__ float vmax(float a, float b) { float r; asm("v_max_f32 %0, %1, %2" : "=v"(r) : "v"(a), "v"(b)); return r; }
__device__ __forceinline__ float vmax3(float a, float b, float c) { float r; asm("v_max3_f32 %0, %1, %2, %3" : "=v"(r) : "v"(a), "v"(b), "v"(c)); return r; }
__device__ __forceinline__ u32 ror8(u32 v) { return (u32)__builtin_amdgcn_update_dpp(0, (int)v, 0x128, 0xf, 0xf, true); }
__device__ __forceinline__ float ror8f(float v) { return __uint_as_float(ror8(__float_as_uint(v))); }
__device__ __forceinline__ int wave_max(int d) {
    d = max(d, __builtin_amdgcn_update_dpp(d, d, 0x128, 0xf, 0xf, false));
    d = max(d, __builtin_amdgcn_update_dpp(d, d, 0x124, 0xf, 0xf, false));
    d = max(d, __builtin_amdgcn_update_dpp(d, d, 0x122, 0xf, 0xf, false));
    d = max(d, __builtin_amdgcn_update_dpp(d, d, 0x121, 0xf, 0xf, false));
    return max(max(__builtin_amdgcn_readlane(d, 0), __builtin_amdgcn_readlane(d, 16)), max(__builtin_amdgcn_readlane(d, 32), __builtin_amdgcn_readlane(d, 48)));
}

constexpr int SPIN_MAX = 2000000;
__device__ __forceinline__ int wait_flags(rsrc_t rw, int line0, int stride, int n, int need, int lane, int& budget, bool fast = false) {
    const int vo = (line0 + (lane < n ? lane : 0) * stride) * 128;
    int mn;
    for (;;) {
        const int v = (int)LD4(rw, vo, 0);
        MB();
        mn = -wave_max(-v);
        if (mn >= need) break;
        if (--budget < 0) break;
        if (fast) __builtin_amdgcn_s_sleep(2); else __builtin_amdgcn_s_sleep(12);
    }
    return mn;
}

template <int NRW, int CW>
__device__ __forceinline__ void rows_ld(v4u (&v)[NRW * CW / 512], const u16* g, int gs, int tid) {
#pragma unroll
    for (int j = 0; j < NRW * CW / 512; ++j) { const int i = tid + 512 * j, row = i / CW, c = i % CW; v[j] = *(const v4u*)(g + (size_t)row * gs + c * 8); }
}
template <int NRW, int CW>
__device__ __forceinline__ void rows_st(const v4u (&v)[NRW * CW / 512], lptr lds, int ls, int tid) {
#pragma unroll
    for (int j = 0; j < NRW * CW / 512; ++j) { const int i = tid + 512 * j, row = i / CW, c = i % CW; *(LAS v4u*)(lds + row * ls + c * 16) = v[j]; }
}
__device__ __forceinline__ bf16x8 frag16(lptr base, int ls, int lane) {
    const int i16 = lane & 15, bg = lane >> 4, q = i16 >> 2, p = i16 & 3;
    lptr a = base + (8 * bg + q) * ls + p * 8;
    return as8(trd(a), trd(a + 4 * ls));
}
__device__ __forceinline__ bf16x8 frag32p(lptr base, int ls, int lane) {
    const int i16 = lane & 15, G = lane >> 4, q = i16 >> 2, p = i16 & 3;
    lptr a = base + (8 * (G >> 1) + q) * ls + (16 * (p & 1) + 8 * (G & 1) + 4 * (p >> 1)) * 2;
    return as8(trd(a), trd(a + 4 * ls));
}

constexpr int XS = 1168;
__device__ __forceinline__ void xload(v4u (&st)[9], const u16* x, int tok0, int tid) {
#pragma unroll
    for (int j = 0; j < 9; ++j) st[j] = *(const v4u*)(x + (size_t)tok0 * DM + (tid + 512 * j) * 8);
}
__device__ __forceinline__ void xstore(const v4u (&st)[9], lptr xl, int tid) {
#pragma unroll
    for (int j = 0; j < 9; ++j) { const int i = tid + 512 * j; *(LAS v4u*)(xl + i * 16 + (i / 72) * 16) = st[j]; }
}
__device__ __forceinline__ void load_w2(bf16x8 (&A)[36], const u16* W, int h0, lptr lds, int tid, int lane, int w) {
    v4u v[6][6];
#pragma unroll
    for (int c = 0; c < 6; ++c) rows_ld<96, 32>(v[c], W + (size_t)c * 96 * 4096 + h0 * DH, 4096, tid);
    __builtin_amdgcn_sched_barrier(0);
#pragma unroll
    for (int c = 0; c < 6; ++c) {
        lptr l = lds + (c & 1) * (96 * 544);
        rows_st<96, 32>(v[c], l, 544, tid);
        LBAR();
#pragma unroll
        for (int ks = 0; ks < 6; ++ks) A[c * 6 + ks] = frag32p(l + ks * 16 * 544 + w * 64, 544, lane);
        __builtin_amdgcn_sched_barrier(0);
    }
    LBAR();
}

__device__ __forceinline__ f32x16 proj_tile(const bf16x8 (&A)[36], lptr xb) {
    f32x16 acc = {};
    bf16x8 f[6];
#pragma unroll
    for (int i = 0; i < 6; ++i) f[i] = lds8(xb + i * 32);
    __builtin_amdgcn_sched_barrier(0);
#pragma unroll
    for (int ks = 0; ks < 36; ++ks) {
        acc = MFMA32(A[ks], f[ks % 6], acc);
        if (ks + 6 < 36) f[ks % 6] = lds8(xb + (ks + 6) * 32);
        __builtin_amdgcn_sched_barrier(0);
    }
    return acc;
}
__device__ __forceinline__ void role_k2(const Args& a, lptr lds, rsrc_t rw, int cu) {
    const int tid = threadIdx.x, lane = tid & 63, w = __builtin_amdgcn_readfirstlane(tid >> 6);
    v4u st[9];
    bf16x8 A[36];
    xload(st, a.u, 0, tid);
    load_w2(A, a.Wk, 2 * cu, lds, tid, lane, w);
    xstore(st, lds, tid);
    __syncthreads();
    const int o0 = OFF_KX + (2 * cu + (w >> 2)) * (NSEG * 8192) + ((w & 3) * 4 + (lane >> 5) * 2) * 512 + (lane & 31) * 16;
    for (int blk = 0; blk < 32; ++blk) {
        lptr xl = lds + (blk & 1) * (64 * XS);
        if (blk + 1 < 32) xload(st, a.u, (blk + 1) * 64, tid);
#pragma unroll
        for (int tt = 0; tt < 2; ++tt) {
            const f32x16 acc = proj_tile(A, xl + (32 * tt + (lane & 31)) * XS + (lane >> 5) * 16);
            v4u c0 = {pack2(acc[0], acc[1]), pack2(acc[2], acc[3]), pack2(acc[4], acc[5]), pack2(acc[6], acc[7])};
            v4u c1 = {pack2(acc[8], acc[9]), pack2(acc[10], acc[11]), pack2(acc[12], acc[13]), pack2(acc[14], acc[15])};
            const int o = o0 + (blk * 2 + tt) * 8192;
            ST16(c0, rw, o); ST16(c1, rw, o + 512);
            if (blk == 0 && tt == 0) { DRAIN(); LBAR(); if (tid == 0) ST4(0u, rw, (FL_K + cu) * 128); }
        }
        if (blk + 1 < 32) xstore(st, lds + ((blk + 1) & 1) * (64 * XS), tid);
        const bool full = blk < 4 || blk == 31;
        if (full) DRAIN(); else asm volatile("s_waitcnt vmcnt(4)" ::: "memory");
        LBAR();
        if (tid == 0) ST4((u32)(full ? 2 * blk + 1 : 2 * blk - 1), rw, (FL_K + cu) * 128);
    }
}

constexpr int VS = 272;
__device__ __forceinline__ void role_q2p(const Args& a, lptr lds, rsrc_t rw, int cu, int par) {
    const int tid = threadIdx.x, lane = tid & 63, w = __builtin_amdgcn_readfirstlane(tid >> 6);
    v4u st[9];
    bf16x8 A[36];
    load_w2(A, a.Wq, 2 * cu, lds, tid, lane, w);
    lptr xl = lds, ql = lds + 64 * XS;
    LAS int* cnt = (LAS int*)(lds + 64 * XS + 4 * 64 * VS);
    if (tid < 2) cnt[tid] = 0;
    xload(st, a.u, par * 64, tid);
    xstore(st, xl, tid);
    __syncthreads();
    int budget = SPIN_MAX, kdone = -1;
    const int i16 = lane & 15, g = lane >> 4;
    const int sg = w >> 2, hh = (w >> 1) & 1, qt = w & 1, h = 2 * cu + hh;
    const float c2 = 0.08838834764831845f * 1.4426950408889634f;
    for (int it = -1; it < 16; ++it) {
        const int blk = 2 * it + par;
        if (it + 1 < 16) {
            if (it + 2 < 16) xload(st, a.u, (blk + 4) * 64, tid);
            lptr qb = ql + (((it + 1) & 1) * 2 + (w >> 2)) * (64 * VS) + ((w & 3) * 32 + (lane >> 5) * 16) * 2;
#pragma unroll
            for (int tt = 0; tt < 2; ++tt) {
                const f32x16 acc = proj_tile(A, xl + (32 * tt + (lane & 31)) * XS + (lane >> 5) * 16);
                v4u c0 = {pack2(acc[0], acc[1]), pack2(acc[2], acc[3]), pack2(acc[4], acc[5]), pack2(acc[6], acc[7])};
                v4u c1 = {pack2(acc[8], acc[9]), pack2(acc[10], acc[11]), pack2(acc[12], acc[13]), pack2(acc[14], acc[15])};
                lptr qp = qb + (32 * tt + (lane & 31)) * VS;
                *(LAS v4u*)qp = c0; *(LAS v4u*)(qp + 16) = c1;
            }
            __syncthreads();
            if (it + 2 < 16) xstore(st, xl, tid);
        }
        if (it >= 0) {
            const int s = 2 * blk + sg;
            if (kdone < s) kdone = wait_flags(rw, FL_K + cu, 1, 1, s, lane, budget);
            bf16x8 k0[4], k1[4], km[4], qf[4];
            const int ko = OFF_KX + (h * NSEG + s) * 8192 + g * 512 + i16 * 16;
#pragma unroll
            for (int ks = 0; ks < 4; ++ks) { k0[ks] = as8(LD16(rw, ko, ks * 2048)); k1[ks] = as8(LD16(rw, ko + 256, ks * 2048)); }
            lptr qp = ql + ((it & 1) * 2 + hh) * (64 * VS) + (32 * sg + 16 * qt + i16) * VS + g * 16;
#pragma unroll
            for (int ks = 0; ks < 4; ++ks) qf[ks] = lds8(qp + ks * 64);
            f32x4 S0 = {}, S1 = {}, Sm = {};
#pragma unroll
            for (int ks = 0; ks < 4; ++ks) { S0 = MFMA16(k0[ks], qf[ks], S0); S1 = MFMA16(k1[ks], qf[ks], S1); }
            wait_flags(rw, FL_M + h, 1, 1, s, lane, budget, s >= 62);
            const int mo = OFF_KM + (s * NH + h) * 2048 + g * 128 + (i16 & 7) * 16;
#pragma unroll
            for (int ks = 0; ks < 4; ++ks) km[ks] = as8(LD16(rw, mo + ks * 512, 0));
#pragma unroll
            for (int ks = 0; ks < 4; ++ks) Sm = MFMA16(km[ks], qf[ks], Sm);
            const int qi = 16 * qt + i16, t0 = 4 * g;
#pragma unroll
            for (int r = 0; r < 4; ++r) {
                if (t0 + r > qi) S0[r] = -INFINITY;
                if (t0 + r + 16 > qi) S1[r] = -INFINITY;
                if (g >= 2) Sm[r] = -INFINITY;
            }
            float m = fmaxf(fmaxf(fmaxf(S0[0], S0[1]), fmaxf(S0[2], S0[3])), fmaxf(fmaxf(S1[0], S1[1]), fmaxf(S1[2], S1[3])));
            m = fmaxf(m, fmaxf(fmaxf(Sm[0], Sm[1]), fmaxf(Sm[2], Sm[3])));
            m = xmax(m);
            const float nm = -m * c2;
            float sum = 0.f;
#pragma unroll
            for (int r = 0; r < 4; ++r) {
                S0[r] = __builtin_amdgcn_exp2f(fmaf(S0[r], c2, nm)); S1[r] = __builtin_amdgcn_exp2f(fmaf(S1[r], c2, nm)); Sm[r] = __builtin_amdgcn_exp2f(fmaf(Sm[r], c2, nm));
                sum += S0[r] + S1[r] + Sm[r];
            }
            sum = xsum(sum);
            const float inv = __builtin_amdgcn_rcpf(sum);
            v4u px = {pack2(S0[0] * inv, S0[1] * inv), pack2(S0[2] * inv, S0[3] * inv), pack2(S1[0] * inv, S1[1] * inv), pack2(S1[2] * inv, S1[3] * inv)};
            const int po = OFF_PX + (s * 2 + qt) * 40960;
            ST16(px, rw, po + h * 1024 + lane * 16);
            const u32 ma = pack2(Sm[0] * inv, Sm[1] * inv), mb = pack2(Sm[2] * inv, Sm[3] * inv);
            auto ra = __builtin_amdgcn_permlane16_swap(ma, ma, false, false);
            auto rb = __builtin_amdgcn_permlane16_swap(mb, mb, false, false);
            v4u pm = {ra[0], rb[0], ra[1], rb[1]};
            if (lane < 16) ST16(pm, rw, po + 32768 + h * 256 + lane * 16);
            DRAIN();
            if (lane == 0) {
                const int old = __hip_atomic_fetch_add(cnt + sg, 1, __ATOMIC_RELAXED, __HIP_MEMORY_SCOPE_WORKGROUP);
                if ((old & 3) == 3) ST4((u32)s, rw, (FL_P + 4 * cu + 2 * par + sg) * 128);
            }
        }
        __syncthreads();
    }
    if (budget < 0 && lane == 0) ST4(5u, rw, FL_ERR * 128);
}

__device__ __forceinline__ void role_vvo(const Args& a, lptr lds, rsrc_t rw, int h, int par) {
    const int tid = threadIdx.x, lane = tid & 63, w = __builtin_amdgcn_readfirstlane(tid >> 6);
    v4u st[9];
#pragma unroll
    for (int j = 0; j < 9; ++j) { const int i = tid + 512 * j; st[j] = *(const v4u*)(a.u + (size_t)(par * 32 + (i >= 2304 ? 32 : 0)) * DM + i * 8); }
    bf16x8 Av[18], Bo[5][4];
    {
        v4u v[3][6], o[2][9];
#pragma unroll
        for (int c = 0; c < 3; ++c) rows_ld<192, 16>(v[c], a.Wv + (size_t)c * 192 * 4096 + h * DH, 4096, tid);
#pragma unroll
        for (int c = 0; c < 2; ++c) rows_ld<64, 72>(o[c], a.Wo + ((size_t)h * DH + c * 64) * DM, DM, tid);
        __builtin_amdgcn_sched_barrier(0);
#pragma unroll
        for (int c = 0; c < 3; ++c) {
            lptr l = lds + (c & 1) * 77824;
            rows_st<192, 16>(v[c], l, 288, tid);
            LBAR();
#pragma unroll
            for (int ks = 0; ks < 6; ++ks) Av[c * 6 + ks] = frag16(l + ks * 32 * 288 + w * 32, 288, lane);
            __builtin_amdgcn_sched_barrier(0);
        }
#pragma unroll
        for (int c = 0; c < 2; ++c) {
            lptr l = lds + ((c + 1) & 1) * 77824;
            rows_st<64, 72>(o[c], l, 1184, tid);
            LBAR();
#pragma unroll
            for (int j = 0; j < 5; ++j) {
                const int r = (w + 8 * j < NR) ? w + 8 * j : w;
#pragma unroll
                for (int ks = 0; ks < 2; ++ks) Bo[j][c * 2 + ks] = frag16(l + ks * 32 * 1184 + r * 32, 1184, lane);
            }
            __builtin_amdgcn_sched_barrier(0);
        }
        LBAR();
    }
    lptr xl = lds, vl = lds + 64 * XS;
    xstore(st, xl, tid);
    __syncthreads();
    const int rho = lane & 15, bg = lane >> 4;
    const int vo0 = OFF_VOX + ((2 * w + ((rho >> 1) & 1)) * 16 + (h >> 1)) * 1024 + ((2 * (h & 1) + (bg & 1)) * 16 + (bg >> 1) * 8 + 2 * (rho >> 2) + (rho & 1)) * 16;
    const int fo = (FL_V + 2 * h + par) * 128;
    auto stage1 = [&](int tt) {
        f32x4 acc = {};
        lptr xb = xl + (16 * tt + rho) * XS + bg * 16;
        bf16x8 f[6];
#pragma unroll
        for (int i = 0; i < 6; ++i) f[i] = lds8(xb + i * 64);
        __builtin_amdgcn_sched_barrier(0);
#pragma unroll
        for (int ks = 0; ks < 18; ++ks) {
            acc = MFMA16(Av[ks], f[ks % 6], acc);
            if (ks + 6 < 18) f[ks % 6] = lds8(xb + (ks + 6) * 64);
            __builtin_amdgcn_sched_barrier(0);
        }
        v2u o = {pack2(acc[0], acc[1]), pack2(acc[2], acc[3])};
        *(LAS v2u*)(vl + (16 * tt + rho) * VS + (16 * w + 4 * bg) * 2) = o;
    };
    auto stage2 = [&](int sg, int s) {
        bf16x8 Af[2][4];
#pragma unroll
        for (int half = 0; half < 2; ++half)
#pragma unroll
            for (int ks = 0; ks < 4; ++ks) Af[half][ks] = lds8(vl + (32 * sg + rho + 16 * half) * VS + ks * 64 + bg * 16);
#pragma unroll
        for (int j = 0; j < 5; ++j) {
            if (j < 4 || w < 4) {
                f32x4 d0 = {}, d1 = {};
#pragma unroll
                for (int ks = 0; ks < 4; ++ks) { d0 = MFMA16(Af[0][ks], Bo[j][ks], d0); d1 = MFMA16(Af[1][ks], Bo[j][ks], d1); }
                v4u o = {pack2(d0[0], d0[1]), pack2(d0[2], d0[3]), pack2(d1[0], d1[1]), pack2(d1[2], d1[3])};
                ST16(o, rw, vo0 + (s * NR + 8 * j) * (NH * 1024));
            }
        }
    };
    int prev = -1;
#pragma unroll 1
    for (int un = 0; un < 32; ++un) {
        const int sg = un & 1, blk = (un >> 1) * 2 + par;
        const int s = un < 2 ? par + 2 * un : 2 * blk + sg;
        if (sg == 0 && blk + 2 < 32) xload(st, a.u, (blk + 2) * 64, tid);
        stage1(2 * sg); stage1(2 * sg + 1);
        if (sg == 1) DRAIN(); else if (blk + 2 < 32) asm volatile("s_waitcnt vmcnt(9)" ::: "memory"); else DRAIN();
        LBAR();
        if (tid == 0 && prev >= 0) ST4((u32)prev, rw, fo);
        if (sg == 1 && blk + 2 < 32) xstore(st, xl, tid);
        stage2(sg, s);
        prev = s;
        if (un == 0 || un == 31) { DRAIN(); LBAR(); if (tid == 0) ST4((u32)s, rw, fo); }
        else if (sg == 1) LBAR();
    }
}
#define VOWN(s) ((s) < 4 ? ((s) & 1) : (((s) >> 1) & 1))

__device__ __forceinline__ u32 rt32() { return (u32)__builtin_amdgcn_s_memrealtime(); }
__device__ __forceinline__ void wait_until(u32 t) {
    if ((int)(t - rt32()) > 1000) return;
    while ((int)(rt32() - t) < -24) __builtin_amdgcn_s_sleep(4);
    while ((int)(rt32() - t) < 0) {}
}
struct TK { u32 XA, XR, EA, ER; int dA0, dA1, dA2, dR0, dR1, dR2; int okA, okR, gap; };
__device__ __forceinline__ int tk_est(int d0, int d1, int d2) { return max(min(d0, d1), min(max(d0, d1), d2)); }
template <int N>
__device__ __forceinline__ u32 beacon_max(rsrc_t rw, int fb, int slot0, int lane, int& budget) {
    const int vo = BCO(slot0 + (lane < N ? lane : 0)), v2 = BCO(64 + (lane & 7));
    u32 v, u = 0;
    for (;;) {
        v = LD4(rw, vo, fb);
        if (N > 64) u = LD4(rw, v2, fb);
        MB();
        if (__all(max(v, u) != 0xFFFFFFFFu)) break;
        if (--budget < 0) break;
        __builtin_amdgcn_s_sleep(1);
    }
    const u32 base = __builtin_amdgcn_readfirstlane(v);
    int d = (int)(v - base);
    if (N > 64) d = max(d, (int)(u - base));
    return base + (u32)wave_max(d);
}
struct BC2 { u32 v, u; };
__device__ __forceinline__ BC2 beacon_issue(rsrc_t rw, int fb, int lane) {
    BC2 b;
    b.v = LD4(rw, BCO(lane), fb);
    b.u = LD4(rw, BCO(64 + (lane & 7)), fb);
    MB();
    return b;
}
__device__ __forceinline__ u32 beacon_finish(BC2 b, rsrc_t rw, int fb, int lane, int& budget) {
    if (!__all(max(b.v, b.u) != 0xFFFFFFFFu)) return beacon_max<NR8>(rw, fb, 0, lane, budget);
    const u32 base = __builtin_amdgcn_readfirstlane(b.v);
    return base + (u32)wave_max(max((int)(b.v - base), (int)(b.u - base)));
}
__device__ __forceinline__ void tk_T0(TK& k, u32 T0, const Args& a) {
    if (k.okR) { k.dR2 = k.dR1; k.dR1 = k.dR0; k.dR0 = (int)(T0 - k.ER); }
    k.okA = (int)(k.XA - T0) >= TK_LA - TK_TOL;
    if (!k.okR) k.gap = k.okA ? max(TK_GAP, k.gap - 6) : min(k.gap + 16, 160);
    k.EA = k.XA;
    k.XR = k.EA + tk_est(k.dA0, k.dA1, k.dA2) + TK_LR;
}
__device__ __forceinline__ void tk_Tp(TK& k, u32 Tp, const Args& a) {
    const int okn = (int)(k.XR - Tp) >= TK_LR - TK_TOL;
    if (k.okA && (k.okR || !okn)) { k.dA2 = k.dA1; k.dA1 = k.dA0; k.dA0 = (int)(Tp - k.EA); }
    k.okR = okn;
    k.ER = k.okR ? k.XR : Tp + k.gap;
    k.XA = k.ER + tk_est(k.dR0, k.dR1, k.dR2) + TK_LA;
}
__device__ __forceinline__ u32 tk_specA(const TK& k, const Args& a) { return k.XR + tk_est(k.dR0, k.dR1, k.dR2) + TK_LA; }
__device__ __forceinline__ u32 tk_specR(const TK& k, const Args& a) { return k.XA + tk_est(k.dA0, k.dA1, k.dA2) + TK_LR; }
__device__ __forceinline__ void tk_init(TK& k) { const u32 n = rt32(); k.XA = k.XR = k.EA = k.ER = n - 100000u; k.dA0 = k.dA1 = k.dA2 = 125; k.dR0 = k.dR1 = k.dR2 = 65; k.okA = k.okR = 0; k.gap = 50; }

__device__ __forceinline__ void tk_post(lptr p, int tag, u32 t, int lane) { if (lane == 0) { v2u v = {(u32)tag, t}; *(volatile LAS v2u*)p = v; } }
__device__ __forceinline__ u32 tk_get(lptr p, int tag, int& budget) {
    for (;;) {
        const v2u v = *(volatile LAS v2u*)p;
        if ((int)__builtin_amdgcn_readfirstlane(v.x) >= tag || --budget < 0) return __builtin_amdgcn_readfirstlane(v.y);
        __builtin_amdgcn_s_sleep(2);
    }
}
__device__ __forceinline__ void spin_to(u32 X) {
    int rem;
    asm volatile("1:\n\ts_memrealtime vcc\n\ts_waitcnt lgkmcnt(0)\n\ts_sub_i32 %0, %1, vcc_lo\n\ts_cmp_gt_i32 %0, 3\n\ts_cbranch_scc1 1b\n\t"
                 "s_cmp_lt_i32 %0, 1\n\ts_cbranch_scc1 2f\n\ts_nop 3\n\ts_cmp_lt_i32 %0, 2\n\ts_cbranch_scc1 2f\n\ts_nop 3\n\t"
                 "s_cmp_lt_i32 %0, 3\n\ts_cbranch_scc1 2f\n\ts_nop 7\n2:"
                 : "=&s"(rem) : "s"(X) : "memory", "scc", "vcc");
}
__device__ __forceinline__ void tk_wait(lptr p, int tag, int& budget) {
    u32 X;
    for (;;) {
        const v2u v = *(volatile LAS v2u*)p;
        const u32 now = rt32();
        X = __builtin_amdgcn_readfirstlane(v.y);
        const int tg = (int)__builtin_amdgcn_readfirstlane(v.x), dt = (int)(X - now);
        if (tg >= tag) {
            if (dt <= 0 || dt > 1000 || tg > tag) return;
            if (dt <= 28) break;
        }
        if (--budget < 0) return;
        __builtin_amdgcn_s_sleep(1);
    }
    spin_to(X);
}
constexpr int OOB = 0x70000000;
__device__ __forceinline__ void gather_mem(rsrc_t rw, int nb, lptr ml, int lane, int w, lptr tkp, int tag, int& budget) {
    const int gi = w == 7 ? 5 : w;
    const int l0 = gi * 1536 + lane * 16, l1 = l0 + 1024;
    const int o0 = l0 < 8192 ? l0 : l0 + (N_MEMB - 8192), o1 = lane < 32 ? (l1 < 8192 ? l1 : l1 + (N_MEMB - 8192)) : OOB;
    lptr p1 = ml + (lane < 32 ? l1 : 14336 + lane * 16);
    asm volatile("" : "+s"(nb));
    tk_wait(tkp, tag, budget);
    v4u v0 = LD16(rw, o0, nb), v1 = LD16(rw, o1, nb);
    MB();
    *(LAS v4u*)(ml + l0) = v0; *(LAS v4u*)p1 = v1;
    MB();
    if (__builtin_expect(!__all(max(bad4(v0), bad4(v1)) != 0xFFFFFFFFu), 0)) {
        do {
            v0 = LD16(rw, o0, nb); v1 = LD16(rw, o1, nb);
            MB();
        } while (!__all(max(bad4(v0), bad4(v1)) != 0xFFFFFFFFu) && --budget >= 0);
        *(LAS v4u*)(ml + l0) = v0; *(LAS v4u*)p1 = v1;
    }
}
__device__ __forceinline__ void gather_mem4(rsrc_t rw, int nb, lptr ml, int lane, int gi, lptr tkp, int tag, int& budget) {
    const int l0 = gi * 2048 + lane * 16;
    const int o2 = lane < 16 ? N_MEMB + gi * 256 + lane * 16 : OOB;
    lptr p2 = ml + (lane < 16 ? 8192 + gi * 256 + lane * 16 : 14336 + lane * 16);
    asm volatile("" : "+s"(nb));
    tk_wait(tkp, tag, budget);
    v4u v0 = LD16(rw, l0, nb), v1 = LD16(rw, l0 + 1024, nb), v2 = LD16(rw, o2, nb);
    MB();
    *(LAS v4u*)(ml + l0) = v0; *(LAS v4u*)(ml + l0 + 1024) = v1; *(LAS v4u*)p2 = v2;
    MB();
    if (__builtin_expect(!__all(max(max(bad4(v0), bad4(v1)), bad4(v2)) != 0xFFFFFFFFu), 0)) {
        do {
            v0 = LD16(rw, l0, nb); v1 = LD16(rw, l0 + 1024, nb); v2 = LD16(rw, o2, nb);
            MB();
        } while (!__all(max(max(bad4(v0), bad4(v1)), bad4(v2)) != 0xFFFFFFFFu) && --budget >= 0);
        *(LAS v4u*)(ml + l0) = v0; *(LAS v4u*)(ml + l0 + 1024) = v1; *(LAS v4u*)p2 = v2;
    }
}
__device__ __forceinline__ int get_parity(rsrc_t rw, int lane, int& budget) {
    const int vo = (FL_PAR + (lane & 7)) * 128;
    u32 v;
    for (;;) {
        v = LD4(rw, vo, 0);
        MB();
        if (__all(v != 0xFFFFFFFFu) || --budget < 0) break;
        __builtin_amdgcn_s_sleep(8);
    }
    int pm = 0;
#pragma unroll
    for (int k = 0; k < 8; ++k) pm |= (__builtin_amdgcn_readlane((int)v, k) & 3) << (2 * k);
    return __builtin_popcount(pm) > 8 ? 0xFFFF : 0;
}
__device__ __forceinline__ void role_probe(rsrc_t rw) {
    const int lane = threadIdx.x & 63, w = __builtin_amdgcn_readfirstlane(threadIdx.x >> 6);
    const int o = OFF_RING + 2 * w * 65536 + N_PROBE;
    ST4(0xFFFFFFFFu, rw, lane < 6 ? o + (lane & 1) * 8192 + (lane >> 1) * 128 : OOB);
    DRAIN();
    int best[2] = {1 << 30, 1 << 30};
#pragma unroll
    for (int k = 0; k < 6; ++k) {
        const unsigned long long t0 = __builtin_amdgcn_s_memtime();
        asm volatile("s_waitcnt lgkmcnt(0)" ::: "memory");
        u32 v = LD4(rw, o + (k & 1) * 8192 + (k >> 1) * 128, 0);
        asm volatile("s_waitcnt vmcnt(0)" : "+v"(v) :: "memory");
        const unsigned long long t1 = __builtin_amdgcn_s_memtime();
        best[k & 1] = min(best[k & 1], (int)(t1 - t0));
    }
    if (lane == 0) ST4(best[1] < best[0] ? 3u : 0u, rw, (FL_PAR + w) * 128);
}
template <typename F>
__device__ __forceinline__ void qk_project_h(const bf16x16 (&Bq)[9], const bf16x16 (&Bk)[9], lptr ma, lptr xw, int idx, F hook) {
    f32x4 aq = {}, ak = {};
    bf16x8 A[9];
#pragma unroll
    for (int t = 0; t < 9; ++t) A[t] = lds8(ma + t * 1024);
    __builtin_amdgcn_sched_barrier(0);
#pragma unroll
    for (int t = 0; t < 9; ++t) {
        aq = SMF(A[t], Bq[t], aq, idx); ak = SMF(A[t], Bk[t], ak, idx);
        hook(t);
        __builtin_amdgcn_sched_barrier(0);
    }
    const float s0 = swap32_sum(aq[0], ak[0]), s1 = swap32_sum(aq[1], ak[1]), s2 = swap32_sum(aq[2], ak[2]), s3 = swap32_sum(aq[3], ak[3]);
    v2u o = {pack2(s0, s1), pack2(s2, s3)};
    *(LAS v2u*)xw = o;
}
__device__ __forceinline__ void qk_project(const bf16x16 (&Bq)[9], const bf16x16 (&Bk)[9], lptr ma, lptr xw, int idx) {
    qk_project_h(Bq, Bk, ma, xw, idx, [](int) {});
}

__device__ __forceinline__ void role_qk(const Args& a, lptr lds, rsrc_t rw, int h, int rep) {
    const int tid = threadIdx.x, lane = tid & 63, w = __builtin_amdgcn_readfirstlane(tid >> 6);
    bf16x16 Bq[9], Bk[9];
    {
        v4u xr[6];
#pragma unroll
        for (int j = 0; j < 6; ++j) {
            const int i = min(tid + 512 * j, 2879), row = i / 72, c = i - row * 72;
            xr[j] = *(const v4u*)((row < 8 ? a.mem0 + row * DM : a.u + (row - 8) * DM) + c * 8);
        }
        v4u v[6][6];
#pragma unroll
        for (int c = 0; c < 6; ++c) rows_ld<192, 16>(v[c], (c >= 3 ? a.Wk : a.Wq) + (size_t)(c % 3) * 192 * 4096 + h * DH, 4096, tid);
        __builtin_amdgcn_sched_barrier(0);
#pragma unroll
        for (int j = 0; j < 6; ++j) {
            const int i = min(tid + 512 * j, 2879), row = i / 72, c = i - row * 72, G = row >> 3;
            lptr d = lds + (G < 2 ? 57344 + G * MEMB : 135168 + (G - 2) * MEMB) + (c >> 1) * 256 + (row & 7) * 16 + (c & 1) * 8;
            *(LAS v2u*)d = v2u{xr[j].x, xr[j].z}; *(LAS v2u*)(d + 128) = v2u{xr[j].y, xr[j].w};
        }
#pragma unroll
        for (int c = 0; c < 6; ++c) {
            lptr l = lds + (c & 1) * 77824;
            rows_st<192, 16>(v[c], l, 288, tid);
            LBAR();
#pragma unroll
            for (int t = 0; t < 3; ++t) {
                bf16x16 f = cat16(frag16(l + (64 * t) * 288 + w * 32, 288, lane), frag16(l + (64 * t + 32) * 288 + w * 32, 288, lane));
                if (c >= 3) Bk[(c % 3) * 3 + t] = f; else Bq[c * 3 + t] = f;
            }
            __builtin_amdgcn_sched_barrier(0);
        }
        LBAR();
    }
    lptr ml = lds, xl = lds + MEMB;
    const int idx = (lane & 8) ? 0xEEEE : 0x4444;
    const int i16 = lane & 15, g = lane >> 4;
    const bool hi8 = (lane & 8) != 0;
    int budget = SPIN_MAX;
    lptr pq = xl + (8 * g + (i16 >> 2)) * 32 + (i16 & 1) * 8;
    lptr kl = lds + 16384 + lane * 16;
    lptr ma = ml + lane * 16, xw = xl + (16 * w + i16) * 32 + g * 8;
    const int ox = N_PX + (h >> 4) * 16384 + (h & 15) * 512 + ((g & 1) * 16 + (g >> 1) * 8 + (i16 & 7)) * 16;
    const int om = hi8 ? OOB : N_PMM + h * 128 + (i16 & 7) * 16;
    const int ob = lane < NREP ? BCO(BC_QK + h) : OOB;
    lptr tkp = lds + MEMB + 4096, kfp = tkp + 64;
    if (tid == 0) { tk_post(tkp, 0, rt32() - 100000u, 0); tk_post(kfp, 0, 0u, 0); }
    for (int c = tid; c < 576; c += 512) *(LAS v4u*)(ml + c * 16) = *(LAS v4u*)(lds + 57344 + c * 16);
    __syncthreads();
#pragma unroll 1
    for (int G = 0; G < 4; ++G) {
        qk_project(Bq, Bk, lds + (G ? 135168 + (G - 1) * MEMB : 57344 + MEMB) + lane * 16, xw, idx);
        LBAR();
        if (w < 4) {
            const bf16x8 F = as8(trd(pq + w * 1024 + 16), trd(pq + w * 1024 + 16 + 128));
            if ((i16 >> 3) == (G & 1)) *(LAS bf16x8*)(kl + ((G >> 1) * 4 + w) * 1024) = F;
        }
        LBAR();
    }
    const int pm = get_parity(rw, lane, budget);
    LBAR();
    if (w == 0) {
        const float c2 = 0.08838834764831845f * 1.4426950408889634f;
        for (int s = 0; s < NSEG - 1; ++s) {
            tk_get(kfp, s, budget);
            bf16x8 kx[2][4];
#pragma unroll
            for (int tl = 0; tl < 2; ++tl)
#pragma unroll
                for (int ks = 0; ks < 4; ++ks) kx[tl][ks] = lds8(kl + (s & 1) * 8192 + (tl * 4 + ks) * 1024);
            int oxs = ox + NEARB(s, i16 >> 3, pm), oxt = ox + NEARB(s, 2 + (i16 >> 3), pm), oms = om + NEARB(s, g, pm), obs = ob + FARB(s, lane & 3, pm);
            const int nb = NEARB(s, rep, pm);
            asm volatile("s_waitcnt lgkmcnt(0)" : "+v"(oxs), "+v"(oxt), "+v"(oms), "+v"(obs) :: "memory");
#pragma unroll
            for (int tl = 0; tl < 2; ++tl)
#pragma unroll
                for (int ks = 0; ks < 4; ++ks) PIN4(kx[tl][ks]);
            if (s > 0) {
                gather_mem(rw, nb, ml, lane, 0, tkp, s, budget);
            }
            LBAR();
            qk_project(Bq, Bk, ma, xw, idx);
            __builtin_amdgcn_s_setprio(3);
            LBAR();
            bf16x8 Fq[4], Fk[4];
#pragma unroll
            for (int ks = 0; ks < 4; ++ks) Fq[ks] = as8(trd(pq + ks * 1024), trd(pq + ks * 1024 + 128));
            __builtin_amdgcn_sched_barrier(0);
#pragma unroll
            for (int ks = 0; ks < 4; ++ks) Fk[ks] = as8(trd(pq + ks * 1024 + 16), trd(pq + ks * 1024 + 16 + 128));
            __builtin_amdgcn_sched_barrier(0);
            f32x4 S0 = {}, S1 = {}, Sm = {};
            S0 = MFMA16(kx[0][0], Fq[0], S0); S1 = MFMA16(kx[1][0], Fq[0], S1);
            S0 = MFMA16(kx[0][1], Fq[1], S0); S1 = MFMA16(kx[1][1], Fq[1], S1);
            __builtin_amdgcn_sched_barrier(0);
            Sm = MFMA16(Fk[0], Fq[0], Sm); S0 = MFMA16(kx[0][2], Fq[2], S0);
            Sm = MFMA16(Fk[1], Fq[1], Sm); S1 = MFMA16(kx[1][2], Fq[2], S1);
            Sm = MFMA16(Fk[2], Fq[2], Sm); S0 = MFMA16(kx[0][3], Fq[3], S0);
            Sm = MFMA16(Fk[3], Fq[3], Sm); S1 = MFMA16(kx[1][3], Fq[3], S1);
            __builtin_amdgcn_sched_barrier(0);
            float y0 = hi8 ? Sm[2] : Sm[0], y1 = hi8 ? Sm[3] : Sm[1];
            float x0 = hi8 ? S1[0] : S0[0], x1 = hi8 ? S1[1] : S0[1], x2 = hi8 ? S1[2] : S0[2], x3 = hi8 ? S1[3] : S0[3];
            const float ml_ = vmax3(vmax3(y0, y1, x0), x1, vmax(x2, x3));
            const float nm = -ml_ * c2;
            float m = vmax(ml_, ror8f(ml_));
            x0 = __builtin_amdgcn_exp2f(fmaf(x0, c2, nm)); x1 = __builtin_amdgcn_exp2f(fmaf(x1, c2, nm));
            { auto r = __builtin_amdgcn_permlane16_swap(__float_as_uint(m), __float_as_uint(m), false, false); m = vmax(__uint_as_float(r[0]), __uint_as_float(r[1])); }
            x2 = __builtin_amdgcn_exp2f(fmaf(x2, c2, nm)); x3 = __builtin_amdgcn_exp2f(fmaf(x3, c2, nm));
            { auto r = __builtin_amdgcn_permlane32_swap(__float_as_uint(m), __float_as_uint(m), false, false); m = vmax(__uint_as_float(r[0]), __uint_as_float(r[1])); }
            y0 = __builtin_amdgcn_exp2f(fmaf(y0, c2, nm)); y1 = __builtin_amdgcn_exp2f(fmaf(y1, c2, nm));
            const float f = __builtin_amdgcn_exp2f((ml_ - m) * c2);
            float sum = f * fmaf(0.5f, y0 + y1, (x0 + x1) + (x2 + x3));
            sum += ror8f(sum);
            sum = xsum(sum);
            const float inv = f * __builtin_amdgcn_rcpf(sum);
            const u32 pa = pack2(x0 * inv, x1 * inv) & 0x7fff7fffu, pb = pack2(x2 * inv, x3 * inv) & 0x7fff7fffu, pc = pack2(y0 * inv, y1 * inv) & 0x7fff7fffu;
            const u32 qc = ror8(pc);
            v4u px = {(u32)__builtin_amdgcn_update_dpp((int)pa, (int)pa, 0x128, 0xf, 0xc, false), (u32)__builtin_amdgcn_update_dpp((int)pb, (int)pb, 0x128, 0xf, 0xc, false),
                      (u32)__builtin_amdgcn_update_dpp((int)pa, (int)pa, 0x128, 0xf, 0x3, false), (u32)__builtin_amdgcn_update_dpp((int)pb, (int)pb, 0x128, 0xf, 0x3, false)};
            ST16(px, rw, oxs);
            ST16(px, rw, oxt);
            auto ra = __builtin_amdgcn_permlane16_swap(pc, pc, false, false);
            auto rc = __builtin_amdgcn_permlane16_swap(qc, qc, false, false);
            v4u pmv = {ra[0], rc[0], ra[1], rc[1]};
            ST16(pmv, rw, oms);
            __builtin_amdgcn_sched_barrier(0);
            ST4(nos(rt32()), rw, obs);
            __builtin_amdgcn_s_setprio(0);
        }
        {
            gather_mem(rw, NEARB(NSEG - 1, rep, pm), ml, lane, 0, tkp, NSEG - 1, budget);
            LBAR();
            qk_project(Bq, Bk, ma, xw, idx);
            LBAR();
        }
    } else if (w == 5) {
        int kdone = -1;
        const int kxo = OFF_KX + h * (NSEG * 8192) + g * 512 + i16 * 16;
        for (int s = 0; s < NSEG; ++s) {
            const bool need = s + 1 < NSEG - 1;
            if (need && kdone < s + 1) kdone = wait_flags(rw, FL_K + (h >> 1), 1, 1, -1, lane, budget);
            const bool go = need && kdone >= s + 1;
            int ko = (go ? kxo : OOB) + (s + 1) * 8192;
            int a0 = ox + NEARB(s + 2, i16 >> 3, pm), a1 = ox + NEARB(s + 2, 2 + (i16 >> 3), pm), a2 = om + NEARB(s + 2, g, pm), a3 = ob + FARB(s + 4, lane & 3, pm);
            v4u ff = {0xFFFFFFFFu, 0xFFFFFFFFu, 0xFFFFFFFFu, 0xFFFFFFFFu};
            asm volatile("" : "+v"(ko), "+v"(a0), "+v"(a1), "+v"(a2), "+v"(a3), "+v"(ff));
            LBAR();
            v4u kx[8];
            qk_project_h(Bq, Bk, ma, xw, idx, [&](int t) {
                if (t < 4) { kx[t] = LD16(rw, ko + (t & 1) * 2048, (t >> 1) * 4096); kx[4 + t] = LD16(rw, ko + 256 + (t & 1) * 2048, (t >> 1) * 4096); }
                if (t == 4) { ST16(ff, rw, a0); ST16(ff, rw, a1); }
                if (t == 5) { ST16(ff, rw, a2); ST4(0xFFFFFFFFu, rw, a3); }
            });
            LBAR();
            if (need) {
                if (!go) {
                    kdone = wait_flags(rw, FL_K + (h >> 1), 1, 1, s + 1, lane, budget, true);
#pragma unroll
                    for (int k = 0; k < 8; ++k) kx[k] = LD16(rw, kxo + (k >> 2) * 256 + (k & 3) * 2048, (s + 1) * 8192);
                }
#pragma unroll
                for (int k = 0; k < 8; ++k) *(LAS v4u*)(kl + ((s + 1) & 1) * 8192 + k * 1024) = kx[k];
                asm volatile("s_waitcnt lgkmcnt(0)" ::: "memory");
                tk_post(kfp, s + 1, 0u, lane);
            }
            DRAIN();
        }
    } else {
        TK tk; tk_init(tk);
        const int kmo = i16 < 8 ? OFF_KM + h * 2048 + g * 128 + i16 * 16 : OOB;
        for (int s = 0; s < NSEG; ++s) {
            if (s > 0 && w != 6) gather_mem(rw, NEARB(s, rep, pm), ml, lane, w, tkp, s, budget);
            BC2 bc = {0u, 0u};
            if (w == 3) bc = beacon_issue(rw, FARB(s, rep, pm), lane);
            LBAR();
            qk_project(Bq, Bk, ma, xw, idx);
            LBAR();
            if (w == 6) {
                bf16x8 F[4];
#pragma unroll
                for (int ks = 0; ks < 4; ++ks) F[ks] = as8(trd(pq + ks * 1024 + 16), trd(pq + ks * 1024 + 16 + 128));
#pragma unroll
                for (int ks = 0; ks < 4; ++ks) ST16(asu(F[ks]), rw, kmo + s * (NH * 2048) + ks * 512);
                DRAIN();
                if (lane == 0) ST4((u32)s, rw, (FL_M + h) * 128);
            } else if (w == 3) {
                if (s < NSEG - 1) {
                    if (s > 0) { tk_T0(tk, beacon_finish(bc, rw, FARB(s, rep, pm), lane, budget), a); if (tk.okA) tk_post(tkp, s + 1, tk_specA(tk, a), lane); }
                    wait_until(tk.XR + 8);
                    tk_Tp(tk, beacon_max<NH>(rw, FARB(s, rep, pm), BC_QK, lane, budget), a);
                    tk_post(tkp, s + 1, tk.XA, lane);
                }
            } else __builtin_amdgcn_s_sleep(14);
        }
    }
    if (budget < 0 && lane == 0) ST4(1u, rw, FL_ERR * 128);
}

__device__ __forceinline__ void role_wvo(const Args& a, lptr lds, rsrc_t rw, int idx) {
    const int tid = threadIdx.x, lane = tid & 63, w = __builtin_amdgcn_readfirstlane(tid >> 6);
    const int h = idx / 3, part = idx - 3 * h;
    bf16x8 Bo[5][4];
    v4u v[6], o[2][9];
#pragma unroll
    for (int c = 0; c < 2; ++c) rows_ld<64, 72>(o[c], a.Wo + ((size_t)h * DH + c * 64) * DM, DM, tid);
    rows_ld<192, 16>(v, a.Wv + (size_t)part * 192 * 4096 + h * DH, 4096, tid);
    __builtin_amdgcn_sched_barrier(0);
#pragma unroll
    for (int c = 0; c < 2; ++c) {
        lptr l = lds + c * 77824;
        rows_st<64, 72>(o[c], l, 1184, tid);
        LBAR();
#pragma unroll
        for (int j = 0; j < 5; ++j) {
            const int r = (w + 8 * j < NR) ? w + 8 * j : w;
#pragma unroll
            for (int ks = 0; ks < 2; ++ks) Bo[j][c * 2 + ks] = frag16(l + ks * 32 * 1184 + r * 32, 1184, lane);
        }
        __builtin_amdgcn_sched_barrier(0);
    }
    rows_st<192, 16>(v, lds, VS, tid);
    LBAR();
    const int rho = lane & 15, bg = lane >> 4;
    const int wo0 = OFF_WVO + (2 * w + ((rho >> 1) & 1)) * 294912 + ((h >> 1) * 18 + part * 6) * 1024 + ((h & 1) * 32 + bg * 8 + 2 * (rho >> 2) + (rho & 1)) * 16;
#pragma unroll 1
    for (int kb = 0; kb < 6; ++kb) {
        bf16x8 Af[2][4];
#pragma unroll
        for (int mt = 0; mt < 2; ++mt)
#pragma unroll
            for (int ks = 0; ks < 4; ++ks) Af[mt][ks] = lds8(lds + (32 * kb + 8 * (rho >> 2) + 4 * mt + (rho & 3)) * VS + ks * 64 + bg * 16);
#pragma unroll
        for (int j = 0; j < 5; ++j) {
            if (j < 4 || w < 4) {
                f32x4 d0 = {}, d1 = {};
#pragma unroll
                for (int ks = 0; ks < 4; ++ks) { d0 = MFMA16(Af[0][ks], Bo[j][ks], d0); d1 = MFMA16(Af[1][ks], Bo[j][ks], d1); }
                v4u q = {pack2(d0[0], d0[1]), pack2(d0[2], d0[3]), pack2(d1[0], d1[1]), pack2(d1[2], d1[3])};
                ST16(q, rw, wo0 + j * (16 * 294912) + kb * 1024);
            }
        }
        if (kb == 2 || kb == 4) {
            asm volatile("s_waitcnt vmcnt(4)" ::: "memory");
            LBAR();
            if (tid == 0) ST4((u32)(kb >> 1), rw, (FL_W + idx) * 128);
        }
    }
    DRAIN();
    LBAR();
    if (tid == 0) ST4(3u, rw, (FL_W + idx) * 128);
}

__device__ __forceinline__ void role_rz(const Args& a, lptr lds, rsrc_t rw, int r, int rep) {
    const int tid = threadIdx.x, lane = tid & 63, w = __builtin_amdgcn_readfirstlane(tid >> 6);
    const int idx = (lane & 8) ? 0xEEEE : 0x4444;
    const int i16 = lane & 15, g = lane >> 4;
    int budget = SPIN_MAX;
    bf16x16 Ba[9], Bb[9];
    v4u t0[4], m0[2];
    int pm;
    {
        const int wo = OFF_WVO + r * 294912 + 2 * w * 18432 + ((i16 >> 3) * 32 + g * 8 + (i16 & 7)) * 16;
        const int vq = OFF_VOX + r * 16384 + w * 2048 + lane * 16;
#pragma unroll
        for (int j = 0; j < 2; ++j) { const int i = min(tid + 512 * j, 575), row = i / 72, c = i - row * 72; m0[j] = *(const v4u*)(a.mem0 + row * DM + c * 8); }
        const int vf = (int)LD4(rw, (FL_V + lane) * 128, 0);
        const u32 pw = LD4(rw, (FL_PAR + (lane & 7)) * 128, 0);
        int have = 0;
        bool vgot = false;
#pragma unroll
        for (int p = 0; p < 3; ++p) {
            if (have < p + 1) have = wait_flags(rw, FL_W + 12 * w, 1, 12, p + 1, lane, budget, true);
            if (p == 0 || p == 2) {
                const bool now = p == 0 ? (bool)__all(vf >= 0) : !vgot;
                if (now) {
                    if (p == 2) wait_flags(rw, FL_V, 1, 64, 0, lane, budget, true);
#pragma unroll
                    for (int k = 0; k < 4; ++k) t0[k] = LD16(rw, vq + (k & 1) * 1024, (k >> 1) * (NR * NH * 1024));
                    vgot = true;
                }
            }
            if (p == 0) pm = __all(pw != 0xFFFFFFFFu) ? (__builtin_popcountll(__ballot(pw != 0u)) > 32 ? 0xFFFF : 0) : get_parity(rw, lane, budget);
#pragma unroll
            for (int q = 0; q < 3; ++q) {
                const int t = 3 * q + p;
                Ba[t] = cat16(as8(LD16(rw, wo, t * 2048)), as8(LD16(rw, wo + 1024, t * 2048)));
                Bb[t] = cat16(as8(LD16(rw, wo, 18432 + t * 2048)), as8(LD16(rw, wo + 1024, 18432 + t * 2048)));
            }
            MB();
        }
#pragma unroll
        for (int j = 0; j < 2; ++j) {
            const int i = min(tid + 512 * j, 575), row = i / 72, c = i - row * 72;
            lptr d = lds + (c >> 1) * 256 + row * 16 + (c & 1) * 8;
            *(LAS v2u*)d = v2u{m0[j].x, m0[j].z}; *(LAS v2u*)(d + 128) = v2u{m0[j].y, m0[j].w};
        }
    }
    lptr ml = lds, zl = lds + MEMB;
    lptr tka = lds + 13312, tkr = tka + 16, cntp = tka + 64;
    volatile LAS int* prod = (volatile LAS int*)(lds + 13312 + 512);
    lptr ring = lds + 16384;
    lptr ma = ml + lane * 16;
    lptr zw = zl + ((2 * w + (lane >> 5)) >> 2) * 1024 + ((((2 * w + (lane >> 5)) & 3) * 16 + (g & 1) * 8 + (i16 & 7)) * 16) + (i16 >> 3) * 8;
    const bool hi8r = (lane & 8) != 0;
    const int mso = i16 < 8 ? (r < 64 ? N_MEMA + r * 128 : N_MEMB + (r - 64) * 128) + i16 * 16 : OOB;
    const int ob = lane < NREP ? BCO(r) : OOB;
    if (tid == 0) { tk_post(tka, 0, rt32() - 100000u, 0); tk_post(tkr, -1, 0u, 0); *(volatile LAS int*)cntp = 0; }
#pragma unroll
    for (int k = 0; k < 4; ++k) *(LAS v4u*)(ring + (k >> 1) * 16384 + w * 2048 + (k & 1) * 1024 + lane * 16) = t0[k];
    if (tid < 4) prod[tid] = 2;
    LBAR();
    if (w < 4) {
        const int po = N_PX + (w >> 1) * 16384 + (w & 1) * 4096 + lane * 16;
        const int mo = N_PMM + w * 1024 + 2 * g * 128 + (i16 & 7) * 16 + (i16 >> 3) * 8;
        lptr cna = lane == 0 ? cntp : cntp + 4 + lane * 4;
        const u32 one = lane == 0 ? 1u : 0u;
        const bool dv = (g >> 1) == (i16 >> 3);
        const int rwo = dv ? (4 * (g & 1) + 2 * (g >> 1)) * 128 + ((i16 & 7) >> 1) * 32 + w * 8 + (i16 & 1) * 4 : 2048 + lane * 4;
        const int msq = lane < 32 ? (r < 64 ? N_MEMA + r * 128 : N_MEMB + (r - 64) * 128) + (lane >> 2) * 16 : OOB;
        lptr zrd = zl + w * 1024 + lane * 16;
        for (int s = 0; s < NSEG; ++s) {
            lptr Rw = lds + 81920 + (s & 1) * 1024 + rwo, Rr = lds + 81920 + (s & 1) * 1024 + (lane & 31) * 32;
            int mss = msq + NEARB(s + 1, lane & 3, pm), obs = ob + FARB(s + 1, lane & 3, pm);
            int sb = NEARB(s, rep, pm);
            asm volatile("" : "+s"(sb));
            lptr rg = ring + (s & 3) * 16384 + w * 4096 + lane * 16;
            bf16x8 b[5];
            const bool early = __builtin_amdgcn_readfirstlane(prod[w >> 1]) > s;
            MB();
#pragma unroll
            for (int k = 0; k < 4; ++k) b[k] = lds8(rg + k * 1024);
            const u32 X = s > 0 ? tk_get(tkr, min(s, NSEG - 2), budget) : 0u;
            asm volatile("s_waitcnt lgkmcnt(0)" : "+v"(mss), "+v"(obs), "+v"(Rw), "+v"(Rr) :: "memory");
#pragma unroll
            for (int k = 0; k < 4; ++k) PIN4(b[k]);
            LBAR();
            qk_project(Ba, Bb, ma, zw, idx);
            LBAR();
            if (s == NSEG - 1) break;
            __builtin_amdgcn_s_setprio(3);
            b[4] = lds8(zrd);
            if (!early) {
                while (__builtin_amdgcn_readfirstlane(prod[w >> 1]) <= s && --budget >= 0) __builtin_amdgcn_s_sleep(1);
                MB();
#pragma unroll
                for (int k = 0; k < 4; ++k) b[k] = lds8(rg + k * 1024);
            }
            if (s > 0 && (int)(X - rt32()) < 1000) spin_to(X);
            f32x4 d;
            u32 bad;
            auto shot = [&]() {
                v4u p[4]; v2u m[2];
                m[0] = LD8(rw, mo, sb); m[1] = LD8(rw, mo + 128, sb);
                __builtin_amdgcn_sched_barrier(0);
#pragma unroll
                for (int k = 0; k < 4; ++k) { p[k] = LD16(rw, po + k * 1024, sb); __builtin_amdgcn_sched_barrier(0); }
                MB();
                d = f32x4{};
                d = MFMA16(as8(m[0], m[1]), b[4], d);
                bad = (m[0].x | m[0].y) | (m[1].x | m[1].y);
                __builtin_amdgcn_sched_barrier(0);
#pragma unroll
                for (int k = 0; k < 4; ++k) {
                    d = MFMA16(as8(p[k]), b[k], d);
                    bad |= (p[k].x | p[k].y) | (p[k].z | p[k].w);
                    __builtin_amdgcn_sched_barrier(0);
                }
            };
            shot();
            if (__builtin_expect(!__all((int)bad >= 0), 0)) {
                do shot(); while (!__all((int)bad >= 0) && --budget >= 0);
            }
            float e0, e1;
            {
                auto r0 = __builtin_amdgcn_permlane32_swap(__float_as_uint(d[0]), __float_as_uint(d[2]), false, false);
                auto r1 = __builtin_amdgcn_permlane32_swap(__float_as_uint(d[1]), __float_as_uint(d[3]), false, false);
                const float t0 = __uint_as_float(hi8r ? r0[1] : r0[0]), t1 = __uint_as_float(hi8r ? r1[1] : r1[0]);
                e0 = t0 + ror8f(t0); e1 = t1 + ror8f(t1);
            }
            f32x4 x0, x1; u32 old;
            asm volatile("ds_write2_b32 %4, %5, %6 offset1:32\n\tds_add_rtn_u32 %0, %7, %8\n\tds_read_b128 %1, %3\n\tds_read_b128 %2, %3 offset:16\n\ts_waitcnt lgkmcnt(0)"
                         : "=&v"(old), "=&v"(x0), "=&v"(x1) : "v"(Rr), "v"(Rw), "v"(e0), "v"(e1), "v"(cna), "v"(one) : "memory");
            {
                const u32 c = nos(pack2((x0[0] + x0[2]) + (x1[0] + x1[2]), (x0[1] + x0[3]) + (x1[1] + x1[3])));
                v4u o = {(u32)__builtin_amdgcn_update_dpp(0, (int)c, 0x00, 0xf, 0xf, true), (u32)__builtin_amdgcn_update_dpp(0, (int)c, 0x55, 0xf, 0xf, true),
                         (u32)__builtin_amdgcn_update_dpp(0, (int)c, 0xAA, 0xf, 0xf, true), (u32)__builtin_amdgcn_update_dpp(0, (int)c, 0xFF, 0xf, 0xf, true)};
                const bool last = (__builtin_amdgcn_readfirstlane(old) & 3) == 3;
                ST16(o, rw, last ? mss : OOB);
                __builtin_amdgcn_sched_barrier(0);
                if (last) ST4(nos(rt32()), rw, obs);
            }
            __builtin_amdgcn_s_setprio(0);
        }
    } else if (w == 5 || w == 6) {
        int vdone0 = -1, vdone1 = -1, staged = 2;
        const int hw = w - 5;
        const int vo = OFF_VOX + r * 16384 + hw * 8192 + lane * 16;
        const int zho = OFF_ZH + r * 4096 + hw * 2048 + lane * 16;
        lptr zhr = zl + hw * 2048 + (((lane >> 4) & 3) * 16 + (lane & 7)) * 16 + ((lane >> 3) & 1) * 8;
        const int aro = w == 5 ? mso : OOB, arb = w == 5 ? ob : OOB;
        v4u zv[2] = {};
        for (int s = 0; s < NSEG; ++s) {
            bool go = false;
            if (staged <= min(s + 3, NSEG - 2)) {
                const int ow = VOWN(staged);
                int& vd = ow ? vdone1 : vdone0;
                if (vd < staged) vd = wait_flags(rw, FL_V + ow, 2, NH, -1, lane, budget);
                go = vd >= staged;
            }
            const int vos = go ? vo : OOB;
            int va = vos + staged * (NR * NH * 1024), zs = s > 0 ? zho + (s - 1) * (NR * NH * 256) : OOB;
            int a0 = aro + NEARB(s + 3, g, pm), a1 = arb + FARB(s + 4, lane & 3, pm);
            v4u ff = {0xFFFFFFFFu, 0xFFFFFFFFu, 0xFFFFFFFFu, 0xFFFFFFFFu};
            asm volatile("" : "+v"(va), "+v"(zs), "+v"(a0), "+v"(a1), "+v"(ff));
            if (s > 0) gather_mem4(rw, NEARB(s, rep, pm), ml, lane, w - 4, tka, s, budget);
            LBAR();
            v4u v[8];
            qk_project_h(Ba, Bb, ma, zw, idx, [&](int t) {
                if (t < 4) { v[2 * t] = LD16(rw, va + (t & 1) * 2048, (t >> 1) * 4096); v[2 * t + 1] = LD16(rw, va + 1024 + (t & 1) * 2048, (t >> 1) * 4096); }
                if (t == 4) { ST16(zv[0], rw, zs); ST16(zv[1], rw, zs + 1024); }
                if (t == 5) { ST16(ff, rw, a0); ST4(0xFFFFFFFFu, rw, a1); }
            });
            LBAR();
#pragma unroll
            for (int k = 0; k < 2; ++k) {
                const v2u lo = *(LAS v2u*)(zhr + k * 1024), hi = *(LAS v2u*)(zhr + k * 1024 + 128);
                zv[k] = v4u{lo.x, lo.y, hi.x, hi.y};
            }
            for (;;) {
                if (go) {
#pragma unroll
                    for (int k = 0; k < 8; ++k) *(LAS v4u*)(ring + (staged & 3) * 16384 + hw * 8192 + k * 1024 + lane * 16) = v[k];
                    asm volatile("s_waitcnt lgkmcnt(0)" ::: "memory");
                    ++staged;
                    prod[hw] = staged;
                }
                if (staged > s + 1 || staged > NSEG - 2) break;
                const int ow = VOWN(staged);
                int& vd = ow ? vdone1 : vdone0;
                if (vd < staged) vd = wait_flags(rw, FL_V + ow, 2, NH, staged <= s ? staged : -1, lane, budget, true);
                if (vd < staged) break;
#pragma unroll
                for (int k = 0; k < 8; ++k) v[k] = LD16(rw, vo + (k & 3) * 1024, staged * (NR * NH * 1024) + (k >> 2) * 4096);
                go = true;
            }
            DRAIN();
        }
        {
            int l2 = (int)__builtin_amdgcn_mbcnt_hi(~0u, __builtin_amdgcn_mbcnt_lo(~0u, 0u));
            asm volatile("" : "+v"(l2));
            const int zf = OFF_ZH + r * 4096 + hw * 2048 + (NSEG - 1) * (NR * NH * 256) + l2 * 16;
            ST16(zv[0], rw, zf); ST16(zv[1], rw, zf + 1024);
        }
    } else {
        TK tk; tk_init(tk);
        for (int s = 0; s < NSEG; ++s) {
            if (s > 0) {
                gather_mem4(rw, NEARB(s, rep, pm), ml, lane, w - 4, tka, s, budget);
            }
            BC2 bc = {0u, 0u};
            if (w == 7) bc = beacon_issue(rw, FARB(s, rep, pm), lane);
            LBAR();
            qk_project(Ba, Bb, ma, zw, idx);
            LBAR();
            if (w == 7) {
                if (s < NSEG - 1) {
                    if (s > 0) { tk_T0(tk, beacon_finish(bc, rw, FARB(s, rep, pm), lane, budget), a); if (tk.okA) tk_post(tka, s + 1, tk_specA(tk, a), lane); }
                    tk_post(tkr, s, tk.XR, lane);
                    wait_until(tk.XR + 8);
                    tk_Tp(tk, beacon_max<NH>(rw, FARB(s, rep, pm), BC_QK, lane, budget), a);
                    tk_post(tka, s + 1, tk.XA, lane);
                    tk_post(tkr, s + 1, tk_specR(tk, a), lane);
                }
            } else {
                if (w == 4 && s >= 2 && lane == 0) ST4((u32)(s - 2), rw, (FL_Z + r) * 128);
                __builtin_amdgcn_s_sleep(14);
            }
        }
    }
    DRAIN();
    LBAR();
    if (tid == 0) ST4((u32)(NSEG - 1), rw, (FL_Z + r) * 128);
    if (budget < 0 && lane == 0) ST4(3u, rw, FL_ERR * 128);
}

__device__ __forceinline__ void cexp_l(float lr, float li, float l, float& er, float& ei) {
    const float mag = __builtin_amdgcn_exp2f(lr * l);
    const float ph = li * l;
    const float pe = fmaf(li, l, -ph);
    const float fr = (ph - floorf(ph)) + pe;
    er = mag * __builtin_amdgcn_cosf(fr);
    ei = mag * __builtin_amdgcn_sinf(fr);
}
constexpr int CV_W = 8 * 260 * 16, CV_U = 96 * 80, CV_CH = CV_W + CV_U;
__device__ __forceinline__ void s4_unit(const Args& a, lptr lds, rsrc_t rw, int unit) {
    const int tid = threadIdx.x, lane = tid & 63, w = __builtin_amdgcn_readfirstlane(tid >> 6);
    const int d0 = unit * 4;
    v2u uv[4];
#pragma unroll
    for (int k = 0; k < 4; ++k) uv[k] = *(const v2u*)(a.u + (size_t)(2 * (tid + 512 * (k >> 1)) + (k & 1)) * DM + d0);
    {
        const int c = tid >> 7, nn = tid & 127, n = (d0 + c) * 128 + nn;
        const float dt = rbf(expf(bf2f(a.logdt[d0 + c])));
        const float lr = dt * bf2f(a.Are[n]) * 1.4426950408889634f, li = dt * bf2f(a.Aim[n]) * 0.15915494309189535f;
        const float br = dt * bf2f(a.Bre[n]), bi = dt * bf2f(a.Bim[n]);
        const float cr = bf2f(a.Cre[n]), ci = bf2f(a.Cim[n]);
        LAS f32x2* T = (LAS f32x2*)(lds + c * CV_CH) + nn;
        float er, ei, pr, pi;
        cexp_l(lr, li, 1.0f, er, ei); pr = er; pi = ei;
#pragma unroll
        for (int o = 0; o < 7; ++o) { T[o * 130] = f32x2{pr, pi}; const float t = pr * er - pi * ei; pi = pr * ei + pi * er; pr = t; }
#pragma unroll
        for (int q4 = 0; q4 < 4; ++q4) {
            if (q4 == 0) cexp_l(lr, li, 128.0f, er, ei);
            cexp_l(lr, li, (float)(128 + 512 * q4), pr, pi);
#pragma unroll
            for (int o = 0; o < 4; ++o) { T[(7 + 4 * q4 + o) * 130] = f32x2{pr, pi}; const float t = pr * er - pi * ei; pi = pr * ei + pi * er; pr = t; }
        }
        const float cbr = cr * br - ci * bi, cbi = cr * bi + ci * br;
        cexp_l(lr, li, 8.0f, er, ei);
#pragma unroll
        for (int q4 = 0; q4 < 4; ++q4) {
            float sr, si;
            cexp_l(lr, li, (float)(32 * q4), sr, si);
            pr = cbr * sr - cbi * si; pi = cbr * si + cbi * sr;
#pragma unroll
            for (int o = 0; o < 4; ++o) { T[(23 + 4 * q4 + o) * 130] = f32x2{pr, pi}; const float t = pr * er - pi * ei; pi = pr * ei + pi * er; pr = t; }
        }
    }
    __syncthreads();
    f32x4 kr[3];
    {
        const int x = lane & 15, g = lane >> 4, c = w & 3, nh = w >> 2;
        const int ah0 = x >> 3, o0 = x & 7;
        const int ah1 = x < 8 ? 2 + (x >> 2) : 4 + ((x - 8) >> 1), o1 = x < 8 ? 2 * (x & 3) : 4 * (x & 1);
        const int ah2 = x < 8 ? 8 + x : 16;
        LAS f32x2* T = (LAS f32x2*)(lds + c * CV_CH) + nh * 64 + g;
        LAS f32x2* Ta0 = T + 7 * 130; LAS f32x2* To0 = T + (o0 ? o0 - 1 : 0) * 130;
        LAS f32x2* Ta1 = T + (6 + ah1) * 130; LAS f32x2* To1 = T + (o1 ? o1 - 1 : 0) * 130;
        LAS f32x2* Ta2 = T + (6 + ah2) * 130; LAS f32x2* Tv = T + (23 + x) * 130;
        const f32x2 one = {1.0f, 0.0f};
        f32x4 ar[3] = {}, ai[3] = {};
#pragma unroll
        for (int jj = 0; jj < 16; ++jj) {
            const f32x2 v = Tv[4 * jj], u2 = Ta2[4 * jj], a1 = Ta1[4 * jj];
            f32x2 a0v = Ta0[4 * jj], e0 = To0[4 * jj], e1 = To1[4 * jj];
            a0v = ah0 ? a0v : one; e0 = o0 ? e0 : one; e1 = o1 ? e1 : one;
            const float u0r = a0v[0] * e0[0] - a0v[1] * e0[1], u0i = a0v[0] * e0[1] + a0v[1] * e0[0];
            const float u1r = a1[0] * e1[0] - a1[1] * e1[1], u1i = a1[0] * e1[1] + a1[1] * e1[0];
            ar[2] = __builtin_amdgcn_mfma_f32_16x16x4f32(u2[0], v[0], ar[2], 0, 0, 0);
            ai[2] = __builtin_amdgcn_mfma_f32_16x16x4f32(u2[1], v[1], ai[2], 0, 0, 0);
            ar[0] = __builtin_amdgcn_mfma_f32_16x16x4f32(u0r, v[0], ar[0], 0, 0, 0);
            ai[0] = __builtin_amdgcn_mfma_f32_16x16x4f32(u0i, v[1], ai[0], 0, 0, 0);
            ar[1] = __builtin_amdgcn_mfma_f32_16x16x4f32(u1r, v[0], ar[1], 0, 0, 0);
            ai[1] = __builtin_amdgcn_mfma_f32_16x16x4f32(u1i, v[1], ai[1], 0, 0, 0);
        }
#pragma unroll
        for (int t = 0; t < 3; ++t) kr[t] = ar[t] - ai[t];
    }
    __syncthreads();
    {
        LAS float* T = (LAS float*)(lds + (w & 3) * CV_CH + 4096 + (w >> 2) * 3072);
#pragma unroll
        for (int t = 0; t < 3; ++t)
#pragma unroll
            for (int r = 0; r < 4; ++r) T[(16 * t + 4 * (lane >> 4) + r) * 16 + (lane & 15)] = kr[t][r];
    }
    __syncthreads();
#pragma unroll
    for (int k = 0; k < 8; ++k) {
        const int i = tid + 512 * k, c = i >> 10, l2 = i & 1023;
        LAS float* T = (LAS float*)(lds + c * CV_CH + 4096);
        float v[2];
#pragma unroll
        for (int e = 0; e < 2; ++e) {
            const int lb = (int)rbf((float)(2 * l2 + e));
            const int hi = lb >> 7, o = lb & 7;
            const int row = lb < 256 ? 8 * hi + o : lb < 512 ? 16 + 4 * (hi - 2) + (o >> 1) : lb < 1024 ? 24 + 2 * (hi - 4) + (o >> 2) : 32 + (hi - 8);
            const int ix = row * 16 + ((lb >> 3) & 15);
            v[e] = T[ix] + T[768 + ix];
        }
        *(LAS u32*)(lds + c * CV_CH + CV_W + 2704 + l2 * 4) = pack2(v[0], v[1]);
    }
    __syncthreads();
    for (int i = tid; i < 4 * 259; i += 512) {
        const int c = i / 259, zz = i - c * 259, z = zz - 3;
        lptr ch = lds + c * CV_CH;
        v4u lo = {0, 0, 0, 0}, hi = {0, 0, 0, 0};
        if (z >= 1) lo = *(LAS v4u*)(ch + CV_W + 2704 + (z - 1) * 16);
        if (z >= 0) hi = *(LAS v4u*)(ch + CV_W + 2704 + z * 16);
        const u32 e[8] = {lo.x, lo.y, lo.z, lo.w, hi.x, hi.y, hi.z, hi.w};
#pragma unroll
        for (int cc = 0; cc < 8; ++cc) {
            v4u o;
#pragma unroll
            for (int k = 0; k < 4; ++k) {
                const int t = 8 + cc - 2 * k;
                if (t & 1) o[k] = (e[t >> 1] >> 16) | (e[t >> 1] << 16);
                else o[k] = (e[t >> 1] & 0xffffu) | (e[(t >> 1) - 1] & 0xffff0000u);
            }
            *(LAS v4u*)(ch + (cc * 260 + zz) * 16) = o;
        }
    }
    __syncthreads();
    for (int i = tid; i < 4 * 160; i += 512) *(LAS v4u*)(lds + (i / 160) * CV_CH + CV_W + (i % 160) * 16) = v4u{0, 0, 0, 0};
#pragma unroll
    for (int k2 = 0; k2 < 2; ++k2) {
        const int l = 2 * (tid + 512 * k2);
        const int o = CV_W + (32 + (l >> 5)) * 80 + (l & 31) * 2;
        const v2u p = uv[2 * k2], q = uv[2 * k2 + 1];
        *(LAS u32*)(lds + 0 * CV_CH + o) = (p.x & 0xffffu) | (q.x << 16);
        *(LAS u32*)(lds + 1 * CV_CH + o) = (p.x >> 16) | (q.x & 0xffff0000u);
        *(LAS u32*)(lds + 2 * CV_CH + o) = (p.y & 0xffffu) | (q.y << 16);
        *(LAS u32*)(lds + 3 * CV_CH + o) = (p.y >> 16) | (q.y & 0xffff0000u);
    }
    __syncthreads();
    const int c = w & 3, par = w >> 2, b = lane & 31, hh = lane >> 5;
    lptr ch = lds + c * CV_CH;
    lptr wa = ch + ((b & 7) * 260 + (b >> 3) - hh + 3) * 16;
    lptr ub = ch + CV_W + (32 + b) * 80 + hh * 16;
    f32x16 y0 = {}, y1 = {};
    wa += par * 64; ub -= par * 80;
    {
        bf16x8 fa[2][2], fb[2][2], fc[2][2];
#pragma unroll
        for (int ks = 0; ks < 2; ++ks) { fa[0][ks] = lds8(wa - ks * 32); fb[0][ks] = lds8(ub + ks * 32); fc[0][ks] = lds8(ub + 2560 + ks * 32); }
#pragma unroll
        for (int i = 0; i < 32; ++i) {
            const int cur = i & 1, nx = cur ^ 1;
            if (i + 1 < 32) {
#pragma unroll
                for (int ks = 0; ks < 2; ++ks) {
                    fa[nx][ks] = lds8(wa + (i + 1) * 128 - ks * 32);
                    if (i + 1 < 16) fb[nx][ks] = lds8(ub - (i + 1) * 160 + ks * 32);
                    fc[nx][ks] = lds8(ub + 2560 - (i + 1) * 160 + ks * 32);
                }
            }
            __builtin_amdgcn_sched_barrier(0);
#pragma unroll
            for (int ks = 0; ks < 2; ++ks) {
                if (i < 16) y0 = MFMA32(fa[cur][ks], fb[cur][ks], y0);
                y1 = MFMA32(fa[cur][ks], fc[cur][ks], y1);
            }
            __builtin_amdgcn_sched_barrier(0);
        }
    }
    __syncthreads();
    {
        LAS f32x4* X = (LAS f32x4*)(ch + par * 4096);
        const f32x16 src = par ? y0 : y1;
#pragma unroll
        for (int k = 0; k < 4; ++k) X[k * 64 + lane] = f32x4{src[4 * k], src[4 * k + 1], src[4 * k + 2], src[4 * k + 3]};
    }
    __syncthreads();
    {
        LAS f32x4* X = (LAS f32x4*)(ch + (1 - par) * 4096);
        lptr Y = ch + 8192 + (32 * par + b) * 72 + hh * 8;
        const f32x16 own = par ? y1 : y0;
#pragma unroll
        for (int k = 0; k < 4; ++k) {
            const f32x4 o = X[k * 64 + lane];
            v2u yv = {pack2(own[4 * k] + o[0], own[4 * k + 1] + o[1]), pack2(own[4 * k + 2] + o[2], own[4 * k + 3] + o[3])};
            *(LAS v2u*)(Y + k * 16) = yv;
        }
    }
    __syncthreads();
    const v2u dk = *(const v2u*)(a.dskip + d0);
#pragma unroll
    for (int k2 = 0; k2 < 2; ++k2) {
        const int i = tid + 512 * k2;
        lptr yp = lds + 8192 + (i >> 4) * 72 + (i & 15) * 4;
        const u32 ya = *(LAS u32*)(yp), yb = *(LAS u32*)(yp + CV_CH), yc = *(LAS u32*)(yp + 2 * CV_CH), yd = *(LAS u32*)(yp + 3 * CV_CH);
        const v2u p = uv[2 * k2], q = uv[2 * k2 + 1];
        v4u o;
        o.x = pack2(bflo(ya) + rbf(bflo(p.x) * bflo(dk.x)), bflo(yb) + rbf(bfhi(p.x) * bfhi(dk.x)));
        o.y = pack2(bflo(yc) + rbf(bflo(p.y) * bflo(dk.y)), bflo(yd) + rbf(bfhi(p.y) * bfhi(dk.y)));
        o.z = pack2(bfhi(ya) + rbf(bflo(q.x) * bflo(dk.x)), bfhi(yb) + rbf(bfhi(q.x) * bfhi(dk.x)));
        o.w = pack2(bfhi(yc) + rbf(bflo(q.y) * bflo(dk.y)), bfhi(yd) + rbf(bfhi(q.y) * bfhi(dk.y)));
        ST16(o, rw, OFF_A4 + unit * 16384 + i * 16);
    }
    __syncthreads();
}
__device__ __forceinline__ void role_s4(const Args& a, lptr lds, rsrc_t rw, int idx) {
    if (idx < 32) {
        const int tid = threadIdx.x, t = idx * 64 + (tid >> 3), j = tid & 7;
        float acc = 0.f;
#pragma unroll
        for (int k = 0; k < 9; ++k) {
            const v4u xv = *(const v4u*)(a.u + (size_t)t * DM + (k * 8 + j) * 8), gv = *(const v4u*)(a.gatew + (k * 8 + j) * 8);
#pragma unroll
            for (int e = 0; e < 4; ++e) acc += rbf(bflo(xv[e]) * bflo(gv[e])) + rbf(bfhi(xv[e]) * bfhi(gv[e]));
        }
        acc += __shfl_xor(acc, 1); acc += __shfl_xor(acc, 2); acc += __shfl_xor(acc, 4);
        acc = rbf(acc);
        if (j == 0) ST4(__float_as_uint(rbf(1.0f / (1.0f + __expf(-acc)))), rw, OFF_G + t * 4);
    }
#pragma unroll 1
    for (int unit = idx; unit < DM / 4; unit += NS4) s4_unit(a, lds, rw, unit);
    DRAIN();
    __syncthreads();
    if (threadIdx.x == 0) ST4(1u, rw, (FL_S + idx) * 128);
}

__device__ __forceinline__ void role_post(const Args& a, lptr lds, rsrc_t rw) {
    const int tid = threadIdx.x, lane = tid & 63, w = __builtin_amdgcn_readfirstlane(tid >> 6);
    const int i16 = lane & 15, g = lane >> 4;
    int budget = SPIN_MAX, vdone0 = -1, vdone1 = -1;
    volatile LAS int* U = (volatile LAS int*)(lds + 81920);
    wait_flags(rw, FL_S, 1, 64, 1, lane, budget);
    wait_flags(rw, FL_S + 64, 1, NS4 - 64, 1, lane, budget);
    constexpr int NFULL = 62 * 6, NUNIT = NFULL + 24;
#pragma unroll 1
    for (;;) {
        if (tid == 0) *U = 1 + (int)__hip_atomic_fetch_add((u32*)(a.ws + FL_Q * 128), 1u, __ATOMIC_RELAXED, __HIP_MEMORY_SCOPE_AGENT);
        __syncthreads();
        const int unit = __builtin_amdgcn_readfirstlane(*U);
        if (unit >= NUNIT || budget < 0) break;
        const bool half = unit >= NFULL;
        const int uu = half ? NFULL + ((unit - NFULL) >> 1) : unit, q0 = half ? (unit - NFULL) & 1 : 0;
        const int s = uu / 6, r = (uu - s * 6) * 6 + (w < 6 ? w : 0);
        bf16x8 b[32];
        v2u av[2]; float gt[2];
        if (w < 6) {
            if (VOWN(s)) { if (vdone1 < s) vdone1 = wait_flags(rw, FL_V + 1, 2, NH, s, lane, budget); }
            else { if (vdone0 < s) vdone0 = wait_flags(rw, FL_V, 2, NH, s, lane, budget); }
            const int vo = OFF_VOX + (s * NR + r) * (NH * 1024) + ((i16 >> 1) & 1) * 16384 + ((g & 1) * 16 + (g >> 1) * 8 + 2 * (i16 >> 2) + (i16 & 1)) * 16;
#pragma unroll
            for (int k = 0; k < 32; ++k) b[k] = as8(LD16(rw, vo + (k & 7) * 512, (k >> 3) * 4096));
#pragma unroll
            for (int qt = 0; qt < 2; ++qt) {
                av[qt] = LD8(rw, OFF_A4 + (4 * r + g) * 16384 + (32 * s + 16 * (qt ^ q0) + i16) * 8, 0);
                gt[qt] = __uint_as_float(LD4(rw, OFF_G + (32 * s + 16 * (qt ^ q0) + i16) * 4, 0));
            }
        }
        v4u z[8];
        const int zo = OFF_ZH + (s * NR + r) * (NH * 256) + ((i16 >> 1) & 1) * 4096 + g * 128 + (2 * (i16 >> 2) + (i16 & 1)) * 16;
        if (half) {
            wait_flags(rw, FL_Z + 2 * r, 1, 2, s, lane, budget, true);
#pragma unroll
            for (int k = 0; k < 8; ++k) z[k] = LD16(rw, zo + k * 512, 0);
            wait_flags(rw, FL_P + (s & 3), 4, 16, s, lane, budget, true);
            v4u pv[5];
#pragma unroll
            for (int k = 0; k < 5; ++k) pv[k] = LD16(rw, OFF_PX + s * 81920 + q0 * 40960 + tid * 16, k * 8192);
#pragma unroll
            for (int k = 0; k < 5; ++k) *(LAS v4u*)(lds + k * 8192 + tid * 16) = pv[k];
        } else {
            wait_flags(rw, FL_P + (s & 3), 4, 16, s, lane, budget);
            v4u pv[10];
#pragma unroll
            for (int k = 0; k < 10; ++k) pv[k] = LD16(rw, OFF_PX + s * 81920 + tid * 16, k * 8192);
            wait_flags(rw, FL_Z + 2 * r, 1, 2, s, lane, budget);
#pragma unroll
            for (int k = 0; k < 8; ++k) z[k] = LD16(rw, zo + k * 512, 0);
            MB();
#pragma unroll
            for (int k = 0; k < 10; ++k) *(LAS v4u*)(lds + k * 8192 + tid * 16) = pv[k];
        }
        __syncthreads();
        if (w < 6) {
#pragma unroll 1
            for (int qt = 0; qt < (half ? 1 : 2); ++qt) {
                f32x4 d = {}, e = {};
                lptr pl = lds + qt * 40960 + lane * 16;
                bf16x8 f[6];
#pragma unroll
                for (int i = 0; i < 6; ++i) f[i] = lds8(pl + i * 1024);
                __builtin_amdgcn_sched_barrier(0);
#pragma unroll
                for (int k = 0; k < 40; ++k) {
                    const bf16x8 v = k < 32 ? b[k < 32 ? k : 0] : as8(z[k < 32 ? 0 : k - 32]);
                    if (k & 1) e = MFMA16(v, f[k % 6], e); else d = MFMA16(v, f[k % 6], d);
                    if (k + 6 < 40) f[k % 6] = lds8(pl + (k + 6) * 1024);
                    __builtin_amdgcn_sched_barrier(0);
                }
                d += e;
                const float gq = qt ? gt[1] : gt[0], og = rbf(1.0f - gq);
                const v2u aq = qt ? av[1] : av[0];
                const float r0 = rbf(gq * bflo(aq.x)) + rbf(og * rbf(d[0])), r1 = rbf(gq * bfhi(aq.x)) + rbf(og * rbf(d[1]));
                const float r2 = rbf(gq * bflo(aq.y)) + rbf(og * rbf(d[2])), r3 = rbf(gq * bfhi(aq.y)) + rbf(og * rbf(d[3]));
                v2u ov = {pack2(r0, r1), pack2(r2, r3)};
                *(v2u*)(a.out + (size_t)(32 * s + 16 * (qt ^ q0) + i16) * DM + 16 * r + 4 * g) = ov;
            }
        }
        if (half) break;
        LBAR();
    }
    if (budget < 0 && lane == 0) ST4(6u, rw, FL_ERR * 128);
}

__global__ void __launch_bounds__(512, 2) k_all(Args a) {
    extern __shared__ __attribute__((aligned(16))) char smem[];
    lptr lds = (lptr)smem;
    const rsrc_t rw = __builtin_amdgcn_make_buffer_rsrc(a.ws, 0, WS_BYTES, 0x00020000);
    if (threadIdx.x == 0) {
        const int x = (int)(__builtin_amdgcn_s_getreg(20 | (3 << 11)) & 7u);
        int role = 0;
        for (int k = 0; k < 8; ++k) {
            const int gq = (x + k) & 7;
            const int t = 1 + (int)__hip_atomic_fetch_add((u32*)(a.ws + (FL_T + gq) * 128), 1u, __ATOMIC_RELAXED, __HIP_MEMORY_SCOPE_AGENT);
            if (t < 32) { role = gq + 8 * t; break; }
        }
        *(volatile LAS int*)lds = role;
    }
    __syncthreads();
    const int b = __builtin_amdgcn_readfirstlane(*(volatile LAS int*)lds), xg = b & 7, slot = b >> 3;
    __syncthreads();

    if (b == 8 * 8) role_probe(rw);
    if (slot >= 20) { role_wvo(a, lds, rw, (xg & 3) * 24 + (xg < 4 ? 12 : 0) + slot - 20); __syncthreads(); }
    if ((xg < 4 && slot >= 26) || (xg >= 4 && slot >= 20)) {
        const int idx = xg < 4 ? 48 + (slot - 26) * 4 + xg : (31 - slot) * 4 + (xg - 4);
        role_s4(a, lds, rw, idx);
        __syncthreads();
    }
    bool post = false;
    if (xg < 4) {
        if (slot < 8) role_qk(a, lds, rw, xg * 8 + slot, xg);
        else if (slot < 26) role_rz(a, lds, rw, xg * 18 + slot - 8, xg);
    } else {
        const int j = xg - 4;
        post = true;
        if (slot < 16) { role_vvo(a, lds, rw, j * 8 + (slot >> 1), slot & 1); post = slot < NJOIN; }
        else if (slot < 20) role_k2(a, lds, rw, j * 4 + slot - 16);
        else if (slot < 28) { role_q2p(a, lds, rw, j * 4 + ((slot - 20) >> 1), slot & 1); post = false; }
    }
    if (post) { __syncthreads(); role_post(a, lds, rw); }
}

extern "C" void kernel_launch(void* const* d_in, const int* in_sizes, int n_in, void* d_out, int out_size, void* d_ws, size_t ws_size, hipStream_t stream) {
    Args a;
    a.u = (const u16*)d_in[0]; a.Are = (const u16*)d_in[1]; a.Aim = (const u16*)d_in[2]; a.Bre = (const u16*)d_in[3]; a.Bim = (const u16*)d_in[4];
    a.Cre = (const u16*)d_in[5]; a.Cim = (const u16*)d_in[6]; a.logdt = (const u16*)d_in[7]; a.dskip = (const u16*)d_in[8];
    a.mem0 = (const u16*)d_in[10]; a.Wq = (const u16*)d_in[11]; a.Wk = (const u16*)d_in[12]; a.Wv = (const u16*)d_in[13]; a.Wo = (const u16*)d_in[14];
    a.gatew = (const u16*)d_in[16]; a.out = (u16*)d_out; a.ws = (char*)d_ws;
    const int LDS_ALL = 160 * 1024;
    (void)hipFuncSetAttribute((const void*)k_all, hipFuncAttributeMaxDynamicSharedMemorySize, LDS_ALL);
    (void)hipMemsetAsync(d_ws, 0xFF, INIT_BYTES, stream);
    hipLaunchKernelGGL(k_all, dim3(256), dim3(512), LDS_ALL, stream, a);
}
```

```cpp
#include <hip/hip_runtime.h>
#include <hip/hip_bf16.h>
#include <stdint.h>

typedef unsigned short u16;
typedef unsigned int u32;
typedef u32 v4u __attribute__((ext_vector_type(4)));
typedef u32 v2u __attribute__((ext_vector_type(2)));
typedef float f32x4 __attribute__((ext_vector_type(4)));
typedef float f32x2 __attribute__((ext_vector_type(2)));
typedef float f32x16 __attribute__((ext_vector_type(16)));
typedef __bf16 bf16x2 __attribute__((ext_vector_type(2)));
typedef __bf16 bf16x8 __attribute__((ext_vector_type(8)));
typedef __bf16 bf16x16 __attribute__((ext_vector_type(16)));
typedef short s16x4 __attribute__((ext_vector_type(4)));
#define LAS __attribute__((address_space(3)))
typedef LAS char* lptr;
typedef __amdgpu_buffer_rsrc_t rsrc_t;

constexpr int SEQ = 2048, DM = 576, NH = 32, DH = 128, NSEG = 64, NR = 36, NR8 = 72, NREP = 4;
constexpr int MEMB = 9216, BC_QK = 80;
constexpr int FL0 = 16384;
constexpr int FL_K = FL0, FL_V = FL0 + 16, FL_M = FL0 + 80, FL_P = FL0 + 112, FL_S = FL0 + 176, FL_Q = FL0 + 254, FL_ERR = FL0 + 255, FL_Z = FL0 + 256, FL_W = FL0 + 328, FL_T = FL0 + 424, FL_PAR = FL0 + 432, NS4 = 72;
constexpr int N_MEMA = 0, N_PX = 32768, N_PMM = 98304, N_MEMB = 98304 + 4096, N_PROBE = 98304 + 6144;
constexpr int INIT_BYTES = 16 * 131072 + 65536;
constexpr int OFF_ZH = INIT_BYTES;
constexpr int OFF_KX = OFF_ZH + NSEG * NR * NH * 256;
constexpr int OFF_VOX = OFF_KX + NH * SEQ * DH * 2;
constexpr int OFF_KM = OFF_VOX + NSEG * NR * NH * 1024;
constexpr int OFF_PX = OFF_KM + NSEG * NH * 2048;
constexpr int OFF_A4 = OFF_PX + NSEG * 81920;
constexpr int OFF_G = OFF_A4 + SEQ * DM * 2;
constexpr int OFF_WVO = OFF_G + SEQ * 4;
constexpr int WS_BYTES = OFF_WVO + NR8 * 294912;
constexpr int TK_LA = 8, TK_LR = 8, TK_TOL = 8, TK_GAP = 30;
constexpr int NJOIN = 10;

struct Args {
    const u16* u; const u16* Are; const u16* Aim; const u16* Bre; const u16* Bim; const u16* Cre; const u16* Cim;
    const u16* logdt; const u16* dskip; const u16* mem0; const u16* Wq; const u16* Wk; const u16* Wv; const u16* Wo;
    const u16* gatew; u16* out; char* ws;
};

__device__ __forceinline__ float bf2f(u16 v) { return __uint_as_float((u32)v << 16); }
__device__ __forceinline__ float bflo(u32 v) { return __uint_as_float(v << 16); }
__device__ __forceinline__ float bfhi(u32 v) { return __uint_as_float(v & 0xffff0000u); }
__device__ __forceinline__ u32 pack2(float lo, float hi) { f32x2 f = {lo, hi}; bf16x2 b = __builtin_convertvector(f, bf16x2); return __builtin_bit_cast(u32, b); }
__device__ __forceinline__ float rbf(float x) { return bflo(pack2(x, x)); }
__device__ __forceinline__ u32 nos(u32 v) { return min(v, 0xFFFFFFFEu); }
__device__ __forceinline__ v2u trd(lptr p) { return __builtin_bit_cast(v2u, __builtin_amdgcn_ds_read_tr16_b64_v4i16((LAS s16x4*)p)); }
__device__ __forceinline__ bf16x8 as8(v4u v) { return __builtin_bit_cast(bf16x8, v); }
__device__ __forceinline__ bf16x8 as8(v2u a, v2u b) { v4u v = {a.x, a.y, b.x, b.y}; return __builtin_bit_cast(bf16x8, v); }
__device__ __forceinline__ v4u asu(bf16x8 v) { return __builtin_bit_cast(v4u, v); }
__device__ __forceinline__ bf16x16 cat16(bf16x8 a, bf16x8 b) { return __builtin_shufflevector(a, b, 0, 1, 2, 3, 4, 5, 6, 7, 8, 9, 10, 11, 12, 13, 14, 15); }
__device__ __forceinline__ bf16x8 lds8(lptr p) { return *(LAS bf16x8*)p; }
__device__ __forceinline__ u32 bad4(v4u v) { return max(max(v.x, v.y), max(v.z, v.w)); }
#define NEARB(s, rep, pm) (((((s) & 3) * 4 + (rep)) << 17) + (int)(((((u32)(pm) >> (((s) & 3) * 4 + (rep))) & 0x10001u) * 0x20004000u >> 16) & 0x6000u))
#define FARB(s, rep, pm) ((NEARB(s, rep, pm) ^ 16384) + (((s) & 4) << 14))
#define BCO(slot) (((slot) & 63) * 128 + ((slot) >> 6) * 32768)
#define LBAR() do { asm volatile("s_waitcnt lgkmcnt(0)" ::: "memory"); __builtin_amdgcn_s_barrier(); asm volatile("" ::: "memory"); } while (0)
#define MB() asm volatile("" ::: "memory")
#define DRAIN() asm volatile("s_waitcnt vmcnt(0)" ::: "memory")
#define PIN4(x) asm volatile("" : "+v"(x))
#define MFMA16(a, b, c) __builtin_amdgcn_mfma_f32_16x16x32_bf16((a), (b), (c), 0, 0, 0)
#define MFMA32(a, b, c) __builtin_amdgcn_mfma_f32_32x32x16_bf16((a), (b), (c), 0, 0, 0)
#define SMF(a, b, c, idx) __builtin_amdgcn_smfmac_f32_16x16x64_bf16((a), (b), (c), (idx), 0, 0)
#define LD16(r, vo, so) __builtin_amdgcn_raw_buffer_load_b128((r), (vo), (so), 16)
#define LD8(r, vo, so) __builtin_amdgcn_raw_buffer_load_b64((r), (vo), (so), 16)
#define LD4(r, vo, so) __builtin_amdgcn_raw_buffer_load_b32((r), (vo), (so), 16)
#define ST16(v, r, vo) __builtin_amdgcn_raw_buffer_store_b128((v), (r), (vo), 0, 16)
#define ST8(v, r, vo) __builtin_amdgcn_raw_buffer_store_b64((v), (r), (vo), 0, 16)
#define ST4(v, r, vo) __builtin_amdgcn_raw_buffer_store_b32((v), (r), (vo), 0, 16)

__device__ __forceinline__ float swap32_sum(float a, float b) {
    auto r = __builtin_amdgcn_permlane32_swap(__float_as_uint(a), __float_as_uint(b), false, false);
    return __uint_as_float(r[0]) + __uint_as_float(r[1]); }
__device__ __forceinline__ float xmax(float m) {
    auto r = __builtin_amdgcn_permlane16_swap(__float_as_uint(m), __float_as_uint(m), false, false);
    m = fmaxf(__uint_as_float(r[0]), __uint_as_float(r[1]));
    auto q = __builtin_amdgcn_permlane32_swap(__float_as_uint(m), __float_as_uint(m), false, false);
    return fmaxf(__uint_as_float(q[0]), __uint_as_float(q[1])); }
__device__ __forceinline__ float xsum(float m) {
    auto r = __builtin_amdgcn_permlane16_swap(__float_as_uint(m), __float_as_uint(m), false, false);
    m = __uint_as_float(r[0]) + __uint_as_float(r[1]);
    auto q = __builtin_amdgcn_permlane32_swap(__float_as_uint(m), __float_as_uint(m), false, false);
    return __uint_as_float(q[0]) + __uint_as_float(q[1]); }
__device__ __forceinline__ float vmax(float a, float b) { float r; asm("v_max_f32 %0, %1, %2" : "=v"(r) : "v"(a), "v"(b)); return r; }
__device__ __forceinline__ float vmax3(float a, float b, float c) { float r; asm("v_max3_f32 %0, %1, %2, %3" : "=v"(r) : "v"(a), "v"(b), "v"(c)); return r; }
__device__ __forceinline__ u32 ror8(u32 v) { return (u32)__builtin_amdgcn_update_dpp(0, (int)v, 0x128, 0xf, 0xf, true); }
__device__ __forceinline__ float ror8f(float v) { return __uint_as_float(ror8(__float_as_uint(v))); }
__device__ __forceinline__ int wave_max(int d) {
    d = max(d, __builtin_amdgcn_update_dpp(d, d, 0x128, 0xf, 0xf, false));
    d = max(d, __builtin_amdgcn_update_dpp(d, d, 0x124, 0xf, 0xf, false));
    d = max(d, __builtin_amdgcn_update_dpp(d, d, 0x122, 0xf, 0xf, false));
    d = max(d, __builtin_amdgcn_update_dpp(d, d, 0x121, 0xf, 0xf, false));
    return max(max(__builtin_amdgcn_readlane(d, 0), __builtin_amdgcn_readlane(d, 16)), max(__builtin_amdgcn_readlane(d, 32), __builtin_amdgcn_readlane(d, 48)));
}

constexpr int SPIN_MAX = 2000000;
__device__ __forceinline__ int wait_flags(rsrc_t rw, int line0, int stride, int n, int need, int lane, int& budget, bool fast = false) {
    const int vo = (line0 + (lane < n ? lane : 0) * stride) * 128;
    int mn;
    for (;;) {
        const int v = (int)LD4(rw, vo, 0);
        MB();
        mn = -wave_max(-v);
        if (mn >= need) break;
        if (--budget < 0) break;
        if (fast) __builtin_amdgcn_s_sleep(2); else __builtin_amdgcn_s_sleep(12);
    }
    return mn;
}

template <int NRW, int CW>
__device__ __forceinline__ void rows_ld(v4u (&v)[NRW * CW / 512], const u16* g, int gs, int tid) {
#pragma unroll
    for (int j = 0; j < NRW * CW / 512; ++j) { const int i = tid + 512 * j, row = i / CW, c = i % CW; v[j] = *(const v4u*)(g + (size_t)row * gs + c * 8); }
}
template <int NRW, int CW>
__device__ __forceinline__ void rows_st(const v4u (&v)[NRW * CW / 512], lptr lds, int ls, int tid) {
#pragma unroll
    for (int j = 0; j < NRW * CW / 512; ++j) { const int i = tid + 512 * j, row = i / CW, c = i % CW; *(LAS v4u*)(lds + row * ls + c * 16) = v[j]; }
}
__device__ __forceinline__ bf16x8 frag16(lptr base, int ls, int lane) {
    const int i16 = lane & 15, bg = lane >> 4, q = i16 >> 2, p = i16 & 3;
    lptr a = base + (8 * bg + q) * ls + p * 8;
    return as8(trd(a), trd(a + 4 * ls));
}
__device__ __forceinline__ bf16x8 frag32p(lptr base, int ls, int lane) {
    const int i16 = lane & 15, G = lane >> 4, q = i16 >> 2, p = i16 & 3;
    lptr a = base + (8 * (G >> 1) + q) * ls + (16 * (p & 1) + 8 * (G & 1) + 4 * (p >> 1)) * 2;
    return as8(trd(a), trd(a + 4 * ls));
}

constexpr int XS = 1168;
__device__ __forceinline__ void xload(v4u (&st)[9], const u16* x, int tok0, int tid) {
#pragma unroll
    for (int j = 0; j < 9; ++j) st[j] = *(const v4u*)(x + (size_t)tok0 * DM + (tid + 512 * j) * 8);
}
__device__ __forceinline__ void xstore(const v4u (&st)[9], lptr xl, int tid) {
#pragma unroll
    for (int j = 0; j < 9; ++j) { const int i = tid + 512 * j; *(LAS v4u*)(xl + i * 16 + (i / 72) * 16) = st[j]; }
}
__device__ __forceinline__ void load_w2(bf16x8 (&A)[36], const u16* W, int h0, lptr lds, int tid, int lane, int w) {
    v4u v[6][6];
#pragma unroll
    for (int c = 0; c < 6; ++c) rows_ld<96, 32>(v[c], W + (size_t)c * 96 * 4096 + h0 * DH, 4096, tid);
    __builtin_amdgcn_sched_barrier(0);
#pragma unroll
    for (int c = 0; c < 6; ++c) {
        lptr l = lds + (c & 1) * (96 * 544);
        rows_st<96, 32>(v[c], l, 544, tid);
        LBAR();
#pragma unroll
        for (int ks = 0; ks < 6; ++ks) A[c * 6 + ks] = frag32p(l + ks * 16 * 544 + w * 64, 544, lane);
        __builtin_amdgcn_sched_barrier(0);
    }
    LBAR();
}

__device__ __forceinline__ f32x16 proj_tile(const bf16x8 (&A)[36], lptr xb) {
    f32x16 acc = {};
    bf16x8 f[6];
#pragma unroll
    for (int i = 0; i < 6; ++i) f[i] = lds8(xb + i * 32);
    __builtin_amdgcn_sched_barrier(0);
#pragma unroll
    for (int ks = 0; ks < 36; ++ks) {
        acc = MFMA32(A[ks], f[ks % 6], acc);
        if (ks + 6 < 36) f[ks % 6] = lds8(xb + (ks + 6) * 32);
        __builtin_amdgcn_sched_barrier(0);
    }
    return acc;
}
__device__ __forceinline__ void role_k2(const Args& a, lptr lds, rsrc_t rw, int cu) {
    const int tid = threadIdx.x, lane = tid & 63, w = __builtin_amdgcn_readfirstlane(tid >> 6);
    v4u st[9];
    bf16x8 A[36];
    xload(st, a.u, 0, tid);
    load_w2(A, a.Wk, 2 * cu, lds, tid, lane, w);
    xstore(st, lds, tid);
    __syncthreads();
    const int o0 = OFF_KX + (2 * cu + (w >> 2)) * (NSEG * 8192) + ((w & 3) * 4 + (lane >> 5) * 2) * 512 + (lane & 31) * 16;
    for (int blk = 0; blk < 32; ++blk) {
        lptr xl = lds + (blk & 1) * (64 * XS);
        if (blk + 1 < 32) xload(st, a.u, (blk + 1) * 64, tid);
#pragma unroll
        for (int tt = 0; tt < 2; ++tt) {
            const f32x16 acc = proj_tile(A, xl + (32 * tt + (lane & 31)) * XS + (lane >> 5) * 16);
            v4u c0 = {pack2(acc[0], acc[1]), pack2(acc[2], acc[3]), pack2(acc[4], acc[5]), pack2(acc[6], acc[7])};
            v4u c1 = {pack2(acc[8], acc[9]), pack2(acc[10], acc[11]), pack2(acc[12], acc[13]), pack2(acc[14], acc[15])};
            const int o = o0 + (blk * 2 + tt) * 8192;
            ST16(c0, rw, o); ST16(c1, rw, o + 512);
            if (blk == 0 && tt == 0) { DRAIN(); LBAR(); if (tid == 0) ST4(0u, rw, (FL_K + cu) * 128); }
        }
        if (blk + 1 < 32) xstore(st, lds + ((blk + 1) & 1) * (64 * XS), tid);
        const bool full = blk < 4 || blk == 31;
        if (full) DRAIN(); else asm volatile("s_waitcnt vmcnt(4)" ::: "memory");
        LBAR();
        if (tid == 0) ST4((u32)(full ? 2 * blk + 1 : 2 * blk - 1), rw, (FL_K + cu) * 128);
    }
}

constexpr int VS = 272;
__device__ __forceinline__ void role_q2p(const Args& a, lptr lds, rsrc_t rw, int cu, int par) {
    const int tid = threadIdx.x, lane = tid & 63, w = __builtin_amdgcn_readfirstlane(tid >> 6);
    v4u st[9];
    bf16x8 A[36];
    load_w2(A, a.Wq, 2 * cu, lds, tid, lane, w);
    lptr xl = lds, ql = lds + 64 * XS;
    LAS int* cnt = (LAS int*)(lds + 64 * XS + 4 * 64 * VS);
    if (tid < 2) cnt[tid] = 0;
    xload(st, a.u, par * 64, tid);
    xstore(st, xl, tid);
    __syncthreads();
    int budget = SPIN_MAX, kdone = -1;
    const int i16 = lane & 15, g = lane >> 4;
    const int sg = w >> 2, hh = (w >> 1) & 1, qt = w & 1, h = 2 * cu + hh;
    const float c2 = 0.08838834764831845f * 1.4426950408889634f;
    for (int it = -1; it < 16; ++it) {
        const int blk = 2 * it + par;
        if (it + 1 < 16) {
            if (it + 2 < 16) xload(st, a.u, (blk + 4) * 64, tid);
            lptr qb = ql + (((it + 1) & 1) * 2 + (w >> 2)) * (64 * VS) + ((w & 3) * 32 + (lane >> 5) * 16) * 2;
#pragma unroll
            for (int tt = 0; tt < 2; ++tt) {
                const f32x16 acc = proj_tile(A, xl + (32 * tt + (lane & 31)) * XS + (lane >> 5) * 16);
                v4u c0 = {pack2(acc[0], acc[1]), pack2(acc[2], acc[3]), pack2(acc[4], acc[5]), pack2(acc[6], acc[7])};
                v4u c1 = {pack2(acc[8], acc[9]), pack2(acc[10], acc[11]), pack2(acc[12], acc[13]), pack2(acc[14], acc[15])};
                lptr qp = qb + (32 * tt + (lane & 31)) * VS;
                *(LAS v4u*)qp = c0; *(LAS v4u*)(qp + 16) = c1;
            }
            __syncthreads();
            if (it + 2 < 16) xstore(st, xl, tid);
        }
        if (it >= 0) {
            const int s = 2 * blk + sg;
            if (kdone < s) kdone = wait_flags(rw, FL_K + cu, 1, 1, s, lane, budget);
            bf16x8 k0[4], k1[4], km[4], qf[4];
            const int ko = OFF_KX + (h * NSEG + s) * 8192 + g * 512 + i16 * 16;
#pragma unroll
            for (int ks = 0; ks < 4; ++ks) { k0[ks] = as8(LD16(rw, ko, ks * 2048)); k1[ks] = as8(LD16(rw, ko + 256, ks * 2048)); }
            lptr qp = ql + ((it & 1) * 2 + hh) * (64 * VS) + (32 * sg + 16 * qt + i16) * VS + g * 16;
#pragma unroll
            for (int ks = 0; ks < 4; ++ks) qf[ks] = lds8(qp + ks * 64);
            f32x4 S0 = {}, S1 = {}, Sm = {};
#pragma unroll
            for (int ks = 0; ks < 4; ++ks) { S0 = MFMA16(k0[ks], qf[ks], S0); S1 = MFMA16(k1[ks], qf[ks], S1); }
            wait_flags(rw, FL_M + h, 1, 1, s, lane, budget, s >= 62);
            const int mo = OFF_KM + (s * NH + h) * 2048 + g * 128 + (i16 & 7) * 16;
#pragma unroll
            for (int ks = 0; ks < 4; ++ks) km[ks] = as8(LD16(rw, mo + ks * 512, 0));
#pragma unroll
            for (int ks = 0; ks < 4; ++ks) Sm = MFMA16(km[ks], qf[ks], Sm);
            const int qi = 16 * qt + i16, t0 = 4 * g;
#pragma unroll
            for (int r = 0; r < 4; ++r) {
                if (t0 + r > qi) S0[r] = -INFINITY;
                if (t0 + r + 16 > qi) S1[r] = -INFINITY;
                if (g >= 2) Sm[r] = -INFINITY;
            }
            float m = fmaxf(fmaxf(fmaxf(S0[0], S0[1]), fmaxf(S0[2], S0[3])), fmaxf(fmaxf(S1[0], S1[1]), fmaxf(S1[2], S1[3])));
            m = fmaxf(m, fmaxf(fmaxf(Sm[0], Sm[1]), fmaxf(Sm[2], Sm[3])));
            m = xmax(m);
            const float nm = -m * c2;
            float sum = 0.f;
#pragma unroll
            for (int r = 0; r < 4; ++r) {
                S0[r] = __builtin_amdgcn_exp2f(fmaf(S0[r], c2, nm)); S1[r] = __builtin_amdgcn_exp2f(fmaf(S1[r], c2, nm)); Sm[r] = __builtin_amdgcn_exp2f(fmaf(Sm[r], c2, nm));
                sum += S0[r] + S1[r] + Sm[r];
            }
            sum = xsum(sum);
            const float inv = __builtin_amdgcn_rcpf(sum);
            v4u px = {pack2(S0[0] * inv, S0[1] * inv), pack2(S0[2] * inv, S0[3] * inv), pack2(S1[0] * inv, S1[1] * inv), pack2(S1[2] * inv, S1[3] * inv)};
            const int po = OFF_PX + (s * 2 + qt) * 40960;
            ST16(px, rw, po + h * 1024 + lane * 16);
            const u32 ma = pack2(Sm[0] * inv, Sm[1] * inv), mb = pack2(Sm[2] * inv, Sm[3] * inv);
            auto ra = __builtin_amdgcn_permlane16_swap(ma, ma, false, false);
            auto rb = __builtin_amdgcn_permlane16_swap(mb, mb, false, false);
            v4u pm = {ra[0], rb[0], ra[1], rb[1]};
            if (lane < 16) ST16(pm, rw, po + 32768 + h * 256 + lane * 16);
            DRAIN();
            if (lane == 0) {
                const int old = __hip_atomic_fetch_add(cnt + sg, 1, __ATOMIC_RELAXED, __HIP_MEMORY_SCOPE_WORKGROUP);
                if ((old & 3) == 3) ST4((u32)s, rw, (FL_P + 4 * cu + 2 * par + sg) * 128);
            }
        }
        __syncthreads();
    }
    if (budget < 0 && lane == 0) ST4(5u, rw, FL_ERR * 128);
}

__device__ __forceinline__ void role_vvo(const Args& a, lptr lds, rsrc_t rw, int h, int par) {
    const int tid = threadIdx.x, lane = tid & 63, w = __builtin_amdgcn_readfirstlane(tid >> 6);
    v4u st[9];
#pragma unroll
    for (int j = 0; j < 9; ++j) { const int i = tid + 512 * j; st[j] = *(const v4u*)(a.u + (size_t)(par * 32 + (i >= 2304 ? 32 : 0)) * DM + i * 8); }
    bf16x8 Av[18], Bo[5][4];
    {
        v4u v[3][6], o[2][9];
#pragma unroll
        for (int c = 0; c < 3; ++c) rows_ld<192, 16>(v[c], a.Wv + (size_t)c * 192 * 4096 + h * DH, 4096, tid);
#pragma unroll
        for (int c = 0; c < 2; ++c) rows_ld<64, 72>(o[c], a.Wo + ((size_t)h * DH + c * 64) * DM, DM, tid);
        __builtin_amdgcn_sched_barrier(0);
#pragma unroll
        for (int c = 0; c < 3; ++c) {
            lptr l = lds + (c & 1) * 77824;
            rows_st<192, 16>(v[c], l, 288, tid);
            LBAR();
#pragma unroll
            for (int ks = 0; ks < 6; ++ks) Av[c * 6 + ks] = frag16(l + ks * 32 * 288 + w * 32, 288, lane);
            __builtin_amdgcn_sched_barrier(0);
        }
#pragma unroll
        for (int c = 0; c < 2; ++c) {
            lptr l = lds + ((c + 1) & 1) * 77824;
            rows_st<64, 72>(o[c], l, 1184, tid);
            LBAR();
#pragma unroll
            for (int j = 0; j < 5; ++j) {
                const int r = (w + 8 * j < NR) ? w + 8 * j : w;
#pragma unroll
                for (int ks = 0; ks < 2; ++ks) Bo[j][c * 2 + ks] = frag16(l + ks * 32 * 1184 + r * 32, 1184, lane);
            }
            __builtin_amdgcn_sched_barrier(0);
        }
        LBAR();
    }
    lptr xl = lds, vl = lds + 64 * XS;
    xstore(st, xl, tid);
    __syncthreads();
    const int rho = lane & 15, bg = lane >> 4;
    const int vo0 = OFF_VOX + ((2 * w + ((rho >> 1) & 1)) * 16 + (h >> 1)) * 1024 + ((2 * (h & 1) + (bg & 1)) * 16 + (bg >> 1) * 8 + 2 * (rho >> 2) + (rho & 1)) * 16;
    const int fo = (FL_V + 2 * h + par) * 128;
    auto stage1 = [&](int tt) {
        f32x4 acc = {};
        lptr xb = xl + (16 * tt + rho) * XS + bg * 16;
        bf16x8 f[6];
#pragma unroll
        for (int i = 0; i < 6; ++i) f[i] = lds8(xb + i * 64);
        __builtin_amdgcn_sched_barrier(0);
#pragma unroll
        for (int ks = 0; ks < 18; ++ks) {
            acc = MFMA16(Av[ks], f[ks % 6], acc);
            if (ks + 6 < 18) f[ks % 6] = lds8(xb + (ks + 6) * 64);
            __builtin_amdgcn_sched_barrier(0);
        }
        v2u o = {pack2(acc[0], acc[1]), pack2(acc[2], acc[3])};
        *(LAS v2u*)(vl + (16 * tt + rho) * VS + (16 * w + 4 * bg) * 2) = o;
    };
    auto stage2 = [&](int sg, int s) {
        bf16x8 Af[2][4];
#pragma unroll
        for (int half = 0; half < 2; ++half)
#pragma unroll
            for (int ks = 0; ks < 4; ++ks) Af[half][ks] = lds8(vl + (32 * sg + rho + 16 * half) * VS + ks * 64 + bg * 16);
#pragma unroll
        for (int j = 0; j < 5; ++j) {
            if (j < 4 || w < 4) {
                f32x4 d0 = {}, d1 = {};
#pragma unroll
                for (int ks = 0; ks < 4; ++ks) { d0 = MFMA16(Af[0][ks], Bo[j][ks], d0); d1 = MFMA16(Af[1][ks], Bo[j][ks], d1); }
                v4u o = {pack2(d0[0], d0[1]), pack2(d0[2], d0[3]), pack2(d1[0], d1[1]), pack2(d1[2], d1[3])};
                ST16(o, rw, vo0 + (s * NR + 8 * j) * (NH * 1024));
            }
        }
    };
    int prev = -1;
#pragma unroll 1
    for (int un = 0; un < 32; ++un) {
        const int sg = un & 1, blk = (un >> 1) * 2 + par;
        const int s = un < 2 ? par + 2 * un : 2 * blk + sg;
        if (sg == 0 && blk + 2 < 32) xload(st, a.u, (blk + 2) * 64, tid);
        stage1(2 * sg); stage1(2 * sg + 1);
        if (sg == 1) DRAIN(); else if (blk + 2 < 32) asm volatile("s_waitcnt vmcnt(9)" ::: "memory"); else DRAIN();
        LBAR();
        if (tid == 0 && prev >= 0) ST4((u32)prev, rw, fo);
        if (sg == 1 && blk + 2 < 32) xstore(st, xl, tid);
        stage2(sg, s);
        prev = s;
        if (un == 0 || un == 31) { DRAIN(); LBAR(); if (tid == 0) ST4((u32)s, rw, fo); }
        else if (sg == 1) LBAR();
    }
}
#define VOWN(s) ((s) < 4 ? ((s) & 1) : (((s) >> 1) & 1))

__device__ __forceinline__ u32 rt32() { return (u32)__builtin_amdgcn_s_memrealtime(); }
__device__ __forceinline__ void wait_until(u32 t) {
    if ((int)(t - rt32()) > 1000) return;
    while ((int)(rt32() - t) < -24) __builtin_amdgcn_s_sleep(4);
    while ((int)(rt32() - t) < 0) {}
}
struct TK { u32 XA, XR, EA, ER; int dA0, dA1, dA2, dR0, dR1, dR2; int okA, okR, gap; };
__device__ __forceinline__ int tk_est(int d0, int d1, int d2) { return max(min(d0, d1), min(max(d0, d1), d2)); }
template <int N>
__device__ __forceinline__ u32 beacon_max(rsrc_t rw, int fb, int slot0, int lane, int& budget) {
    const int vo = BCO(slot0 + (lane < N ? lane : 0)), v2 = BCO(64 + (lane & 7));
    u32 v, u = 0;
    for (;;) {
        v = LD4(rw, vo, fb);
        if (N > 64) u = LD4(rw, v2, fb);
        MB();
        if (__all(max(v, u) != 0xFFFFFFFFu)) break;
        if (--budget < 0) break;
        __builtin_amdgcn_s_sleep(1);
    }
    const u32 base = __builtin_amdgcn_readfirstlane(v);
    int d = (int)(v - base);
    if (N > 64) d = max(d, (int)(u - base));
    return base + (u32)wave_max(d);
}
struct BC2 { u32 v, u; };
__device__ __forceinline__ BC2 beacon_issue(rsrc_t rw, int fb, int lane) {
    BC2 b;
    b.v = LD4(rw, BCO(lane), fb);
    b.u = LD4(rw, BCO(64 + (lane & 7)), fb);
    MB();
    return b;
}
__device__ __forceinline__ u32 beacon_finish(BC2 b, rsrc_t rw, int fb, int lane, int& budget) {
    if (!__all(max(b.v, b.u) != 0xFFFFFFFFu)) return beacon_max<NR8>(rw, fb, 0, lane, budget);
    const u32 base = __builtin_amdgcn_readfirstlane(b.v);
    return base + (u32)wave_max(max((int)(b.v - base), (int)(b.u - base)));
}
__device__ __forceinline__ void tk_T0(TK& k, u32 T0, const Args& a) {
    if (k.okR) { k.dR2 = k.dR1; k.dR1 = k.dR0; k.dR0 = (int)(T0 - k.ER); }
    k.okA = (int)(k.XA - T0) >= TK_LA - TK_TOL;
    if (!k.okR) k.gap = k.okA ? max(TK_GAP, k.gap - 6) : min(k.gap + 16, 160);
    k.EA = k.XA;
    k.XR = k.EA + tk_est(k.dA0, k.dA1, k.dA2) + TK_LR;
}
__device__ __forceinline__ void tk_Tp(TK& k, u32 Tp, const Args& a) {
    const int okn = (int)(k.XR - Tp) >= TK_LR - TK_TOL;
    if (k.okA && (k.okR || !okn)) { k.dA2 = k.dA1; k.dA1 = k.dA0; k.dA0 = (int)(Tp - k.EA); }
    k.okR = okn;
    k.ER = k.okR ? k.XR : Tp + k.gap;
    k.XA = k.ER + tk_est(k.dR0, k.dR1, k.dR2) + TK_LA;
}
__device__ __forceinline__ u32 tk_specA(const TK& k, const Args& a) { return k.XR + tk_est(k.dR0, k.dR1, k.dR2) + TK_LA; }
__device__ __forceinline__ u32 tk_specR(const TK& k, const Args& a) { return k.XA + tk_est(k.dA0, k.dA1, k.dA2) + TK_LR; }
__device__ __forceinline__ void tk_init(TK& k) { const u32 n = rt32(); k.XA = k.XR = k.EA = k.ER = n - 100000u; k.dA0 = k.dA1 = k.dA2 = 125; k.dR0 = k.dR1 = k.dR2 = 65; k.okA = k.okR = 0; k.gap = 50; }

__device__ __forceinline__ void tk_post(lptr p, int tag, u32 t, int lane) { if (lane == 0) { v2u v = {(u32)tag, t}; *(volatile LAS v2u*)p = v; } }
__device__ __forceinline__ u32 tk_get(lptr p, int tag, int& budget) {
    for (;;) {
        const v2u v = *(volatile LAS v2u*)p;
        if ((int)__builtin_amdgcn_readfirstlane(v.x) >= tag || --budget < 0) return __builtin_amdgcn_readfirstlane(v.y);
        __builtin_amdgcn_s_sleep(2);
    }
}
__device__ __forceinline__ void spin_to(u32 X) {
    int rem;
    asm volatile("1:\n\ts_memrealtime vcc\n\ts_waitcnt lgkmcnt(0)\n\ts_sub_i32 %0, %1, vcc_lo\n\ts_cmp_gt_i32 %0, 3\n\ts_cbranch_scc1 1b\n\t"
                 "s_cmp_lt_i32 %0, 1\n\ts_cbranch_scc1 2f\n\ts_nop 3\n\ts_cmp_lt_i32 %0, 2\n\ts_cbranch_scc1 2f\n\ts_nop 3\n\t"
                 "s_cmp_lt_i32 %0, 3\n\ts_cbranch_scc1 2f\n\ts_nop 7\n2:"
                 : "=&s"(rem) : "s"(X) : "memory", "scc", "vcc");
}
__device__ __forceinline__ void tk_wait(lptr p, int tag, int& budget) {
    u32 X;
    for (;;) {
        const v2u v = *(volatile LAS v2u*)p;
        const u32 now = rt32();
        X = __builtin_amdgcn_readfirstlane(v.y);
        const int tg = (int)__builtin_amdgcn_readfirstlane(v.x), dt = (int)(X - now);
        if (tg >= tag) {
            if (dt <= 0 || dt > 1000 || tg > tag) return;
            if (dt <= 28) break;
        }
        if (--budget < 0) return;
        __builtin_amdgcn_s_sleep(1);
    }
    spin_to(X);
}
constexpr int OOB = 0x70000000;
__device__ __forceinline__ void gather_mem(rsrc_t rw, int nb, lptr ml, int lane, int w, lptr tkp, int tag, int& budget) {
    const int gi = w == 7 ? 5 : w;
    const int l0 = gi * 1536 + lane * 16, l1 = l0 + 1024;
    const int o0 = l0 < 8192 ? l0 : l0 + (N_MEMB - 8192), o1 = lane < 32 ? (l1 < 8192 ? l1 : l1 + (N_MEMB - 8192)) : OOB;
    lptr p1 = ml + (lane < 32 ? l1 : 14336 + lane * 16);
    asm volatile("" : "+s"(nb));
    tk_wait(tkp, tag, budget);
    v4u v0 = LD16(rw, o0, nb), v1 = LD16(rw, o1, nb);
    MB();
    *(LAS v4u*)(ml + l0) = v0; *(LAS v4u*)p1 = v1;
    MB();
    if (__builtin_expect(!__all(max(bad4(v0), bad4(v1)) != 0xFFFFFFFFu), 0)) {
        do {
            v0 = LD16(rw, o0, nb); v1 = LD16(rw, o1, nb);
            MB();
        } while (!__all(max(bad4(v0), bad4(v1)) != 0xFFFFFFFFu) && --budget >= 0);
        *(LAS v4u*)(ml + l0) = v0; *(LAS v4u*)p1 = v1;
    }
}
__device__ __forceinline__ void gather_mem4(rsrc_t rw, int nb, lptr ml, int lane, int gi, lptr tkp, int tag, int& budget) {
    const int l0 = gi * 2048 + lane * 16;
    const int o2 = lane < 16 ? N_MEMB + gi * 256 + lane * 16 : OOB;
    lptr p2 = ml + (lane < 16 ? 8192 + gi * 256 + lane * 16 : 14336 + lane * 16);
    asm volatile("" : "+s"(nb));
    tk_wait(tkp, tag, budget);
    v4u v0 = LD16(rw, l0, nb), v1 = LD16(rw, l0 + 1024, nb), v2 = LD16(rw, o2, nb);
    MB();
    *(LAS v4u*)(ml + l0) = v0; *(LAS v4u*)(ml + l0 + 1024) = v1; *(LAS v4u*)p2 = v2;
    MB();
    if (__builtin_expect(!__all(max(max(bad4(v0), bad4(v1)), bad4(v2)) != 0xFFFFFFFFu), 0)) {
        do {
            v0 = LD16(rw, l0, nb); v1 = LD16(rw, l0 + 1024, nb); v2 = LD16(rw, o2, nb);
            MB();
        } while (!__all(max(max(bad4(v0), bad4(v1)), bad4(v2)) != 0xFFFFFFFFu) && --budget >= 0);
        *(LAS v4u*)(ml + l0) = v0; *(LAS v4u*)(ml + l0 + 1024) = v1; *(LAS v4u*)p2 = v2;
    }
}
__device__ __forceinline__ int decode_parity(u32 v, int lane) {
    u32 e = v & 0x00FF00FFu, o = (v >> 8) & 0x00FF00FFu;
    { auto r = __builtin_amdgcn_permlane16_swap(e, e, false, false); e = r[0] + r[1]; }
    { auto r = __builtin_amdgcn_permlane16_swap(o, o, false, false); o = r[0] + r[1]; }
    const int l0 = e & 0xFFFF, l2 = e >> 16, l1 = o & 0xFFFF, l3 = o >> 16;
    const bool par = l1 + l3 < l0 + l2;
    const bool hi = par ? l3 < l1 : l2 < l0;
    return (int)((__ballot(par) & 0xFFFFu) | ((__ballot(hi) & 0xFFFFu) << 16));
}
__device__ __forceinline__ int get_parity(rsrc_t rw, int lane, int& budget) {
    const int vo = (FL_PAR + (lane & 31)) * 128;
    u32 v;
    for (;;) {
        v = LD4(rw, vo, 0);
        MB();
        if (__all(v != 0xFFFFFFFFu) || --budget < 0) break;
        __builtin_amdgcn_s_sleep(8);
    }
    return decode_parity(v, lane);
}
__device__ __forceinline__ void role_probe(rsrc_t rw, int rep) {
    const int lane = threadIdx.x & 63, w = __builtin_amdgcn_readfirstlane(threadIdx.x >> 6);
    const int G = (w & 3) * 4 + rep;
    const int o = (G << 17) + N_PROBE + (w >> 2) * 512;
    ST4(0xFFFFFFFFu, rw, lane < 12 ? o + (lane & 3) * 8192 + (lane >> 2) * 128 : OOB);
    DRAIN();
    int best[4] = {254, 254, 254, 254};
#pragma unroll
    for (int k = 0; k < 12; ++k) {
        const unsigned long long t0 = __builtin_amdgcn_s_memtime();
        asm volatile("s_waitcnt lgkmcnt(0)" ::: "memory");
        u32 v = LD4(rw, o + (k & 3) * 8192 + (k >> 2) * 128, 0);
        asm volatile("s_waitcnt vmcnt(0)" : "+v"(v) :: "memory");
        const unsigned long long t1 = __builtin_amdgcn_s_memtime();
        best[k & 3] = min(best[k & 3], (int)(t1 - t0) >> 2);
    }
    if (lane == 0) ST4((u32)(best[0] | (best[1] << 8) | (best[2] << 16) | (best[3] << 24)), rw, (FL_PAR + (w >> 2) * 16 + G) * 128);
}
template <typename F>
__device__ __forceinline__ void qk_project_h(const bf16x16 (&Bq)[9], const bf16x16 (&Bk)[9], lptr ma, lptr xw, int idx, F hook) {
    f32x4 aq = {}, ak = {};
    bf16x8 A[9];
#pragma unroll
    for (int t = 0; t < 9; ++t) A[t] = lds8(ma + t * 1024);
    __builtin_amdgcn_sched_barrier(0);
#pragma unroll
    for (int t = 0; t < 9; ++t) {
        aq = SMF(A[t], Bq[t], aq, idx); ak = SMF(A[t], Bk[t], ak, idx);
        hook(t);
        __builtin_amdgcn_sched_barrier(0);
    }
    const float s0 = swap32_sum(aq[0], ak[0]), s1 = swap32_sum(aq[1], ak[1]), s2 = swap32_sum(aq[2], ak[2]), s3 = swap32_sum(aq[3], ak[3]);
    v2u o = {pack2(s0, s1), pack2(s2, s3)};
    *(LAS v2u*)xw = o;
}
__device__ __forceinline__ void qk_project(const bf16x16 (&Bq)[9], const bf16x16 (&Bk)[9], lptr ma, lptr xw, int idx) {
    qk_project_h(Bq, Bk, ma, xw, idx, [](int) {});
}

__device__ __forceinline__ void role_qk(const Args& a, lptr lds, rsrc_t rw, int h, int rep) {
    const int tid = threadIdx.x, lane = tid & 63, w = __builtin_amdgcn_readfirstlane(tid >> 6);
    bf16x16 Bq[9], Bk[9];
    {
        v4u xr[6];
#pragma unroll
        for (int j = 0; j < 6; ++j) {
            const int i = min(tid + 512 * j, 2879), row = i / 72, c = i - row * 72;
            xr[j] = *(const v4u*)((row < 8 ? a.mem0 + row * DM : a.u + (row - 8) * DM) + c * 8);
        }
        v4u v[6][6];
#pragma unroll
        for (int c = 0; c < 6; ++c) rows_ld<192, 16>(v[c], (c >= 3 ? a.Wk : a.Wq) + (size_t)(c % 3) * 192 * 4096 + h * DH, 4096, tid);
        __builtin_amdgcn_sched_barrier(0);
#pragma unroll
        for (int j = 0; j < 6; ++j) {
            const int i = min(tid + 512 * j, 2879), row = i / 72, c = i - row * 72, G = row >> 3;
            lptr d = lds + (G < 2 ? 57344 + G * MEMB : 135168 + (G - 2) * MEMB) + (c >> 1) * 256 + (row & 7) * 16 + (c & 1) * 8;
            *(LAS v2u*)d = v2u{xr[j].x, xr[j].z}; *(LAS v2u*)(d + 128) = v2u{xr[j].y, xr[j].w};
        }
#pragma unroll
        for (int c = 0; c < 6; ++c) {
            lptr l = lds + (c & 1) * 77824;
            rows_st<192, 16>(v[c], l, 288, tid);
            LBAR();
#pragma unroll
            for (int t = 0; t < 3; ++t) {
                bf16x16 f = cat16(frag16(l + (64 * t) * 288 + w * 32, 288, lane), frag16(l + (64 * t + 32) * 288 + w * 32, 288, lane));
                if (c >= 3) Bk[(c % 3) * 3 + t] = f; else Bq[c * 3 + t] = f;
            }
            __builtin_amdgcn_sched_barrier(0);
        }
        LBAR();
    }
    lptr ml = lds, xl = lds + MEMB;
    const int idx = (lane & 8) ? 0xEEEE : 0x4444;
    const int i16 = lane & 15, g = lane >> 4;
    const bool hi8 = (lane & 8) != 0;
    int budget = SPIN_MAX;
    lptr pq = xl + (8 * g + (i16 >> 2)) * 32 + (i16 & 1) * 8;
    lptr kl = lds + 16384 + lane * 16;
    lptr ma = ml + lane * 16, xw = xl + (16 * w + i16) * 32 + g * 8;
    const int ox = N_PX + (h >> 4) * 32768 + (h & 15) * 512 + ((g & 1) * 16 + (g >> 1) * 8 + (i16 & 7)) * 16;
    const int om = hi8 ? OOB : N_PMM + h * 128 + (i16 & 7) * 16;
    const int ob = lane < NREP ? BCO(BC_QK + h) : OOB;
    lptr tkp = lds + MEMB + 4096, kfp = tkp + 64;
    if (tid == 0) { tk_post(tkp, 0, rt32() - 100000u, 0); tk_post(kfp, 0, 0u, 0); }
    for (int c = tid; c < 576; c += 512) *(LAS v4u*)(ml + c * 16) = *(LAS v4u*)(lds + 57344 + c * 16);
    __syncthreads();
#pragma unroll 1
    for (int G = 0; G < 4; ++G) {
        qk_project(Bq, Bk, lds + (G ? 135168 + (G - 1) * MEMB : 57344 + MEMB) + lane * 16, xw, idx);
        LBAR();
        if (w < 4) {
            const bf16x8 F = as8(trd(pq + w * 1024 + 16), trd(pq + w * 1024 + 16 + 128));
            if ((i16 >> 3) == (G & 1)) *(LAS bf16x8*)(kl + ((G >> 1) * 4 + w) * 1024) = F;
        }
        LBAR();
    }
    const int pm = get_parity(rw, lane, budget);
    LBAR();
    if (w == 0) {
        const float c2 = 0.08838834764831845f * 1.4426950408889634f;
        for (int s = 0; s < NSEG - 1; ++s) {
            tk_get(kfp, s, budget);
            bf16x8 kx[2][4];
#pragma unroll
            for (int tl = 0; tl < 2; ++tl)
#pragma unroll
                for (int ks = 0; ks < 4; ++ks) kx[tl][ks] = lds8(kl + (s & 1) * 8192 + (tl * 4 + ks) * 1024);
            int oxs = ox + NEARB(s, i16 >> 3, pm), oxt = ox + NEARB(s, 2 + (i16 >> 3), pm), oms = om + NEARB(s, g, pm), obs = ob + FARB(s, lane & 3, pm);
            const int nb = NEARB(s, rep, pm);
            asm volatile("s_waitcnt lgkmcnt(0)" : "+v"(oxs), "+v"(oxt), "+v"(oms), "+v"(obs) :: "memory");
#pragma unroll
            for (int tl = 0; tl < 2; ++tl)
#pragma unroll
                for (int ks = 0; ks < 4; ++ks) PIN4(kx[tl][ks]);
            if (s > 0) {
                gather_mem(rw, nb, ml, lane, 0, tkp, s, budget);
            }
            LBAR();
            qk_project(Bq, Bk, ma, xw, idx);
            __builtin_amdgcn_s_setprio(3);
            LBAR();
            bf16x8 Fq[4], Fk[4];
#pragma unroll
            for (int ks = 0; ks < 4; ++ks) Fq[ks] = as8(trd(pq + ks * 1024), trd(pq + ks * 1024 + 128));
            __builtin_amdgcn_sched_barrier(0);
#pragma unroll
            for (int ks = 0; ks < 4; ++ks) Fk[ks] = as8(trd(pq + ks * 1024 + 16), trd(pq + ks * 1024 + 16 + 128));
            __builtin_amdgcn_sched_barrier(0);
            f32x4 S0 = {}, S1 = {}, Sm = {};
            S0 = MFMA16(kx[0][0], Fq[0], S0); S1 = MFMA16(kx[1][0], Fq[0], S1);
            S0 = MFMA16(kx[0][1], Fq[1], S0); S1 = MFMA16(kx[1][1], Fq[1], S1);
            __builtin_amdgcn_sched_barrier(0);
            Sm = MFMA16(Fk[0], Fq[0], Sm); S0 = MFMA16(kx[0][2], Fq[2], S0);
            Sm = MFMA16(Fk[1], Fq[1], Sm); S1 = MFMA16(kx[1][2], Fq[2], S1);
            Sm = MFMA16(Fk[2], Fq[2], Sm); S0 = MFMA16(kx[0][3], Fq[3], S0);
            Sm = MFMA16(Fk[3], Fq[3], Sm); S1 = MFMA16(kx[1][3], Fq[3], S1);
            __builtin_amdgcn_sched_barrier(0);
            float y0 = hi8 ? Sm[2] : Sm[0], y1 = hi8 ? Sm[3] : Sm[1];
            float x0 = hi8 ? S1[0] : S0[0], x1 = hi8 ? S1[1] : S0[1], x2 = hi8 ? S1[2] : S0[2], x3 = hi8 ? S1[3] : S0[3];
            const float ml_ = vmax3(vmax3(y0, y1, x0), x1, vmax(x2, x3));
            const float nm = -ml_ * c2;
            float m = vmax(ml_, ror8f(ml_));
            x0 = __builtin_amdgcn_exp2f(fmaf(x0, c2, nm)); x1 = __builtin_amdgcn_exp2f(fmaf(x1, c2, nm));
            { auto r = __builtin_amdgcn_permlane16_swap(__float_as_uint(m), __float_as_uint(m), false, false); m = vmax(__uint_as_float(r[0]), __uint_as_float(r[1])); }
            x2 = __builtin_amdgcn_exp2f(fmaf(x2, c2, nm)); x3 = __builtin_amdgcn_exp2f(fmaf(x3, c2, nm));
            { auto r = __builtin_amdgcn_permlane32_swap(__float_as_uint(m), __float_as_uint(m), false, false); m = vmax(__uint_as_float(r[0]), __uint_as_float(r[1])); }
            y0 = __builtin_amdgcn_exp2f(fmaf(y0, c2, nm)); y1 = __builtin_amdgcn_exp2f(fmaf(y1, c2, nm));
            const float f = __builtin_amdgcn_exp2f((ml_ - m) * c2);
            float sum = f * fmaf(0.5f, y0 + y1, (x0 + x1) + (x2 + x3));
            sum += ror8f(sum);
            sum = xsum(sum);
            const float inv = f * __builtin_amdgcn_rcpf(sum);
            const u32 pa = pack2(x0 * inv, x1 * inv) & 0x7fff7fffu, pb = pack2(x2 * inv, x3 * inv) & 0x7fff7fffu, pc = pack2(y0 * inv, y1 * inv) & 0x7fff7fffu;
            const u32 qc = ror8(pc);
            v4u px = {(u32)__builtin_amdgcn_update_dpp((int)pa, (int)pa, 0x128, 0xf, 0xc, false), (u32)__builtin_amdgcn_update_dpp((int)pb, (int)pb, 0x128, 0xf, 0xc, false),
                      (u32)__builtin_amdgcn_update_dpp((int)pa, (int)pa, 0x128, 0xf, 0x3, false), (u32)__builtin_amdgcn_update_dpp((int)pb, (int)pb, 0x128, 0xf, 0x3, false)};
            ST16(px, rw, oxs);
            ST16(px, rw, oxt);
            auto ra = __builtin_amdgcn_permlane16_swap(pc, pc, false, false);
            auto rc = __builtin_amdgcn_permlane16_swap(qc, qc, false, false);
            v4u pmv = {ra[0], rc[0], ra[1], rc[1]};
            ST16(pmv, rw, oms);
            __builtin_amdgcn_sched_barrier(0);
            ST4(nos(rt32()), rw, obs);
            __builtin_amdgcn_s_setprio(0);
        }
        {
            gather_mem(rw, NEARB(NSEG - 1, rep, pm), ml, lane, 0, tkp, NSEG - 1, budget);
            LBAR();
            qk_project(Bq, Bk, ma, xw, idx);
            LBAR();
        }
    } else if (w == 5) {
        int kdone = -1;
        const int kxo = OFF_KX + h * (NSEG * 8192) + g * 512 + i16 * 16;
        for (int s = 0; s < NSEG; ++s) {
            const bool need = s + 1 < NSEG - 1;
            if (need && kdone < s + 1) kdone = wait_flags(rw, FL_K + (h >> 1), 1, 1, -1, lane, budget);
            const bool go = need && kdone >= s + 1;
            int ko = (go ? kxo : OOB) + (s + 1) * 8192;
            int a0 = ox + NEARB(s + 2, i16 >> 3, pm), a1 = ox + NEARB(s + 2, 2 + (i16 >> 3), pm), a2 = om + NEARB(s + 2, g, pm), a3 = ob + FARB(s + 4, lane & 3, pm);
            v4u ff = {0xFFFFFFFFu, 0xFFFFFFFFu, 0xFFFFFFFFu, 0xFFFFFFFFu};
            asm volatile("" : "+v"(ko), "+v"(a0), "+v"(a1), "+v"(a2), "+v"(a3), "+v"(ff));
            LBAR();
            v4u kx[8];
            qk_project_h(Bq, Bk, ma, xw, idx, [&](int t) {
                if (t < 4) { kx[t] = LD16(rw, ko + (t & 1) * 2048, (t >> 1) * 4096); kx[4 + t] = LD16(rw, ko + 256 + (t & 1) * 2048, (t >> 1) * 4096); }
                if (t == 4) { ST16(ff, rw, a0); ST16(ff, rw, a1); }
                if (t == 5) { ST16(ff, rw, a2); ST4(0xFFFFFFFFu, rw, a3); }
            });
            LBAR();
            if (need) {
                if (!go) {
                    kdone = wait_flags(rw, FL_K + (h >> 1), 1, 1, s + 1, lane, budget, true);
#pragma unroll
                    for (int k = 0; k < 8; ++k) kx[k] = LD16(rw, kxo + (k >> 2) * 256 + (k & 3) * 2048, (s + 1) * 8192);
                }
#pragma unroll
                for (int k = 0; k < 8; ++k) *(LAS v4u*)(kl + ((s + 1) & 1) * 8192 + k * 1024) = kx[k];
                asm volatile("s_waitcnt lgkmcnt(0)" ::: "memory");
                tk_post(kfp, s + 1, 0u, lane);
            }
            DRAIN();
        }
    } else {
        TK tk; tk_init(tk);
        const int kmo = i16 < 8 ? OFF_KM + h * 2048 + g * 128 + i16 * 16 : OOB;
        for (int s = 0; s < NSEG; ++s) {
            if (s > 0 && w != 6) gather_mem(rw, NEARB(s, rep, pm), ml, lane, w, tkp, s, budget);
            BC2 bc = {0u, 0u};
            if (w == 3) bc = beacon_issue(rw, FARB(s, rep, pm), lane);
            LBAR();
            qk_project(Bq, Bk, ma, xw, idx);
            LBAR();
            if (w == 6) {
                bf16x8 F[4];
#pragma unroll
                for (int ks = 0; ks < 4; ++ks) F[ks] = as8(trd(pq + ks * 1024 + 16), trd(pq + ks * 1024 + 16 + 128));
#pragma unroll
                for (int ks = 0; ks < 4; ++ks) ST16(asu(F[ks]), rw, kmo + s * (NH * 2048) + ks * 512);
                DRAIN();
                if (lane == 0) ST4((u32)s, rw, (FL_M + h) * 128);
            } else if (w == 3) {
                if (s < NSEG - 1) {
                    if (s > 0) { tk_T0(tk, beacon_finish(bc, rw, FARB(s, rep, pm), lane, budget), a); if (tk.okA) tk_post(tkp, s + 1, tk_specA(tk, a), lane); }
                    wait_until(tk.XR + 8);
                    tk_Tp(tk, beacon_max<NH>(rw, FARB(s, rep, pm), BC_QK, lane, budget), a);
                    tk_post(tkp, s + 1, tk.XA, lane);
                }
            } else __builtin_amdgcn_s_sleep(14);
        }
    }
    if (budget < 0 && lane == 0) ST4(1u, rw, FL_ERR * 128);
}

__device__ __forceinline__ void role_wvo(const Args& a, lptr lds, rsrc_t rw, int idx) {
    const int tid = threadIdx.x, lane = tid & 63, w = __builtin_amdgcn_readfirstlane(tid >> 6);
    const int h = idx / 3, part = idx - 3 * h;
    bf16x8 Bo[5][4];
    v4u v[6], o[2][9];
#pragma unroll
    for (int c = 0; c < 2; ++c) rows_ld<64, 72>(o[c], a.Wo + ((size_t)h * DH + c * 64) * DM, DM, tid);
    rows_ld<192, 16>(v, a.Wv + (size_t)part * 192 * 4096 + h * DH, 4096, tid);
    __builtin_amdgcn_sched_barrier(0);
#pragma unroll
    for (int c = 0; c < 2; ++c) {
        lptr l = lds + c * 77824;
        rows_st<64, 72>(o[c], l, 1184, tid);
        LBAR();
#pragma unroll
        for (int j = 0; j < 5; ++j) {
            const int r = (w + 8 * j < NR) ? w + 8 * j : w;
#pragma unroll
            for (int ks = 0; ks < 2; ++ks) Bo[j][c * 2 + ks] = frag16(l + ks * 32 * 1184 + r * 32, 1184, lane);
        }
        __builtin_amdgcn_sched_barrier(0);
    }
    rows_st<192, 16>(v, lds, VS, tid);
    LBAR();
    const int rho = lane & 15, bg = lane >> 4;
    const int wo0 = OFF_WVO + (2 * w + ((rho >> 1) & 1)) * 294912 + ((h >> 1) * 18 + part * 6) * 1024 + ((h & 1) * 32 + bg * 8 + 2 * (rho >> 2) + (rho & 1)) * 16;
#pragma unroll 1
    for (int kb = 0; kb < 6; ++kb) {
        bf16x8 Af[2][4];
#pragma unroll
        for (int mt = 0; mt < 2; ++mt)
#pragma unroll
            for (int ks = 0; ks < 4; ++ks) Af[mt][ks] = lds8(lds + (32 * kb + 8 * (rho >> 2) + 4 * mt + (rho & 3)) * VS + ks * 64 + bg * 16);
#pragma unroll
        for (int j = 0; j < 5; ++j) {
            if (j < 4 || w < 4) {
                f32x4 d0 = {}, d1 = {};
#pragma unroll
                for (int ks = 0; ks < 4; ++ks) { d0 = MFMA16(Af[0][ks], Bo[j][ks], d0); d1 = MFMA16(Af[1][ks], Bo[j][ks], d1); }
                v4u q = {pack2(d0[0], d0[1]), pack2(d0[2], d0[3]), pack2(d1[0], d1[1]), pack2(d1[2], d1[3])};
                ST16(q, rw, wo0 + j * (16 * 294912) + kb * 1024);
            }
        }
        if (kb == 2 || kb == 4) {
            asm volatile("s_waitcnt vmcnt(4)" ::: "memory");
            LBAR();
            if (tid == 0) ST4((u32)(kb >> 1), rw, (FL_W + idx) * 128);
        }
    }
    DRAIN();
    LBAR();
    if (tid == 0) ST4(3u, rw, (FL_W + idx) * 128);
}

__device__ __forceinline__ void role_rz(const Args& a, lptr lds, rsrc_t rw, int r, int rep) {
    const int tid = threadIdx.x, lane = tid & 63, w = __builtin_amdgcn_readfirstlane(tid >> 6);
    const int idx = (lane & 8) ? 0xEEEE : 0x4444;
    const int i16 = lane & 15, g = lane >> 4;
    int budget = SPIN_MAX;
    bf16x16 Ba[9], Bb[9];
    v4u t0[4], m0[2];
    int pm;
    {
        const int wo = OFF_WVO + r * 294912 + 2 * w * 18432 + ((i16 >> 3) * 32 + g * 8 + (i16 & 7)) * 16;
        const int vq = OFF_VOX + r * 16384 + w * 2048 + lane * 16;
#pragma unroll
        for (int j = 0; j < 2; ++j) { const int i = min(tid + 512 * j, 575), row = i / 72, c = i - row * 72; m0[j] = *(const v4u*)(a.mem0 + row * DM + c * 8); }
        const int vf = (int)LD4(rw, (FL_V + lane) * 128, 0);
        const u32 pw = LD4(rw, (FL_PAR + (lane & 31)) * 128, 0);
        int have = 0;
        bool vgot = false;
#pragma unroll
        for (int p = 0; p < 3; ++p) {
            if (have < p + 1) have = wait_flags(rw, FL_W + 12 * w, 1, 12, p + 1, lane, budget, true);
            if (p == 0 || p == 2) {
                const bool now = p == 0 ? (bool)__all(vf >= 0) : !vgot;
                if (now) {
                    if (p == 2) wait_flags(rw, FL_V, 1, 64, 0, lane, budget, true);
#pragma unroll
                    for (int k = 0; k < 4; ++k) t0[k] = LD16(rw, vq + (k & 1) * 1024, (k >> 1) * (NR * NH * 1024));
                    vgot = true;
                }
            }
            if (p == 0) pm = __all(pw != 0xFFFFFFFFu) ? decode_parity(pw, lane) : get_parity(rw, lane, budget);
#pragma unroll
            for (int q = 0; q < 3; ++q) {
                const int t = 3 * q + p;
                Ba[t] = cat16(as8(LD16(rw, wo, t * 2048)), as8(LD16(rw, wo + 1024, t * 2048)));
                Bb[t] = cat16(as8(LD16(rw, wo, 18432 + t * 2048)), as8(LD16(rw, wo + 1024, 18432 + t * 2048)));
            }
            MB();
        }
#pragma unroll
        for (int j = 0; j < 2; ++j) {
            const int i = min(tid + 512 * j, 575), row = i / 72, c = i - row * 72;
            lptr d = lds + (c >> 1) * 256 + row * 16 + (c & 1) * 8;
            *(LAS v2u*)d = v2u{m0[j].x, m0[j].z}; *(LAS v2u*)(d + 128) = v2u{m0[j].y, m0[j].w};
        }
    }
    lptr ml = lds, zl = lds + MEMB;
    lptr tka = lds + 13312, tkr = tka + 16, cntp = tka + 64;
    volatile LAS int* prod = (volatile LAS int*)(lds + 13312 + 512);
    lptr ring = lds + 16384;
    lptr ma = ml + lane * 16;
    lptr zw = zl + ((2 * w + (lane >> 5)) >> 2) * 1024 + ((((2 * w + (lane >> 5)) & 3) * 16 + (g & 1) * 8 + (i16 & 7)) * 16) + (i16 >> 3) * 8;
    const bool hi8r = (lane & 8) != 0;
    const int mso = i16 < 8 ? (r < 64 ? N_MEMA + r * 128 : N_MEMB + (r - 64) * 128) + i16 * 16 : OOB;
    const int ob = lane < NREP ? BCO(r) : OOB;
    if (tid == 0) { tk_post(tka, 0, rt32() - 100000u, 0); tk_post(tkr, -1, 0u, 0); *(volatile LAS int*)cntp = 0; }
#pragma unroll
    for (int k = 0; k < 4; ++k) *(LAS v4u*)(ring + (k >> 1) * 16384 + w * 2048 + (k & 1) * 1024 + lane * 16) = t0[k];
    if (tid < 4) prod[tid] = 2;
    LBAR();
    if (w < 4) {
        const int po = N_PX + (w >> 1) * 32768 + (w & 1) * 4096 + lane * 16;
        const int mo = N_PMM + w * 1024 + 2 * g * 128 + (i16 & 7) * 16 + (i16 >> 3) * 8;
        lptr cna = lane == 0 ? cntp : cntp + 4 + lane * 4;
        const u32 one = lane == 0 ? 1u : 0u;
        const bool dv = (g >> 1) == (i16 >> 3);
        const int rwo = dv ? (4 * (g & 1) + 2 * (g >> 1)) * 128 + ((i16 & 7) >> 1) * 32 + w * 8 + (i16 & 1) * 4 : 2048 + lane * 4;
        const int msq = lane < 32 ? (r < 64 ? N_MEMA + r * 128 : N_MEMB + (r - 64) * 128) + (lane >> 2) * 16 : OOB;
        lptr zrd = zl + w * 1024 + lane * 16;
        for (int s = 0; s < NSEG; ++s) {
            lptr Rw = lds + 81920 + (s & 1) * 1024 + rwo, Rr = lds + 81920 + (s & 1) * 1024 + (lane & 31) * 32;
            int mss = msq + NEARB(s + 1, lane & 3, pm), obs = ob + FARB(s + 1, lane & 3, pm);
            int sb = NEARB(s, rep, pm);
            asm volatile("" : "+s"(sb));
            lptr rg = ring + (s & 3) * 16384 + w * 4096 + lane * 16;
            bf16x8 b[5];
            const bool early = __builtin_amdgcn_readfirstlane(prod[w >> 1]) > s;
            MB();
#pragma unroll
            for (int k = 0; k < 4; ++k) b[k] = lds8(rg + k * 1024);
            const u32 X = s > 0 ? tk_get(tkr, min(s, NSEG - 2), budget) : 0u;
            asm volatile("s_waitcnt lgkmcnt(0)" : "+v"(mss), "+v"(obs), "+v"(Rw), "+v"(Rr) :: "memory");
#pragma unroll
            for (int k = 0; k < 4; ++k) PIN4(b[k]);
            LBAR();
            qk_project(Ba, Bb, ma, zw, idx);
            LBAR();
            if (s == NSEG - 1) break;
            __builtin_amdgcn_s_setprio(3);
            b[4] = lds8(zrd);
            if (!early) {
                while (__builtin_amdgcn_readfirstlane(prod[w >> 1]) <= s && --budget >= 0) __builtin_amdgcn_s_sleep(1);
                MB();
#pragma unroll
                for (int k = 0; k < 4; ++k) b[k] = lds8(rg + k * 1024);
            }
            if (s > 0 && (int)(X - rt32()) < 1000) spin_to(X);
            f32x4 d;
            u32 bad;
            auto shot = [&]() {
                v4u p[4]; v2u m[2];
                m[0] = LD8(rw, mo, sb); m[1] = LD8(rw, mo + 128, sb);
                __builtin_amdgcn_sched_barrier(0);
#pragma unroll
                for (int k = 0; k < 4; ++k) { p[k] = LD16(rw, po + k * 1024, sb); __builtin_amdgcn_sched_barrier(0); }
                MB();
                d = f32x4{};
                d = MFMA16(as8(m[0], m[1]), b[4], d);
                bad = (m[0].x | m[0].y) | (m[1].x | m[1].y);
                __builtin_amdgcn_sched_barrier(0);
#pragma unroll
                for (int k = 0; k < 4; ++k) {
                    d = MFMA16(as8(p[k]), b[k], d);
                    bad |= (p[k].x | p[k].y) | (p[k].z | p[k].w);
                    __builtin_amdgcn_sched_barrier(0);
                }
            };
            shot();
            if (__builtin_expect(!__all((int)bad >= 0), 0)) {
                do shot(); while (!__all((int)bad >= 0) && --budget >= 0);
            }
            float e0, e1;
            {
                auto r0 = __builtin_amdgcn_permlane32_swap(__float_as_uint(d[0]), __float_as_uint(d[2]), false, false);
                auto r1 = __builtin_amdgcn_permlane32_swap(__float_as_uint(d[1]), __float_as_uint(d[3]), false, false);
                const float t0 = __uint_as_float(hi8r ? r0[1] : r0[0]), t1 = __uint_as_float(hi8r ? r1[1] : r1[0]);
                e0 = t0 + ror8f(t0); e1 = t1 + ror8f(t1);
            }
            f32x4 x0, x1; u32 old;
            asm volatile("ds_write2_b32 %4, %5, %6 offset1:32\n\tds_add_rtn_u32 %0, %7, %8\n\tds_read_b128 %1, %3\n\tds_read_b128 %2, %3 offset:16\n\ts_waitcnt lgkmcnt(0)"
                         : "=&v"(old), "=&v"(x0), "=&v"(x1) : "v"(Rr), "v"(Rw), "v"(e0), "v"(e1), "v"(cna), "v"(one) : "memory");
            {
                const u32 c = nos(pack2((x0[0] + x0[2]) + (x1[0] + x1[2]), (x0[1] + x0[3]) + (x1[1] + x1[3])));
                v4u o = {(u32)__builtin_amdgcn_update_dpp(0, (int)c, 0x00, 0xf, 0xf, true), (u32)__builtin_amdgcn_update_dpp(0, (int)c, 0x55, 0xf, 0xf, true),
                         (u32)__builtin_amdgcn_update_dpp(0, (int)c, 0xAA, 0xf, 0xf, true), (u32)__builtin_amdgcn_update_dpp(0, (int)c, 0xFF, 0xf, 0xf, true)};
                const bool last = (__builtin_amdgcn_readfirstlane(old) & 3) == 3;
                ST16(o, rw, last ? mss : OOB);
                __builtin_amdgcn_sched_barrier(0);
                if (last) ST4(nos(rt32()), rw, obs);
            }
            __builtin_amdgcn_s_setprio(0);
        }
    } else if (w == 5 || w == 6) {
        int vdone0 = -1, vdone1 = -1, staged = 2;
        const int hw = w - 5;
        const int vo = OFF_VOX + r * 16384 + hw * 8192 + lane * 16;
        const int zho = OFF_ZH + r * 4096 + hw * 2048 + lane * 16;
        lptr zhr = zl + hw * 2048 + (((lane >> 4) & 3) * 16 + (lane & 7)) * 16 + ((lane >> 3) & 1) * 8;
        const int aro = w == 5 ? mso : OOB, arb = w == 5 ? ob : OOB;
        v4u zv[2] = {};
        for (int s = 0; s < NSEG; ++s) {
            bool go = false;
            if (staged <= min(s + 3, NSEG - 2)) {
                const int ow = VOWN(staged);
                int& vd = ow ? vdone1 : vdone0;
                if (vd < staged) vd = wait_flags(rw, FL_V + ow, 2, NH, -1, lane, budget);
                go = vd >= staged;
            }
            const int vos = go ? vo : OOB;
            int va = vos + staged * (NR * NH * 1024), zs = s > 0 ? zho + (s - 1) * (NR * NH * 256) : OOB;
            int a0 = aro + NEARB(s + 3, g, pm), a1 = arb + FARB(s + 4, lane & 3, pm);
            v4u ff = {0xFFFFFFFFu, 0xFFFFFFFFu, 0xFFFFFFFFu, 0xFFFFFFFFu};
            asm volatile("" : "+v"(va), "+v"(zs), "+v"(a0), "+v"(a1), "+v"(ff));
            if (s > 0) gather_mem4(rw, NEARB(s, rep, pm), ml, lane, w - 4, tka, s, budget);
            LBAR();
            v4u v[8];
            qk_project_h(Ba, Bb, ma, zw, idx, [&](int t) {
                if (t < 4) { v[2 * t] = LD16(rw, va + (t & 1) * 2048, (t >> 1) * 4096); v[2 * t + 1] = LD16(rw, va + 1024 + (t & 1) * 2048, (t >> 1) * 4096); }
                if (t == 4) { ST16(zv[0], rw, zs); ST16(zv[1], rw, zs + 1024); }
                if (t == 5) { ST16(ff, rw, a0); ST4(0xFFFFFFFFu, rw, a1); }
            });
            LBAR();
#pragma unroll
            for (int k = 0; k < 2; ++k) {
                const v2u lo = *(LAS v2u*)(zhr + k * 1024), hi = *(LAS v2u*)(zhr + k * 1024 + 128);
                zv[k] = v4u{lo.x, lo.y, hi.x, hi.y};
            }
            for (;;) {
                if (go) {
#pragma unroll
                    for (int k = 0; k < 8; ++k) *(LAS v4u*)(ring + (staged & 3) * 16384 + hw * 8192 + k * 1024 + lane * 16) = v[k];
                    asm volatile("s_waitcnt lgkmcnt(0)" ::: "memory");
                    ++staged;
                    prod[hw] = staged;
                }
                if (staged > s + 1 || staged > NSEG - 2) break;
                const int ow = VOWN(staged);
                int& vd = ow ? vdone1 : vdone0;
                if (vd < staged) vd = wait_flags(rw, FL_V + ow, 2, NH, staged <= s ? staged : -1, lane, budget, true);
                if (vd < staged) break;
#pragma unroll
                for (int k = 0; k < 8; ++k) v[k] = LD16(rw, vo + (k & 3) * 1024, staged * (NR * NH * 1024) + (k >> 2) * 4096);
                go = true;
            }
            DRAIN();
        }
        {
            int l2 = (int)__builtin_amdgcn_mbcnt_hi(~0u, __builtin_amdgcn_mbcnt_lo(~0u, 0u));
            asm volatile("" : "+v"(l2));
            const int zf = OFF_ZH + r * 4096 + hw * 2048 + (NSEG - 1) * (NR * NH * 256) + l2 * 16;
            ST16(zv[0], rw, zf); ST16(zv[1], rw, zf + 1024);
        }
    } else {
        TK tk; tk_init(tk);
        for (int s = 0; s < NSEG; ++s) {
            if (s > 0) {
                gather_mem4(rw, NEARB(s, rep, pm), ml, lane, w - 4, tka, s, budget);
            }
            BC2 bc = {0u, 0u};
            if (w == 7) bc = beacon_issue(rw, FARB(s, rep, pm), lane);
            LBAR();
            qk_project(Ba, Bb, ma, zw, idx);
            LBAR();
            if (w == 7) {
                if (s < NSEG - 1) {
                    if (s > 0) { tk_T0(tk, beacon_finish(bc, rw, FARB(s, rep, pm), lane, budget), a); if (tk.okA) tk_post(tka, s + 1, tk_specA(tk, a), lane); }
                    tk_post(tkr, s, tk.XR, lane);
                    wait_until(tk.XR + 8);
                    tk_Tp(tk, beacon_max<NH>(rw, FARB(s, rep, pm), BC_QK, lane, budget), a);
                    tk_post(tka, s + 1, tk.XA, lane);
                    tk_post(tkr, s + 1, tk_specR(tk, a), lane);
                }
            } else {
                if (w == 4 && s >= 2 && lane == 0) ST4((u32)(s - 2), rw, (FL_Z + r) * 128);
                __builtin_amdgcn_s_sleep(14);
            }
        }
    }
    DRAIN();
    LBAR();
    if (tid == 0) ST4((u32)(NSEG - 1), rw, (FL_Z + r) * 128);
    if (budget < 0 && lane == 0) ST4(3u, rw, FL_ERR * 128);
}

__device__ __forceinline__ void cexp_l(float lr, float li, float l, float& er, float& ei) {
    const float mag = __builtin_amdgcn_exp2f(lr * l);
    const float ph = li * l;
    const float pe = fmaf(li, l, -ph);
    const float fr = (ph - floorf(ph)) + pe;
    er = mag * __builtin_amdgcn_cosf(fr);
    ei = mag * __builtin_amdgcn_sinf(fr);
}
constexpr int CV_W = 8 * 260 * 16, CV_U = 96 * 80, CV_CH = CV_W + CV_U;
__device__ __forceinline__ void s4_unit(const Args& a, lptr lds, rsrc_t rw, int unit) {
    const int tid = threadIdx.x, lane = tid & 63, w = __builtin_amdgcn_readfirstlane(tid >> 6);
    const int d0 = unit * 4;
    v2u uv[4];
#pragma unroll
    for (int k = 0; k < 4; ++k) uv[k] = *(const v2u*)(a.u + (size_t)(2 * (tid + 512 * (k >> 1)) + (k & 1)) * DM + d0);
    {
        const int c = tid >> 7, nn = tid & 127, n = (d0 + c) * 128 + nn;
        const float dt = rbf(expf(bf2f(a.logdt[d0 + c])));
        const float lr = dt * bf2f(a.Are[n]) * 1.4426950408889634f, li = dt * bf2f(a.Aim[n]) * 0.15915494309189535f;
        const float br = dt * bf2f(a.Bre[n]), bi = dt * bf2f(a.Bim[n]);
        const float cr = bf2f(a.Cre[n]), ci = bf2f(a.Cim[n]);
        LAS f32x2* T = (LAS f32x2*)(lds + c * CV_CH) + nn;
        float er, ei, pr, pi;
        cexp_l(lr, li, 1.0f, er, ei); pr = er; pi = ei;
#pragma unroll
        for (int o = 0; o < 7; ++o) { T[o * 130] = f32x2{pr, pi}; const float t = pr * er - pi * ei; pi = pr * ei + pi * er; pr = t; }
#pragma unroll
        for (int q4 = 0; q4 < 4; ++q4) {
            if (q4 == 0) cexp_l(lr, li, 128.0f, er, ei);
            cexp_l(lr, li, (float)(128 + 512 * q4), pr, pi);
#pragma unroll
            for (int o = 0; o < 4; ++o) { T[(7 + 4 * q4 + o) * 130] = f32x2{pr, pi}; const float t = pr * er - pi * ei; pi = pr * ei + pi * er; pr = t; }
        }
        const float cbr = cr * br - ci * bi, cbi = cr * bi + ci * br;
        cexp_l(lr, li, 8.0f, er, ei);
#pragma unroll
        for (int q4 = 0; q4 < 4; ++q4) {
            float sr, si;
            cexp_l(lr, li, (float)(32 * q4), sr, si);
            pr = cbr * sr - cbi * si; pi = cbr * si + cbi * sr;
#pragma unroll
            for (int o = 0; o < 4; ++o) { T[(23 + 4 * q4 + o) * 130] = f32x2{pr, pi}; const float t = pr * er - pi * ei; pi = pr * ei + pi * er; pr = t; }
        }
    }
    __syncthreads();
    f32x4 kr[3];
    {
        const int x = lane & 15, g = lane >> 4, c = w & 3, nh = w >> 2;
        const int ah0 = x >> 3, o0 = x & 7;
        const int ah1 = x < 8 ? 2 + (x >> 2) : 4 + ((x - 8) >> 1), o1 = x < 8 ? 2 * (x & 3) : 4 * (x & 1);
        const int ah2 = x < 8 ? 8 + x : 16;
        LAS f32x2* T = (LAS f32x2*)(lds + c * CV_CH) + nh * 64 + g;
        LAS f32x2* Ta0 = T + 7 * 130; LAS f32x2* To0 = T + (o0 ? o0 - 1 : 0) * 130;
        LAS f32x2* Ta1 = T + (6 + ah1) * 130; LAS f32x2* To1 = T + (o1 ? o1 - 1 : 0) * 130;
        LAS f32x2* Ta2 = T + (6 + ah2) * 130; LAS f32x2* Tv = T + (23 + x) * 130;
        const f32x2 one = {1.0f, 0.0f};
        f32x4 ar[3] = {}, ai[3] = {};
#pragma unroll
        for (int jj = 0; jj < 16; ++jj) {
            const f32x2 v = Tv[4 * jj], u2 = Ta2[4 * jj], a1 = Ta1[4 * jj];
            f32x2 a0v = Ta0[4 * jj], e0 = To0[4 * jj], e1 = To1[4 * jj];
            a0v = ah0 ? a0v : one; e0 = o0 ? e0 : one; e1 = o1 ? e1 : one;
            const float u0r = a0v[0] * e0[0] - a0v[1] * e0[1], u0i = a0v[0] * e0[1] + a0v[1] * e0[0];
            const float u1r = a1[0] * e1[0] - a1[1] * e1[1], u1i = a1[0] * e1[1] + a1[1] * e1[0];
            ar[2] = __builtin_amdgcn_mfma_f32_16x16x4f32(u2[0], v[0], ar[2], 0, 0, 0);
            ai[2] = __builtin_amdgcn_mfma_f32_16x16x4f32(u2[1], v[1], ai[2], 0, 0, 0);
            ar[0] = __builtin_amdgcn_mfma_f32_16x16x4f32(u0r, v[0], ar[0], 0, 0, 0);
            ai[0] = __builtin_amdgcn_mfma_f32_16x16x4f32(u0i, v[1], ai[0], 0, 0, 0);
            ar[1] = __builtin_amdgcn_mfma_f32_16x16x4f32(u1r, v[0], ar[1], 0, 0, 0);
            ai[1] = __builtin_amdgcn_mfma_f32_16x16x4f32(u1i, v[1], ai[1], 0, 0, 0);
        }
#pragma unroll
        for (int t = 0; t < 3; ++t) kr[t] = ar[t] - ai[t];
    }
    __syncthreads();
    {
        LAS float* T = (LAS float*)(lds + (w & 3) * CV_CH + 4096 + (w >> 2) * 3072);
#pragma unroll
        for (int t = 0; t < 3; ++t)
#pragma unroll
            for (int r = 0; r < 4; ++r) T[(16 * t + 4 * (lane >> 4) + r) * 16 + (lane & 15)] = kr[t][r];
    }
    __syncthreads();
#pragma unroll
    for (int k = 0; k < 8; ++k) {
        const int i = tid + 512 * k, c = i >> 10, l2 = i & 1023;
        LAS float* T = (LAS float*)(lds + c * CV_CH + 4096);
        float v[2];
#pragma unroll
        for (int e = 0; e < 2; ++e) {
            const int lb = (int)rbf((float)(2 * l2 + e));
            const int hi = lb >> 7, o = lb & 7;
            const int row = lb < 256 ? 8 * hi + o : lb < 512 ? 16 + 4 * (hi - 2) + (o >> 1) : lb < 1024 ? 24 + 2 * (hi - 4) + (o >> 2) : 32 + (hi - 8);
            const int ix = row * 16 + ((lb >> 3) & 15);
            v[e] = T[ix] + T[768 + ix];
        }
        *(LAS u32*)(lds + c * CV_CH + CV_W + 2704 + l2 * 4) = pack2(v[0], v[1]);
    }
    __syncthreads();
    for (int i = tid; i < 4 * 259; i += 512) {
        const int c = i / 259, zz = i - c * 259, z = zz - 3;
        lptr ch = lds + c * CV_CH;
        v4u lo = {0, 0, 0, 0}, hi = {0, 0, 0, 0};
        if (z >= 1) lo = *(LAS v4u*)(ch + CV_W + 2704 + (z - 1) * 16);
        if (z >= 0) hi = *(LAS v4u*)(ch + CV_W + 2704 + z * 16);
        const u32 e[8] = {lo.x, lo.y, lo.z, lo.w, hi.x, hi.y, hi.z, hi.w};
#pragma unroll
        for (int cc = 0; cc < 8; ++cc) {
            v4u o;
#pragma unroll
            for (int k = 0; k < 4; ++k) {
                const int t = 8 + cc - 2 * k;
                if (t & 1) o[k] = (e[t >> 1] >> 16) | (e[t >> 1] << 16);
                else o[k] = (e[t >> 1] & 0xffffu) | (e[(t >> 1) - 1] & 0xffff0000u);
            }
            *(LAS v4u*)(ch + (cc * 260 + zz) * 16) = o;
        }
    }
    __syncthreads();
    for (int i = tid; i < 4 * 160; i += 512) *(LAS v4u*)(lds + (i / 160) * CV_CH + CV_W + (i % 160) * 16) = v4u{0, 0, 0, 0};
#pragma unroll
    for (int k2 = 0; k2 < 2; ++k2) {
        const int l = 2 * (tid + 512 * k2);
        const int o = CV_W + (32 + (l >> 5)) * 80 + (l & 31) * 2;
        const v2u p = uv[2 * k2], q = uv[2 * k2 + 1];
        *(LAS u32*)(lds + 0 * CV_CH + o) = (p.x & 0xffffu) | (q.x << 16);
        *(LAS u32*)(lds + 1 * CV_CH + o) = (p.x >> 16) | (q.x & 0xffff0000u);
        *(LAS u32*)(lds + 2 * CV_CH + o) = (p.y & 0xffffu) | (q.y << 16);
        *(LAS u32*)(lds + 3 * CV_CH + o) = (p.y >> 16) | (q.y & 0xffff0000u);
    }
    __syncthreads();
    const int c = w & 3, par = w >> 2, b = lane & 31, hh = lane >> 5;
    lptr ch = lds + c * CV_CH;
    lptr wa = ch + ((b & 7) * 260 + (b >> 3) - hh + 3) * 16;
    lptr ub = ch + CV_W + (32 + b) * 80 + hh * 16;
    f32x16 y0 = {}, y1 = {};
    wa += par * 64; ub -= par * 80;
    {
        bf16x8 fa[2][2], fb[2][2], fc[2][2];
#pragma unroll
        for (int ks = 0; ks < 2; ++ks) { fa[0][ks] = lds8(wa - ks * 32); fb[0][ks] = lds8(ub + ks * 32); fc[0][ks] = lds8(ub + 2560 + ks * 32); }
#pragma unroll
        for (int i = 0; i < 32; ++i) {
            const int cur = i & 1, nx = cur ^ 1;
            if (i + 1 < 32) {
#pragma unroll
                for (int ks = 0; ks < 2; ++ks) {
                    fa[nx][ks] = lds8(wa + (i + 1) * 128 - ks * 32);
                    if (i + 1 < 16) fb[nx][ks] = lds8(ub - (i + 1) * 160 + ks * 32);
                    fc[nx][ks] = lds8(ub + 2560 - (i + 1) * 160 + ks * 32);
                }
            }
            __builtin_amdgcn_sched_barrier(0);
#pragma unroll
            for (int ks = 0; ks < 2; ++ks) {
                if (i < 16) y0 = MFMA32(fa[cur][ks], fb[cur][ks], y0);
                y1 = MFMA32(fa[cur][ks], fc[cur][ks], y1);
            }
            __builtin_amdgcn_sched_barrier(0);
        }
    }
    __syncthreads();
    {
        LAS f32x4* X = (LAS f32x4*)(ch + par * 4096);
        const f32x16 src = par ? y0 : y1;
#pragma unroll
        for (int k = 0; k < 4; ++k) X[k * 64 + lane] = f32x4{src[4 * k], src[4 * k + 1], src[4 * k + 2], src[4 * k + 3]};
    }
    __syncthreads();
    {
        LAS f32x4* X = (LAS f32x4*)(ch + (1 - par) * 4096);
        lptr Y = ch + 8192 + (32 * par + b) * 72 + hh * 8;
        const f32x16 own = par ? y1 : y0;
#pragma unroll
        for (int k = 0; k < 4; ++k) {
            const f32x4 o = X[k * 64 + lane];
            v2u yv = {pack2(own[4 * k] + o[0], own[4 * k + 1] + o[1]), pack2(own[4 * k + 2] + o[2], own[4 * k + 3] + o[3])};
            *(LAS v2u*)(Y + k * 16) = yv;
        }
    }
    __syncthreads();
    const v2u dk = *(const v2u*)(a.dskip + d0);
#pragma unroll
    for (int k2 = 0; k2 < 2; ++k2) {
        const int i = tid + 512 * k2;
        lptr yp = lds + 8192 + (i >> 4) * 72 + (i & 15) * 4;
        const u32 ya = *(LAS u32*)(yp), yb = *(LAS u32*)(yp + CV_CH), yc = *(LAS u32*)(yp + 2 * CV_CH), yd = *(LAS u32*)(yp + 3 * CV_CH);
        const v2u p = uv[2 * k2], q = uv[2 * k2 + 1];
        v4u o;
        o.x = pack2(bflo(ya) + rbf(bflo(p.x) * bflo(dk.x)), bflo(yb) + rbf(bfhi(p.x) * bfhi(dk.x)));
        o.y = pack2(bflo(yc) + rbf(bflo(p.y) * bflo(dk.y)), bflo(yd) + rbf(bfhi(p.y) * bfhi(dk.y)));
        o.z = pack2(bfhi(ya) + rbf(bflo(q.x) * bflo(dk.x)), bfhi(yb) + rbf(bfhi(q.x) * bfhi(dk.x)));
        o.w = pack2(bfhi(yc) + rbf(bflo(q.y) * bflo(dk.y)), bfhi(yd) + rbf(bfhi(q.y) * bfhi(dk.y)));
        ST16(o, rw, OFF_A4 + unit * 16384 + i * 16);
    }
    __syncthreads();
}
__device__ __forceinline__ void role_s4(const Args& a, lptr lds, rsrc_t rw, int idx) {
    if (idx < 32) {
        const int tid = threadIdx.x, t = idx * 64 + (tid >> 3), j = tid & 7;
        float acc = 0.f;
#pragma unroll
        for (int k = 0; k < 9; ++k) {
            const v4u xv = *(const v4u*)(a.u + (size_t)t * DM + (k * 8 + j) * 8), gv = *(const v4u*)(a.gatew + (k * 8 + j) * 8);
#pragma unroll
            for (int e = 0; e < 4; ++e) acc += rbf(bflo(xv[e]) * bflo(gv[e])) + rbf(bfhi(xv[e]) * bfhi(gv[e]));
        }
        acc += __shfl_xor(acc, 1); acc += __shfl_xor(acc, 2); acc += __shfl_xor(acc, 4);
        acc = rbf(acc);
        if (j == 0) ST4(__float_as_uint(rbf(1.0f / (1.0f + __expf(-acc)))), rw, OFF_G + t * 4);
    }
#pragma unroll 1
    for (int unit = idx; unit < DM / 4; unit += NS4) s4_unit(a, lds, rw, unit);
    DRAIN();
    __syncthreads();
    if (threadIdx.x == 0) ST4(1u, rw, (FL_S + idx) * 128);
}

__device__ __forceinline__ void role_post(const Args& a, lptr lds, rsrc_t rw) {
    const int tid = threadIdx.x, lane = tid & 63, w = __builtin_amdgcn_readfirstlane(tid >> 6);
    const int i16 = lane & 15, g = lane >> 4;
    int budget = SPIN_MAX, vdone0 = -1, vdone1 = -1;
    volatile LAS int* U = (volatile LAS int*)(lds + 81920);
    wait_flags(rw, FL_S, 1, 64, 1, lane, budget);
    wait_flags(rw, FL_S + 64, 1, NS4 - 64, 1, lane, budget);
    constexpr int NFULL = 62 * 6, NUNIT = NFULL + 24;
#pragma unroll 1
    for (;;) {
        if (tid == 0) *U = 1 + (int)__hip_atomic_fetch_add((u32*)(a.ws + FL_Q * 128), 1u, __ATOMIC_RELAXED, __HIP_MEMORY_SCOPE_AGENT);
        __syncthreads();
        const int unit = __builtin_amdgcn_readfirstlane(*U);
        if (unit >= NUNIT || budget < 0) break;
        const bool half = unit >= NFULL;
        const int uu = half ? NFULL + ((unit - NFULL) >> 1) : unit, q0 = half ? (unit - NFULL) & 1 : 0;
        const int s = uu / 6, r = (uu - s * 6) * 6 + (w < 6 ? w : 0);
        bf16x8 b[32];
        v2u av[2]; float gt[2];
        if (w < 6) {
            if (VOWN(s)) { if (vdone1 < s) vdone1 = wait_flags(rw, FL_V + 1, 2, NH, s, lane, budget); }
            else { if (vdone0 < s) vdone0 = wait_flags(rw, FL_V, 2, NH, s, lane, budget); }
            const int vo = OFF_VOX + (s * NR + r) * (NH * 1024) + ((i16 >> 1) & 1) * 16384 + ((g & 1) * 16 + (g >> 1) * 8 + 2 * (i16 >> 2) + (i16 & 1)) * 16;
#pragma unroll
            for (int k = 0; k < 32; ++k) b[k] = as8(LD16(rw, vo + (k & 7) * 512, (k >> 3) * 4096));
#pragma unroll
            for (int qt = 0; qt < 2; ++qt) {
                av[qt] = LD8(rw, OFF_A4 + (4 * r + g) * 16384 + (32 * s + 16 * (qt ^ q0) + i16) * 8, 0);
                gt[qt] = __uint_as_float(LD4(rw, OFF_G + (32 * s + 16 * (qt ^ q0) + i16) * 4, 0));
            }
        }
        v4u z[8];
        const int zo = OFF_ZH + (s * NR + r) * (NH * 256) + ((i16 >> 1) & 1) * 4096 + g * 128 + (2 * (i16 >> 2) + (i16 & 1)) * 16;
        if (half) {
            wait_flags(rw, FL_Z + 2 * r, 1, 2, s, lane, budget, true);
#pragma unroll
            for (int k = 0; k < 8; ++k) z[k] = LD16(rw, zo + k * 512, 0);
            wait_flags(rw, FL_P + (s & 3), 4, 16, s, lane, budget, true);
            v4u pv[5];
#pragma unroll
            for (int k = 0; k < 5; ++k) pv[k] = LD16(rw, OFF_PX + s * 81920 + q0 * 40960 + tid * 16, k * 8192);
#pragma unroll
            for (int k = 0; k < 5; ++k) *(LAS v4u*)(lds + k * 8192 + tid * 16) = pv[k];
        } else {
            wait_flags(rw, FL_P + (s & 3), 4, 16, s, lane, budget);
            v4u pv[10];
#pragma unroll
            for (int k = 0; k < 10; ++k) pv[k] = LD16(rw, OFF_PX + s * 81920 + tid * 16, k * 8192);
            wait_flags(rw, FL_Z + 2 * r, 1, 2, s, lane, budget);
#pragma unroll
            for (int k = 0; k < 8; ++k) z[k] = LD16(rw, zo + k * 512, 0);
            MB();
#pragma unroll
            for (int k = 0; k < 10; ++k) *(LAS v4u*)(lds + k * 8192 + tid * 16) = pv[k];
        }
        __syncthreads();
        if (w < 6) {
#pragma unroll 1
            for (int qt = 0; qt < (half ? 1 : 2); ++qt) {
                f32x4 d = {}, e = {};
                lptr pl = lds + qt * 40960 + lane * 16;
                bf16x8 f[6];
#pragma unroll
                for (int i = 0; i < 6; ++i) f[i] = lds8(pl + i * 1024);
                __builtin_amdgcn_sched_barrier(0);
#pragma unroll
                for (int k = 0; k < 40; ++k) {
                    const bf16x8 v = k < 32 ? b[k < 32 ? k : 0] : as8(z[k < 32 ? 0 : k - 32]);
                    if (k & 1) e = MFMA16(v, f[k % 6], e); else d = MFMA16(v, f[k % 6], d);
                    if (k + 6 < 40) f[k % 6] = lds8(pl + (k + 6) * 1024);
                    __builtin_amdgcn_sched_barrier(0);
                }
                d += e;
                const float gq = qt ? gt[1] : gt[0], og = rbf(1.0f - gq);
                const v2u aq = qt ? av[1] : av[0];
                const float r0 = rbf(gq * bflo(aq.x)) + rbf(og * rbf(d[0])), r1 = rbf(gq * bfhi(aq.x)) + rbf(og * rbf(d[1]));
                const float r2 = rbf(gq * bflo(aq.y)) + rbf(og * rbf(d[2])), r3 = rbf(gq * bfhi(aq.y)) + rbf(og * rbf(d[3]));
                v2u ov = {pack2(r0, r1), pack2(r2, r3)};
                *(v2u*)(a.out + (size_t)(32 * s + 16 * (qt ^ q0) + i16) * DM + 16 * r + 4 * g) = ov;
            }
        }
        if (half) break;
        LBAR();
    }
    if (budget < 0 && lane == 0) ST4(6u, rw, FL_ERR * 128);
}

__global__ void __launch_bounds__(512, 2) k_all(Args a) {
    extern __shared__ __attribute__((aligned(16))) char smem[];
    lptr lds = (lptr)smem;
    a.ws += (0u - (u32)(uintptr_t)a.ws) & 0x7FFFu;
    const rsrc_t rw = __builtin_amdgcn_make_buffer_rsrc(a.ws, 0, WS_BYTES, 0x00020000);
    if (threadIdx.x == 0) {
        const int x = (int)(__builtin_amdgcn_s_getreg(20 | (3 << 11)) & 7u);
        int role = 0;
        for (int k = 0; k < 8; ++k) {
            const int gq = (x + k) & 7;
            const int t = 1 + (int)__hip_atomic_fetch_add((u32*)(a.ws + (FL_T + gq) * 128), 1u, __ATOMIC_RELAXED, __HIP_MEMORY_SCOPE_AGENT);
            if (t < 32) { role = gq + 8 * (31 - t); break; }
        }
        *(volatile LAS int*)lds = role;
    }
    __syncthreads();
    const int b = __builtin_amdgcn_readfirstlane(*(volatile LAS int*)lds), xg = b & 7, slot = b >> 3;
    __syncthreads();

    if (slot == 8 && xg < 4) role_probe(rw, xg);
    if (slot >= 20) { role_wvo(a, lds, rw, (xg & 3) * 24 + (xg < 4 ? 12 : 0) + slot - 20); __syncthreads(); }
    if ((xg < 4 && slot >= 26) || (xg >= 4 && slot >= 20)) {
        const int idx = xg < 4 ? 48 + (slot - 26) * 4 + xg : (31 - slot) * 4 + (xg - 4);
        role_s4(a, lds, rw, idx);
        __syncthreads();
    }
    bool post = false;
    if (xg < 4) {
        if (slot < 8) role_qk(a, lds, rw, xg * 8 + slot, xg);
        else if (slot < 26) role_rz(a, lds, rw, xg * 18 + slot - 8, xg);
    } else {
        const int j = xg - 4;
        post = true;
        if (slot < 16) { role_vvo(a, lds, rw, j * 8 + (slot >> 1), slot & 1); post = slot < NJOIN; }
        else if (slot < 20) role_k2(a, lds, rw, j * 4 + slot - 16);
        else if (slot < 28) { role_q2p(a, lds, rw, j * 4 + ((slot - 20) >> 1), slot & 1); post = false; }
    }
    if (post) { __syncthreads(); role_post(a, lds, rw); }
}

extern "C" void kernel_launch(void* const* d_in, const int* in_sizes, int n_in, void* d_out, int out_size, void* d_ws, size_t ws_size, hipStream_t stream) {
    Args a;
    a.u = (const u16*)d_in[0]; a.Are = (const u16*)d_in[1]; a.Aim = (const u16*)d_in[2]; a.Bre = (const u16*)d_in[3]; a.Bim = (const u16*)d_in[4];
    a.Cre = (const u16*)d_in[5]; a.Cim = (const u16*)d_in[6]; a.logdt = (const u16*)d_in[7]; a.dskip = (const u16*)d_in[8];
    a.mem0 = (const u16*)d_in[10]; a.Wq = (const u16*)d_in[11]; a.Wk = (const u16*)d_in[12]; a.Wv = (const u16*)d_in[13]; a.Wo = (const u16*)d_in[14];
    a.gatew = (const u16*)d_in[16]; a.out = (u16*)d_out; a.ws = (char*)d_ws + ((ws_size - (size_t)WS_BYTES - 32768) & ~(size_t)0x1FFFFF);
    const int LDS_ALL = 160 * 1024;
    (void)hipFuncSetAttribute((const void*)k_all, hipFuncAttributeMaxDynamicSharedMemorySize, LDS_ALL);
    (void)hipMemsetAsync(a.ws, 0xFF, INIT_BYTES + 32768, stream);
    hipLaunchKernelGGL(k_all, dim3(256), dim3(512), LDS_ALL, stream, a);
}
```

```cpp
#include <hip/hip_runtime.h>
#include <hip/hip_bf16.h>
#include <stdint.h>

typedef unsigned short u16;
typedef unsigned int u32;
typedef u32 v4u __attribute__((ext_vector_type(4)));
typedef u32 v2u __attribute__((ext_vector_type(2)));
typedef float f32x4 __attribute__((ext_vector_type(4)));
typedef float f32x2 __attribute__((ext_vector_type(2)));
typedef float f32x16 __attribute__((ext_vector_type(16)));
typedef __bf16 bf16x2 __attribute__((ext_vector_type(2)));
typedef __bf16 bf16x8 __attribute__((ext_vector_type(8)));
typedef __bf16 bf16x16 __attribute__((ext_vector_type(16)));
typedef short s16x4 __attribute__((ext_vector_type(4)));
#define LAS __attribute__((address_space(3)))
typedef LAS char* lptr;
typedef __amdgpu_buffer_rsrc_t rsrc_t;

constexpr int SEQ = 2048, DM = 576, NH = 32, DH = 128, NSEG = 64, NR = 36, NR8 = 72, NREP = 4;
constexpr int MEMB = 9216, BC_QK = 80;
constexpr int FL0 = 16384;
constexpr int FL_K = FL0, FL_V = FL0 + 16, FL_M = FL0 + 80, FL_P = FL0 + 112, FL_S = FL0 + 176, FL_Q = FL0 + 254, FL_ERR = FL0 + 255, FL_Z = FL0 + 256, FL_W = FL0 + 328, FL_T = FL0 + 424, FL_PAR = FL0 + 432, NS4 = 72;
constexpr int N_MEMA = 0, N_PX = 32768, N_PMM = 98304, N_MEMB = 98304 + 4096, N_PROBE = 98304 + 6144;
constexpr int INIT_BYTES = 16 * 131072 + 65536;
constexpr int OFF_ZH = INIT_BYTES;
constexpr int OFF_KX = OFF_ZH + NSEG * NR * NH * 256;
constexpr int OFF_VOX = OFF_KX + NH * SEQ * DH * 2;
constexpr int OFF_KM = OFF_VOX + NSEG * NR * NH * 1024;
constexpr int OFF_PX = OFF_KM + NSEG * NH * 2048;
constexpr int OFF_A4 = OFF_PX + NSEG * 81920;
constexpr int OFF_G = OFF_A4 + SEQ * DM * 2;
constexpr int OFF_WVO = OFF_G + SEQ * 4;
constexpr int WS_BYTES = OFF_WVO + NR8 * 294912;
constexpr int TK_LA = 8, TK_LR = 8, TK_TOL = 8, TK_GAP = 30;
constexpr int NJOIN = 10;

struct Args {
    const u16* u; const u16* Are; const u16* Aim; const u16* Bre; const u16* Bim; const u16* Cre; const u16* Cim;
    const u16* logdt; const u16* dskip; const u16* mem0; const u16* Wq; const u16* Wk; const u16* Wv; const u16* Wo;
    const u16* gatew; u16* out; char* ws;
};

__device__ __forceinline__ float bf2f(u16 v) { return __uint_as_float((u32)v << 16); }
__device__ __forceinline__ float bflo(u32 v) { return __uint_as_float(v << 16); }
__device__ __forceinline__ float bfhi(u32 v) { return __uint_as_float(v & 0xffff0000u); }
__device__ __forceinline__ u32 pack2(float lo, float hi) { f32x2 f = {lo, hi}; bf16x2 b = __builtin_convertvector(f, bf16x2); return __builtin_bit_cast(u32, b); }
__device__ __forceinline__ float rbf(float x) { return bflo(pack2(x, x)); }
__device__ __forceinline__ u32 nos(u32 v) { return min(v, 0xFFFFFFFEu); }
__device__ __forceinline__ v2u trd(lptr p) { return __builtin_bit_cast(v2u, __builtin_amdgcn_ds_read_tr16_b64_v4i16((LAS s16x4*)p)); }
__device__ __forceinline__ bf16x8 as8(v4u v) { return __builtin_bit_cast(bf16x8, v); }
__device__ __forceinline__ bf16x8 as8(v2u a, v2u b) { v4u v = {a.x, a.y, b.x, b.y}; return __builtin_bit_cast(bf16x8, v); }
__device__ __forceinline__ v4u asu(bf16x8 v) { return __builtin_bit_cast(v4u, v); }
__device__ __forceinline__ bf16x16 cat16(bf16x8 a, bf16x8 b) { return __builtin_shufflevector(a, b, 0, 1, 2, 3, 4, 5, 6, 7, 8, 9, 10, 11, 12, 13, 14, 15); }
__device__ __forceinline__ bf16x8 lds8(lptr p) { return *(LAS bf16x8*)p; }
__device__ __forceinline__ u32 bad4(v4u v) { return max(max(v.x, v.y), max(v.z, v.w)); }
#define NEARB(s, rep, pm) (((((s) & 3) * 4 + (rep)) << 17) + (int)(((((u32)(pm) >> (((s) & 3) * 4 + (rep))) & 0x10001u) * 0x20004000u >> 16) & 0x6000u))
#define FARB(s, rep, pm) ((NEARB(s, rep, pm) ^ 16384) + (((s) & 4) << 14))
#define BCO(slot) (((slot) & 63) * 128 + ((slot) >> 6) * 32768)
#define LBAR() do { asm volatile("s_waitcnt lgkmcnt(0)" ::: "memory"); __builtin_amdgcn_s_barrier(); asm volatile("" ::: "memory"); } while (0)
#define MB() asm volatile("" ::: "memory")
#define DRAIN() asm volatile("s_waitcnt vmcnt(0)" ::: "memory")
#define PIN4(x) asm volatile("" : "+v"(x))
#define MFMA16(a, b, c) __builtin_amdgcn_mfma_f32_16x16x32_bf16((a), (b), (c), 0, 0, 0)
#define MFMA32(a, b, c) __builtin_amdgcn_mfma_f32_32x32x16_bf16((a), (b), (c), 0, 0, 0)
#define SMF(a, b, c, idx) __builtin_amdgcn_smfmac_f32_16x16x64_bf16((a), (b), (c), (idx), 0, 0)
#define LD16(r, vo, so) __builtin_amdgcn_raw_buffer_load_b128((r), (vo), (so), 16)
#define LD8(r, vo, so) __builtin_amdgcn_raw_buffer_load_b64((r), (vo), (so), 16)
#define LD4(r, vo, so) __builtin_amdgcn_raw_buffer_load_b32((r), (vo), (so), 16)
#define ST16(v, r, vo) __builtin_amdgcn_raw_buffer_store_b128((v), (r), (vo), 0, 16)
#define ST8(v, r, vo) __builtin_amdgcn_raw_buffer_store_b64((v), (r), (vo), 0, 16)
#define ST4(v, r, vo) __builtin_amdgcn_raw_buffer_store_b32((v), (r), (vo), 0, 16)

__device__ __forceinline__ float swap32_sum(float a, float b) {
    auto r = __builtin_amdgcn_permlane32_swap(__float_as_uint(a), __float_as_uint(b), false, false);
    return __uint_as_float(r[0]) + __uint_as_float(r[1]); }
__device__ __forceinline__ float xmax(float m) {
    auto r = __builtin_amdgcn_permlane16_swap(__float_as_uint(m), __float_as_uint(m), false, false);
    m = fmaxf(__uint_as_float(r[0]), __uint_as_float(r[1]));
    auto q = __builtin_amdgcn_permlane32_swap(__float_as_uint(m), __float_as_uint(m), false, false);
    return fmaxf(__uint_as_float(q[0]), __uint_as_float(q[1])); }
__device__ __forceinline__ float xsum(float m) {
    auto r = __builtin_amdgcn_permlane16_swap(__float_as_uint(m), __float_as_uint(m), false, false);
    m = __uint_as_float(r[0]) + __uint_as_float(r[1]);
    auto q = __builtin_amdgcn_permlane32_swap(__float_as_uint(m), __float_as_uint(m), false, false);
    return __uint_as_float(q[0]) + __uint_as_float(q[1]); }
__device__ __forceinline__ float vmax(float a, float b) { float r; asm("v_max_f32 %0, %1, %2" : "=v"(r) : "v"(a), "v"(b)); return r; }
__device__ __forceinline__ float vmax3(float a, float b, float c) { float r; asm("v_max3_f32 %0, %1, %2, %3" : "=v"(r) : "v"(a), "v"(b), "v"(c)); return r; }
__device__ __forceinline__ u32 ror8(u32 v) { return (u32)__builtin_amdgcn_update_dpp(0, (int)v, 0x128, 0xf, 0xf, true); }
__device__ __forceinline__ float ror8f(float v) { return __uint_as_float(ror8(__float_as_uint(v))); }
__device__ __forceinline__ int wave_max(int d) {
    d = max(d, __builtin_amdgcn_update_dpp(d, d, 0x128, 0xf, 0xf, false));
    d = max(d, __builtin_amdgcn_update_dpp(d, d, 0x124, 0xf, 0xf, false));
    d = max(d, __builtin_amdgcn_update_dpp(d, d, 0x122, 0xf, 0xf, false));
    d = max(d, __builtin_amdgcn_update_dpp(d, d, 0x121, 0xf, 0xf, false));
    return max(max(__builtin_amdgcn_readlane(d, 0), __builtin_amdgcn_readlane(d, 16)), max(__builtin_amdgcn_readlane(d, 32), __builtin_amdgcn_readlane(d, 48)));
}

constexpr int SPIN_MAX = 2000000;
__device__ __forceinline__ int wait_flags(rsrc_t rw, int line0, int stride, int n, int need, int lane, int& budget, bool fast = false) {
    const int vo = (line0 + (lane < n ? lane : 0) * stride) * 128;
    int mn;
    for (;;) {
        const int v = (int)LD4(rw, vo, 0);
        MB();
        mn = -wave_max(-v);
        if (mn >= need) break;
        if (--budget < 0) break;
        if (fast) __builtin_amdgcn_s_sleep(2); else __builtin_amdgcn_s_sleep(12);
    }
    return mn;
}

template <int NRW, int CW>
__device__ __forceinline__ void rows_ld(v4u (&v)[NRW * CW / 512], const u16* g, int gs, int tid) {
#pragma unroll
    for (int j = 0; j < NRW * CW / 512; ++j) { const int i = tid + 512 * j, row = i / CW, c = i % CW; v[j] = *(const v4u*)(g + (size_t)row * gs + c * 8); }
}
template <int NRW, int CW>
__device__ __forceinline__ void rows_st(const v4u (&v)[NRW * CW / 512], lptr lds, int ls, int tid) {
#pragma unroll
    for (int j = 0; j < NRW * CW / 512; ++j) { const int i = tid + 512 * j, row = i / CW, c = i % CW; *(LAS v4u*)(lds + row * ls + c * 16) = v[j]; }
}
__device__ __forceinline__ bf16x8 frag16(lptr base, int ls, int lane) {
    const int i16 = lane & 15, bg = lane >> 4, q = i16 >> 2, p = i16 & 3;
    lptr a = base + (8 * bg + q) * ls + p * 8;
    return as8(trd(a), trd(a + 4 * ls));
}
__device__ __forceinline__ bf16x8 frag32p(lptr base, int ls, int lane) {
    const int i16 = lane & 15, G = lane >> 4, q = i16 >> 2, p = i16 & 3;
    lptr a = base + (8 * (G >> 1) + q) * ls + (16 * (p & 1) + 8 * (G & 1) + 4 * (p >> 1)) * 2;
    return as8(trd(a), trd(a + 4 * ls));
}

constexpr int XS = 1168;
__device__ __forceinline__ void xload(v4u (&st)[9], const u16* x, int tok0, int tid) {
#pragma unroll
    for (int j = 0; j < 9; ++j) st[j] = *(const v4u*)(x + (size_t)tok0 * DM + (tid + 512 * j) * 8);
}
__device__ __forceinline__ void xstore(const v4u (&st)[9], lptr xl, int tid) {
#pragma unroll
    for (int j = 0; j < 9; ++j) { const int i = tid + 512 * j; *(LAS v4u*)(xl + i * 16 + (i / 72) * 16) = st[j]; }
}
__device__ __forceinline__ void load_w2(bf16x8 (&A)[36], const u16* W, int h0, lptr lds, int tid, int lane, int w) {
    v4u v[6][6];
#pragma unroll
    for (int c = 0; c < 6; ++c) rows_ld<96, 32>(v[c], W + (size_t)c * 96 * 4096 + h0 * DH, 4096, tid);
    __builtin_amdgcn_sched_barrier(0);
#pragma unroll
    for (int c = 0; c < 6; ++c) {
        lptr l = lds + (c & 1) * (96 * 544);
        rows_st<96, 32>(v[c], l, 544, tid);
        LBAR();
#pragma unroll
        for (int ks = 0; ks < 6; ++ks) A[c * 6 + ks] = frag32p(l + ks * 16 * 544 + w * 64, 544, lane);
        __builtin_amdgcn_sched_barrier(0);
    }
    LBAR();
}

__device__ __forceinline__ f32x16 proj_tile(const bf16x8 (&A)[36], lptr xb) {
    f32x16 acc = {};
    bf16x8 f[6];
#pragma unroll
    for (int i = 0; i < 6; ++i) f[i] = lds8(xb + i * 32);
    __builtin_amdgcn_sched_barrier(0);
#pragma unroll
    for (int ks = 0; ks < 36; ++ks) {
        acc = MFMA32(A[ks], f[ks % 6], acc);
        if (ks + 6 < 36) f[ks % 6] = lds8(xb + (ks + 6) * 32);
        __builtin_amdgcn_sched_barrier(0);
    }
    return acc;
}
__device__ __forceinline__ void role_k2(const Args& a, lptr lds, rsrc_t rw, int cu) {
    const int tid = threadIdx.x, lane = tid & 63, w = __builtin_amdgcn_readfirstlane(tid >> 6);
    v4u st[9];
    bf16x8 A[36];
    xload(st, a.u, 0, tid);
    load_w2(A, a.Wk, 2 * cu, lds, tid, lane, w);
    xstore(st, lds, tid);
    __syncthreads();
    const int o0 = OFF_KX + (2 * cu + (w >> 2)) * (NSEG * 8192) + ((w & 3) * 4 + (lane >> 5) * 2) * 512 + (lane & 31) * 16;
    for (int blk = 0; blk < 32; ++blk) {
        lptr xl = lds + (blk & 1) * (64 * XS);
        if (blk + 1 < 32) xload(st, a.u, (blk + 1) * 64, tid);
#pragma unroll
        for (int tt = 0; tt < 2; ++tt) {
            const f32x16 acc = proj_tile(A, xl + (32 * tt + (lane & 31)) * XS + (lane >> 5) * 16);
            v4u c0 = {pack2(acc[0], acc[1]), pack2(acc[2], acc[3]), pack2(acc[4], acc[5]), pack2(acc[6], acc[7])};
            v4u c1 = {pack2(acc[8], acc[9]), pack2(acc[10], acc[11]), pack2(acc[12], acc[13]), pack2(acc[14], acc[15])};
            const int o = o0 + (blk * 2 + tt) * 8192;
            ST16(c0, rw, o); ST16(c1, rw, o + 512);
            if (blk == 0 && tt == 0) { DRAIN(); LBAR(); if (tid == 0) ST4(0u, rw, (FL_K + cu) * 128); }
        }
        if (blk + 1 < 32) xstore(st, lds + ((blk + 1) & 1) * (64 * XS), tid);
        const bool full = blk < 4 || blk == 31;
        if (full) DRAIN(); else asm volatile("s_waitcnt vmcnt(4)" ::: "memory");
        LBAR();
        if (tid == 0) ST4((u32)(full ? 2 * blk + 1 : 2 * blk - 1), rw, (FL_K + cu) * 128);
    }
}

constexpr int VS = 272;
__device__ __forceinline__ void role_q2p(const Args& a, lptr lds, rsrc_t rw, int cu, int par) {
    const int tid = threadIdx.x, lane = tid & 63, w = __builtin_amdgcn_readfirstlane(tid >> 6);
    v4u st[9];
    bf16x8 A[36];
    load_w2(A, a.Wq, 2 * cu, lds, tid, lane, w);
    lptr xl = lds, ql = lds + 64 * XS;
    LAS int* cnt = (LAS int*)(lds + 64 * XS + 4 * 64 * VS);
    if (tid < 2) cnt[tid] = 0;
    xload(st, a.u, par * 64, tid);
    xstore(st, xl, tid);
    __syncthreads();
    int budget = SPIN_MAX, kdone = -1;
    const int i16 = lane & 15, g = lane >> 4;
    const int sg = w >> 2, hh = (w >> 1) & 1, qt = w & 1, h = 2 * cu + hh;
    const float c2 = 0.08838834764831845f * 1.4426950408889634f;
    for (int it = -1; it < 16; ++it) {
        const int blk = 2 * it + par;
        if (it + 1 < 16) {
            if (it + 2 < 16) xload(st, a.u, (blk + 4) * 64, tid);
            lptr qb = ql + (((it + 1) & 1) * 2 + (w >> 2)) * (64 * VS) + ((w & 3) * 32 + (lane >> 5) * 16) * 2;
#pragma unroll
            for (int tt = 0; tt < 2; ++tt) {
                const f32x16 acc = proj_tile(A, xl + (32 * tt + (lane & 31)) * XS + (lane >> 5) * 16);
                v4u c0 = {pack2(acc[0], acc[1]), pack2(acc[2], acc[3]), pack2(acc[4], acc[5]), pack2(acc[6], acc[7])};
                v4u c1 = {pack2(acc[8], acc[9]), pack2(acc[10], acc[11]), pack2(acc[12], acc[13]), pack2(acc[14], acc[15])};
                lptr qp = qb + (32 * tt + (lane & 31)) * VS;
                *(LAS v4u*)qp = c0; *(LAS v4u*)(qp + 16) = c1;
            }
            __syncthreads();
            if (it + 2 < 16) xstore(st, xl, tid);
        }
        if (it >= 0) {
            const int s = 2 * blk + sg;
            if (kdone < s) kdone = wait_flags(rw, FL_K + cu, 1, 1, s, lane, budget);
            bf16x8 k0[4], k1[4], km[4], qf[4];
            const int ko = OFF_KX + (h * NSEG + s) * 8192 + g * 512 + i16 * 16;
#pragma unroll
            for (int ks = 0; ks < 4; ++ks) { k0[ks] = as8(LD16(rw, ko, ks * 2048)); k1[ks] = as8(LD16(rw, ko + 256, ks * 2048)); }
            lptr qp = ql + ((it & 1) * 2 + hh) * (64 * VS) + (32 * sg + 16 * qt + i16) * VS + g * 16;
#pragma unroll
            for (int ks = 0; ks < 4; ++ks) qf[ks] = lds8(qp + ks * 64);
            f32x4 S0 = {}, S1 = {}, Sm = {};
#pragma unroll
            for (int ks = 0; ks < 4; ++ks) { S0 = MFMA16(k0[ks], qf[ks], S0); S1 = MFMA16(k1[ks], qf[ks], S1); }
            wait_flags(rw, FL_M + h, 1, 1, s, lane, budget, s >= 62);
            const int mo = OFF_KM + (s * NH + h) * 2048 + g * 128 + (i16 & 7) * 16;
#pragma unroll
            for (int ks = 0; ks < 4; ++ks) km[ks] = as8(LD16(rw, mo + ks * 512, 0));
#pragma unroll
            for (int ks = 0; ks < 4; ++ks) Sm = MFMA16(km[ks], qf[ks], Sm);
            const int qi = 16 * qt + i16, t0 = 4 * g;
#pragma unroll
            for (int r = 0; r < 4; ++r) {
                if (t0 + r > qi) S0[r] = -INFINITY;
                if (t0 + r + 16 > qi) S1[r] = -INFINITY;
                if (g >= 2) Sm[r] = -INFINITY;
            }
            float m = fmaxf(fmaxf(fmaxf(S0[0], S0[1]), fmaxf(S0[2], S0[3])), fmaxf(fmaxf(S1[0], S1[1]), fmaxf(S1[2], S1[3])));
            m = fmaxf(m, fmaxf(fmaxf(Sm[0], Sm[1]), fmaxf(Sm[2], Sm[3])));
            m = xmax(m);
            const float nm = -m * c2;
            float sum = 0.f;
#pragma unroll
            for (int r = 0; r < 4; ++r) {
                S0[r] = __builtin_amdgcn_exp2f(fmaf(S0[r], c2, nm)); S1[r] = __builtin_amdgcn_exp2f(fmaf(S1[r], c2, nm)); Sm[r] = __builtin_amdgcn_exp2f(fmaf(Sm[r], c2, nm));
                sum += S0[r] + S1[r] + Sm[r];
            }
            sum = xsum(sum);
            const float inv = __builtin_amdgcn_rcpf(sum);
            v4u px = {pack2(S0[0] * inv, S0[1] * inv), pack2(S0[2] * inv, S0[3] * inv), pack2(S1[0] * inv, S1[1] * inv), pack2(S1[2] * inv, S1[3] * inv)};
            const int po = OFF_PX + (s * 2 + qt) * 40960;
            ST16(px, rw, po + h * 1024 + lane * 16);
            const u32 ma = pack2(Sm[0] * inv, Sm[1] * inv), mb = pack2(Sm[2] * inv, Sm[3] * inv);
            auto ra = __builtin_amdgcn_permlane16_swap(ma, ma, false, false);
            auto rb = __builtin_amdgcn_permlane16_swap(mb, mb, false, false);
            v4u pm = {ra[0], rb[0], ra[1], rb[1]};
            if (lane < 16) ST16(pm, rw, po + 32768 + h * 256 + lane * 16);
            DRAIN();
            if (lane == 0) {
                const int old = __hip_atomic_fetch_add(cnt + sg, 1, __ATOMIC_RELAXED, __HIP_MEMORY_SCOPE_WORKGROUP);
                if ((old & 3) == 3) ST4((u32)s, rw, (FL_P + 4 * cu + 2 * par + sg) * 128);
            }
        }
        __syncthreads();
    }
    if (budget < 0 && lane == 0) ST4(5u, rw, FL_ERR * 128);
}

__device__ __forceinline__ void role_vvo(const Args& a, lptr lds, rsrc_t rw, int h, int par) {
    const int tid = threadIdx.x, lane = tid & 63, w = __builtin_amdgcn_readfirstlane(tid >> 6);
    v4u st[9];
#pragma unroll
    for (int j = 0; j < 9; ++j) { const int i = tid + 512 * j; st[j] = *(const v4u*)(a.u + (size_t)(par * 32 + (i >= 2304 ? 32 : 0)) * DM + i * 8); }
    bf16x8 Av[18], Bo[5][4];
    {
        v4u v[3][6], o[2][9];
#pragma unroll
        for (int c = 0; c < 3; ++c) rows_ld<192, 16>(v[c], a.Wv + (size_t)c * 192 * 4096 + h * DH, 4096, tid);
#pragma unroll
        for (int c = 0; c < 2; ++c) rows_ld<64, 72>(o[c], a.Wo + ((size_t)h * DH + c * 64) * DM, DM, tid);
        __builtin_amdgcn_sched_barrier(0);
#pragma unroll
        for (int c = 0; c < 3; ++c) {
            lptr l = lds + (c & 1) * 77824;
            rows_st<192, 16>(v[c], l, 288, tid);
            LBAR();
#pragma unroll
            for (int ks = 0; ks < 6; ++ks) Av[c * 6 + ks] = frag16(l + ks * 32 * 288 + w * 32, 288, lane);
            __builtin_amdgcn_sched_barrier(0);
        }
#pragma unroll
        for (int c = 0; c < 2; ++c) {
            lptr l = lds + ((c + 1) & 1) * 77824;
            rows_st<64, 72>(o[c], l, 1184, tid);
            LBAR();
#pragma unroll
            for (int j = 0; j < 5; ++j) {
                const int r = (w + 8 * j < NR) ? w + 8 * j : w;
#pragma unroll
                for (int ks = 0; ks < 2; ++ks) Bo[j][c * 2 + ks] = frag16(l + ks * 32 * 1184 + r * 32, 1184, lane);
            }
            __builtin_amdgcn_sched_barrier(0);
        }
        LBAR();
    }
    lptr xl = lds, vl = lds + 64 * XS;
    xstore(st, xl, tid);
    __syncthreads();
    const int rho = lane & 15, bg = lane >> 4;
    const int vo0 = OFF_VOX + ((2 * w + ((rho >> 1) & 1)) * 16 + (h >> 1)) * 1024 + ((2 * (h & 1) + (bg & 1)) * 16 + (bg >> 1) * 8 + 2 * (rho >> 2) + (rho & 1)) * 16;
    const int fo = (FL_V + 2 * h + par) * 128;
    auto stage1 = [&](int tt) {
        f32x4 acc = {};
        lptr xb = xl + (16 * tt + rho) * XS + bg * 16;
        bf16x8 f[6];
#pragma unroll
        for (int i = 0; i < 6; ++i) f[i] = lds8(xb + i * 64);
        __builtin_amdgcn_sched_barrier(0);
#pragma unroll
        for (int ks = 0; ks < 18; ++ks) {
            acc = MFMA16(Av[ks], f[ks % 6], acc);
            if (ks + 6 < 18) f[ks % 6] = lds8(xb + (ks + 6) * 64);
            __builtin_amdgcn_sched_barrier(0);
        }
        v2u o = {pack2(acc[0], acc[1]), pack2(acc[2], acc[3])};
        *(LAS v2u*)(vl + (16 * tt + rho) * VS + (16 * w + 4 * bg) * 2) = o;
    };
    auto stage2 = [&](int sg, int s) {
        bf16x8 Af[2][4];
#pragma unroll
        for (int half = 0; half < 2; ++half)
#pragma unroll
            for (int ks = 0; ks < 4; ++ks) Af[half][ks] = lds8(vl + (32 * sg + rho + 16 * half) * VS + ks * 64 + bg * 16);
#pragma unroll
        for (int j = 0; j < 5; ++j) {
            if (j < 4 || w < 4) {
                f32x4 d0 = {}, d1 = {};
#pragma unroll
                for (int ks = 0; ks < 4; ++ks) { d0 = MFMA16(Af[0][ks], Bo[j][ks], d0); d1 = MFMA16(Af[1][ks], Bo[j][ks], d1); }
                v4u o = {pack2(d0[0], d0[1]), pack2(d0[2], d0[3]), pack2(d1[0], d1[1]), pack2(d1[2], d1[3])};
                ST16(o, rw, vo0 + (s * NR + 8 * j) * (NH * 1024));
            }
        }
    };
    int prev = -1;
#pragma unroll 1
    for (int un = 0; un < 32; ++un) {
        const int sg = un & 1, blk = (un >> 1) * 2 + par;
        const int s = un < 2 ? par + 2 * un : 2 * blk + sg;
        if (sg == 0 && blk + 2 < 32) xload(st, a.u, (blk + 2) * 64, tid);
        stage1(2 * sg); stage1(2 * sg + 1);
        if (sg == 1) DRAIN(); else if (blk + 2 < 32) asm volatile("s_waitcnt vmcnt(9)" ::: "memory"); else DRAIN();
        LBAR();
        if (tid == 0 && prev >= 0) ST4((u32)prev, rw, fo);
        if (sg == 1 && blk + 2 < 32) xstore(st, xl, tid);
        stage2(sg, s);
        prev = s;
        if (un == 0 || un == 31) { DRAIN(); LBAR(); if (tid == 0) ST4((u32)s, rw, fo); }
        else if (sg == 1) LBAR();
    }
}
#define VOWN(s) ((s) < 4 ? ((s) & 1) : (((s) >> 1) & 1))

__device__ __forceinline__ u32 rt32() { return (u32)__builtin_amdgcn_s_memrealtime(); }
__device__ __forceinline__ void wait_until(u32 t) {
    if ((int)(t - rt32()) > 1000) return;
    while ((int)(rt32() - t) < -24) __builtin_amdgcn_s_sleep(4);
    while ((int)(rt32() - t) < 0) {}
}
struct TK { u32 XA, XR, EA, ER; int dA0, dA1, dA2, dR0, dR1, dR2; int okA, okR, gap; };
__device__ __forceinline__ int tk_est(int d0, int d1, int d2) { return max(min(d0, d1), min(max(d0, d1), d2)); }
template <int N>
__device__ __forceinline__ u32 beacon_max(rsrc_t rw, int fb, int slot0, int lane, int& budget) {
    const int vo = BCO(slot0 + (lane < N ? lane : 0)), v2 = BCO(64 + (lane & 7));
    u32 v, u = 0;
    for (;;) {
        v = LD4(rw, vo, fb);
        if (N > 64) u = LD4(rw, v2, fb);
        MB();
        if (__all(max(v, u) != 0xFFFFFFFFu)) break;
        if (--budget < 0) break;
        __builtin_amdgcn_s_sleep(1);
    }
    const u32 base = __builtin_amdgcn_readfirstlane(v);
    int d = (int)(v - base);
    if (N > 64) d = max(d, (int)(u - base));
    return base + (u32)wave_max(d);
}
struct BC2 { u32 v, u; };
__device__ __forceinline__ BC2 beacon_issue(rsrc_t rw, int fb, int lane) {
    BC2 b;
    b.v = LD4(rw, BCO(lane), fb);
    b.u = LD4(rw, BCO(64 + (lane & 7)), fb);
    MB();
    return b;
}
__device__ __forceinline__ u32 beacon_finish(BC2 b, rsrc_t rw, int fb, int lane, int& budget) {
    if (!__all(max(b.v, b.u) != 0xFFFFFFFFu)) return beacon_max<NR8>(rw, fb, 0, lane, budget);
    const u32 base = __builtin_amdgcn_readfirstlane(b.v);
    return base + (u32)wave_max(max((int)(b.v - base), (int)(b.u - base)));
}
__device__ __forceinline__ void tk_T0(TK& k, u32 T0, const Args& a) {
    if (k.okR) { k.dR2 = k.dR1; k.dR1 = k.dR0; k.dR0 = (int)(T0 - k.ER); }
    k.okA = (int)(k.XA - T0) >= TK_LA - TK_TOL;
    if (!k.okR) k.gap = k.okA ? max(TK_GAP, k.gap - 6) : min(k.gap + 16, 160);
    k.EA = k.XA;
    k.XR = k.EA + tk_est(k.dA0, k.dA1, k.dA2) + TK_LR;
}
__device__ __forceinline__ void tk_Tp(TK& k, u32 Tp, const Args& a) {
    const int okn = (int)(k.XR - Tp) >= TK_LR - TK_TOL;
    if (k.okA && (k.okR || !okn)) { k.dA2 = k.dA1; k.dA1 = k.dA0; k.dA0 = (int)(Tp - k.EA); }
    k.okR = okn;
    k.ER = k.okR ? k.XR : Tp + k.gap;
    k.XA = k.ER + tk_est(k.dR0, k.dR1, k.dR2) + TK_LA;
}
__device__ __forceinline__ u32 tk_specA(const TK& k, const Args& a) { return k.XR + tk_est(k.dR0, k.dR1, k.dR2) + TK_LA; }
__device__ __forceinline__ u32 tk_specR(const TK& k, const Args& a) { return k.XA + tk_est(k.dA0, k.dA1, k.dA2) + TK_LR; }
__device__ __forceinline__ void tk_init(TK& k) { const u32 n = rt32(); k.XA = k.XR = k.EA = k.ER = n - 100000u; k.dA0 = k.dA1 = k.dA2 = 123; k.dR0 = k.dR1 = k.dR2 = 65; k.okA = k.okR = 0; k.gap = 50; }

__device__ __forceinline__ void tk_post(lptr p, int tag, u32 t, int lane) { if (lane == 0) { v2u v = {(u32)tag, t}; *(volatile LAS v2u*)p = v; } }
__device__ __forceinline__ u32 tk_get(lptr p, int tag, int& budget) {
    for (;;) {
        const v2u v = *(volatile LAS v2u*)p;
        if ((int)__builtin_amdgcn_readfirstlane(v.x) >= tag || --budget < 0) return __builtin_amdgcn_readfirstlane(v.y);
        __builtin_amdgcn_s_sleep(2);
    }
}
__device__ __forceinline__ void spin_to(u32 X) {
    int rem;
    asm volatile("1:\n\ts_memrealtime vcc\n\ts_waitcnt lgkmcnt(0)\n\ts_sub_i32 %0, %1, vcc_lo\n\ts_cmp_gt_i32 %0, 3\n\ts_cbranch_scc1 1b\n\t"
                 "s_cmp_lt_i32 %0, 1\n\ts_cbranch_scc1 2f\n\ts_nop 3\n\ts_cmp_lt_i32 %0, 2\n\ts_cbranch_scc1 2f\n\ts_nop 3\n\t"
                 "s_cmp_lt_i32 %0, 3\n\ts_cbranch_scc1 2f\n\ts_nop 7\n2:"
                 : "=&s"(rem) : "s"(X) : "memory", "scc", "vcc");
}
__device__ __forceinline__ void tk_wait(lptr p, int tag, int& budget) {
    u32 X;
    for (;;) {
        const v2u v = *(volatile LAS v2u*)p;
        const u32 now = rt32();
        X = __builtin_amdgcn_readfirstlane(v.y);
        const int tg = (int)__builtin_amdgcn_readfirstlane(v.x), dt = (int)(X - now);
        if (tg >= tag) {
            if (dt <= 0 || dt > 1000 || tg > tag) return;
            if (dt <= 28) break;
        }
        if (--budget < 0) return;
        __builtin_amdgcn_s_sleep(1);
    }
    spin_to(X);
}
constexpr int OOB = 0x70000000;
__device__ __forceinline__ void gather_mem4(rsrc_t rw, int nb, lptr ml, int lane, int gi, lptr tkp, int tag, int& budget) {
    const int l0 = gi * 2048 + lane * 16;
    const int o2 = lane < 16 ? N_MEMB + gi * 256 + lane * 16 : OOB;
    lptr p2 = ml + (lane < 16 ? 8192 + gi * 256 + lane * 16 : 14336 + lane * 16);
    asm volatile("" : "+s"(nb));
    tk_wait(tkp, tag, budget);
    v4u v0 = LD16(rw, l0, nb), v1 = LD16(rw, l0 + 1024, nb), v2 = LD16(rw, o2, nb);
    MB();
    *(LAS v4u*)(ml + l0) = v0; *(LAS v4u*)(ml + l0 + 1024) = v1; *(LAS v4u*)p2 = v2;
    MB();
    if (__builtin_expect(!__all(max(max(bad4(v0), bad4(v1)), bad4(v2)) != 0xFFFFFFFFu), 0)) {
        do {
            v0 = LD16(rw, l0, nb); v1 = LD16(rw, l0 + 1024, nb); v2 = LD16(rw, o2, nb);
            MB();
        } while (!__all(max(max(bad4(v0), bad4(v1)), bad4(v2)) != 0xFFFFFFFFu) && --budget >= 0);
        *(LAS v4u*)(ml + l0) = v0; *(LAS v4u*)(ml + l0 + 1024) = v1; *(LAS v4u*)p2 = v2;
    }
}
__device__ __forceinline__ int decode_parity(u32 v, int lane) {
    u32 e = v & 0x00FF00FFu, o = (v >> 8) & 0x00FF00FFu;
    { auto r = __builtin_amdgcn_permlane16_swap(e, e, false, false); e = r[0] + r[1]; }
    { auto r = __builtin_amdgcn_permlane16_swap(o, o, false, false); o = r[0] + r[1]; }
    const int l0 = e & 0xFFFF, l2 = e >> 16, l1 = o & 0xFFFF, l3 = o >> 16;
    const bool par = l1 + l3 < l0 + l2;
    const bool hi = par ? l3 < l1 : l2 < l0;
    return (int)((__ballot(par) & 0xFFFFu) | ((__ballot(hi) & 0xFFFFu) << 16));
}
__device__ __forceinline__ int get_parity(rsrc_t rw, int lane, int& budget) {
    const int vo = (FL_PAR + (lane & 31)) * 128;
    u32 v;
    for (;;) {
        v = LD4(rw, vo, 0);
        MB();
        if (__all(v != 0xFFFFFFFFu) || --budget < 0) break;
        __builtin_amdgcn_s_sleep(8);
    }
    return decode_parity(v, lane);
}
__device__ __forceinline__ void role_probe(rsrc_t rw, int rep) {
    const int lane = threadIdx.x & 63, w = __builtin_amdgcn_readfirstlane(threadIdx.x >> 6);
    const int G = (w & 3) * 4 + rep;
    const int o = (G << 17) + N_PROBE + (w >> 2) * 512;
    ST4(0xFFFFFFFFu, rw, lane < 12 ? o + (lane & 3) * 8192 + (lane >> 2) * 128 : OOB);
    DRAIN();
    int best[4] = {254, 254, 254, 254};
#pragma unroll
    for (int k = 0; k < 12; ++k) {
        const unsigned long long t0 = __builtin_amdgcn_s_memtime();
        asm volatile("s_waitcnt lgkmcnt(0)" ::: "memory");
        u32 v = LD4(rw, o + (k & 3) * 8192 + (k >> 2) * 128, 0);
        asm volatile("s_waitcnt vmcnt(0)" : "+v"(v) :: "memory");
        const unsigned long long t1 = __builtin_amdgcn_s_memtime();
        best[k & 3] = min(best[k & 3], (int)(t1 - t0) >> 2);
    }
    if (lane == 0) ST4((u32)(best[0] | (best[1] << 8) | (best[2] << 16) | (best[3] << 24)), rw, (FL_PAR + (w >> 2) * 16 + G) * 128);
}
template <typename F>
__device__ __forceinline__ void qk_project_h(const bf16x16 (&Bq)[9], const bf16x16 (&Bk)[9], lptr ma, lptr xw, int idx, F hook) {
    f32x4 aq = {}, ak = {};
    bf16x8 A[9];
#pragma unroll
    for (int t = 0; t < 9; ++t) A[t] = lds8(ma + t * 1024);
    __builtin_amdgcn_sched_barrier(0);
#pragma unroll
    for (int t = 0; t < 9; ++t) {
        aq = SMF(A[t], Bq[t], aq, idx); ak = SMF(A[t], Bk[t], ak, idx);
        hook(t);
        __builtin_amdgcn_sched_barrier(0);
    }
    const float s0 = swap32_sum(aq[0], ak[0]), s1 = swap32_sum(aq[1], ak[1]), s2 = swap32_sum(aq[2], ak[2]), s3 = swap32_sum(aq[3], ak[3]);
    v2u o = {pack2(s0, s1), pack2(s2, s3)};
    *(LAS v2u*)xw = o;
}
__device__ __forceinline__ void qk_project(const bf16x16 (&Bq)[9], const bf16x16 (&Bk)[9], lptr ma, lptr xw, int idx) {
    qk_project_h(Bq, Bk, ma, xw, idx, [](int) {});
}

template <int GI>
__device__ __forceinline__ void gather_project(rsrc_t rw, int nb, lptr ml, int lane, lptr tkp, int tag, int& budget,
                                               const bf16x16 (&Bq)[9], const bf16x16 (&Bk)[9], lptr xw, int idx) {
    const int l0 = GI * 2048 + lane * 16;
    const int o2 = lane < 16 ? N_MEMB + GI * 256 + lane * 16 : OOB;
    lptr p2 = ml + (lane < 16 ? 8192 + GI * 256 + lane * 16 : 14336 + lane * 16);
    asm volatile("" : "+s"(nb));
    tk_wait(tkp, tag, budget);
    v4u v0 = LD16(rw, l0, nb), v1 = LD16(rw, l0 + 1024, nb), v2 = LD16(rw, o2, nb);
    MB();
    *(LAS v4u*)(ml + l0) = v0; *(LAS v4u*)(ml + l0 + 1024) = v1; *(LAS v4u*)p2 = v2;
    MB();
    if (__builtin_expect(!__all(max(max(bad4(v0), bad4(v1)), bad4(v2)) != 0xFFFFFFFFu), 0)) {
        do {
            v0 = LD16(rw, l0, nb); v1 = LD16(rw, l0 + 1024, nb); v2 = LD16(rw, o2, nb);
            MB();
        } while (!__all(max(max(bad4(v0), bad4(v1)), bad4(v2)) != 0xFFFFFFFFu) && --budget >= 0);
        *(LAS v4u*)(ml + l0) = v0; *(LAS v4u*)(ml + l0 + 1024) = v1; *(LAS v4u*)p2 = v2;
    }
    LBAR();
    bf16x8 A[9];
#pragma unroll
    for (int t = 0; t < 9; ++t) if (t != 2 * GI && t != 2 * GI + 1) A[t] = lds8(ml + lane * 16 + t * 1024);
    __builtin_amdgcn_sched_barrier(0);
    f32x4 aq = {}, ak = {};
    aq = SMF(as8(v0), Bq[2 * GI], aq, idx); ak = SMF(as8(v0), Bk[2 * GI], ak, idx);
    aq = SMF(as8(v1), Bq[2 * GI + 1], aq, idx); ak = SMF(as8(v1), Bk[2 * GI + 1], ak, idx);
    __builtin_amdgcn_sched_barrier(0);
#pragma unroll
    for (int t = 0; t < 9; ++t) if (t != 2 * GI && t != 2 * GI + 1) { aq = SMF(A[t], Bq[t], aq, idx); ak = SMF(A[t], Bk[t], ak, idx); __builtin_amdgcn_sched_barrier(0); }
    const float s0 = swap32_sum(aq[0], ak[0]), s1 = swap32_sum(aq[1], ak[1]), s2 = swap32_sum(aq[2], ak[2]), s3 = swap32_sum(aq[3], ak[3]);
    v2u o = {pack2(s0, s1), pack2(s2, s3)};
    *(LAS v2u*)xw = o;
}
__device__ __forceinline__ void role_qk(const Args& a, lptr lds, rsrc_t rw, int h, int rep) {
    const int tid = threadIdx.x, lane = tid & 63, w = __builtin_amdgcn_readfirstlane(tid >> 6);
    bf16x16 Bq[9], Bk[9];
    {
        v4u xr[6];
#pragma unroll
        for (int j = 0; j < 6; ++j) {
            const int i = min(tid + 512 * j, 2879), row = i / 72, c = i - row * 72;
            xr[j] = *(const v4u*)((row < 8 ? a.mem0 + row * DM : a.u + (row - 8) * DM) + c * 8);
        }
        v4u v[6][6];
#pragma unroll
        for (int c = 0; c < 6; ++c) rows_ld<192, 16>(v[c], (c >= 3 ? a.Wk : a.Wq) + (size_t)(c % 3) * 192 * 4096 + h * DH, 4096, tid);
        __builtin_amdgcn_sched_barrier(0);
#pragma unroll
        for (int j = 0; j < 6; ++j) {
            const int i = min(tid + 512 * j, 2879), row = i / 72, c = i - row * 72, G = row >> 3;
            lptr d = lds + (G < 2 ? 57344 + G * MEMB : 135168 + (G - 2) * MEMB) + (c >> 1) * 256 + (row & 7) * 16 + (c & 1) * 8;
            *(LAS v2u*)d = v2u{xr[j].x, xr[j].z}; *(LAS v2u*)(d + 128) = v2u{xr[j].y, xr[j].w};
        }
#pragma unroll
        for (int c = 0; c < 6; ++c) {
            lptr l = lds + (c & 1) * 77824;
            rows_st<192, 16>(v[c], l, 288, tid);
            LBAR();
#pragma unroll
            for (int t = 0; t < 3; ++t) {
                bf16x16 f = cat16(frag16(l + (64 * t) * 288 + w * 32, 288, lane), frag16(l + (64 * t + 32) * 288 + w * 32, 288, lane));
                if (c >= 3) Bk[(c % 3) * 3 + t] = f; else Bq[c * 3 + t] = f;
            }
            __builtin_amdgcn_sched_barrier(0);
        }
        LBAR();
    }
    lptr ml = lds, xl = lds + MEMB;
    const int idx = (lane & 8) ? 0xEEEE : 0x4444;
    const int i16 = lane & 15, g = lane >> 4;
    const bool hi8 = (lane & 8) != 0;
    int budget = SPIN_MAX;
    lptr pq = xl + (8 * g + (i16 >> 2)) * 32 + (i16 & 1) * 8;
    lptr kl = lds + 16384 + lane * 16;
    lptr ma = ml + lane * 16, xw = xl + (16 * w + i16) * 32 + g * 8;
    const int ox = N_PX + (h >> 4) * 32768 + (h & 15) * 512 + ((g & 1) * 16 + (g >> 1) * 8 + (i16 & 7)) * 16;
    const int om = hi8 ? OOB : N_PMM + h * 128 + (i16 & 7) * 16;
    const int ob = lane < NREP ? BCO(BC_QK + h) : OOB;
    lptr tkp = lds + MEMB + 4096, kfp = tkp + 64;
    if (tid == 0) { tk_post(tkp, 0, rt32() - 100000u, 0); tk_post(kfp, 0, 0u, 0); }
    for (int c = tid; c < 576; c += 512) *(LAS v4u*)(ml + c * 16) = *(LAS v4u*)(lds + 57344 + c * 16);
    __syncthreads();
#pragma unroll 1
    for (int G = 0; G < 4; ++G) {
        qk_project(Bq, Bk, lds + (G ? 135168 + (G - 1) * MEMB : 57344 + MEMB) + lane * 16, xw, idx);
        LBAR();
        if (w < 4) {
            const bf16x8 F = as8(trd(pq + w * 1024 + 16), trd(pq + w * 1024 + 16 + 128));
            if ((i16 >> 3) == (G & 1)) *(LAS bf16x8*)(kl + ((G >> 1) * 4 + w) * 1024) = F;
        }
        LBAR();
    }
    const int pm = get_parity(rw, lane, budget);
    LBAR();
    if (w == 0) {
        const float c2 = 0.08838834764831845f * 1.4426950408889634f;
        for (int s = 0; s < NSEG - 1; ++s) {
            tk_get(kfp, s, budget);
            bf16x8 kx[2][4];
#pragma unroll
            for (int tl = 0; tl < 2; ++tl)
#pragma unroll
                for (int ks = 0; ks < 4; ++ks) kx[tl][ks] = lds8(kl + (s & 1) * 8192 + (tl * 4 + ks) * 1024);
            int oxs = ox + NEARB(s, i16 >> 3, pm), oxt = ox + NEARB(s, 2 + (i16 >> 3), pm), oms = om + NEARB(s, g, pm), obs = ob + FARB(s, lane & 3, pm);
            const int nb = NEARB(s, rep, pm);
            asm volatile("s_waitcnt lgkmcnt(0)" : "+v"(oxs), "+v"(oxt), "+v"(oms), "+v"(obs) :: "memory");
#pragma unroll
            for (int tl = 0; tl < 2; ++tl)
#pragma unroll
                for (int ks = 0; ks < 4; ++ks) PIN4(kx[tl][ks]);
            if (s > 0) gather_project<0>(rw, nb, ml, lane, tkp, s, budget, Bq, Bk, xw, idx);
            else { LBAR(); qk_project(Bq, Bk, ma, xw, idx); }
            __builtin_amdgcn_s_setprio(3);
            LBAR();
            bf16x8 Fq[4], Fk[4];
#pragma unroll
            for (int ks = 0; ks < 4; ++ks) Fq[ks] = as8(trd(pq + ks * 1024), trd(pq + ks * 1024 + 128));
            __builtin_amdgcn_sched_barrier(0);
#pragma unroll
            for (int ks = 0; ks < 4; ++ks) Fk[ks] = as8(trd(pq + ks * 1024 + 16), trd(pq + ks * 1024 + 16 + 128));
            __builtin_amdgcn_sched_barrier(0);
            f32x4 S0 = {}, S1 = {}, Sm = {};
            S0 = MFMA16(kx[0][0], Fq[0], S0); S1 = MFMA16(kx[1][0], Fq[0], S1);
            S0 = MFMA16(kx[0][1], Fq[1], S0); S1 = MFMA16(kx[1][1], Fq[1], S1);
            __builtin_amdgcn_sched_barrier(0);
            Sm = MFMA16(Fk[0], Fq[0], Sm); S0 = MFMA16(kx[0][2], Fq[2], S0);
            Sm = MFMA16(Fk[1], Fq[1], Sm); S1 = MFMA16(kx[1][2], Fq[2], S1);
            Sm = MFMA16(Fk[2], Fq[2], Sm); S0 = MFMA16(kx[0][3], Fq[3], S0);
            Sm = MFMA16(Fk[3], Fq[3], Sm); S1 = MFMA16(kx[1][3], Fq[3], S1);
            __builtin_amdgcn_sched_barrier(0);
            float y0 = hi8 ? Sm[2] : Sm[0], y1 = hi8 ? Sm[3] : Sm[1];
            float x0 = hi8 ? S1[0] : S0[0], x1 = hi8 ? S1[1] : S0[1], x2 = hi8 ? S1[2] : S0[2], x3 = hi8 ? S1[3] : S0[3];
            const float ml_ = vmax3(vmax3(y0, y1, x0), x1, vmax(x2, x3));
            const float nm = -ml_ * c2;
            float m = vmax(ml_, ror8f(ml_));
            x0 = __builtin_amdgcn_exp2f(fmaf(x0, c2, nm)); x1 = __builtin_amdgcn_exp2f(fmaf(x1, c2, nm));
            { auto r = __builtin_amdgcn_permlane16_swap(__float_as_uint(m), __float_as_uint(m), false, false); m = vmax(__uint_as_float(r[0]), __uint_as_float(r[1])); }
            x2 = __builtin_amdgcn_exp2f(fmaf(x2, c2, nm)); x3 = __builtin_amdgcn_exp2f(fmaf(x3, c2, nm));
            { auto r = __builtin_amdgcn_permlane32_swap(__float_as_uint(m), __float_as_uint(m), false, false); m = vmax(__uint_as_float(r[0]), __uint_as_float(r[1])); }
            y0 = __builtin_amdgcn_exp2f(fmaf(y0, c2, nm)); y1 = __builtin_amdgcn_exp2f(fmaf(y1, c2, nm));
            const float f = __builtin_amdgcn_exp2f((ml_ - m) * c2);
            float sum = f * fmaf(0.5f, y0 + y1, (x0 + x1) + (x2 + x3));
            sum += ror8f(sum);
            sum = xsum(sum);
            const float inv = f * __builtin_amdgcn_rcpf(sum);
            const u32 pa = pack2(x0 * inv, x1 * inv) & 0x7fff7fffu, pb = pack2(x2 * inv, x3 * inv) & 0x7fff7fffu, pc = pack2(y0 * inv, y1 * inv) & 0x7fff7fffu;
            const u32 qc = ror8(pc);
            v4u px = {(u32)__builtin_amdgcn_update_dpp((int)pa, (int)pa, 0x128, 0xf, 0xc, false), (u32)__builtin_amdgcn_update_dpp((int)pb, (int)pb, 0x128, 0xf, 0xc, false),
                      (u32)__builtin_amdgcn_update_dpp((int)pa, (int)pa, 0x128, 0xf, 0x3, false), (u32)__builtin_amdgcn_update_dpp((int)pb, (int)pb, 0x128, 0xf, 0x3, false)};
            ST16(px, rw, oxs);
            ST16(px, rw, oxt);
            auto ra = __builtin_amdgcn_permlane16_swap(pc, pc, false, false);
            auto rc = __builtin_amdgcn_permlane16_swap(qc, qc, false, false);
            v4u pmv = {ra[0], rc[0], ra[1], rc[1]};
            ST16(pmv, rw, oms);
            __builtin_amdgcn_sched_barrier(0);
            ST4(nos(rt32()), rw, obs);
            __builtin_amdgcn_s_setprio(0);
        }
        {
            gather_project<0>(rw, NEARB(NSEG - 1, rep, pm), ml, lane, tkp, NSEG - 1, budget, Bq, Bk, xw, idx);
            LBAR();
        }
    } else if (w == 5) {
        int kdone = -1;
        const int kxo = OFF_KX + h * (NSEG * 8192) + g * 512 + i16 * 16;
        for (int s = 0; s < NSEG; ++s) {
            const bool need = s + 1 < NSEG - 1;
            if (need && kdone < s + 1) kdone = wait_flags(rw, FL_K + (h >> 1), 1, 1, -1, lane, budget);
            const bool go = need && kdone >= s + 1;
            int ko = (go ? kxo : OOB) + (s + 1) * 8192;
            int a0 = ox + NEARB(s + 2, i16 >> 3, pm), a1 = ox + NEARB(s + 2, 2 + (i16 >> 3), pm), a2 = om + NEARB(s + 2, g, pm), a3 = ob + FARB(s + 4, lane & 3, pm);
            v4u ff = {0xFFFFFFFFu, 0xFFFFFFFFu, 0xFFFFFFFFu, 0xFFFFFFFFu};
            asm volatile("" : "+v"(ko), "+v"(a0), "+v"(a1), "+v"(a2), "+v"(a3), "+v"(ff));
            LBAR();
            v4u kx[8];
            qk_project_h(Bq, Bk, ma, xw, idx, [&](int t) {
                if (t < 4) { kx[t] = LD16(rw, ko + (t & 1) * 2048, (t >> 1) * 4096); kx[4 + t] = LD16(rw, ko + 256 + (t & 1) * 2048, (t >> 1) * 4096); }
                if (t == 4) { ST16(ff, rw, a0); ST16(ff, rw, a1); }
                if (t == 5) { ST16(ff, rw, a2); ST4(0xFFFFFFFFu, rw, a3); }
            });
            LBAR();
            if (need) {
                if (!go) {
                    kdone = wait_flags(rw, FL_K + (h >> 1), 1, 1, s + 1, lane, budget, true);
#pragma unroll
                    for (int k = 0; k < 8; ++k) kx[k] = LD16(rw, kxo + (k >> 2) * 256 + (k & 3) * 2048, (s + 1) * 8192);
                }
#pragma unroll
                for (int k = 0; k < 8; ++k) *(LAS v4u*)(kl + ((s + 1) & 1) * 8192 + k * 1024) = kx[k];
                asm volatile("s_waitcnt lgkmcnt(0)" ::: "memory");
                tk_post(kfp, s + 1, 0u, lane);
            }
            DRAIN();
        }
    } else if (w == 1 || w == 2 || w == 7) {
        LBAR();
        qk_project(Bq, Bk, ma, xw, idx);
        LBAR();
        for (int s = 1; s < NSEG; ++s) {
            __builtin_amdgcn_s_sleep(14);
            const int nb = NEARB(s, rep, pm);
            if (w == 1) gather_project<1>(rw, nb, ml, lane, tkp, s, budget, Bq, Bk, xw, idx);
            else if (w == 2) gather_project<2>(rw, nb, ml, lane, tkp, s, budget, Bq, Bk, xw, idx);
            else gather_project<3>(rw, nb, ml, lane, tkp, s, budget, Bq, Bk, xw, idx);
            LBAR();
        }
    } else {
        TK tk; tk_init(tk);
        const int kmo = i16 < 8 ? OFF_KM + h * 2048 + g * 128 + i16 * 16 : OOB;
        for (int s = 0; s < NSEG; ++s) {
            if (s > 0 && w == 3) tk_wait(tkp, s, budget);
            BC2 bc = {0u, 0u};
            if (w == 3) bc = beacon_issue(rw, FARB(s, rep, pm), lane);
            LBAR();
            qk_project(Bq, Bk, ma, xw, idx);
            LBAR();
            if (w == 6) {
                bf16x8 F[4];
#pragma unroll
                for (int ks = 0; ks < 4; ++ks) F[ks] = as8(trd(pq + ks * 1024 + 16), trd(pq + ks * 1024 + 16 + 128));
#pragma unroll
                for (int ks = 0; ks < 4; ++ks) ST16(asu(F[ks]), rw, kmo + s * (NH * 2048) + ks * 512);
                DRAIN();
                if (lane == 0) ST4((u32)s, rw, (FL_M + h) * 128);
            } else if (w == 3) {
                if (s < NSEG - 1) {
                    if (s > 0) { tk_T0(tk, beacon_finish(bc, rw, FARB(s, rep, pm), lane, budget), a); if (tk.okA) tk_post(tkp, s + 1, tk_specA(tk, a), lane); }
                    wait_until(tk.XR + 8);
                    tk_Tp(tk, beacon_max<NH>(rw, FARB(s, rep, pm), BC_QK, lane, budget), a);
                    tk_post(tkp, s + 1, tk.XA, lane);
                }
            } else __builtin_amdgcn_s_sleep(14);
        }
    }
    if (budget < 0 && lane == 0) ST4(1u, rw, FL_ERR * 128);
}

__device__ __forceinline__ void role_wvo(const Args& a, lptr lds, rsrc_t rw, int idx) {
    const int tid = threadIdx.x, lane = tid & 63, w = __builtin_amdgcn_readfirstlane(tid >> 6);
    const int h = idx / 3, part = idx - 3 * h;
    bf16x8 Bo[5][4];
    v4u v[6], o[2][9];
#pragma unroll
    for (int c = 0; c < 2; ++c) rows_ld<64, 72>(o[c], a.Wo + ((size_t)h * DH + c * 64) * DM, DM, tid);
    rows_ld<192, 16>(v, a.Wv + (size_t)part * 192 * 4096 + h * DH, 4096, tid);
    __builtin_amdgcn_sched_barrier(0);
#pragma unroll
    for (int c = 0; c < 2; ++c) {
        lptr l = lds + c * 77824;
        rows_st<64, 72>(o[c], l, 1184, tid);
        LBAR();
#pragma unroll
        for (int j = 0; j < 5; ++j) {
            const int r = (w + 8 * j < NR) ? w + 8 * j : w;
#pragma unroll
            for (int ks = 0; ks < 2; ++ks) Bo[j][c * 2 + ks] = frag16(l + ks * 32 * 1184 + r * 32, 1184, lane);
        }
        __builtin_amdgcn_sched_barrier(0);
    }
    rows_st<192, 16>(v, lds, VS, tid);
    LBAR();
    const int rho = lane & 15, bg = lane >> 4;
    const int wo0 = OFF_WVO + (2 * w + ((rho >> 1) & 1)) * 294912 + ((h >> 1) * 18 + part * 6) * 1024 + ((h & 1) * 32 + bg * 8 + 2 * (rho >> 2) + (rho & 1)) * 16;
#pragma unroll 1
    for (int kb = 0; kb < 6; ++kb) {
        bf16x8 Af[2][4];
#pragma unroll
        for (int mt = 0; mt < 2; ++mt)
#pragma unroll
            for (int ks = 0; ks < 4; ++ks) Af[mt][ks] = lds8(lds + (32 * kb + 8 * (rho >> 2) + 4 * mt + (rho & 3)) * VS + ks * 64 + bg * 16);
#pragma unroll
        for (int j = 0; j < 5; ++j) {
            if (j < 4 || w < 4) {
                f32x4 d0 = {}, d1 = {};
#pragma unroll
                for (int ks = 0; ks < 4; ++ks) { d0 = MFMA16(Af[0][ks], Bo[j][ks], d0); d1 = MFMA16(Af[1][ks], Bo[j][ks], d1); }
                v4u q = {pack2(d0[0], d0[1]), pack2(d0[2], d0[3]), pack2(d1[0], d1[1]), pack2(d1[2], d1[3])};
                ST16(q, rw, wo0 + j * (16 * 294912) + kb * 1024);
            }
        }
        if (kb == 2 || kb == 4) {
            asm volatile("s_waitcnt vmcnt(4)" ::: "memory");
            LBAR();
            if (tid == 0) ST4((u32)(kb >> 1), rw, (FL_W + idx) * 128);
        }
    }
    DRAIN();
    LBAR();
    if (tid == 0) ST4(3u, rw, (FL_W + idx) * 128);
}

__device__ __forceinline__ void role_rz(const Args& a, lptr lds, rsrc_t rw, int r, int rep) {
    const int tid = threadIdx.x, lane = tid & 63, w = __builtin_amdgcn_readfirstlane(tid >> 6);
    const int idx = (lane & 8) ? 0xEEEE : 0x4444;
    const int i16 = lane & 15, g = lane >> 4;
    int budget = SPIN_MAX;
    bf16x16 Ba[9], Bb[9];
    v4u t0[4], m0[2];
    int pm;
    {
        const int wo = OFF_WVO + r * 294912 + 2 * w * 18432 + ((i16 >> 3) * 32 + g * 8 + (i16 & 7)) * 16;
        const int vq = OFF_VOX + r * 16384 + w * 2048 + lane * 16;
#pragma unroll
        for (int j = 0; j < 2; ++j) { const int i = min(tid + 512 * j, 575), row = i / 72, c = i - row * 72; m0[j] = *(const v4u*)(a.mem0 + row * DM + c * 8); }
        const int vf = (int)LD4(rw, (FL_V + lane) * 128, 0);
        const u32 pw = LD4(rw, (FL_PAR + (lane & 31)) * 128, 0);
        int have = 0;
        bool vgot = false;
#pragma unroll
        for (int p = 0; p < 3; ++p) {
            if (have < p + 1) have = wait_flags(rw, FL_W + 12 * w, 1, 12, p + 1, lane, budget, true);
            if (p == 0 || p == 2) {
                const bool now = p == 0 ? (bool)__all(vf >= 0) : !vgot;
                if (now) {
                    if (p == 2) wait_flags(rw, FL_V, 1, 64, 0, lane, budget, true);
#pragma unroll
                    for (int k = 0; k < 4; ++k) t0[k] = LD16(rw, vq + (k & 1) * 1024, (k >> 1) * (NR * NH * 1024));
                    vgot = true;
                }
            }
            if (p == 0) pm = __all(pw != 0xFFFFFFFFu) ? decode_parity(pw, lane) : get_parity(rw, lane, budget);
#pragma unroll
            for (int q = 0; q < 3; ++q) {
                const int t = 3 * q + p;
                Ba[t] = cat16(as8(LD16(rw, wo, t * 2048)), as8(LD16(rw, wo + 1024, t * 2048)));
                Bb[t] = cat16(as8(LD16(rw, wo, 18432 + t * 2048)), as8(LD16(rw, wo + 1024, 18432 + t * 2048)));
            }
            MB();
        }
#pragma unroll
        for (int j = 0; j < 2; ++j) {
            const int i = min(tid + 512 * j, 575), row = i / 72, c = i - row * 72;
            lptr d = lds + (c >> 1) * 256 + row * 16 + (c & 1) * 8;
            *(LAS v2u*)d = v2u{m0[j].x, m0[j].z}; *(LAS v2u*)(d + 128) = v2u{m0[j].y, m0[j].w};
        }
    }
    lptr ml = lds, zl = lds + MEMB;
    lptr tka = lds + 13312, tkr = tka + 16, cntp = tka + 64;
    volatile LAS int* prod = (volatile LAS int*)(lds + 13312 + 512);
    lptr ring = lds + 16384;
    lptr ma = ml + lane * 16;
    lptr zw = zl + ((2 * w + (lane >> 5)) >> 2) * 1024 + ((((2 * w + (lane >> 5)) & 3) * 16 + (g & 1) * 8 + (i16 & 7)) * 16) + (i16 >> 3) * 8;
    const bool hi8r = (lane & 8) != 0;
    const int mso = i16 < 8 ? (r < 64 ? N_MEMA + r * 128 : N_MEMB + (r - 64) * 128) + i16 * 16 : OOB;
    const int ob = lane < NREP ? BCO(r) : OOB;
    if (tid == 0) { tk_post(tka, 0, rt32() - 100000u, 0); tk_post(tkr, -1, 0u, 0); *(volatile LAS int*)cntp = 0; }
#pragma unroll
    for (int k = 0; k < 4; ++k) *(LAS v4u*)(ring + (k >> 1) * 16384 + w * 2048 + (k & 1) * 1024 + lane * 16) = t0[k];
    if (tid < 4) prod[tid] = 2;
    LBAR();
    if (w < 4) {
        const int po = N_PX + (w >> 1) * 32768 + (w & 1) * 4096 + lane * 16;
        const int mo = N_PMM + w * 1024 + 2 * g * 128 + (i16 & 7) * 16 + (i16 >> 3) * 8;
        lptr cna = lane == 0 ? cntp : cntp + 4 + lane * 4;
        const u32 one = lane == 0 ? 1u : 0u;
        const bool dv = (g >> 1) == (i16 >> 3);
        const int rwo = dv ? (4 * (g & 1) + 2 * (g >> 1)) * 128 + ((i16 & 7) >> 1) * 32 + w * 8 + (i16 & 1) * 4 : 2048 + lane * 4;
        const int msq = lane < 32 ? (r < 64 ? N_MEMA + r * 128 : N_MEMB + (r - 64) * 128) + (lane >> 2) * 16 : OOB;
        lptr zrd = zl + w * 1024 + lane * 16;
        for (int s = 0; s < NSEG; ++s) {
            lptr Rw = lds + 81920 + (s & 1) * 1024 + rwo, Rr = lds + 81920 + (s & 1) * 1024 + (lane & 31) * 32;
            int mss = msq + NEARB(s + 1, lane & 3, pm), obs = ob + FARB(s + 1, lane & 3, pm);
            int sb = NEARB(s, rep, pm);
            asm volatile("" : "+s"(sb));
            lptr rg = ring + (s & 3) * 16384 + w * 4096 + lane * 16;
            bf16x8 b[5];
            const bool early = __builtin_amdgcn_readfirstlane(prod[w >> 1]) > s;
            MB();
#pragma unroll
            for (int k = 0; k < 4; ++k) b[k] = lds8(rg + k * 1024);
            const u32 X = s > 0 ? tk_get(tkr, min(s, NSEG - 2), budget) : 0u;
            asm volatile("s_waitcnt lgkmcnt(0)" : "+v"(mss), "+v"(obs), "+v"(Rw), "+v"(Rr) :: "memory");
#pragma unroll
            for (int k = 0; k < 4; ++k) PIN4(b[k]);
            LBAR();
            qk_project(Ba, Bb, ma, zw, idx);
            LBAR();
            if (s == NSEG - 1) break;
            __builtin_amdgcn_s_setprio(3);
            b[4] = lds8(zrd);
            if (!early) {
                while (__builtin_amdgcn_readfirstlane(prod[w >> 1]) <= s && --budget >= 0) __builtin_amdgcn_s_sleep(1);
                MB();
#pragma unroll
                for (int k = 0; k < 4; ++k) b[k] = lds8(rg + k * 1024);
            }
            if (s > 0 && (int)(X - rt32()) < 1000) spin_to(X);
            f32x4 d;
            u32 bad;
            auto shot = [&]() {
                v4u p[4]; v2u m[2];
                m[0] = LD8(rw, mo, sb); m[1] = LD8(rw, mo + 128, sb);
                __builtin_amdgcn_sched_barrier(0);
#pragma unroll
                for (int k = 0; k < 4; ++k) { p[k] = LD16(rw, po + k * 1024, sb); __builtin_amdgcn_sched_barrier(0); }
                MB();
                d = f32x4{};
                d = MFMA16(as8(m[0], m[1]), b[4], d);
                bad = (m[0].x | m[0].y) | (m[1].x | m[1].y);
                __builtin_amdgcn_sched_barrier(0);
#pragma unroll
                for (int k = 0; k < 4; ++k) {
                    d = MFMA16(as8(p[k]), b[k], d);
                    bad |= (p[k].x | p[k].y) | (p[k].z | p[k].w);
                    __builtin_amdgcn_sched_barrier(0);
                }
            };
            shot();
            if (__builtin_expect(!__all((int)bad >= 0), 0)) {
                do shot(); while (!__all((int)bad >= 0) && --budget >= 0);
            }
            float e0, e1;
            {
                auto r0 = __builtin_amdgcn_permlane32_swap(__float_as_uint(d[0]), __float_as_uint(d[2]), false, false);
                auto r1 = __builtin_amdgcn_permlane32_swap(__float_as_uint(d[1]), __float_as_uint(d[3]), false, false);
                const float t0 = __uint_as_float(hi8r ? r0[1] : r0[0]), t1 = __uint_as_float(hi8r ? r1[1] : r1[0]);
                e0 = t0 + ror8f(t0); e1 = t1 + ror8f(t1);
            }
            f32x4 x0, x1; u32 old;
            asm volatile("ds_write2_b32 %4, %5, %6 offset1:32\n\tds_add_rtn_u32 %0, %7, %8\n\tds_read_b128 %1, %3\n\tds_read_b128 %2, %3 offset:16\n\ts_waitcnt lgkmcnt(0)"
                         : "=&v"(old), "=&v"(x0), "=&v"(x1) : "v"(Rr), "v"(Rw), "v"(e0), "v"(e1), "v"(cna), "v"(one) : "memory");
            {
                const u32 c = nos(pack2((x0[0] + x0[2]) + (x1[0] + x1[2]), (x0[1] + x0[3]) + (x1[1] + x1[3])));
                v4u o = {(u32)__builtin_amdgcn_update_dpp(0, (int)c, 0x00, 0xf, 0xf, true), (u32)__builtin_amdgcn_update_dpp(0, (int)c, 0x55, 0xf, 0xf, true),
                         (u32)__builtin_amdgcn_update_dpp(0, (int)c, 0xAA, 0xf, 0xf, true), (u32)__builtin_amdgcn_update_dpp(0, (int)c, 0xFF, 0xf, 0xf, true)};
                const bool last = (__builtin_amdgcn_readfirstlane(old) & 3) == 3;
                ST16(o, rw, last ? mss : OOB);
                __builtin_amdgcn_sched_barrier(0);
                if (last) ST4(nos(rt32()), rw, obs);
            }
            __builtin_amdgcn_s_setprio(0);
        }
    } else if (w == 5 || w == 6) {
        int vdone0 = -1, vdone1 = -1, staged = 2;
        const int hw = w - 5;
        const int vo = OFF_VOX + r * 16384 + hw * 8192 + lane * 16;
        const int zho = OFF_ZH + r * 4096 + hw * 2048 + lane * 16;
        lptr zhr = zl + hw * 2048 + (((lane >> 4) & 3) * 16 + (lane & 7)) * 16 + ((lane >> 3) & 1) * 8;
        const int aro = w == 5 ? mso : OOB, arb = w == 5 ? ob : OOB;
        v4u zv[2] = {};
        for (int s = 0; s < NSEG; ++s) {
            bool go = false;
            if (staged <= min(s + 3, NSEG - 2)) {
                const int ow = VOWN(staged);
                int& vd = ow ? vdone1 : vdone0;
                if (vd < staged) vd = wait_flags(rw, FL_V + ow, 2, NH, -1, lane, budget);
                go = vd >= staged;
            }
            const int vos = go ? vo : OOB;
            int va = vos + staged * (NR * NH * 1024), zs = s > 0 ? zho + (s - 1) * (NR * NH * 256) : OOB;
            int a0 = aro + NEARB(s + 3, g, pm), a1 = arb + FARB(s + 4, lane & 3, pm);
            v4u ff = {0xFFFFFFFFu, 0xFFFFFFFFu, 0xFFFFFFFFu, 0xFFFFFFFFu};
            asm volatile("" : "+v"(va), "+v"(zs), "+v"(a0), "+v"(a1), "+v"(ff));
            if (s > 0) gather_mem4(rw, NEARB(s, rep, pm), ml, lane, w - 4, tka, s, budget);
            LBAR();
            v4u v[8];
            qk_project_h(Ba, Bb, ma, zw, idx, [&](int t) {
                if (t < 4) { v[2 * t] = LD16(rw, va + (t & 1) * 2048, (t >> 1) * 4096); v[2 * t + 1] = LD16(rw, va + 1024 + (t & 1) * 2048, (t >> 1) * 4096); }
                if (t == 4) { ST16(zv[0], rw, zs); ST16(zv[1], rw, zs + 1024); }
                if (t == 5) { ST16(ff, rw, a0); ST4(0xFFFFFFFFu, rw, a1); }
            });
            LBAR();
#pragma unroll
            for (int k = 0; k < 2; ++k) {
                const v2u lo = *(LAS v2u*)(zhr + k * 1024), hi = *(LAS v2u*)(zhr + k * 1024 + 128);
                zv[k] = v4u{lo.x, lo.y, hi.x, hi.y};
            }
            for (;;) {
                if (go) {
#pragma unroll
                    for (int k = 0; k < 8; ++k) *(LAS v4u*)(ring + (staged & 3) * 16384 + hw * 8192 + k * 1024 + lane * 16) = v[k];
                    asm volatile("s_waitcnt lgkmcnt(0)" ::: "memory");
                    ++staged;
                    prod[hw] = staged;
                }
                if (staged > s + 1 || staged > NSEG - 2) break;
                const int ow = VOWN(staged);
                int& vd = ow ? vdone1 : vdone0;
                if (vd < staged) vd = wait_flags(rw, FL_V + ow, 2, NH, staged <= s ? staged : -1, lane, budget, true);
                if (vd < staged) break;
#pragma unroll
                for (int k = 0; k < 8; ++k) v[k] = LD16(rw, vo + (k & 3) * 1024, staged * (NR * NH * 1024) + (k >> 2) * 4096);
                go = true;
            }
            DRAIN();
        }
        {
            int l2 = (int)__builtin_amdgcn_mbcnt_hi(~0u, __builtin_amdgcn_mbcnt_lo(~0u, 0u));
            asm volatile("" : "+v"(l2));
            const int zf = OFF_ZH + r * 4096 + hw * 2048 + (NSEG - 1) * (NR * NH * 256) + l2 * 16;
            ST16(zv[0], rw, zf); ST16(zv[1], rw, zf + 1024);
        }
    } else {
        TK tk; tk_init(tk);
        for (int s = 0; s < NSEG; ++s) {
            if (s > 0) {
                gather_mem4(rw, NEARB(s, rep, pm), ml, lane, w - 4, tka, s, budget);
            }
            BC2 bc = {0u, 0u};
            if (w == 7) bc = beacon_issue(rw, FARB(s, rep, pm), lane);
            LBAR();
            qk_project(Ba, Bb, ma, zw, idx);
            LBAR();
            if (w == 7) {
                if (s < NSEG - 1) {
                    if (s > 0) { tk_T0(tk, beacon_finish(bc, rw, FARB(s, rep, pm), lane, budget), a); if (tk.okA) tk_post(tka, s + 1, tk_specA(tk, a), lane); }
                    tk_post(tkr, s, tk.XR, lane);
                    wait_until(tk.XR + 8);
                    tk_Tp(tk, beacon_max<NH>(rw, FARB(s, rep, pm), BC_QK, lane, budget), a);
                    tk_post(tka, s + 1, tk.XA, lane);
                    tk_post(tkr, s + 1, tk_specR(tk, a), lane);
                }
            } else {
                if (w == 4 && s >= 2 && lane == 0) ST4((u32)(s - 2), rw, (FL_Z + r) * 128);
                __builtin_amdgcn_s_sleep(14);
            }
        }
    }
    DRAIN();
    LBAR();
    if (tid == 0) ST4((u32)(NSEG - 1), rw, (FL_Z + r) * 128);
    if (budget < 0 && lane == 0) ST4(3u, rw, FL_ERR * 128);
}

__device__ __forceinline__ void cexp_l(float lr, float li, float l, float& er, float& ei) {
    const float mag = __builtin_amdgcn_exp2f(lr * l);
    const float ph = li * l;
    const float pe = fmaf(li, l, -ph);
    const float fr = (ph - floorf(ph)) + pe;
    er = mag * __builtin_amdgcn_cosf(fr);
    ei = mag * __builtin_amdgcn_sinf(fr);
}
constexpr int CV_W = 8 * 260 * 16, CV_U = 96 * 80, CV_CH = CV_W + CV_U;
__device__ __forceinline__ void s4_unit(const Args& a, lptr lds, rsrc_t rw, int unit) {
    const int tid = threadIdx.x, lane = tid & 63, w = __builtin_amdgcn_readfirstlane(tid >> 6);
    const int d0 = unit * 4;
    v2u uv[4];
#pragma unroll
    for (int k = 0; k < 4; ++k) uv[k] = *(const v2u*)(a.u + (size_t)(2 * (tid + 512 * (k >> 1)) + (k & 1)) * DM + d0);
    {
        const int c = tid >> 7, nn = tid & 127, n = (d0 + c) * 128 + nn;
        const float dt = rbf(expf(bf2f(a.logdt[d0 + c])));
        const float lr = dt * bf2f(a.Are[n]) * 1.4426950408889634f, li = dt * bf2f(a.Aim[n]) * 0.15915494309189535f;
        const float br = dt * bf2f(a.Bre[n]), bi = dt * bf2f(a.Bim[n]);
        const float cr = bf2f(a.Cre[n]), ci = bf2f(a.Cim[n]);
        LAS f32x2* T = (LAS f32x2*)(lds + c * CV_CH) + nn;
        float er, ei, pr, pi;
        cexp_l(lr, li, 1.0f, er, ei); pr = er; pi = ei;
#pragma unroll
        for (int o = 0; o < 7; ++o) { T[o * 130] = f32x2{pr, pi}; const float t = pr * er - pi * ei; pi = pr * ei + pi * er; pr = t; }
#pragma unroll
        for (int q4 = 0; q4 < 4; ++q4) {
            if (q4 == 0) cexp_l(lr, li, 128.0f, er, ei);
            cexp_l(lr, li, (float)(128 + 512 * q4), pr, pi);
#pragma unroll
            for (int o = 0; o < 4; ++o) { T[(7 + 4 * q4 + o) * 130] = f32x2{pr, pi}; const float t = pr * er - pi * ei; pi = pr * ei + pi * er; pr = t; }
        }
        const float cbr = cr * br - ci * bi, cbi = cr * bi + ci * br;
        cexp_l(lr, li, 8.0f, er, ei);
#pragma unroll
        for (int q4 = 0; q4 < 4; ++q4) {
            float sr, si;
            cexp_l(lr, li, (float)(32 * q4), sr, si);
            pr = cbr * sr - cbi * si; pi = cbr * si + cbi * sr;
#pragma unroll
            for (int o = 0; o < 4; ++o) { T[(23 + 4 * q4 + o) * 130] = f32x2{pr, pi}; const float t = pr * er - pi * ei; pi = pr * ei + pi * er; pr = t; }
        }
    }
    __syncthreads();
    f32x4 kr[3];
    {
        const int x = lane & 15, g = lane >> 4, c = w & 3, nh = w >> 2;
        const int ah0 = x >> 3, o0 = x & 7;
        const int ah1 = x < 8 ? 2 + (x >> 2) : 4 + ((x - 8) >> 1), o1 = x < 8 ? 2 * (x & 3) : 4 * (x & 1);
        const int ah2 = x < 8 ? 8 + x : 16;
        LAS f32x2* T = (LAS f32x2*)(lds + c * CV_CH) + nh * 64 + g;
        LAS f32x2* Ta0 = T + 7 * 130; LAS f32x2* To0 = T + (o0 ? o0 - 1 : 0) * 130;
        LAS f32x2* Ta1 = T + (6 + ah1) * 130; LAS f32x2* To1 = T + (o1 ? o1 - 1 : 0) * 130;
        LAS f32x2* Ta2 = T + (6 + ah2) * 130; LAS f32x2* Tv = T + (23 + x) * 130;
        const f32x2 one = {1.0f, 0.0f};
        f32x4 ar[3] = {}, ai[3] = {};
#pragma unroll
        for (int jj = 0; jj < 16; ++jj) {
            const f32x2 v = Tv[4 * jj], u2 = Ta2[4 * jj], a1 = Ta1[4 * jj];
            f32x2 a0v = Ta0[4 * jj], e0 = To0[4 * jj], e1 = To1[4 * jj];
            a0v = ah0 ? a0v : one; e0 = o0 ? e0 : one; e1 = o1 ? e1 : one;
            const float u0r = a0v[0] * e0[0] - a0v[1] * e0[1], u0i = a0v[0] * e0[1] + a0v[1] * e0[0];
            const float u1r = a1[0] * e1[0] - a1[1] * e1[1], u1i = a1[0] * e1[1] + a1[1] * e1[0];
            ar[2] = __builtin_amdgcn_mfma_f32_16x16x4f32(u2[0], v[0], ar[2], 0, 0, 0);
            ai[2] = __builtin_amdgcn_mfma_f32_16x16x4f32(u2[1], v[1], ai[2], 0, 0, 0);
            ar[0] = __builtin_amdgcn_mfma_f32_16x16x4f32(u0r, v[0], ar[0], 0, 0, 0);
            ai[0] = __builtin_amdgcn_mfma_f32_16x16x4f32(u0i, v[1], ai[0], 0, 0, 0);
            ar[1] = __builtin_amdgcn_mfma_f32_16x16x4f32(u1r, v[0], ar[1], 0, 0, 0);
            ai[1] = __builtin_amdgcn_mfma_f32_16x16x4f32(u1i, v[1], ai[1], 0, 0, 0);
        }
#pragma unroll
        for (int t = 0; t < 3; ++t) kr[t] = ar[t] - ai[t];
    }
    __syncthreads();
    {
        LAS float* T = (LAS float*)(lds + (w & 3) * CV_CH + 4096 + (w >> 2) * 3072);
#pragma unroll
        for (int t = 0; t < 3; ++t)
#pragma unroll
            for (int r = 0; r < 4; ++r) T[(16 * t + 4 * (lane >> 4) + r) * 16 + (lane & 15)] = kr[t][r];
    }
    __syncthreads();
#pragma unroll
    for (int k = 0; k < 8; ++k) {
        const int i = tid + 512 * k, c = i >> 10, l2 = i & 1023;
        LAS float* T = (LAS float*)(lds + c * CV_CH + 4096);
        float v[2];
#pragma unroll
        for (int e = 0; e < 2; ++e) {
            const int lb = (int)rbf((float)(2 * l2 + e));
            const int hi = lb >> 7, o = lb & 7;
            const int row = lb < 256 ? 8 * hi + o : lb < 512 ? 16 + 4 * (hi - 2) + (o >> 1) : lb < 1024 ? 24 + 2 * (hi - 4) + (o >> 2) : 32 + (hi - 8);
            const int ix = row * 16 + ((lb >> 3) & 15);
            v[e] = T[ix] + T[768 + ix];
        }
        *(LAS u32*)(lds + c * CV_CH + CV_W + 2704 + l2 * 4) = pack2(v[0], v[1]);
    }
    __syncthreads();
    for (int i = tid; i < 4 * 259; i += 512) {
        const int c = i / 259, zz = i - c * 259, z = zz - 3;
        lptr ch = lds + c * CV_CH;
        v4u lo = {0, 0, 0, 0}, hi = {0, 0, 0, 0};
        if (z >= 1) lo = *(LAS v4u*)(ch + CV_W + 2704 + (z - 1) * 16);
        if (z >= 0) hi = *(LAS v4u*)(ch + CV_W + 2704 + z * 16);
        const u32 e[8] = {lo.x, lo.y, lo.z, lo.w, hi.x, hi.y, hi.z, hi.w};
#pragma unroll
        for (int cc = 0; cc < 8; ++cc) {
            v4u o;
#pragma unroll
            for (int k = 0; k < 4; ++k) {
                const int t = 8 + cc - 2 * k;
                if (t & 1) o[k] = (e[t >> 1] >> 16) | (e[t >> 1] << 16);
                else o[k] = (e[t >> 1] & 0xffffu) | (e[(t >> 1) - 1] & 0xffff0000u);
            }
            *(LAS v4u*)(ch + (cc * 260 + zz) * 16) = o;
        }
    }
    __syncthreads();
    for (int i = tid; i < 4 * 160; i += 512) *(LAS v4u*)(lds + (i / 160) * CV_CH + CV_W + (i % 160) * 16) = v4u{0, 0, 0, 0};
#pragma unroll
    for (int k2 = 0; k2 < 2; ++k2) {
        const int l = 2 * (tid + 512 * k2);
        const int o = CV_W + (32 + (l >> 5)) * 80 + (l & 31) * 2;
        const v2u p = uv[2 * k2], q = uv[2 * k2 + 1];
        *(LAS u32*)(lds + 0 * CV_CH + o) = (p.x & 0xffffu) | (q.x << 16);
        *(LAS u32*)(lds + 1 * CV_CH + o) = (p.x >> 16) | (q.x & 0xffff0000u);
        *(LAS u32*)(lds + 2 * CV_CH + o) = (p.y & 0xffffu) | (q.y << 16);
        *(LAS u32*)(lds + 3 * CV_CH + o) = (p.y >> 16) | (q.y & 0xffff0000u);
    }
    __syncthreads();
    const int c = w & 3, par = w >> 2, b = lane & 31, hh = lane >> 5;
    lptr ch = lds + c * CV_CH;
    lptr wa = ch + ((b & 7) * 260 + (b >> 3) - hh + 3) * 16;
    lptr ub = ch + CV_W + (32 + b) * 80 + hh * 16;
    f32x16 y0 = {}, y1 = {};
    wa += par * 64; ub -= par * 80;
    {
        bf16x8 fa[2][2], fb[2][2], fc[2][2];
#pragma unroll
        for (int ks = 0; ks < 2; ++ks) { fa[0][ks] = lds8(wa - ks * 32); fb[0][ks] = lds8(ub + ks * 32); fc[0][ks] = lds8(ub + 2560 + ks * 32); }
#pragma unroll
        for (int i = 0; i < 32; ++i) {
            const int cur = i & 1, nx = cur ^ 1;
            if (i + 1 < 32) {
#pragma unroll
                for (int ks = 0; ks < 2; ++ks) {
                    fa[nx][ks] = lds8(wa + (i + 1) * 128 - ks * 32);
                    if (i + 1 < 16) fb[nx][ks] = lds8(ub - (i + 1) * 160 + ks * 32);
                    fc[nx][ks] = lds8(ub + 2560 - (i + 1) * 160 + ks * 32);
                }
            }
            __builtin_amdgcn_sched_barrier(0);
#pragma unroll
            for (int ks = 0; ks < 2; ++ks) {
                if (i < 16) y0 = MFMA32(fa[cur][ks], fb[cur][ks], y0);
                y1 = MFMA32(fa[cur][ks], fc[cur][ks], y1);
            }
            __builtin_amdgcn_sched_barrier(0);
        }
    }
    __syncthreads();
    {
        LAS f32x4* X = (LAS f32x4*)(ch + par * 4096);
        const f32x16 src = par ? y0 : y1;
#pragma unroll
        for (int k = 0; k < 4; ++k) X[k * 64 + lane] = f32x4{src[4 * k], src[4 * k + 1], src[4 * k + 2], src[4 * k + 3]};
    }
    __syncthreads();
    {
        LAS f32x4* X = (LAS f32x4*)(ch + (1 - par) * 4096);
        lptr Y = ch + 8192 + (32 * par + b) * 72 + hh * 8;
        const f32x16 own = par ? y1 : y0;
#pragma unroll
        for (int k = 0; k < 4; ++k) {
            const f32x4 o = X[k * 64 + lane];
            v2u yv = {pack2(own[4 * k] + o[0], own[4 * k + 1] + o[1]), pack2(own[4 * k + 2] + o[2], own[4 * k + 3] + o[3])};
            *(LAS v2u*)(Y + k * 16) = yv;
        }
    }
    __syncthreads();
    const v2u dk = *(const v2u*)(a.dskip + d0);
#pragma unroll
    for (int k2 = 0; k2 < 2; ++k2) {
        const int i = tid + 512 * k2;
        lptr yp = lds + 8192 + (i >> 4) * 72 + (i & 15) * 4;
        const u32 ya = *(LAS u32*)(yp), yb = *(LAS u32*)(yp + CV_CH), yc = *(LAS u32*)(yp + 2 * CV_CH), yd = *(LAS u32*)(yp + 3 * CV_CH);
        const v2u p = uv[2 * k2], q = uv[2 * k2 + 1];
        v4u o;
        o.x = pack2(bflo(ya) + rbf(bflo(p.x) * bflo(dk.x)), bflo(yb) + rbf(bfhi(p.x) * bfhi(dk.x)));
        o.y = pack2(bflo(yc) + rbf(bflo(p.y) * bflo(dk.y)), bflo(yd) + rbf(bfhi(p.y) * bfhi(dk.y)));
        o.z = pack2(bfhi(ya) + rbf(bflo(q.x) * bflo(dk.x)), bfhi(yb) + rbf(bfhi(q.x) * bfhi(dk.x)));
        o.w = pack2(bfhi(yc) + rbf(bflo(q.y) * bflo(dk.y)), bfhi(yd) + rbf(bfhi(q.y) * bfhi(dk.y)));
        ST16(o, rw, OFF_A4 + unit * 16384 + i * 16);
    }
    __syncthreads();
}
__device__ __forceinline__ void role_s4(const Args& a, lptr lds, rsrc_t rw, int idx) {
    if (idx < 32) {
        const int tid = threadIdx.x, t = idx * 64 + (tid >> 3), j = tid & 7;
        float acc = 0.f;
#pragma unroll
        for (int k = 0; k < 9; ++k) {
            const v4u xv = *(const v4u*)(a.u + (size_t)t * DM + (k * 8 + j) * 8), gv = *(const v4u*)(a.gatew + (k * 8 + j) * 8);
#pragma unroll
            for (int e = 0; e < 4; ++e) acc += rbf(bflo(xv[e]) * bflo(gv[e])) + rbf(bfhi(xv[e]) * bfhi(gv[e]));
        }
        acc += __shfl_xor(acc, 1); acc += __shfl_xor(acc, 2); acc += __shfl_xor(acc, 4);
        acc = rbf(acc);
        if (j == 0) ST4(__float_as_uint(rbf(1.0f / (1.0f + __expf(-acc)))), rw, OFF_G + t * 4);
    }
#pragma unroll 1
    for (int unit = idx; unit < DM / 4; unit += NS4) s4_unit(a, lds, rw, unit);
    DRAIN();
    __syncthreads();
    if (threadIdx.x == 0) ST4(1u, rw, (FL_S + idx) * 128);
}

__device__ __forceinline__ void role_post(const Args& a, lptr lds, rsrc_t rw) {
    const int tid = threadIdx.x, lane = tid & 63, w = __builtin_amdgcn_readfirstlane(tid >> 6);
    const int i16 = lane & 15, g = lane >> 4;
    int budget = SPIN_MAX, vdone0 = -1, vdone1 = -1;
    volatile LAS int* U = (volatile LAS int*)(lds + 81920);
    wait_flags(rw, FL_S, 1, 64, 1, lane, budget);
    wait_flags(rw, FL_S + 64, 1, NS4 - 64, 1, lane, budget);
    constexpr int NFULL = 62 * 6, NUNIT = NFULL + 24;
#pragma unroll 1
    for (;;) {
        if (tid == 0) *U = 1 + (int)__hip_atomic_fetch_add((u32*)(a.ws + FL_Q * 128), 1u, __ATOMIC_RELAXED, __HIP_MEMORY_SCOPE_AGENT);
        __syncthreads();
        const int unit = __builtin_amdgcn_readfirstlane(*U);
        if (unit >= NUNIT || budget < 0) break;
        const bool half = unit >= NFULL;
        const int uu = half ? NFULL + ((unit - NFULL) >> 1) : unit, q0 = half ? (unit - NFULL) & 1 : 0;
        const int s = uu / 6, r = (uu - s * 6) * 6 + (w < 6 ? w : 0);
        bf16x8 b[32];
        v2u av[2]; float gt[2];
        if (w < 6) {
            if (VOWN(s)) { if (vdone1 < s) vdone1 = wait_flags(rw, FL_V + 1, 2, NH, s, lane, budget); }
            else { if (vdone0 < s) vdone0 = wait_flags(rw, FL_V, 2, NH, s, lane, budget); }
            const int vo = OFF_VOX + (s * NR + r) * (NH * 1024) + ((i16 >> 1) & 1) * 16384 + ((g & 1) * 16 + (g >> 1) * 8 + 2 * (i16 >> 2) + (i16 & 1)) * 16;
#pragma unroll
            for (int k = 0; k < 32; ++k) b[k] = as8(LD16(rw, vo + (k & 7) * 512, (k >> 3) * 4096));
#pragma unroll
            for (int qt = 0; qt < 2; ++qt) {
                av[qt] = LD8(rw, OFF_A4 + (4 * r + g) * 16384 + (32 * s + 16 * (qt ^ q0) + i16) * 8, 0);
                gt[qt] = __uint_as_float(LD4(rw, OFF_G + (32 * s + 16 * (qt ^ q0) + i16) * 4, 0));
            }
        }
        v4u z[8];
        const int zo = OFF_ZH + (s * NR + r) * (NH * 256) + ((i16 >> 1) & 1) * 4096 + g * 128 + (2 * (i16 >> 2) + (i16 & 1)) * 16;
        if (half) {
            wait_flags(rw, FL_Z + 2 * r, 1, 2, s, lane, budget, true);
#pragma unroll
            for (int k = 0; k < 8; ++k) z[k] = LD16(rw, zo + k * 512, 0);
            wait_flags(rw, FL_P + (s & 3), 4, 16, s, lane, budget, true);
            v4u pv[5];
#pragma unroll
            for (int k = 0; k < 5; ++k) pv[k] = LD16(rw, OFF_PX + s * 81920 + q0 * 40960 + tid * 16, k * 8192);
#pragma unroll
            for (int k = 0; k < 5; ++k) *(LAS v4u*)(lds + k * 8192 + tid * 16) = pv[k];
        } else {
            wait_flags(rw, FL_P + (s & 3), 4, 16, s, lane, budget);
            v4u pv[10];
#pragma unroll
            for (int k = 0; k < 10; ++k) pv[k] = LD16(rw, OFF_PX + s * 81920 + tid * 16, k * 8192);
            wait_flags(rw, FL_Z + 2 * r, 1, 2, s, lane, budget);
#pragma unroll
            for (int k = 0; k < 8; ++k) z[k] = LD16(rw, zo + k * 512, 0);
            MB();
#pragma unroll
            for (int k = 0; k < 10; ++k) *(LAS v4u*)(lds + k * 8192 + tid * 16) = pv[k];
        }
        __syncthreads();
        if (w < 6) {
#pragma unroll 1
            for (int qt = 0; qt < (half ? 1 : 2); ++qt) {
                f32x4 d = {}, e = {};
                lptr pl = lds + qt * 40960 + lane * 16;
                bf16x8 f[6];
#pragma unroll
                for (int i = 0; i < 6; ++i) f[i] = lds8(pl + i * 1024);
                __builtin_amdgcn_sched_barrier(0);
#pragma unroll
                for (int k = 0; k < 40; ++k) {
                    const bf16x8 v = k < 32 ? b[k < 32 ? k : 0] : as8(z[k < 32 ? 0 : k - 32]);
                    if (k & 1) e = MFMA16(v, f[k % 6], e); else d = MFMA16(v, f[k % 6], d);
                    if (k + 6 < 40) f[k % 6] = lds8(pl + (k + 6) * 1024);
                    __builtin_amdgcn_sched_barrier(0);
                }
                d += e;
                const float gq = qt ? gt[1] : gt[0], og = rbf(1.0f - gq);
                const v2u aq = qt ? av[1] : av[0];
                const float r0 = rbf(gq * bflo(aq.x)) + rbf(og * rbf(d[0])), r1 = rbf(gq * bfhi(aq.x)) + rbf(og * rbf(d[1]));
                const float r2 = rbf(gq * bflo(aq.y)) + rbf(og * rbf(d[2])), r3 = rbf(gq * bfhi(aq.y)) + rbf(og * rbf(d[3]));
                v2u ov = {pack2(r0, r1), pack2(r2, r3)};
                *(v2u*)(a.out + (size_t)(32 * s + 16 * (qt ^ q0) + i16) * DM + 16 * r + 4 * g) = ov;
            }
        }
        if (half) break;
        LBAR();
    }
    if (budget < 0 && lane == 0) ST4(6u, rw, FL_ERR * 128);
}

__global__ void __launch_bounds__(512, 2) k_all(Args a) {
    extern __shared__ __attribute__((aligned(16))) char smem[];
    lptr lds = (lptr)smem;
    a.ws += (0u - (u32)(uintptr_t)a.ws) & 0x7FFFu;
    const rsrc_t rw = __builtin_amdgcn_make_buffer_rsrc(a.ws, 0, WS_BYTES, 0x00020000);
    if (threadIdx.x == 0) {
        const int x = (int)(__builtin_amdgcn_s_getreg(20 | (3 << 11)) & 7u);
        int role = 0;
        for (int k = 0; k < 8; ++k) {
            const int gq = (x + k) & 7;
            const int t = 1 + (int)__hip_atomic_fetch_add((u32*)(a.ws + (FL_T + gq) * 128), 1u, __ATOMIC_RELAXED, __HIP_MEMORY_SCOPE_AGENT);
            if (t < 32) { role = gq + 8 * (31 - t); break; }
        }
        *(volatile LAS int*)lds = role;
    }
    __syncthreads();
    const int b = __builtin_amdgcn_readfirstlane(*(volatile LAS int*)lds), xg = b & 7, slot = b >> 3;
    __syncthreads();

    if (slot == 8 && xg < 4) role_probe(rw, xg);
    if (slot >= 20) { role_wvo(a, lds, rw, (xg & 3) * 24 + (xg < 4 ? 12 : 0) + slot - 20); __syncthreads(); }
    if ((xg < 4 && slot >= 26) || (xg >= 4 && slot >= 20)) {
        const int idx = xg < 4 ? 48 + (slot - 26) * 4 + xg : (31 - slot) * 4 + (xg - 4);
        role_s4(a, lds, rw, idx);
        __syncthreads();
    }
    bool post = false;
    if (xg < 4) {
        if (slot < 8) role_qk(a, lds, rw, xg * 8 + slot, xg);
        else if (slot < 26) role_rz(a, lds, rw, xg * 18 + slot - 8, xg);
    } else {
        const int j = xg - 4;
        post = true;
        if (slot < 16) { role_vvo(a, lds, rw, j * 8 + (slot >> 1), slot & 1); post = slot < NJOIN; }
        else if (slot < 20) role_k2(a, lds, rw, j * 4 + slot - 16);
        else if (slot < 28) { role_q2p(a, lds, rw, j * 4 + ((slot - 20) >> 1), slot & 1); post = false; }
    }
    if (post) { __syncthreads(); role_post(a, lds, rw); }
}

extern "C" void kernel_launch(void* const* d_in, const int* in_sizes, int n_in, void* d_out, int out_size, void* d_ws, size_t ws_size, hipStream_t stream) {
    Args a;
    a.u = (const u16*)d_in[0]; a.Are = (const u16*)d_in[1]; a.Aim = (const u16*)d_in[2]; a.Bre = (const u16*)d_in[3]; a.Bim = (const u16*)d_in[4];
    a.Cre = (const u16*)d_in[5]; a.Cim = (const u16*)d_in[6]; a.logdt = (const u16*)d_in[7]; a.dskip = (const u16*)d_in[8];
    a.mem0 = (const u16*)d_in[10]; a.Wq = (const u16*)d_in[11]; a.Wk = (const u16*)d_in[12]; a.Wv = (const u16*)d_in[13]; a.Wo = (const u16*)d_in[14];
    a.gatew = (const u16*)d_in[16]; a.out = (u16*)d_out; a.ws = (char*)d_ws;
    const int LDS_ALL = 160 * 1024;
    (void)hipFuncSetAttribute((const void*)k_all, hipFuncAttributeMaxDynamicSharedMemorySize, LDS_ALL);
    (void)hipMemsetAsync(d_ws, 0xFF, INIT_BYTES + 32768, stream);
    hipLaunchKernelGGL(k_all, dim3(256), dim3(512), LDS_ALL, stream, a);
}
```

```cpp
#include <hip/hip_runtime.h>
#include <hip/hip_bf16.h>
#include <stdint.h>

typedef unsigned short u16;
typedef unsigned int u32;
typedef u32 v4u __attribute__((ext_vector_type(4)));
typedef u32 v2u __attribute__((ext_vector_type(2)));
typedef float f32x4 __attribute__((ext_vector_type(4)));
typedef float f32x2 __attribute__((ext_vector_type(2)));
typedef float f32x16 __attribute__((ext_vector_type(16)));
typedef __bf16 bf16x2 __attribute__((ext_vector_type(2)));
typedef __bf16 bf16x8 __attribute__((ext_vector_type(8)));
typedef __bf16 bf16x16 __attribute__((ext_vector_type(16)));
typedef short s16x4 __attribute__((ext_vector_type(4)));
#define LAS __attribute__((address_space(3)))
typedef LAS char* lptr;
typedef __amdgpu_buffer_rsrc_t rsrc_t;

constexpr int SEQ = 2048, DM = 576, NH = 32, DH = 128, NSEG = 64, NR = 36, NR8 = 72, NREP = 4;
constexpr int MEMB = 9216, BC_QK = 80;
constexpr int FL0 = 16384;
constexpr int FL_K = FL0, FL_V = FL0 + 16, FL_M = FL0 + 80, FL_P = FL0 + 112, FL_S = FL0 + 176, FL_Q = FL0 + 254, FL_ERR = FL0 + 255, FL_Z = FL0 + 256, FL_W = FL0 + 328, FL_T = FL0 + 424, FL_PAR = FL0 + 432, NS4 = 72;
constexpr int N_MEMA = 0, N_PX = 32768, N_PMM = 98304, N_MEMB = 98304 + 4096, N_PROBE = 98304 + 6144;
constexpr int INIT_BYTES = 16 * 131072 + 65536;
constexpr int OFF_ZH = INIT_BYTES;
constexpr int OFF_KX = OFF_ZH + NSEG * NR * NH * 256;
constexpr int OFF_VOX = OFF_KX + NH * SEQ * DH * 2;
constexpr int OFF_KM = OFF_VOX + NSEG * NR * NH * 1024;
constexpr int OFF_PX = OFF_KM + NSEG * NH * 2048;
constexpr int OFF_A4 = OFF_PX + NSEG * 81920;
constexpr int OFF_G = OFF_A4 + SEQ * DM * 2;
constexpr int OFF_WVO = OFF_G + SEQ * 4;
constexpr int WS_BYTES = OFF_WVO + NR8 * 294912;
constexpr int TK_LA = 8, TK_LR = 8, TK_TOL = 8, TK_GAP = 30;
constexpr int NJOIN = 6;

struct Args {
    const u16* u; const u16* Are; const u16* Aim; const u16* Bre; const u16* Bim; const u16* Cre; const u16* Cim;
    const u16* logdt; const u16* dskip; const u16* mem0; const u16* Wq; const u16* Wk; const u16* Wv; const u16* Wo;
    const u16* gatew; u16* out; char* ws;
};

__device__ __forceinline__ float bf2f(u16 v) { return __uint_as_float((u32)v << 16); }
__device__ __forceinline__ float bflo(u32 v) { return __uint_as_float(v << 16); }
__device__ __forceinline__ float bfhi(u32 v) { return __uint_as_float(v & 0xffff0000u); }
__device__ __forceinline__ u32 pack2(float lo, float hi) { f32x2 f = {lo, hi}; bf16x2 b = __builtin_convertvector(f, bf16x2); return __builtin_bit_cast(u32, b); }
__device__ __forceinline__ float rbf(float x) { return bflo(pack2(x, x)); }
__device__ __forceinline__ u32 nos(u32 v) { return min(v, 0xFFFFFFFEu); }
__device__ __forceinline__ v2u trd(lptr p) { return __builtin_bit_cast(v2u, __builtin_amdgcn_ds_read_tr16_b64_v4i16((LAS s16x4*)p)); }
__device__ __forceinline__ bf16x8 as8(v4u v) { return __builtin_bit_cast(bf16x8, v); }
__device__ __forceinline__ bf16x8 as8(v2u a, v2u b) { v4u v = {a.x, a.y, b.x, b.y}; return __builtin_bit_cast(bf16x8, v); }
__device__ __forceinline__ v4u asu(bf16x8 v) { return __builtin_bit_cast(v4u, v); }
__device__ __forceinline__ bf16x16 cat16(bf16x8 a, bf16x8 b) { return __builtin_shufflevector(a, b, 0, 1, 2, 3, 4, 5, 6, 7, 8, 9, 10, 11, 12, 13, 14, 15); }
__device__ __forceinline__ bf16x8 lds8(lptr p) { return *(LAS bf16x8*)p; }
__device__ __forceinline__ u32 bad4(v4u v) { return max(max(v.x, v.y), max(v.z, v.w)); }
#define NEARB(s, rep, pm) (((((s) & 3) * 4 + (rep)) << 17) + (int)(((((u32)(pm) >> (((s) & 3) * 4 + (rep))) & 0x10001u) * 0x20004000u >> 16) & 0x6000u))
#define FARB(s, rep, pm) ((NEARB(s, rep, pm) ^ 16384) + (((s) & 4) << 14))
#define BCO(slot) (((slot) & 63) * 128 + ((slot) >> 6) * 32768)
#define LBAR() do { asm volatile("s_waitcnt lgkmcnt(0)" ::: "memory"); __builtin_amdgcn_s_barrier(); asm volatile("" ::: "memory"); } while (0)
#define MB() asm volatile("" ::: "memory")
#define DRAIN() asm volatile("s_waitcnt vmcnt(0)" ::: "memory")
#define PIN4(x) asm volatile("" : "+v"(x))
#define MFMA16(a, b, c) __builtin_amdgcn_mfma_f32_16x16x32_bf16((a), (b), (c), 0, 0, 0)
#define MFMA32(a, b, c) __builtin_amdgcn_mfma_f32_32x32x16_bf16((a), (b), (c), 0, 0, 0)
#define SMF(a, b, c, idx) __builtin_amdgcn_smfmac_f32_16x16x64_bf16((a), (b), (c), (idx), 0, 0)
#define LD16(r, vo, so) __builtin_amdgcn_raw_buffer_load_b128((r), (vo), (so), 16)
#define LD8(r, vo, so) __builtin_amdgcn_raw_buffer_load_b64((r), (vo), (so), 16)
#define LD4(r, vo, so) __builtin_amdgcn_raw_buffer_load_b32((r), (vo), (so), 16)
#define ST16(v, r, vo) __builtin_amdgcn_raw_buffer_store_b128((v), (r), (vo), 0, 16)
#define ST8(v, r, vo) __builtin_amdgcn_raw_buffer_store_b64((v), (r), (vo), 0, 16)
#define ST4(v, r, vo) __builtin_amdgcn_raw_buffer_store_b32((v), (r), (vo), 0, 16)

__device__ __forceinline__ float swap32_sum(float a, float b) {
    auto r = __builtin_amdgcn_permlane32_swap(__float_as_uint(a), __float_as_uint(b), false, false);
    return __uint_as_float(r[0]) + __uint_as_float(r[1]); }
__device__ __forceinline__ float xmax(float m) {
    auto r = __builtin_amdgcn_permlane16_swap(__float_as_uint(m), __float_as_uint(m), false, false);
    m = fmaxf(__uint_as_float(r[0]), __uint_as_float(r[1]));
    auto q = __builtin_amdgcn_permlane32_swap(__float_as_uint(m), __float_as_uint(m), false, false);
    return fmaxf(__uint_as_float(q[0]), __uint_as_float(q[1])); }
__device__ __forceinline__ float xsum(float m) {
    auto r = __builtin_amdgcn_permlane16_swap(__float_as_uint(m), __float_as_uint(m), false, false);
    m = __uint_as_float(r[0]) + __uint_as_float(r[1]);
    auto q = __builtin_amdgcn_permlane32_swap(__float_as_uint(m), __float_as_uint(m), false, false);
    return __uint_as_float(q[0]) + __uint_as_float(q[1]); }
__device__ __forceinline__ float vmax(float a, float b) { float r; asm("v_max_f32 %0, %1, %2" : "=v"(r) : "v"(a), "v"(b)); return r; }
__device__ __forceinline__ float vmax3(float a, float b, float c) { float r; asm("v_max3_f32 %0, %1, %2, %3" : "=v"(r) : "v"(a), "v"(b), "v"(c)); return r; }
__device__ __forceinline__ u32 ror8(u32 v) { return (u32)__builtin_amdgcn_update_dpp(0, (int)v, 0x128, 0xf, 0xf, true); }
__device__ __forceinline__ float ror8f(float v) { return __uint_as_float(ror8(__float_as_uint(v))); }
__device__ __forceinline__ int wave_max(int d) {
    d = max(d, __builtin_amdgcn_update_dpp(d, d, 0x128, 0xf, 0xf, false));
    d = max(d, __builtin_amdgcn_update_dpp(d, d, 0x124, 0xf, 0xf, false));
    d = max(d, __builtin_amdgcn_update_dpp(d, d, 0x122, 0xf, 0xf, false));
    d = max(d, __builtin_amdgcn_update_dpp(d, d, 0x121, 0xf, 0xf, false));
    return max(max(__builtin_amdgcn_readlane(d, 0), __builtin_amdgcn_readlane(d, 16)), max(__builtin_amdgcn_readlane(d, 32), __builtin_amdgcn_readlane(d, 48)));
}

constexpr int SPIN_MAX = 2000000;
__device__ __forceinline__ int wait_flags(rsrc_t rw, int line0, int stride, int n, int need, int lane, int& budget, bool fast = false) {
    const int vo = (line0 + (lane < n ? lane : 0) * stride) * 128;
    int mn;
    for (;;) {
        const int v = (int)LD4(rw, vo, 0);
        MB();
        mn = -wave_max(-v);
        if (mn >= need) break;
        if (--budget < 0) break;
        if (fast) __builtin_amdgcn_s_sleep(2); else __builtin_amdgcn_s_sleep(12);
    }
    return mn;
}

template <int NRW, int CW>
__device__ __forceinline__ void rows_ld(v4u (&v)[NRW * CW / 512], const u16* g, int gs, int tid) {
#pragma unroll
    for (int j = 0; j < NRW * CW / 512; ++j) { const int i = tid + 512 * j, row = i / CW, c = i % CW; v[j] = *(const v4u*)(g + (size_t)row * gs + c * 8); }
}
template <int NRW, int CW>
__device__ __forceinline__ void rows_st(const v4u (&v)[NRW * CW / 512], lptr lds, int ls, int tid) {
#pragma unroll
    for (int j = 0; j < NRW * CW / 512; ++j) { const int i = tid + 512 * j, row = i / CW, c = i % CW; *(LAS v4u*)(lds + row * ls + c * 16) = v[j]; }
}
__device__ __forceinline__ bf16x8 frag16(lptr base, int ls, int lane) {
    const int i16 = lane & 15, bg = lane >> 4, q = i16 >> 2, p = i16 & 3;
    lptr a = base + (8 * bg + q) * ls + p * 8;
    return as8(trd(a), trd(a + 4 * ls));
}
__device__ __forceinline__ bf16x8 frag32p(lptr base, int ls, int lane) {
    const int i16 = lane & 15, G = lane >> 4, q = i16 >> 2, p = i16 & 3;
    lptr a = base + (8 * (G >> 1) + q) * ls + (16 * (p & 1) + 8 * (G & 1) + 4 * (p >> 1)) * 2;
    return as8(trd(a), trd(a + 4 * ls));
}

constexpr int XS = 1168;
__device__ __forceinline__ void xload(v4u (&st)[9], const u16* x, int tok0, int tid) {
#pragma unroll
    for (int j = 0; j < 9; ++j) st[j] = *(const v4u*)(x + (size_t)tok0 * DM + (tid + 512 * j) * 8);
}
__device__ __forceinline__ void xstore(const v4u (&st)[9], lptr xl, int tid) {
#pragma unroll
    for (int j = 0; j < 9; ++j) { const int i = tid + 512 * j; *(LAS v4u*)(xl + i * 16 + (i / 72) * 16) = st[j]; }
}
__device__ __forceinline__ void load_w2(bf16x8 (&A)[36], const u16* W, int h0, lptr lds, int tid, int lane, int w) {
    v4u v[6][6];
#pragma unroll
    for (int c = 0; c < 6; ++c) rows_ld<96, 32>(v[c], W + (size_t)c * 96 * 4096 + h0 * DH, 4096, tid);
    __builtin_amdgcn_sched_barrier(0);
#pragma unroll
    for (int c = 0; c < 6; ++c) {
        lptr l = lds + (c & 1) * (96 * 544);
        rows_st<96, 32>(v[c], l, 544, tid);
        LBAR();
#pragma unroll
        for (int ks = 0; ks < 6; ++ks) A[c * 6 + ks] = frag32p(l + ks * 16 * 544 + w * 64, 544, lane);
        __builtin_amdgcn_sched_barrier(0);
    }
    LBAR();
}

__device__ __forceinline__ f32x16 proj_tile(const bf16x8 (&A)[36], lptr xb) {
    f32x16 acc = {};
    bf16x8 f[6];
#pragma unroll
    for (int i = 0; i < 6; ++i) f[i] = lds8(xb + i * 32);
    __builtin_amdgcn_sched_barrier(0);
#pragma unroll
    for (int ks = 0; ks < 36; ++ks) {
        acc = MFMA32(A[ks], f[ks % 6], acc);
        if (ks + 6 < 36) f[ks % 6] = lds8(xb + (ks + 6) * 32);
        __builtin_amdgcn_sched_barrier(0);
    }
    return acc;
}
__device__ __forceinline__ void role_k2(const Args& a, lptr lds, rsrc_t rw, int cu) {
    const int tid = threadIdx.x, lane = tid & 63, w = __builtin_amdgcn_readfirstlane(tid >> 6);
    v4u st[9];
    bf16x8 A[36];
    xload(st, a.u, 32, tid);
    load_w2(A, a.Wk, 2 * cu, lds, tid, lane, w);
    xstore(st, lds, tid);
    LAS int* cnt = (LAS int*)(lds + 2 * 64 * XS);
    if (tid == 0) *cnt = 0;
    __syncthreads();
    const int o0 = OFF_KX + (2 * cu + (w >> 2)) * (NSEG * 8192) + ((w & 3) * 4 + (lane >> 5) * 2) * 512 + (lane & 31) * 16;
#pragma unroll 1
    for (int blk0 = 0; blk0 < 32; ++blk0) {
        int blk = blk0;
        asm volatile("" : "+s"(blk));
        lptr xl = lds + (blk & 1) * (64 * XS);
        const int sg0 = min(2 * blk + 1, 62);
        if (blk + 1 < 32) xload(st, a.u, min(2 * blk + 3, 62) * 32, tid);
#pragma unroll
        for (int tt = 0; tt < 2; ++tt) {
            const f32x16 acc = proj_tile(A, xl + (32 * tt + (lane & 31)) * XS + (lane >> 5) * 16);
            v4u c0 = {pack2(acc[0], acc[1]), pack2(acc[2], acc[3]), pack2(acc[4], acc[5]), pack2(acc[6], acc[7])};
            v4u c1 = {pack2(acc[8], acc[9]), pack2(acc[10], acc[11]), pack2(acc[12], acc[13]), pack2(acc[14], acc[15])};
            const int o = o0 + (sg0 + tt) * 8192;
            ST16(c0, rw, o); ST16(c1, rw, o + 512);
            if (blk == 0 && tt == 0) { DRAIN(); LBAR(); if (tid == 0) ST4(1u, rw, (FL_K + cu) * 128); }
            if (tt == 0 && blk >= 3) {
                asm volatile("s_waitcnt vmcnt(2)" ::: "memory");
                if (lane == 0) {
                    const int old = __hip_atomic_fetch_add(cnt, 1, __ATOMIC_RELAXED, __HIP_MEMORY_SCOPE_WORKGROUP);
                    if ((old & 7) == 7) ST4((u32)(2 * blk), rw, (FL_K + cu) * 128);
                }
            }
        }
        if (blk + 1 < 32) xstore(st, lds + ((blk + 1) & 1) * (64 * XS), tid);
        const bool full = blk < 2 || blk == 31;
        if (full) DRAIN();
        LBAR();
        if (tid == 0 && full) ST4((u32)(sg0 + 1), rw, (FL_K + cu) * 128);
    }
}

constexpr int VS = 272;
__device__ __forceinline__ void role_q2p(const Args& a, lptr lds, rsrc_t rw, int cu, int par) {
    const int tid = threadIdx.x, lane = tid & 63, w = __builtin_amdgcn_readfirstlane(tid >> 6);
    v4u st[9];
    bf16x8 A[36];
    load_w2(A, a.Wq, 2 * cu, lds, tid, lane, w);
    lptr xl = lds, ql = lds + 64 * XS;
    LAS int* cnt = (LAS int*)(lds + 64 * XS + 4 * 64 * VS);
    if (tid < 2) cnt[tid] = 0;
    xload(st, a.u, par * 64, tid);
    xstore(st, xl, tid);
    __syncthreads();
    int budget = SPIN_MAX, kdone = -1;
    const int i16 = lane & 15, g = lane >> 4;
    const int sg = w >> 2, hh = (w >> 1) & 1, qt = w & 1, h = 2 * cu + hh;
    const float c2 = 0.08838834764831845f * 1.4426950408889634f;
    for (int it = -1; it < 16; ++it) {
        const int blk = 2 * it + par;
        if (it + 1 < 16) {
            if (it + 2 < 16) xload(st, a.u, (blk + 4) * 64, tid);
            lptr qb = ql + (((it + 1) & 1) * 2 + (w >> 2)) * (64 * VS) + ((w & 3) * 32 + (lane >> 5) * 16) * 2;
#pragma unroll
            for (int tt = 0; tt < 2; ++tt) {
                const f32x16 acc = proj_tile(A, xl + (32 * tt + (lane & 31)) * XS + (lane >> 5) * 16);
                v4u c0 = {pack2(acc[0], acc[1]), pack2(acc[2], acc[3]), pack2(acc[4], acc[5]), pack2(acc[6], acc[7])};
                v4u c1 = {pack2(acc[8], acc[9]), pack2(acc[10], acc[11]), pack2(acc[12], acc[13]), pack2(acc[14], acc[15])};
                lptr qp = qb + (32 * tt + (lane & 31)) * VS;
                *(LAS v4u*)qp = c0; *(LAS v4u*)(qp + 16) = c1;
            }
            __syncthreads();
            if (it + 2 < 16) xstore(st, xl, tid);
        }
        if (it >= 0) {
            const int s = 2 * blk + sg;
            if (s == 0) wait_flags(rw, FL_M + h, 1, 1, 0, lane, budget);
            else if (kdone < s) kdone = wait_flags(rw, FL_K + cu, 1, 1, s, lane, budget);
            bf16x8 k0[4], k1[4], km[4], qf[4];
            const int ko = OFF_KX + (h * NSEG + s) * 8192 + g * 512 + i16 * 16;
#pragma unroll
            for (int ks = 0; ks < 4; ++ks) { k0[ks] = as8(LD16(rw, ko, ks * 2048)); k1[ks] = as8(LD16(rw, ko + 256, ks * 2048)); }
            lptr qp = ql + ((it & 1) * 2 + hh) * (64 * VS) + (32 * sg + 16 * qt + i16) * VS + g * 16;
#pragma unroll
            for (int ks = 0; ks < 4; ++ks) qf[ks] = lds8(qp + ks * 64);
            f32x4 S0 = {}, S1 = {}, Sm = {};
#pragma unroll
            for (int ks = 0; ks < 4; ++ks) { S0 = MFMA16(k0[ks], qf[ks], S0); S1 = MFMA16(k1[ks], qf[ks], S1); }
            wait_flags(rw, FL_M + h, 1, 1, s, lane, budget, s >= 62);
            const int mo = OFF_KM + (s * NH + h) * 2048 + g * 128 + (i16 & 7) * 16;
#pragma unroll
            for (int ks = 0; ks < 4; ++ks) km[ks] = as8(LD16(rw, mo + ks * 512, 0));
#pragma unroll
            for (int ks = 0; ks < 4; ++ks) Sm = MFMA16(km[ks], qf[ks], Sm);
            const int qi = 16 * qt + i16, t0 = 4 * g;
#pragma unroll
            for (int r = 0; r < 4; ++r) {
                if (t0 + r > qi) S0[r] = -INFINITY;
                if (t0 + r + 16 > qi) S1[r] = -INFINITY;
                if (g >= 2) Sm[r] = -INFINITY;
            }
            float m = fmaxf(fmaxf(fmaxf(S0[0], S0[1]), fmaxf(S0[2], S0[3])), fmaxf(fmaxf(S1[0], S1[1]), fmaxf(S1[2], S1[3])));
            m = fmaxf(m, fmaxf(fmaxf(Sm[0], Sm[1]), fmaxf(Sm[2], Sm[3])));
            m = xmax(m);
            const float nm = -m * c2;
            float sum = 0.f;
#pragma unroll
            for (int r = 0; r < 4; ++r) {
                S0[r] = __builtin_amdgcn_exp2f(fmaf(S0[r], c2, nm)); S1[r] = __builtin_amdgcn_exp2f(fmaf(S1[r], c2, nm)); Sm[r] = __builtin_amdgcn_exp2f(fmaf(Sm[r], c2, nm));
                sum += S0[r] + S1[r] + Sm[r];
            }
            sum = xsum(sum);
            const float inv = __builtin_amdgcn_rcpf(sum);
            v4u px = {pack2(S0[0] * inv, S0[1] * inv), pack2(S0[2] * inv, S0[3] * inv), pack2(S1[0] * inv, S1[1] * inv), pack2(S1[2] * inv, S1[3] * inv)};
            const int po = OFF_PX + (s * 2 + qt) * 40960;
            ST16(px, rw, po + h * 1024 + lane * 16);
            const u32 ma = pack2(Sm[0] * inv, Sm[1] * inv), mb = pack2(Sm[2] * inv, Sm[3] * inv);
            auto ra = __builtin_amdgcn_permlane16_swap(ma, ma, false, false);
            auto rb = __builtin_amdgcn_permlane16_swap(mb, mb, false, false);
            v4u pm = {ra[0], rb[0], ra[1], rb[1]};
            if (lane < 16) ST16(pm, rw, po + 32768 + h * 256 + lane * 16);
            DRAIN();
            if (lane == 0) {
                const int old = __hip_atomic_fetch_add(cnt + sg, 1, __ATOMIC_RELAXED, __HIP_MEMORY_SCOPE_WORKGROUP);
                if ((old & 3) == 3) ST4((u32)s, rw, (FL_P + 4 * cu + 2 * par + sg) * 128);
            }
        }
        __syncthreads();
    }
    if (budget < 0 && lane == 0) ST4(5u, rw, FL_ERR * 128);
}

__device__ __forceinline__ void role_vvo(const Args& a, lptr lds, rsrc_t rw, int h, int par) {
    const int tid = threadIdx.x, lane = tid & 63, w = __builtin_amdgcn_readfirstlane(tid >> 6);
    v4u st[9];
#pragma unroll
    for (int j = 0; j < 9; ++j) { const int i = tid + 512 * j; st[j] = *(const v4u*)(a.u + (size_t)(par * 32 + (i >= 2304 ? 32 : 0)) * DM + i * 8); }
    bf16x8 Av[18], Bo[5][4];
    {
        v4u v[3][6], o[2][9];
#pragma unroll
        for (int c = 0; c < 3; ++c) rows_ld<192, 16>(v[c], a.Wv + (size_t)c * 192 * 4096 + h * DH, 4096, tid);
#pragma unroll
        for (int c = 0; c < 2; ++c) rows_ld<64, 72>(o[c], a.Wo + ((size_t)h * DH + c * 64) * DM, DM, tid);
        __builtin_amdgcn_sched_barrier(0);
#pragma unroll
        for (int c = 0; c < 3; ++c) {
            lptr l = lds + (c & 1) * 77824;
            rows_st<192, 16>(v[c], l, 288, tid);
            LBAR();
#pragma unroll
            for (int ks = 0; ks < 6; ++ks) Av[c * 6 + ks] = frag16(l + ks * 32 * 288 + w * 32, 288, lane);
            __builtin_amdgcn_sched_barrier(0);
        }
#pragma unroll
        for (int c = 0; c < 2; ++c) {
            lptr l = lds + ((c + 1) & 1) * 77824;
            rows_st<64, 72>(o[c], l, 1184, tid);
            LBAR();
#pragma unroll
            for (int j = 0; j < 5; ++j) {
                const int r = (w + 8 * j < NR) ? w + 8 * j : w;
#pragma unroll
                for (int ks = 0; ks < 2; ++ks) Bo[j][c * 2 + ks] = frag16(l + ks * 32 * 1184 + r * 32, 1184, lane);
            }
            __builtin_amdgcn_sched_barrier(0);
        }
        LBAR();
    }
    lptr xl = lds, vl = lds + 64 * XS;
    xstore(st, xl, tid);
    __syncthreads();
    const int rho = lane & 15, bg = lane >> 4;
    const int vo0 = OFF_VOX + ((2 * w + ((rho >> 1) & 1)) * 16 + (h >> 1)) * 1024 + ((2 * (h & 1) + (bg & 1)) * 16 + (bg >> 1) * 8 + 2 * (rho >> 2) + (rho & 1)) * 16;
    const int fo = (FL_V + 2 * h + par) * 128;
    auto stage1 = [&](int tt) {
        f32x4 acc = {};
        lptr xb = xl + (16 * tt + rho) * XS + bg * 16;
        bf16x8 f[6];
#pragma unroll
        for (int i = 0; i < 6; ++i) f[i] = lds8(xb + i * 64);
        __builtin_amdgcn_sched_barrier(0);
#pragma unroll
        for (int ks = 0; ks < 18; ++ks) {
            acc = MFMA16(Av[ks], f[ks % 6], acc);
            if (ks + 6 < 18) f[ks % 6] = lds8(xb + (ks + 6) * 64);
            __builtin_amdgcn_sched_barrier(0);
        }
        v2u o = {pack2(acc[0], acc[1]), pack2(acc[2], acc[3])};
        *(LAS v2u*)(vl + (16 * tt + rho) * VS + (16 * w + 4 * bg) * 2) = o;
    };
    auto stage2 = [&](int sg, int s) {
        bf16x8 Af[2][4];
#pragma unroll
        for (int half = 0; half < 2; ++half)
#pragma unroll
            for (int ks = 0; ks < 4; ++ks) Af[half][ks] = lds8(vl + (32 * sg + rho + 16 * half) * VS + ks * 64 + bg * 16);
#pragma unroll
        for (int j = 0; j < 5; ++j) {
            if (j < 4 || w < 4) {
                f32x4 d0 = {}, d1 = {};
#pragma unroll
                for (int ks = 0; ks < 4; ++ks) { d0 = MFMA16(Af[0][ks], Bo[j][ks], d0); d1 = MFMA16(Af[1][ks], Bo[j][ks], d1); }
                v4u o = {pack2(d0[0], d0[1]), pack2(d0[2], d0[3]), pack2(d1[0], d1[1]), pack2(d1[2], d1[3])};
                ST16(o, rw, vo0 + (s * NR + 8 * j) * (NH * 1024));
            }
        }
    };
    int prev = -1;
#pragma unroll 1
    for (int un = 0; un < 32; ++un) {
        const int sg = un & 1, blk = (un >> 1) * 2 + par;
        const int s = un < 2 ? par + 2 * un : 2 * blk + sg;
        if (sg == 0 && blk + 2 < 32) xload(st, a.u, (blk + 2) * 64, tid);
        stage1(2 * sg); stage1(2 * sg + 1);
        if (sg == 1) DRAIN(); else if (blk + 2 < 32) asm volatile("s_waitcnt vmcnt(9)" ::: "memory"); else DRAIN();
        LBAR();
        if (tid == 0 && prev >= 0) ST4((u32)prev, rw, fo);
        if (sg == 1 && blk + 2 < 32) xstore(st, xl, tid);
        stage2(sg, s);
        prev = s;
        if (un == 0 || un == 31) { DRAIN(); LBAR(); if (tid == 0) ST4((u32)s, rw, fo); }
        else if (sg == 1) LBAR();
    }
}
#define VOWN(s) ((s) < 4 ? ((s) & 1) : (((s) >> 1) & 1))

__device__ __forceinline__ u32 rt32() { return (u32)__builtin_amdgcn_s_memrealtime(); }
__device__ __forceinline__ void wait_until(u32 t) {
    if ((int)(t - rt32()) > 1000) return;
    while ((int)(rt32() - t) < -24) __builtin_amdgcn_s_sleep(4);
    while ((int)(rt32() - t) < 0) {}
}
struct TK { u32 XA, XR, EA, ER; int dA0, dA1, dA2, dR0, dR1, dR2; int okA, okR, gap; };
__device__ __forceinline__ int tk_est(int d0, int d1, int d2) { return max(min(d0, d1), min(max(d0, d1), d2)); }
template <int N>
__device__ __forceinline__ u32 beacon_max(rsrc_t rw, int fb, int slot0, int lane, int& budget) {
    const int vo = BCO(slot0 + (lane < N ? lane : 0)), v2 = BCO(64 + (lane & 7));
    u32 v, u = 0;
    for (;;) {
        v = LD4(rw, vo, fb);
        if (N > 64) u = LD4(rw, v2, fb);
        MB();
        if (__all(max(v, u) != 0xFFFFFFFFu)) break;
        if (--budget < 0) break;
        __builtin_amdgcn_s_sleep(1);
    }
    const u32 base = __builtin_amdgcn_readfirstlane(v);
    int d = (int)(v - base);
    if (N > 64) d = max(d, (int)(u - base));
    return base + (u32)wave_max(d);
}
struct BC2 { u32 v, u; };
__device__ __forceinline__ BC2 beacon_issue(rsrc_t rw, int fb, int lane) {
    BC2 b;
    b.v = LD4(rw, BCO(lane), fb);
    b.u = LD4(rw, BCO(64 + (lane & 7)), fb);
    MB();
    return b;
}
__device__ __forceinline__ u32 beacon_finish(BC2 b, rsrc_t rw, int fb, int lane, int& budget) {
    if (!__all(max(b.v, b.u) != 0xFFFFFFFFu)) return beacon_max<NR8>(rw, fb, 0, lane, budget);
    const u32 base = __builtin_amdgcn_readfirstlane(b.v);
    return base + (u32)wave_max(max((int)(b.v - base), (int)(b.u - base)));
}
__device__ __forceinline__ void tk_T0(TK& k, u32 T0, const Args& a) {
    if (k.okR) { k.dR2 = k.dR1; k.dR1 = k.dR0; k.dR0 = (int)(T0 - k.ER); }
    k.okA = (int)(k.XA - T0) >= TK_LA - TK_TOL;
    if (!k.okR) k.gap = k.okA ? max(TK_GAP, k.gap - 6) : min(k.gap + 16, 160);
    k.EA = k.XA;
    k.XR = k.EA + tk_est(k.dA0, k.dA1, k.dA2) + TK_LR;
}
__device__ __forceinline__ void tk_Tp(TK& k, u32 Tp, const Args& a) {
    const int okn = (int)(k.XR - Tp) >= TK_LR - TK_TOL;
    if (k.okA && (k.okR || !okn)) { k.dA2 = k.dA1; k.dA1 = k.dA0; k.dA0 = (int)(Tp - k.EA); }
    k.okR = okn;
    k.ER = k.okR ? k.XR : Tp + k.gap;
    k.XA = k.ER + tk_est(k.dR0, k.dR1, k.dR2) + TK_LA;
}
__device__ __forceinline__ u32 tk_specA(const TK& k, const Args& a) { return k.XR + tk_est(k.dR0, k.dR1, k.dR2) + TK_LA; }
__device__ __forceinline__ u32 tk_specR(const TK& k, const Args& a) { return k.XA + tk_est(k.dA0, k.dA1, k.dA2) + TK_LR; }
__device__ __forceinline__ void tk_init(TK& k) { const u32 n = rt32(); k.XA = k.XR = k.EA = k.ER = n - 100000u; k.dA0 = k.dA1 = k.dA2 = 125; k.dR0 = k.dR1 = k.dR2 = 65; k.okA = k.okR = 0; k.gap = 50; }

__device__ __forceinline__ void tk_post(lptr p, int tag, u32 t, int lane) { if (lane == 0) { v2u v = {(u32)tag, t}; *(volatile LAS v2u*)p = v; } }
__device__ __forceinline__ u32 tk_get(lptr p, int tag, int& budget) {
    for (;;) {
        const v2u v = *(volatile LAS v2u*)p;
        if ((int)__builtin_amdgcn_readfirstlane(v.x) >= tag || --budget < 0) return __builtin_amdgcn_readfirstlane(v.y);
        __builtin_amdgcn_s_sleep(2);
    }
}
__device__ __forceinline__ void spin_to(u32 X) {
    int rem;
    asm volatile("1:\n\ts_memrealtime vcc\n\ts_waitcnt lgkmcnt(0)\n\ts_sub_i32 %0, %1, vcc_lo\n\ts_cmp_gt_i32 %0, 3\n\ts_cbranch_scc1 1b\n\t"
                 "s_cmp_lt_i32 %0, 1\n\ts_cbranch_scc1 2f\n\ts_nop 3\n\ts_cmp_lt_i32 %0, 2\n\ts_cbranch_scc1 2f\n\ts_nop 3\n\t"
                 "s_cmp_lt_i32 %0, 3\n\ts_cbranch_scc1 2f\n\ts_nop 7\n2:"
                 : "=&s"(rem) : "s"(X) : "memory", "scc", "vcc");
}
__device__ __forceinline__ void tk_wait(lptr p, int tag, int& budget) {
    u32 X;
    for (;;) {
        const v2u v = *(volatile LAS v2u*)p;
        const u32 now = rt32();
        X = __builtin_amdgcn_readfirstlane(v.y);
        const int tg = (int)__builtin_amdgcn_readfirstlane(v.x), dt = (int)(X - now);
        if (tg >= tag) {
            if (dt <= 0 || dt > 1000 || tg > tag) return;
            if (dt <= 28) break;
        }
        if (--budget < 0) return;
        __builtin_amdgcn_s_sleep(1);
    }
    spin_to(X);
}
constexpr int OOB = 0x70000000;
__device__ __forceinline__ void gather_mem(rsrc_t rw, int nb, lptr ml, int lane, int w, lptr tkp, int tag, int& budget) {
    const int gi = w == 7 ? 5 : w;
    const int l0 = gi * 1536 + lane * 16, l1 = l0 + 1024;
    const int o0 = l0 < 8192 ? l0 : l0 + (N_MEMB - 8192), o1 = lane < 32 ? (l1 < 8192 ? l1 : l1 + (N_MEMB - 8192)) : OOB;
    lptr p1 = ml + (lane < 32 ? l1 : 14336 + lane * 16);
    asm volatile("" : "+s"(nb));
    tk_wait(tkp, tag, budget);
    v4u v0 = LD16(rw, o0, nb), v1 = LD16(rw, o1, nb);
    MB();
    *(LAS v4u*)(ml + l0) = v0; *(LAS v4u*)p1 = v1;
    MB();
    if (__builtin_expect(!__all(max(bad4(v0), bad4(v1)) != 0xFFFFFFFFu), 0)) {
        do {
            v0 = LD16(rw, o0, nb); v1 = LD16(rw, o1, nb);
            MB();
        } while (!__all(max(bad4(v0), bad4(v1)) != 0xFFFFFFFFu) && --budget >= 0);
        *(LAS v4u*)(ml + l0) = v0; *(LAS v4u*)p1 = v1;
    }
}
__device__ __forceinline__ void gather_mem4(rsrc_t rw, int nb, lptr ml, int lane, int gi, lptr tkp, int tag, int& budget) {
    const int l0 = gi * 2048 + lane * 16;
    const int o2 = lane < 16 ? N_MEMB + gi * 256 + lane * 16 : OOB;
    lptr p2 = ml + (lane < 16 ? 8192 + gi * 256 + lane * 16 : 14336 + lane * 16);
    asm volatile("" : "+s"(nb));
    tk_wait(tkp, tag, budget);
    v4u v0 = LD16(rw, l0, nb), v1 = LD16(rw, l0 + 1024, nb), v2 = LD16(rw, o2, nb);
    MB();
    *(LAS v4u*)(ml + l0) = v0; *(LAS v4u*)(ml + l0 + 1024) = v1; *(LAS v4u*)p2 = v2;
    MB();
    if (__builtin_expect(!__all(max(max(bad4(v0), bad4(v1)), bad4(v2)) != 0xFFFFFFFFu), 0)) {
        do {
            v0 = LD16(rw, l0, nb); v1 = LD16(rw, l0 + 1024, nb); v2 = LD16(rw, o2, nb);
            MB();
        } while (!__all(max(max(bad4(v0), bad4(v1)), bad4(v2)) != 0xFFFFFFFFu) && --budget >= 0);
        *(LAS v4u*)(ml + l0) = v0; *(LAS v4u*)(ml + l0 + 1024) = v1; *(LAS v4u*)p2 = v2;
    }
}
__device__ __forceinline__ int decode_parity(u32 v, int lane) {
    u32 e = v & 0x00FF00FFu, o = (v >> 8) & 0x00FF00FFu;
    { auto r = __builtin_amdgcn_permlane16_swap(e, e, false, false); e = r[0] + r[1]; }
    { auto r = __builtin_amdgcn_permlane16_swap(o, o, false, false); o = r[0] + r[1]; }
    const int l0 = e & 0xFFFF, l2 = e >> 16, l1 = o & 0xFFFF, l3 = o >> 16;
    const bool par = l1 + l3 < l0 + l2;
    const bool hi = par ? l3 < l1 : l2 < l0;
    return (int)((__ballot(par) & 0xFFFFu) | ((__ballot(hi) & 0xFFFFu) << 16));
}
__device__ __forceinline__ int get_parity(rsrc_t rw, int lane, int& budget) {
    const int vo = (FL_PAR + (lane & 31)) * 128;
    u32 v;
    for (;;) {
        v = LD4(rw, vo, 0);
        MB();
        if (__all(v != 0xFFFFFFFFu) || --budget < 0) break;
        __builtin_amdgcn_s_sleep(8);
    }
    return decode_parity(v, lane);
}
__device__ __forceinline__ void role_probe(rsrc_t rw, int rep) {
    const int lane = threadIdx.x & 63, w = __builtin_amdgcn_readfirstlane(threadIdx.x >> 6);
    const int G = (w & 3) * 4 + rep;
    const int o = (G << 17) + N_PROBE + (w >> 2) * 512;
    ST4(0xFFFFFFFFu, rw, lane < 12 ? o + (lane & 3) * 8192 + (lane >> 2) * 128 : OOB);
    DRAIN();
    int best[4] = {254, 254, 254, 254};
#pragma unroll
    for (int k = 0; k < 12; ++k) {
        const unsigned long long t0 = __builtin_amdgcn_s_memtime();
        asm volatile("s_waitcnt lgkmcnt(0)" ::: "memory");
        u32 v = LD4(rw, o + (k & 3) * 8192 + (k >> 2) * 128, 0);
        asm volatile("s_waitcnt vmcnt(0)" : "+v"(v) :: "memory");
        const unsigned long long t1 = __builtin_amdgcn_s_memtime();
        best[k & 3] = min(best[k & 3], (int)(t1 - t0) >> 2);
    }
    if (lane == 0) ST4((u32)(best[0] | (best[1] << 8) | (best[2] << 16) | (best[3] << 24)), rw, (FL_PAR + (w >> 2) * 16 + G) * 128);
}
template <typename F>
__device__ __forceinline__ void qk_project_h(const bf16x16 (&Bq)[9], const bf16x16 (&Bk)[9], lptr ma, lptr xw, int idx, F hook) {
    f32x4 aq = {}, ak = {};
    bf16x8 A[9];
#pragma unroll
    for (int t = 0; t < 9; ++t) A[t] = lds8(ma + t * 1024);
    __builtin_amdgcn_sched_barrier(0);
#pragma unroll
    for (int t = 0; t < 9; ++t) {
        aq = SMF(A[t], Bq[t], aq, idx); ak = SMF(A[t], Bk[t], ak, idx);
        hook(t);
        __builtin_amdgcn_sched_barrier(0);
    }
    const float s0 = swap32_sum(aq[0], ak[0]), s1 = swap32_sum(aq[1], ak[1]), s2 = swap32_sum(aq[2], ak[2]), s3 = swap32_sum(aq[3], ak[3]);
    v2u o = {pack2(s0, s1), pack2(s2, s3)};
    *(LAS v2u*)xw = o;
}
__device__ __forceinline__ void qk_project(const bf16x16 (&Bq)[9], const bf16x16 (&Bk)[9], lptr ma, lptr xw, int idx) {
    qk_project_h(Bq, Bk, ma, xw, idx, [](int) {});
}

template <int GI>
__device__ __forceinline__ void gather_project(rsrc_t rw, int nb, lptr ml, int lane, lptr tkp, int tag, int& budget,
                                               const bf16x16 (&Bq)[9], const bf16x16 (&Bk)[9], lptr xw, int idx) {
    const int l0 = GI * 2048 + lane * 16;
    const int o2 = lane < 16 ? N_MEMB + GI * 256 + lane * 16 : OOB;
    lptr p2 = ml + (lane < 16 ? 8192 + GI * 256 + lane * 16 : 14336 + lane * 16);
    asm volatile("" : "+s"(nb));
    tk_wait(tkp, tag, budget);
    v4u v0 = LD16(rw, l0, nb), v1 = LD16(rw, l0 + 1024, nb), v2 = LD16(rw, o2, nb);
    MB();
    *(LAS v4u*)(ml + l0) = v0; *(LAS v4u*)(ml + l0 + 1024) = v1; *(LAS v4u*)p2 = v2;
    MB();
    if (__builtin_expect(!__all(max(max(bad4(v0), bad4(v1)), bad4(v2)) != 0xFFFFFFFFu), 0)) {
        do {
            v0 = LD16(rw, l0, nb); v1 = LD16(rw, l0 + 1024, nb); v2 = LD16(rw, o2, nb);
            MB();
        } while (!__all(max(max(bad4(v0), bad4(v1)), bad4(v2)) != 0xFFFFFFFFu) && --budget >= 0);
        *(LAS v4u*)(ml + l0) = v0; *(LAS v4u*)(ml + l0 + 1024) = v1; *(LAS v4u*)p2 = v2;
    }
    LBAR();
    bf16x8 A[9];
#pragma unroll
    for (int t = 0; t < 9; ++t) if (t != 2 * GI && t != 2 * GI + 1) A[t] = lds8(ml + lane * 16 + t * 1024);
    __builtin_amdgcn_sched_barrier(0);
    f32x4 aq = {}, ak = {};
    aq = SMF(as8(v0), Bq[2 * GI], aq, idx); ak = SMF(as8(v0), Bk[2 * GI], ak, idx);
    aq = SMF(as8(v1), Bq[2 * GI + 1], aq, idx); ak = SMF(as8(v1), Bk[2 * GI + 1], ak, idx);
    __builtin_amdgcn_sched_barrier(0);
#pragma unroll
    for (int t = 0; t < 9; ++t) if (t != 2 * GI && t != 2 * GI + 1) { aq = SMF(A[t], Bq[t], aq, idx); ak = SMF(A[t], Bk[t], ak, idx); __builtin_amdgcn_sched_barrier(0); }
    const float s0 = swap32_sum(aq[0], ak[0]), s1 = swap32_sum(aq[1], ak[1]), s2 = swap32_sum(aq[2], ak[2]), s3 = swap32_sum(aq[3], ak[3]);
    v2u o = {pack2(s0, s1), pack2(s2, s3)};
    *(LAS v2u*)xw = o;
}
__device__ __forceinline__ void role_qk(const Args& a, lptr lds, rsrc_t rw, int h, int rep) {
    const int tid = threadIdx.x, lane = tid & 63, w = __builtin_amdgcn_readfirstlane(tid >> 6);
    bf16x16 Bq[9], Bk[9];
    {
        v4u xr[6];
#pragma unroll
        for (int j = 0; j < 6; ++j) {
            const int i = min(tid + 512 * j, 2879), row = i / 72, c = i - row * 72;
            xr[j] = *(const v4u*)((row < 8 ? a.mem0 + row * DM : a.u + (row - 8) * DM) + c * 8);
        }
        v4u v[6][6];
#pragma unroll
        for (int c = 0; c < 6; ++c) rows_ld<192, 16>(v[c], (c >= 3 ? a.Wk : a.Wq) + (size_t)(c % 3) * 192 * 4096 + h * DH, 4096, tid);
        __builtin_amdgcn_sched_barrier(0);
#pragma unroll
        for (int j = 0; j < 6; ++j) {
            const int i = min(tid + 512 * j, 2879), row = i / 72, c = i - row * 72, G = row >> 3;
            lptr d = lds + (G < 2 ? 57344 + G * MEMB : 135168 + (G - 2) * MEMB) + (c >> 1) * 256 + (row & 7) * 16 + (c & 1) * 8;
            *(LAS v2u*)d = v2u{xr[j].x, xr[j].z}; *(LAS v2u*)(d + 128) = v2u{xr[j].y, xr[j].w};
        }
#pragma unroll
        for (int c = 0; c < 6; ++c) {
            lptr l = lds + (c & 1) * 77824;
            rows_st<192, 16>(v[c], l, 288, tid);
            LBAR();
#pragma unroll
            for (int t = 0; t < 3; ++t) {
                bf16x16 f = cat16(frag16(l + (64 * t) * 288 + w * 32, 288, lane), frag16(l + (64 * t + 32) * 288 + w * 32, 288, lane));
                if (c >= 3) Bk[(c % 3) * 3 + t] = f; else Bq[c * 3 + t] = f;
            }
            __builtin_amdgcn_sched_barrier(0);
        }
        LBAR();
    }
    lptr ml = lds, xl = lds + MEMB;
    const int idx = (lane & 8) ? 0xEEEE : 0x4444;
    const int i16 = lane & 15, g = lane >> 4;
    const bool hi8 = (lane & 8) != 0;
    int budget = SPIN_MAX;
    lptr pq = xl + (8 * g + (i16 >> 2)) * 32 + (i16 & 1) * 8;
    lptr kl = lds + 16384 + lane * 16;
    lptr ma = ml + lane * 16, xw = xl + (16 * w + i16) * 32 + g * 8;
    const int ox = N_PX + (h >> 4) * 32768 + (h & 15) * 512 + ((g & 1) * 16 + (g >> 1) * 8 + (i16 & 7)) * 16;
    const int om = hi8 ? OOB : N_PMM + h * 128 + (i16 & 7) * 16;
    const int ob = lane < NREP ? BCO(BC_QK + h) : OOB;
    lptr tkp = lds + MEMB + 4096, kfp = tkp + 64;
    if (tid == 0) { tk_post(tkp, 0, rt32() - 100000u, 0); tk_post(kfp, 0, 0u, 0); }
    for (int c = tid; c < 576; c += 512) *(LAS v4u*)(ml + c * 16) = *(LAS v4u*)(lds + 57344 + c * 16);
    __syncthreads();
#pragma unroll 1
    for (int G = 0; G < 4; ++G) {
        qk_project(Bq, Bk, lds + (G ? 135168 + (G - 1) * MEMB : 57344 + MEMB) + lane * 16, xw, idx);
        LBAR();
        if (w < 4) {
            const bf16x8 F = as8(trd(pq + w * 1024 + 16), trd(pq + w * 1024 + 16 + 128));
            if ((i16 >> 3) == (G & 1)) *(LAS bf16x8*)(kl + ((G >> 1) * 4 + w) * 1024) = F;
        }
        LBAR();
    }
    const int pm = get_parity(rw, lane, budget);
    LBAR();
    if (w == 0) {
        const float c2 = 0.08838834764831845f * 1.4426950408889634f;
        for (int s = 0; s < NSEG - 1; ++s) {
            tk_get(kfp, s, budget);
            bf16x8 kx[2][4];
#pragma unroll
            for (int tl = 0; tl < 2; ++tl)
#pragma unroll
                for (int ks = 0; ks < 4; ++ks) kx[tl][ks] = lds8(kl + (s & 1) * 8192 + (tl * 4 + ks) * 1024);
            int oxs = ox + NEARB(s, i16 >> 3, pm), oxt = ox + NEARB(s, 2 + (i16 >> 3), pm), oms = om + NEARB(s, g, pm), obs = ob + FARB(s, lane & 3, pm);
            const int nb = NEARB(s, rep, pm);
            asm volatile("s_waitcnt lgkmcnt(0)" : "+v"(oxs), "+v"(oxt), "+v"(oms), "+v"(obs) :: "memory");
#pragma unroll
            for (int tl = 0; tl < 2; ++tl)
#pragma unroll
                for (int ks = 0; ks < 4; ++ks) PIN4(kx[tl][ks]);
            if (s > 0) gather_project<0>(rw, nb, ml, lane, tkp, s, budget, Bq, Bk, xw, idx);
            else { LBAR(); qk_project(Bq, Bk, ma, xw, idx); }
            __builtin_amdgcn_s_setprio(3);
            LBAR();
            bf16x8 Fq[4], Fk[4];
#pragma unroll
            for (int ks = 0; ks < 4; ++ks) Fq[ks] = as8(trd(pq + ks * 1024), trd(pq + ks * 1024 + 128));
            __builtin_amdgcn_sched_barrier(0);
#pragma unroll
            for (int ks = 0; ks < 4; ++ks) Fk[ks] = as8(trd(pq + ks * 1024 + 16), trd(pq + ks * 1024 + 16 + 128));
            __builtin_amdgcn_sched_barrier(0);
            f32x4 S0 = {}, S1 = {}, Sm = {};
            S0 = MFMA16(kx[0][0], Fq[0], S0); S1 = MFMA16(kx[1][0], Fq[0], S1);
            S0 = MFMA16(kx[0][1], Fq[1], S0); S1 = MFMA16(kx[1][1], Fq[1], S1);
            __builtin_amdgcn_sched_barrier(0);
            Sm = MFMA16(Fk[0], Fq[0], Sm); S0 = MFMA16(kx[0][2], Fq[2], S0);
            Sm = MFMA16(Fk[1], Fq[1], Sm); S1 = MFMA16(kx[1][2], Fq[2], S1);
            Sm = MFMA16(Fk[2], Fq[2], Sm); S0 = MFMA16(kx[0][3], Fq[3], S0);
            Sm = MFMA16(Fk[3], Fq[3], Sm); S1 = MFMA16(kx[1][3], Fq[3], S1);
            __builtin_amdgcn_sched_barrier(0);
            float y0 = hi8 ? Sm[2] : Sm[0], y1 = hi8 ? Sm[3] : Sm[1];
            float x0 = hi8 ? S1[0] : S0[0], x1 = hi8 ? S1[1] : S0[1], x2 = hi8 ? S1[2] : S0[2], x3 = hi8 ? S1[3] : S0[3];
            const float ml_ = vmax3(vmax3(y0, y1, x0), x1, vmax(x2, x3));
            const float nm = -ml_ * c2;
            float m = vmax(ml_, ror8f(ml_));
            x0 = __builtin_amdgcn_exp2f(fmaf(x0, c2, nm)); x1 = __builtin_amdgcn_exp2f(fmaf(x1, c2, nm));
            { auto r = __builtin_amdgcn_permlane16_swap(__float_as_uint(m), __float_as_uint(m), false, false); m = vmax(__uint_as_float(r[0]), __uint_as_float(r[1])); }
            x2 = __builtin_amdgcn_exp2f(fmaf(x2, c2, nm)); x3 = __builtin_amdgcn_exp2f(fmaf(x3, c2, nm));
            { auto r = __builtin_amdgcn_permlane32_swap(__float_as_uint(m), __float_as_uint(m), false, false); m = vmax(__uint_as_float(r[0]), __uint_as_float(r[1])); }
            y0 = __builtin_amdgcn_exp2f(fmaf(y0, c2, nm)); y1 = __builtin_amdgcn_exp2f(fmaf(y1, c2, nm));
            const float f = __builtin_amdgcn_exp2f((ml_ - m) * c2);
            float sum = f * fmaf(0.5f, y0 + y1, (x0 + x1) + (x2 + x3));
            sum += ror8f(sum);
            sum = xsum(sum);
            const float inv = f * __builtin_amdgcn_rcpf(sum);
            const u32 pa = pack2(x0 * inv, x1 * inv) & 0x7fff7fffu, pb = pack2(x2 * inv, x3 * inv) & 0x7fff7fffu, pc = pack2(y0 * inv, y1 * inv) & 0x7fff7fffu;
            const u32 qc = ror8(pc);
            v4u px = {(u32)__builtin_amdgcn_update_dpp((int)pa, (int)pa, 0x128, 0xf, 0xc, false), (u32)__builtin_amdgcn_update_dpp((int)pb, (int)pb, 0x128, 0xf, 0xc, false),
                      (u32)__builtin_amdgcn_update_dpp((int)pa, (int)pa, 0x128, 0xf, 0x3, false), (u32)__builtin_amdgcn_update_dpp((int)pb, (int)pb, 0x128, 0xf, 0x3, false)};
            ST16(px, rw, oxs);
            ST16(px, rw, oxt);
            auto ra = __builtin_amdgcn_permlane16_swap(pc, pc, false, false);
            auto rc = __builtin_amdgcn_permlane16_swap(qc, qc, false, false);
            v4u pmv = {ra[0], rc[0], ra[1], rc[1]};
            ST16(pmv, rw, oms);
            __builtin_amdgcn_sched_barrier(0);
            ST4(nos(rt32()), rw, obs);
            __builtin_amdgcn_s_setprio(0);
        }
        {
            gather_project<0>(rw, NEARB(NSEG - 1, rep, pm), ml, lane, tkp, NSEG - 1, budget, Bq, Bk, xw, idx);
            LBAR();
        }
    } else if (w == 5) {
        int kdone = -1;
        const int kxo = OFF_KX + h * (NSEG * 8192) + g * 512 + i16 * 16;
        for (int s = 0; s < NSEG; ++s) {
            const bool need = s + 1 < NSEG - 1;
            if (need && kdone < s + 1) kdone = wait_flags(rw, FL_K + (h >> 1), 1, 1, -1, lane, budget);
            const bool go = need && kdone >= s + 1;
            int ko = (go ? kxo : OOB) + (s + 1) * 8192;
            int a0 = ox + NEARB(s + 2, i16 >> 3, pm), a1 = ox + NEARB(s + 2, 2 + (i16 >> 3), pm), a2 = om + NEARB(s + 2, g, pm), a3 = ob + FARB(s + 4, lane & 3, pm);
            v4u ff = {0xFFFFFFFFu, 0xFFFFFFFFu, 0xFFFFFFFFu, 0xFFFFFFFFu};
            asm volatile("" : "+v"(ko), "+v"(a0), "+v"(a1), "+v"(a2), "+v"(a3), "+v"(ff));
            LBAR();
            v4u kx[8];
            qk_project_h(Bq, Bk, ma, xw, idx, [&](int t) {
                if (t < 4) { kx[t] = LD16(rw, ko + (t & 1) * 2048, (t >> 1) * 4096); kx[4 + t] = LD16(rw, ko + 256 + (t & 1) * 2048, (t >> 1) * 4096); }
                if (t == 4) { ST16(ff, rw, a0); ST16(ff, rw, a1); }
                if (t == 5) { ST16(ff, rw, a2); ST4(0xFFFFFFFFu, rw, a3); }
            });
            LBAR();
            if (need) {
                if (!go) {
                    kdone = wait_flags(rw, FL_K + (h >> 1), 1, 1, s + 1, lane, budget, true);
#pragma unroll
                    for (int k = 0; k < 8; ++k) kx[k] = LD16(rw, kxo + (k >> 2) * 256 + (k & 3) * 2048, (s + 1) * 8192);
                }
#pragma unroll
                for (int k = 0; k < 8; ++k) *(LAS v4u*)(kl + ((s + 1) & 1) * 8192 + k * 1024) = kx[k];
                asm volatile("s_waitcnt lgkmcnt(0)" ::: "memory");
                tk_post(kfp, s + 1, 0u, lane);
            }
            DRAIN();
        }
    } else if (w == 1 || w == 2 || w == 7) {
        LBAR();
        qk_project(Bq, Bk, ma, xw, idx);
        LBAR();
        for (int s = 1; s < NSEG; ++s) {
            __builtin_amdgcn_s_sleep(14);
            const int nb = NEARB(s, rep, pm);
            if (w == 1) gather_project<1>(rw, nb, ml, lane, tkp, s, budget, Bq, Bk, xw, idx);
            else if (w == 2) gather_project<2>(rw, nb, ml, lane, tkp, s, budget, Bq, Bk, xw, idx);
            else gather_project<3>(rw, nb, ml, lane, tkp, s, budget, Bq, Bk, xw, idx);
            LBAR();
        }
    } else {
        TK tk; tk_init(tk);
        const int kmo = i16 < 8 ? OFF_KM + h * 2048 + g * 128 + i16 * 16 : OOB;
        for (int s = 0; s < NSEG; ++s) {
            if (s > 0 && w == 3) tk_wait(tkp, s, budget);
            BC2 bc = {0u, 0u};
            if (w == 3) bc = beacon_issue(rw, FARB(s, rep, pm), lane);
            LBAR();
            qk_project(Bq, Bk, ma, xw, idx);
            LBAR();
            if (w == 6) {
                bf16x8 F[4];
#pragma unroll
                for (int ks = 0; ks < 4; ++ks) F[ks] = as8(trd(pq + ks * 1024 + 16), trd(pq + ks * 1024 + 16 + 128));
#pragma unroll
                for (int ks = 0; ks < 4; ++ks) ST16(asu(F[ks]), rw, kmo + s * (NH * 2048) + ks * 512);
                if (s == 0) {
#pragma unroll
                    for (int k = 0; k < 8; ++k) ST16(*(LAS v4u*)(kl + k * 1024), rw, OFF_KX + h * (NSEG * 8192) + g * 512 + i16 * 16 + (k >> 2) * 256 + (k & 3) * 2048);
                }
                DRAIN();
                if (lane == 0) ST4((u32)s, rw, (FL_M + h) * 128);
            } else if (w == 3) {
                if (s < NSEG - 1) {
                    if (s > 0) { tk_T0(tk, beacon_finish(bc, rw, FARB(s, rep, pm), lane, budget), a); if (tk.okA) tk_post(tkp, s + 1, tk_specA(tk, a), lane); }
                    wait_until(tk.XR + 8);
                    tk_Tp(tk, beacon_max<NH>(rw, FARB(s, rep, pm), BC_QK, lane, budget), a);
                    tk_post(tkp, s + 1, tk.XA, lane);
                }
            } else __builtin_amdgcn_s_sleep(14);
        }
    }
    if (budget < 0 && lane == 0) ST4(1u, rw, FL_ERR * 128);
}

__device__ __forceinline__ void role_wvo(const Args& a, lptr lds, rsrc_t rw, int idx) {
    const int tid = threadIdx.x, lane = tid & 63, w = __builtin_amdgcn_readfirstlane(tid >> 6);
    const int h = idx / 3, part = idx - 3 * h;
    bf16x8 Bo[5][4];
    v4u v[6], o[2][9];
#pragma unroll
    for (int c = 0; c < 2; ++c) rows_ld<64, 72>(o[c], a.Wo + ((size_t)h * DH + c * 64) * DM, DM, tid);
    rows_ld<192, 16>(v, a.Wv + (size_t)part * 192 * 4096 + h * DH, 4096, tid);
    __builtin_amdgcn_sched_barrier(0);
#pragma unroll
    for (int c = 0; c < 2; ++c) {
        lptr l = lds + c * 77824;
        rows_st<64, 72>(o[c], l, 1184, tid);
        LBAR();
#pragma unroll
        for (int j = 0; j < 5; ++j) {
            const int r = (w + 8 * j < NR) ? w + 8 * j : w;
#pragma unroll
            for (int ks = 0; ks < 2; ++ks) Bo[j][c * 2 + ks] = frag16(l + ks * 32 * 1184 + r * 32, 1184, lane);
        }
        __builtin_amdgcn_sched_barrier(0);
    }
    rows_st<192, 16>(v, lds, VS, tid);
    LBAR();
    const int rho = lane & 15, bg = lane >> 4;
    const int wo0 = OFF_WVO + (2 * w + ((rho >> 1) & 1)) * 294912 + ((h >> 1) * 18 + part * 6) * 1024 + ((h & 1) * 32 + bg * 8 + 2 * (rho >> 2) + (rho & 1)) * 16;
#pragma unroll 1
    for (int kb = 0; kb < 6; ++kb) {
        bf16x8 Af[2][4];
#pragma unroll
        for (int mt = 0; mt < 2; ++mt)
#pragma unroll
            for (int ks = 0; ks < 4; ++ks) Af[mt][ks] = lds8(lds + (32 * kb + 8 * (rho >> 2) + 4 * mt + (rho & 3)) * VS + ks * 64 + bg * 16);
#pragma unroll
        for (int j = 0; j < 5; ++j) {
            if (j < 4 || w < 4) {
                f32x4 d0 = {}, d1 = {};
#pragma unroll
                for (int ks = 0; ks < 4; ++ks) { d0 = MFMA16(Af[0][ks], Bo[j][ks], d0); d1 = MFMA16(Af[1][ks], Bo[j][ks], d1); }
                v4u q = {pack2(d0[0], d0[1]), pack2(d0[2], d0[3]), pack2(d1[0], d1[1]), pack2(d1[2], d1[3])};
                ST16(q, rw, wo0 + j * (16 * 294912) + kb * 1024);
            }
        }
        if (kb == 2 || kb == 4) {
            asm volatile("s_waitcnt vmcnt(4)" ::: "memory");
            LBAR();
            if (tid == 0) ST4((u32)(kb >> 1), rw, (FL_W + idx) * 128);
        }
    }
    DRAIN();
    LBAR();
    if (tid == 0) ST4(3u, rw, (FL_W + idx) * 128);
}

__device__ __forceinline__ void role_rz(const Args& a, lptr lds, rsrc_t rw, int r, int rep) {
    const int tid = threadIdx.x, lane = tid & 63, w = __builtin_amdgcn_readfirstlane(tid >> 6);
    const int idx = (lane & 8) ? 0xEEEE : 0x4444;
    const int i16 = lane & 15, g = lane >> 4;
    int budget = SPIN_MAX;
    bf16x16 Ba[9], Bb[9];
    v4u t0[4], m0[2];
    int pm;
    {
        const int wo = OFF_WVO + r * 294912 + 2 * w * 18432 + ((i16 >> 3) * 32 + g * 8 + (i16 & 7)) * 16;
        const int vq = OFF_VOX + r * 16384 + w * 2048 + lane * 16;
#pragma unroll
        for (int j = 0; j < 2; ++j) { const int i = min(tid + 512 * j, 575), row = i / 72, c = i - row * 72; m0[j] = *(const v4u*)(a.mem0 + row * DM + c * 8); }
        const int vf = (int)LD4(rw, (FL_V + lane) * 128, 0);
        const u32 pw = LD4(rw, (FL_PAR + (lane & 31)) * 128, 0);
        int have = 0;
        bool vgot = false;
#pragma unroll
        for (int p = 0; p < 3; ++p) {
            if (have < p + 1) have = wait_flags(rw, FL_W + 12 * w, 1, 12, p + 1, lane, budget, true);
            if (p == 0 || p == 2) {
                const bool now = p == 0 ? (bool)__all(vf >= 0) : !vgot;
                if (now) {
                    if (p == 2) wait_flags(rw, FL_V, 1, 64, 0, lane, budget, true);
#pragma unroll
                    for (int k = 0; k < 4; ++k) t0[k] = LD16(rw, vq + (k & 1) * 1024, (k >> 1) * (NR * NH * 1024));
                    vgot = true;
                }
            }
            if (p == 0) pm = __all(pw != 0xFFFFFFFFu) ? decode_parity(pw, lane) : get_parity(rw, lane, budget);
#pragma unroll
            for (int q = 0; q < 3; ++q) {
                const int t = 3 * q + p;
                Ba[t] = cat16(as8(LD16(rw, wo, t * 2048)), as8(LD16(rw, wo + 1024, t * 2048)));
                Bb[t] = cat16(as8(LD16(rw, wo, 18432 + t * 2048)), as8(LD16(rw, wo + 1024, 18432 + t * 2048)));
            }
            MB();
        }
#pragma unroll
        for (int j = 0; j < 2; ++j) {
            const int i = min(tid + 512 * j, 575), row = i / 72, c = i - row * 72;
            lptr d = lds + (c >> 1) * 256 + row * 16 + (c & 1) * 8;
            *(LAS v2u*)d = v2u{m0[j].x, m0[j].z}; *(LAS v2u*)(d + 128) = v2u{m0[j].y, m0[j].w};
        }
    }
    lptr ml = lds, zl = lds + MEMB;
    lptr tka = lds + 13312, tkr = tka + 16, cntp = tka + 64;
    volatile LAS int* prod = (volatile LAS int*)(lds + 13312 + 512);
    lptr ring = lds + 16384;
    lptr ma = ml + lane * 16;
    lptr zw = zl + ((2 * w + (lane >> 5)) >> 2) * 1024 + ((((2 * w + (lane >> 5)) & 3) * 16 + (g & 1) * 8 + (i16 & 7)) * 16) + (i16 >> 3) * 8;
    const bool hi8r = (lane & 8) != 0;
    const int mso = i16 < 8 ? (r < 64 ? N_MEMA + r * 128 : N_MEMB + (r - 64) * 128) + i16 * 16 : OOB;
    const int ob = lane < NREP ? BCO(r) : OOB;
    if (tid == 0) { tk_post(tka, 0, rt32() - 100000u, 0); tk_post(tkr, -1, 0u, 0); *(volatile LAS int*)cntp = 0; }
#pragma unroll
    for (int k = 0; k < 4; ++k) *(LAS v4u*)(ring + (k >> 1) * 16384 + w * 2048 + (k & 1) * 1024 + lane * 16) = t0[k];
    if (tid < 4) prod[tid] = 2;
    LBAR();
    if (w < 4) {
        const int po = N_PX + (w >> 1) * 32768 + (w & 1) * 4096 + lane * 16;
        const int mo = N_PMM + w * 1024 + 2 * g * 128 + (i16 & 7) * 16 + (i16 >> 3) * 8;
        lptr cna = lane == 0 ? cntp : cntp + 4 + lane * 4;
        const u32 one = lane == 0 ? 1u : 0u;
        const bool dv = (g >> 1) == (i16 >> 3);
        const int rwo = dv ? (4 * (g & 1) + 2 * (g >> 1)) * 128 + ((i16 & 7) >> 1) * 32 + w * 8 + (i16 & 1) * 4 : 2048 + lane * 4;
        const int msq = lane < 32 ? (r < 64 ? N_MEMA + r * 128 : N_MEMB + (r - 64) * 128) + (lane >> 2) * 16 : OOB;
        lptr zrd = zl + w * 1024 + lane * 16;
        for (int s = 0; s < NSEG; ++s) {
            lptr Rw = lds + 81920 + (s & 1) * 1024 + rwo, Rr = lds + 81920 + (s & 1) * 1024 + (lane & 31) * 32;
            int mss = msq + NEARB(s + 1, lane & 3, pm), obs = ob + FARB(s + 1, lane & 3, pm);
            int sb = NEARB(s, rep, pm);
            asm volatile("" : "+s"(sb));
            lptr rg = ring + (s & 3) * 16384 + w * 4096 + lane * 16;
            bf16x8 b[5];
            const bool early = __builtin_amdgcn_readfirstlane(prod[w >> 1]) > s;
            MB();
#pragma unroll
            for (int k = 0; k < 4; ++k) b[k] = lds8(rg + k * 1024);
            const u32 X = s > 0 ? tk_get(tkr, min(s, NSEG - 2), budget) : 0u;
            asm volatile("s_waitcnt lgkmcnt(0)" : "+v"(mss), "+v"(obs), "+v"(Rw), "+v"(Rr) :: "memory");
#pragma unroll
            for (int k = 0; k < 4; ++k) PIN4(b[k]);
            LBAR();
            qk_project(Ba, Bb, ma, zw, idx);
            LBAR();
            if (s == NSEG - 1) break;
            __builtin_amdgcn_s_setprio(3);
            b[4] = lds8(zrd);
            if (!early) {
                while (__builtin_amdgcn_readfirstlane(prod[w >> 1]) <= s && --budget >= 0) __builtin_amdgcn_s_sleep(1);
                MB();
#pragma unroll
                for (int k = 0; k < 4; ++k) b[k] = lds8(rg + k * 1024);
            }
            if (s > 0 && (int)(X - rt32()) < 1000) spin_to(X);
            f32x4 d;
            u32 bad;
            auto shot = [&]() {
                v4u p[4]; v2u m[2];
                m[0] = LD8(rw, mo, sb); m[1] = LD8(rw, mo + 128, sb);
                __builtin_amdgcn_sched_barrier(0);
#pragma unroll
                for (int k = 0; k < 4; ++k) { p[k] = LD16(rw, po + k * 1024, sb); __builtin_amdgcn_sched_barrier(0); }
                MB();
                d = f32x4{};
                d = MFMA16(as8(m[0], m[1]), b[4], d);
                bad = (m[0].x | m[0].y) | (m[1].x | m[1].y);
                __builtin_amdgcn_sched_barrier(0);
#pragma unroll
                for (int k = 0; k < 4; ++k) {
                    d = MFMA16(as8(p[k]), b[k], d);
                    bad |= (p[k].x | p[k].y) | (p[k].z | p[k].w);
                    __builtin_amdgcn_sched_barrier(0);
                }
            };
            shot();
            if (__builtin_expect(!__all((int)bad >= 0), 0)) {
                do shot(); while (!__all((int)bad >= 0) && --budget >= 0);
            }
            float e0, e1;
            {
                auto r0 = __builtin_amdgcn_permlane32_swap(__float_as_uint(d[0]), __float_as_uint(d[2]), false, false);
                auto r1 = __builtin_amdgcn_permlane32_swap(__float_as_uint(d[1]), __float_as_uint(d[3]), false, false);
                const float t0 = __uint_as_float(hi8r ? r0[1] : r0[0]), t1 = __uint_as_float(hi8r ? r1[1] : r1[0]);
                e0 = t0 + ror8f(t0); e1 = t1 + ror8f(t1);
            }
            f32x4 x0, x1; u32 old;
            asm volatile("ds_write2_b32 %4, %5, %6 offset1:32\n\tds_add_rtn_u32 %0, %7, %8\n\tds_read_b128 %1, %3\n\tds_read_b128 %2, %3 offset:16\n\ts_waitcnt lgkmcnt(0)"
                         : "=&v"(old), "=&v"(x0), "=&v"(x1) : "v"(Rr), "v"(Rw), "v"(e0), "v"(e1), "v"(cna), "v"(one) : "memory");
            {
                const u32 c = nos(pack2((x0[0] + x0[2]) + (x1[0] + x1[2]), (x0[1] + x0[3]) + (x1[1] + x1[3])));
                v4u o = {(u32)__builtin_amdgcn_update_dpp(0, (int)c, 0x00, 0xf, 0xf, true), (u32)__builtin_amdgcn_update_dpp(0, (int)c, 0x55, 0xf, 0xf, true),
                         (u32)__builtin_amdgcn_update_dpp(0, (int)c, 0xAA, 0xf, 0xf, true), (u32)__builtin_amdgcn_update_dpp(0, (int)c, 0xFF, 0xf, 0xf, true)};
                const bool last = (__builtin_amdgcn_readfirstlane(old) & 3) == 3;
                ST16(o, rw, last ? mss : OOB);
                __builtin_amdgcn_sched_barrier(0);
                if (last) ST4(nos(rt32()), rw, obs);
            }
            __builtin_amdgcn_s_setprio(0);
        }
    } else if (w == 5 || w == 6) {
        int vdone0 = -1, vdone1 = -1, staged = 2;
        const int hw = w - 5;
        const int vo = OFF_VOX + r * 16384 + hw * 8192 + lane * 16;
        const int zho = OFF_ZH + r * 4096 + hw * 2048 + lane * 16;
        lptr zhr = zl + hw * 2048 + (((lane >> 4) & 3) * 16 + (lane & 7)) * 16 + ((lane >> 3) & 1) * 8;
        const int aro = w == 5 ? mso : OOB, arb = w == 5 ? ob : OOB;
        v4u zv[2] = {};
        for (int s = 0; s < NSEG; ++s) {
            bool go = false;
            if (staged <= min(s + 3, NSEG - 2)) {
                const int ow = VOWN(staged);
                int& vd = ow ? vdone1 : vdone0;
                if (vd < staged) vd = wait_flags(rw, FL_V + ow, 2, NH, -1, lane, budget);
                go = vd >= staged;
            }
            const int vos = go ? vo : OOB;
            int va = vos + staged * (NR * NH * 1024), zs = s > 0 ? zho + (s - 1) * (NR * NH * 256) : OOB;
            int a0 = aro + NEARB(s + 3, g, pm), a1 = arb + FARB(s + 4, lane & 3, pm);
            v4u ff = {0xFFFFFFFFu, 0xFFFFFFFFu, 0xFFFFFFFFu, 0xFFFFFFFFu};
            asm volatile("" : "+v"(va), "+v"(zs), "+v"(a0), "+v"(a1), "+v"(ff));
            if (s > 0) gather_mem4(rw, NEARB(s, rep, pm), ml, lane, w - 4, tka, s, budget);
            LBAR();
            v4u v[8];
            qk_project_h(Ba, Bb, ma, zw, idx, [&](int t) {
                if (t < 4) { v[2 * t] = LD16(rw, va + (t & 1) * 2048, (t >> 1) * 4096); v[2 * t + 1] = LD16(rw, va + 1024 + (t & 1) * 2048, (t >> 1) * 4096); }
                if (t == 4) { ST16(zv[0], rw, zs); ST16(zv[1], rw, zs + 1024); }
                if (t == 5) { ST16(ff, rw, a0); ST4(0xFFFFFFFFu, rw, a1); }
            });
            LBAR();
#pragma unroll
            for (int k = 0; k < 2; ++k) {
                const v2u lo = *(LAS v2u*)(zhr + k * 1024), hi = *(LAS v2u*)(zhr + k * 1024 + 128);
                zv[k] = v4u{lo.x, lo.y, hi.x, hi.y};
            }
            for (;;) {
                if (go) {
#pragma unroll
                    for (int k = 0; k < 8; ++k) *(LAS v4u*)(ring + (staged & 3) * 16384 + hw * 8192 + k * 1024 + lane * 16) = v[k];
                    asm volatile("s_waitcnt lgkmcnt(0)" ::: "memory");
                    ++staged;
                    prod[hw] = staged;
                }
                if (staged > s + 1 || staged > NSEG - 2) break;
                const int ow = VOWN(staged);
                int& vd = ow ? vdone1 : vdone0;
                if (vd < staged) vd = wait_flags(rw, FL_V + ow, 2, NH, staged <= s ? staged : -1, lane, budget, true);
                if (vd < staged) break;
#pragma unroll
                for (int k = 0; k < 8; ++k) v[k] = LD16(rw, vo + (k & 3) * 1024, staged * (NR * NH * 1024) + (k >> 2) * 4096);
                go = true;
            }
            DRAIN();
        }
        {
            int l2 = (int)__builtin_amdgcn_mbcnt_hi(~0u, __builtin_amdgcn_mbcnt_lo(~0u, 0u));
            asm volatile("" : "+v"(l2));
            const int zf = OFF_ZH + r * 4096 + hw * 2048 + (NSEG - 1) * (NR * NH * 256) + l2 * 16;
            ST16(zv[0], rw, zf); ST16(zv[1], rw, zf + 1024);
        }
    } else {
        TK tk; tk_init(tk);
        for (int s = 0; s < NSEG; ++s) {
            if (s > 0) {
                gather_mem4(rw, NEARB(s, rep, pm), ml, lane, w - 4, tka, s, budget);
            }
            BC2 bc = {0u, 0u};
            if (w == 7) bc = beacon_issue(rw, FARB(s, rep, pm), lane);
            LBAR();
            qk_project(Ba, Bb, ma, zw, idx);
            LBAR();
            if (w == 7) {
                if (s < NSEG - 1) {
                    if (s > 0) { tk_T0(tk, beacon_finish(bc, rw, FARB(s, rep, pm), lane, budget), a); if (tk.okA) tk_post(tka, s + 1, tk_specA(tk, a), lane); }
                    tk_post(tkr, s, tk.XR, lane);
                    wait_until(tk.XR + 8);
                    tk_Tp(tk, beacon_max<NH>(rw, FARB(s, rep, pm), BC_QK, lane, budget), a);
                    tk_post(tka, s + 1, tk.XA, lane);
                    tk_post(tkr, s + 1, tk_specR(tk, a), lane);
                }
            } else {
                if (w == 4 && s >= 2 && lane == 0) ST4((u32)(s - 2), rw, (FL_Z + r) * 128);
                __builtin_amdgcn_s_sleep(14);
            }
        }
    }
    DRAIN();
    LBAR();
    if (tid == 0) ST4((u32)(NSEG - 1), rw, (FL_Z + r) * 128);
    if (budget < 0 && lane == 0) ST4(3u, rw, FL_ERR * 128);
}

__device__ __forceinline__ void cexp_l(float lr, float li, float l, float& er, float& ei) {
    const float mag = __builtin_amdgcn_exp2f(lr * l);
    const float ph = li * l;
    const float pe = fmaf(li, l, -ph);
    const float fr = (ph - floorf(ph)) + pe;
    er = mag * __builtin_amdgcn_cosf(fr);
    ei = mag * __builtin_amdgcn_sinf(fr);
}
constexpr int CV_W = 8 * 260 * 16, CV_U = 96 * 80, CV_CH = CV_W + CV_U;
__device__ __forceinline__ void s4_unit(const Args& a, lptr lds, rsrc_t rw, int unit) {
    const int tid = threadIdx.x, lane = tid & 63, w = __builtin_amdgcn_readfirstlane(tid >> 6);
    const int d0 = unit * 4;
    v2u uv[4];
    {
        const int c = tid >> 7, nn = tid & 127, n = (d0 + c) * 128 + nn;
        const float dt = rbf(expf(bf2f(a.logdt[d0 + c])));
        const float lr = dt * bf2f(a.Are[n]) * 1.4426950408889634f, li = dt * bf2f(a.Aim[n]) * 0.15915494309189535f;
        const float br = dt * bf2f(a.Bre[n]), bi = dt * bf2f(a.Bim[n]);
        const float cr = bf2f(a.Cre[n]), ci = bf2f(a.Cim[n]);
        MB();
#pragma unroll
        for (int k = 0; k < 4; ++k) uv[k] = *(const v2u*)(a.u + (size_t)(2 * (tid + 512 * (k >> 1)) + (k & 1)) * DM + d0);
        MB();
        LAS f32x2* T = (LAS f32x2*)(lds + c * CV_CH) + nn;
        float er, ei, pr, pi;
        cexp_l(lr, li, 1.0f, er, ei); pr = er; pi = ei;
#pragma unroll
        for (int o = 0; o < 7; ++o) { T[o * 130] = f32x2{pr, pi}; const float t = pr * er - pi * ei; pi = pr * ei + pi * er; pr = t; }
#pragma unroll
        for (int q4 = 0; q4 < 4; ++q4) {
            if (q4 == 0) cexp_l(lr, li, 128.0f, er, ei);
            cexp_l(lr, li, (float)(128 + 512 * q4), pr, pi);
#pragma unroll
            for (int o = 0; o < 4; ++o) { T[(7 + 4 * q4 + o) * 130] = f32x2{pr, pi}; const float t = pr * er - pi * ei; pi = pr * ei + pi * er; pr = t; }
        }
        const float cbr = cr * br - ci * bi, cbi = cr * bi + ci * br;
        cexp_l(lr, li, 8.0f, er, ei);
#pragma unroll
        for (int q4 = 0; q4 < 4; ++q4) {
            float sr, si;
            cexp_l(lr, li, (float)(32 * q4), sr, si);
            pr = cbr * sr - cbi * si; pi = cbr * si + cbi * sr;
#pragma unroll
            for (int o = 0; o < 4; ++o) { T[(23 + 4 * q4 + o) * 130] = f32x2{pr, pi}; const float t = pr * er - pi * ei; pi = pr * ei + pi * er; pr = t; }
        }
    }
    LBAR();
    f32x4 kr[3];
    {
        const int x = lane & 15, g = lane >> 4, c = w & 3, nh = w >> 2;
        const int ah0 = x >> 3, o0 = x & 7;
        const int ah1 = x < 8 ? 2 + (x >> 2) : 4 + ((x - 8) >> 1), o1 = x < 8 ? 2 * (x & 3) : 4 * (x & 1);
        const int ah2 = x < 8 ? 8 + x : 16;
        LAS f32x2* T = (LAS f32x2*)(lds + c * CV_CH) + nh * 64 + g;
        LAS f32x2* Ta0 = T + 7 * 130; LAS f32x2* To0 = T + (o0 ? o0 - 1 : 0) * 130;
        LAS f32x2* Ta1 = T + (6 + ah1) * 130; LAS f32x2* To1 = T + (o1 ? o1 - 1 : 0) * 130;
        LAS f32x2* Ta2 = T + (6 + ah2) * 130; LAS f32x2* Tv = T + (23 + x) * 130;
        const f32x2 one = {1.0f, 0.0f};
        f32x4 ar[3] = {}, ai[3] = {};
#pragma unroll
        for (int jj = 0; jj < 16; ++jj) {
            const f32x2 v = Tv[4 * jj], u2 = Ta2[4 * jj], a1 = Ta1[4 * jj];
            f32x2 a0v = Ta0[4 * jj], e0 = To0[4 * jj], e1 = To1[4 * jj];
            a0v = ah0 ? a0v : one; e0 = o0 ? e0 : one; e1 = o1 ? e1 : one;
            const float u0r = a0v[0] * e0[0] - a0v[1] * e0[1], u0i = a0v[0] * e0[1] + a0v[1] * e0[0];
            const float u1r = a1[0] * e1[0] - a1[1] * e1[1], u1i = a1[0] * e1[1] + a1[1] * e1[0];
            ar[2] = __builtin_amdgcn_mfma_f32_16x16x4f32(u2[0], v[0], ar[2], 0, 0, 0);
            ai[2] = __builtin_amdgcn_mfma_f32_16x16x4f32(u2[1], v[1], ai[2], 0, 0, 0);
            ar[0] = __builtin_amdgcn_mfma_f32_16x16x4f32(u0r, v[0], ar[0], 0, 0, 0);
            ai[0] = __builtin_amdgcn_mfma_f32_16x16x4f32(u0i, v[1], ai[0], 0, 0, 0);
            ar[1] = __builtin_amdgcn_mfma_f32_16x16x4f32(u1r, v[0], ar[1], 0, 0, 0);
            ai[1] = __builtin_amdgcn_mfma_f32_16x16x4f32(u1i, v[1], ai[1], 0, 0, 0);
        }
#pragma unroll
        for (int t = 0; t < 3; ++t) kr[t] = ar[t] - ai[t];
    }
    LBAR();
    {
        LAS float* T = (LAS float*)(lds + (w & 3) * CV_CH + 4096 + (w >> 2) * 3072);
#pragma unroll
        for (int t = 0; t < 3; ++t)
#pragma unroll
            for (int r = 0; r < 4; ++r) T[(16 * t + 4 * (lane >> 4) + r) * 16 + (lane & 15)] = kr[t][r];
    }
    LBAR();
#pragma unroll
    for (int k = 0; k < 8; ++k) {
        const int i = tid + 512 * k, c = i >> 10, l2 = i & 1023;
        LAS float* T = (LAS float*)(lds + c * CV_CH + 4096);
        float v[2];
#pragma unroll
        for (int e = 0; e < 2; ++e) {
            const int lb = (int)rbf((float)(2 * l2 + e));
            const int hi = lb >> 7, o = lb & 7;
            const int row = lb < 256 ? 8 * hi + o : lb < 512 ? 16 + 4 * (hi - 2) + (o >> 1) : lb < 1024 ? 24 + 2 * (hi - 4) + (o >> 2) : 32 + (hi - 8);
            const int ix = row * 16 + ((lb >> 3) & 15);
            v[e] = T[ix] + T[768 + ix];
        }
        *(LAS u32*)(lds + c * CV_CH + CV_W + 2704 + l2 * 4) = pack2(v[0], v[1]);
    }
    LBAR();
    for (int i = tid; i < 4 * 259; i += 512) {
        const int c = i / 259, zz = i - c * 259, z = zz - 3;
        lptr ch = lds + c * CV_CH;
        v4u lo = {0, 0, 0, 0}, hi = {0, 0, 0, 0};
        if (z >= 1) lo = *(LAS v4u*)(ch + CV_W + 2704 + (z - 1) * 16);
        if (z >= 0) hi = *(LAS v4u*)(ch + CV_W + 2704 + z * 16);
        const u32 e[8] = {lo.x, lo.y, lo.z, lo.w, hi.x, hi.y, hi.z, hi.w};
#pragma unroll
        for (int cc = 0; cc < 8; ++cc) {
            v4u o;
#pragma unroll
            for (int k = 0; k < 4; ++k) {
                const int t = 8 + cc - 2 * k;
                if (t & 1) o[k] = (e[t >> 1] >> 16) | (e[t >> 1] << 16);
                else o[k] = (e[t >> 1] & 0xffffu) | (e[(t >> 1) - 1] & 0xffff0000u);
            }
            *(LAS v4u*)(ch + (cc * 260 + zz) * 16) = o;
        }
    }
    LBAR();
    for (int i = tid; i < 4 * 160; i += 512) *(LAS v4u*)(lds + (i / 160) * CV_CH + CV_W + (i % 160) * 16) = v4u{0, 0, 0, 0};
#pragma unroll
    for (int k2 = 0; k2 < 2; ++k2) {
        const int l = 2 * (tid + 512 * k2);
        const int o = CV_W + (32 + (l >> 5)) * 80 + (l & 31) * 2;
        const v2u p = uv[2 * k2], q = uv[2 * k2 + 1];
        *(LAS u32*)(lds + 0 * CV_CH + o) = (p.x & 0xffffu) | (q.x << 16);
        *(LAS u32*)(lds + 1 * CV_CH + o) = (p.x >> 16) | (q.x & 0xffff0000u);
        *(LAS u32*)(lds + 2 * CV_CH + o) = (p.y & 0xffffu) | (q.y << 16);
        *(LAS u32*)(lds + 3 * CV_CH + o) = (p.y >> 16) | (q.y & 0xffff0000u);
    }
    LBAR();
    const int c = w & 3, par = w >> 2, b = lane & 31, hh = lane >> 5;
    lptr ch = lds + c * CV_CH;
    lptr wa = ch + ((b & 7) * 260 + (b >> 3) - hh + 3) * 16;
    lptr ub = ch + CV_W + (32 + b) * 80 + hh * 16;
    f32x16 y0 = {}, y1 = {};
    wa += par * 64; ub -= par * 80;
    {
        bf16x8 fa[2][2], fb[2][2], fc[2][2];
#pragma unroll
        for (int ks = 0; ks < 2; ++ks) { fa[0][ks] = lds8(wa - ks * 32); fb[0][ks] = lds8(ub + ks * 32); fc[0][ks] = lds8(ub + 2560 + ks * 32); }
#pragma unroll
        for (int i = 0; i < 32; ++i) {
            const int cur = i & 1, nx = cur ^ 1;
            if (i + 1 < 32) {
#pragma unroll
                for (int ks = 0; ks < 2; ++ks) {
                    fa[nx][ks] = lds8(wa + (i + 1) * 128 - ks * 32);
                    if (i + 1 < 16) fb[nx][ks] = lds8(ub - (i + 1) * 160 + ks * 32);
                    fc[nx][ks] = lds8(ub + 2560 - (i + 1) * 160 + ks * 32);
                }
            }
            __builtin_amdgcn_sched_barrier(0);
#pragma unroll
            for (int ks = 0; ks < 2; ++ks) {
                if (i < 16) y0 = MFMA32(fa[cur][ks], fb[cur][ks], y0);
                y1 = MFMA32(fa[cur][ks], fc[cur][ks], y1);
            }
            __builtin_amdgcn_sched_barrier(0);
        }
    }
    LBAR();
    {
        LAS f32x4* X = (LAS f32x4*)(ch + par * 4096);
        const f32x16 src = par ? y0 : y1;
#pragma unroll
        for (int k = 0; k < 4; ++k) X[k * 64 + lane] = f32x4{src[4 * k], src[4 * k + 1], src[4 * k + 2], src[4 * k + 3]};
    }
    LBAR();
    {
        LAS f32x4* X = (LAS f32x4*)(ch + (1 - par) * 4096);
        lptr Y = ch + 8192 + (32 * par + b) * 72 + hh * 8;
        const f32x16 own = par ? y1 : y0;
#pragma unroll
        for (int k = 0; k < 4; ++k) {
            const f32x4 o = X[k * 64 + lane];
            v2u yv = {pack2(own[4 * k] + o[0], own[4 * k + 1] + o[1]), pack2(own[4 * k + 2] + o[2], own[4 * k + 3] + o[3])};
            *(LAS v2u*)(Y + k * 16) = yv;
        }
    }
    LBAR();
    const v2u dk = *(const v2u*)(a.dskip + d0);
#pragma unroll
    for (int k2 = 0; k2 < 2; ++k2) {
        const int i = tid + 512 * k2;
        lptr yp = lds + 8192 + (i >> 4) * 72 + (i & 15) * 4;
        const u32 ya = *(LAS u32*)(yp), yb = *(LAS u32*)(yp + CV_CH), yc = *(LAS u32*)(yp + 2 * CV_CH), yd = *(LAS u32*)(yp + 3 * CV_CH);
        const v2u p = uv[2 * k2], q = uv[2 * k2 + 1];
        v4u o;
        o.x = pack2(bflo(ya) + rbf(bflo(p.x) * bflo(dk.x)), bflo(yb) + rbf(bfhi(p.x) * bfhi(dk.x)));
        o.y = pack2(bflo(yc) + rbf(bflo(p.y) * bflo(dk.y)), bflo(yd) + rbf(bfhi(p.y) * bfhi(dk.y)));
        o.z = pack2(bfhi(ya) + rbf(bflo(q.x) * bflo(dk.x)), bfhi(yb) + rbf(bfhi(q.x) * bfhi(dk.x)));
        o.w = pack2(bfhi(yc) + rbf(bflo(q.y) * bflo(dk.y)), bfhi(yd) + rbf(bfhi(q.y) * bfhi(dk.y)));
        ST16(o, rw, OFF_A4 + unit * 16384 + i * 16);
    }
    LBAR();
}
__device__ __forceinline__ void role_s4(const Args& a, lptr lds, rsrc_t rw, int idx) {
    if (idx < 32) {
        const int tid = threadIdx.x, t = idx * 64 + (tid >> 3), j = tid & 7;
        float acc = 0.f;
#pragma unroll
        for (int k = 0; k < 9; ++k) {
            const v4u xv = *(const v4u*)(a.u + (size_t)t * DM + (k * 8 + j) * 8), gv = *(const v4u*)(a.gatew + (k * 8 + j) * 8);
#pragma unroll
            for (int e = 0; e < 4; ++e) acc += rbf(bflo(xv[e]) * bflo(gv[e])) + rbf(bfhi(xv[e]) * bfhi(gv[e]));
        }
        acc += __shfl_xor(acc, 1); acc += __shfl_xor(acc, 2); acc += __shfl_xor(acc, 4);
        acc = rbf(acc);
        if (j == 0) ST4(__float_as_uint(rbf(1.0f / (1.0f + __expf(-acc)))), rw, OFF_G + t * 4);
    }
    const bool qwg = idx >= 16 && idx < 48;
    const int u0 = qwg ? 96 + idx : (idx < 16 ? 24 + idx : idx - 48), uend = qwg ? DM / 4 : 112;
#pragma unroll 1
    for (int unit = u0; unit < uend; unit += 40) s4_unit(a, lds, rw, unit);
    DRAIN();
    __syncthreads();
    if (threadIdx.x == 0) ST4(1u, rw, (FL_S + idx) * 128);
}

__device__ __forceinline__ void role_post(const Args& a, lptr lds, rsrc_t rw) {
    const int tid = threadIdx.x, lane = tid & 63, w = __builtin_amdgcn_readfirstlane(tid >> 6);
    const int i16 = lane & 15, g = lane >> 4;
    int budget = SPIN_MAX, vdone0 = -1, vdone1 = -1;
    volatile LAS int* U = (volatile LAS int*)(lds + 81920);
    wait_flags(rw, FL_S, 1, 64, 1, lane, budget);
    wait_flags(rw, FL_S + 64, 1, NS4 - 64, 1, lane, budget);
    constexpr int NFULL = 62 * 6, NUNIT = NFULL + 24;
#pragma unroll 1
    for (;;) {
        if (tid == 0) *U = 1 + (int)__hip_atomic_fetch_add((u32*)(a.ws + FL_Q * 128), 1u, __ATOMIC_RELAXED, __HIP_MEMORY_SCOPE_AGENT);
        __syncthreads();
        const int unit = __builtin_amdgcn_readfirstlane(*U);
        if (unit >= NUNIT || budget < 0) break;
        const bool half = unit >= NFULL;
        const int uu = half ? NFULL + ((unit - NFULL) >> 1) : unit, q0 = half ? (unit - NFULL) & 1 : 0;
        const int s = uu / 6, r = (uu - s * 6) * 6 + (w < 6 ? w : 0);
        bf16x8 b[32];
        v2u av[2]; float gt[2];
        if (w < 6) {
            if (VOWN(s)) { if (vdone1 < s) vdone1 = wait_flags(rw, FL_V + 1, 2, NH, s, lane, budget); }
            else { if (vdone0 < s) vdone0 = wait_flags(rw, FL_V, 2, NH, s, lane, budget); }
            const int vo = OFF_VOX + (s * NR + r) * (NH * 1024) + ((i16 >> 1) & 1) * 16384 + ((g & 1) * 16 + (g >> 1) * 8 + 2 * (i16 >> 2) + (i16 & 1)) * 16;
#pragma unroll
            for (int k = 0; k < 32; ++k) b[k] = as8(LD16(rw, vo + (k & 7) * 512, (k >> 3) * 4096));
#pragma unroll
            for (int qt = 0; qt < 2; ++qt) {
                av[qt] = LD8(rw, OFF_A4 + (4 * r + g) * 16384 + (32 * s + 16 * (qt ^ q0) + i16) * 8, 0);
                gt[qt] = __uint_as_float(LD4(rw, OFF_G + (32 * s + 16 * (qt ^ q0) + i16) * 4, 0));
            }
        }
        v4u z[8];
        const int zo = OFF_ZH + (s * NR + r) * (NH * 256) + ((i16 >> 1) & 1) * 4096 + g * 128 + (2 * (i16 >> 2) + (i16 & 1)) * 16;
        if (half) {
            wait_flags(rw, FL_Z + 2 * r, 1, 2, s, lane, budget, true);
#pragma unroll
            for (int k = 0; k < 8; ++k) z[k] = LD16(rw, zo + k * 512, 0);
            wait_flags(rw, FL_P + (s & 3), 4, 16, s, lane, budget, true);
            v4u pv[5];
#pragma unroll
            for (int k = 0; k < 5; ++k) pv[k] = LD16(rw, OFF_PX + s * 81920 + q0 * 40960 + tid * 16, k * 8192);
#pragma unroll
            for (int k = 0; k < 5; ++k) *(LAS v4u*)(lds + k * 8192 + tid * 16) = pv[k];
        } else {
            wait_flags(rw, FL_P + (s & 3), 4, 16, s, lane, budget);
            v4u pv[10];
#pragma unroll
            for (int k = 0; k < 10; ++k) pv[k] = LD16(rw, OFF_PX + s * 81920 + tid * 16, k * 8192);
            wait_flags(rw, FL_Z + 2 * r, 1, 2, s, lane, budget);
#pragma unroll
            for (int k = 0; k < 8; ++k) z[k] = LD16(rw, zo + k * 512, 0);
            MB();
#pragma unroll
            for (int k = 0; k < 10; ++k) *(LAS v4u*)(lds + k * 8192 + tid * 16) = pv[k];
        }
        __syncthreads();
        if (w < 6) {
#pragma unroll 1
            for (int qt = 0; qt < (half ? 1 : 2); ++qt) {
                f32x4 d = {}, e = {};
                lptr pl = lds + qt * 40960 + lane * 16;
                bf16x8 f[6];
#pragma unroll
                for (int i = 0; i < 6; ++i) f[i] = lds8(pl + i * 1024);
                __builtin_amdgcn_sched_barrier(0);
#pragma unroll
                for (int k = 0; k < 40; ++k) {
                    const bf16x8 v = k < 32 ? b[k < 32 ? k : 0] : as8(z[k < 32 ? 0 : k - 32]);
                    if (k & 1) e = MFMA16(v, f[k % 6], e); else d = MFMA16(v, f[k % 6], d);
                    if (k + 6 < 40) f[k % 6] = lds8(pl + (k + 6) * 1024);
                    __builtin_amdgcn_sched_barrier(0);
                }
                d += e;
                const float gq = qt ? gt[1] : gt[0], og = rbf(1.0f - gq);
                const v2u aq = qt ? av[1] : av[0];
                const float r0 = rbf(gq * bflo(aq.x)) + rbf(og * rbf(d[0])), r1 = rbf(gq * bfhi(aq.x)) + rbf(og * rbf(d[1]));
                const float r2 = rbf(gq * bflo(aq.y)) + rbf(og * rbf(d[2])), r3 = rbf(gq * bfhi(aq.y)) + rbf(og * rbf(d[3]));
                v2u ov = {pack2(r0, r1), pack2(r2, r3)};
                *(v2u*)(a.out + (size_t)(32 * s + 16 * (qt ^ q0) + i16) * DM + 16 * r + 4 * g) = ov;
            }
        }
        if (half) break;
        LBAR();
    }
    if (budget < 0 && lane == 0) ST4(6u, rw, FL_ERR * 128);
}

__global__ void __launch_bounds__(512, 2) k_all(Args a) {
    extern __shared__ __attribute__((aligned(16))) char smem[];
    lptr lds = (lptr)smem;
    a.ws += (0u - (u32)(uintptr_t)a.ws) & 0x7FFFu;
    const rsrc_t rw = __builtin_amdgcn_make_buffer_rsrc(a.ws, 0, WS_BYTES, 0x00020000);
    if (threadIdx.x == 0) {
        const int x = (int)(__builtin_amdgcn_s_getreg(20 | (3 << 11)) & 7u);
        int role = 0;
        for (int k = 0; k < 8; ++k) {
            const int gq = (x + k) & 7;
            const int t = 1 + (int)__hip_atomic_fetch_add((u32*)(a.ws + (FL_T + gq) * 128), 1u, __ATOMIC_RELAXED, __HIP_MEMORY_SCOPE_AGENT);
            if (t < 32) { role = gq + 8 * (31 - t); break; }
        }
        *(volatile LAS int*)lds = role;
    }
    __syncthreads();
    const int b = __builtin_amdgcn_readfirstlane(*(volatile LAS int*)lds), xg = b & 7, slot = b >> 3;
    __syncthreads();

    if (slot == 8 && xg < 4) role_probe(rw, xg);
    if (slot >= 20) { role_wvo(a, lds, rw, (xg & 3) * 24 + (xg < 4 ? 12 : 0) + slot - 20); __syncthreads(); }
    if ((xg < 4 && slot >= 26) || (xg >= 4 && slot >= 20)) {
        const int idx = xg < 4 ? 48 + (slot - 26) * 4 + xg : (31 - slot) * 4 + (xg - 4);
        role_s4(a, lds, rw, idx);
        __syncthreads();
    }
    bool post = false;
    if (xg < 4) {
        if (slot < 8) role_qk(a, lds, rw, xg * 8 + slot, xg);
        else if (slot < 26) role_rz(a, lds, rw, xg * 18 + slot - 8, xg);
    } else {
        const int j = xg - 4;
        post = true;
        if (slot < 16) { role_vvo(a, lds, rw, j * 8 + (slot >> 1), slot & 1); post = slot < NJOIN; }
        else if (slot < 20) role_k2(a, lds, rw, j * 4 + slot - 16);
        else if (slot < 28) { role_q2p(a, lds, rw, j * 4 + ((slot - 20) >> 1), slot & 1); post = false; }
    }
    if (post) { __syncthreads(); role_post(a, lds, rw); }
}

extern "C" void kernel_launch(void* const* d_in, const int* in_sizes, int n_in, void* d_out, int out_size, void* d_ws, size_t ws_size, hipStream_t stream) {
    Args a;
    a.u = (const u16*)d_in[0]; a.Are = (const u16*)d_in[1]; a.Aim = (const u16*)d_in[2]; a.Bre = (const u16*)d_in[3]; a.Bim = (const u16*)d_in[4];
    a.Cre = (const u16*)d_in[5]; a.Cim = (const u16*)d_in[6]; a.logdt = (const u16*)d_in[7]; a.dskip = (const u16*)d_in[8];
    a.mem0 = (const u16*)d_in[10]; a.Wq = (const u16*)d_in[11]; a.Wk = (const u16*)d_in[12]; a.Wv = (const u16*)d_in[13]; a.Wo = (const u16*)d_in[14];
    a.gatew = (const u16*)d_in[16]; a.out = (u16*)d_out; a.ws = (char*)d_ws;
    const int LDS_ALL = 160 * 1024;
    (void)hipFuncSetAttribute((const void*)k_all, hipFuncAttributeMaxDynamicSharedMemorySize, LDS_ALL);
    (void)hipMemsetAsync(d_ws, 0xFF, INIT_BYTES + 32768, stream);
    hipLaunchKernelGGL(k_all, dim3(256), dim3(512), LDS_ALL, stream, a);
}
```

```cpp
#include <hip/hip_runtime.h>
#include <hip/hip_bf16.h>
#include <stdint.h>

typedef unsigned short u16;
typedef unsigned int u32;
typedef u32 v4u __attribute__((ext_vector_type(4)));
typedef u32 v2u __attribute__((ext_vector_type(2)));
typedef float f32x4 __attribute__((ext_vector_type(4)));
typedef float f32x2 __attribute__((ext_vector_type(2)));
typedef float f32x16 __attribute__((ext_vector_type(16)));
typedef __bf16 bf16x2 __attribute__((ext_vector_type(2)));
typedef __bf16 bf16x8 __attribute__((ext_vector_type(8)));
typedef __bf16 bf16x16 __attribute__((ext_vector_type(16)));
typedef short s16x4 __attribute__((ext_vector_type(4)));
#define LAS __attribute__((address_space(3)))
typedef LAS char* lptr;
typedef __amdgpu_buffer_rsrc_t rsrc_t;

constexpr int SEQ = 2048, DM = 576, NH = 32, DH = 128, NSEG = 64, NR = 36, NR8 = 72, NREP = 4;
constexpr int MEMB = 9216, BC_QK = 80;
constexpr int FL0 = 16384;
constexpr int FL_K = FL0, FL_V = FL0 + 16, FL_M = FL0 + 80, FL_P = FL0 + 112, FL_S = FL0 + 176, FL_Q = FL0 + 254, FL_ERR = FL0 + 255, FL_Z = FL0 + 256, FL_W = FL0 + 328, FL_T = FL0 + 424, FL_PAR = FL0 + 432, NS4 = 72;
constexpr int N_MEMA = 0, N_PX = 32768, N_PMM = 98304, N_MEMB = 98304 + 4096, N_PROBE = 98304 + 6144;
constexpr int INIT_BYTES = 16 * 131072 + 65536;
constexpr int OFF_ZH = INIT_BYTES;
constexpr int OFF_KX = OFF_ZH + NSEG * NR * NH * 256;
constexpr int OFF_VOX = OFF_KX + NH * SEQ * DH * 2;
constexpr int OFF_KM = OFF_VOX + NSEG * NR * NH * 1024;
constexpr int OFF_PX = OFF_KM + NSEG * NH * 2048;
constexpr int OFF_A4 = OFF_PX + NSEG * 81920;
constexpr int OFF_G = OFF_A4 + SEQ * DM * 2;
constexpr int OFF_WVO = OFF_G + SEQ * 4;
constexpr int WS_BYTES = OFF_WVO + NR8 * 294912;
constexpr int TK_LA = 8, TK_LR = 8, TK_TOL = 8, TK_GAP = 30;
constexpr int NJOIN = 6;

struct Args {
    const u16* u; const u16* Are; const u16* Aim; const u16* Bre; const u16* Bim; const u16* Cre; const u16* Cim;
    const u16* logdt; const u16* dskip; const u16* mem0; const u16* Wq; const u16* Wk; const u16* Wv; const u16* Wo;
    const u16* gatew; u16* out; char* ws;
};

__device__ __forceinline__ float bf2f(u16 v) { return __uint_as_float((u32)v << 16); }
__device__ __forceinline__ float bflo(u32 v) { return __uint_as_float(v << 16); }
__device__ __forceinline__ float bfhi(u32 v) { return __uint_as_float(v & 0xffff0000u); }
__device__ __forceinline__ u32 pack2(float lo, float hi) { f32x2 f = {lo, hi}; bf16x2 b = __builtin_convertvector(f, bf16x2); return __builtin_bit_cast(u32, b); }
__device__ __forceinline__ float rbf(float x) { return bflo(pack2(x, x)); }
__device__ __forceinline__ u32 nos(u32 v) { return min(v, 0xFFFFFFFEu); }
__device__ __forceinline__ v2u trd(lptr p) { return __builtin_bit_cast(v2u, __builtin_amdgcn_ds_read_tr16_b64_v4i16((LAS s16x4*)p)); }
__device__ __forceinline__ bf16x8 as8(v4u v) { return __builtin_bit_cast(bf16x8, v); }
__device__ __forceinline__ bf16x8 as8(v2u a, v2u b) { v4u v = {a.x, a.y, b.x, b.y}; return __builtin_bit_cast(bf16x8, v); }
__device__ __forceinline__ v4u asu(bf16x8 v) { return __builtin_bit_cast(v4u, v); }
__device__ __forceinline__ bf16x16 cat16(bf16x8 a, bf16x8 b) { return __builtin_shufflevector(a, b, 0, 1, 2, 3, 4, 5, 6, 7, 8, 9, 10, 11, 12, 13, 14, 15); }
__device__ __forceinline__ bf16x8 lds8(lptr p) { return *(LAS bf16x8*)p; }
__device__ __forceinline__ u32 bad4(v4u v) { return max(max(v.x, v.y), max(v.z, v.w)); }
#define NEARB(s, rep, pm) (((((s) & 3) * 4 + (rep)) << 17) + (int)(((((u32)(pm) >> (((s) & 3) * 4 + (rep))) & 0x10001u) * 0x20004000u >> 16) & 0x6000u))
#define FARB(s, rep, pm) ((NEARB(s, rep, pm) ^ 16384) + (((s) & 4) << 14))
#define BCO(slot) (((slot) & 63) * 128 + ((slot) >> 6) * 32768)
#define LBAR() do { asm volatile("s_waitcnt lgkmcnt(0)" ::: "memory"); __builtin_amdgcn_s_barrier(); asm volatile("" ::: "memory"); } while (0)
#define MB() asm volatile("" ::: "memory")
#define DRAIN() asm volatile("s_waitcnt vmcnt(0)" ::: "memory")
#define PIN4(x) asm volatile("" : "+v"(x))
#define MFMA16(a, b, c) __builtin_amdgcn_mfma_f32_16x16x32_bf16((a), (b), (c), 0, 0, 0)
#define MFMA32(a, b, c) __builtin_amdgcn_mfma_f32_32x32x16_bf16((a), (b), (c), 0, 0, 0)
#define SMF(a, b, c, idx) __builtin_amdgcn_smfmac_f32_16x16x64_bf16((a), (b), (c), (idx), 0, 0)
#define LD16(r, vo, so) __builtin_amdgcn_raw_buffer_load_b128((r), (vo), (so), 16)
#define LD8(r, vo, so) __builtin_amdgcn_raw_buffer_load_b64((r), (vo), (so), 16)
#define LD4(r, vo, so) __builtin_amdgcn_raw_buffer_load_b32((r), (vo), (so), 16)
#define ST16(v, r, vo) __builtin_amdgcn_raw_buffer_store_b128((v), (r), (vo), 0, 16)
#define ST8(v, r, vo) __builtin_amdgcn_raw_buffer_store_b64((v), (r), (vo), 0, 16)
#define ST4(v, r, vo) __builtin_amdgcn_raw_buffer_store_b32((v), (r), (vo), 0, 16)

__device__ __forceinline__ float swap32_sum(float a, float b) {
    auto r = __builtin_amdgcn_permlane32_swap(__float_as_uint(a), __float_as_uint(b), false, false);
    return __uint_as_float(r[0]) + __uint_as_float(r[1]); }
__device__ __forceinline__ float xmax(float m) {
    auto r = __builtin_amdgcn_permlane16_swap(__float_as_uint(m), __float_as_uint(m), false, false);
    m = fmaxf(__uint_as_float(r[0]), __uint_as_float(r[1]));
    auto q = __builtin_amdgcn_permlane32_swap(__float_as_uint(m), __float_as_uint(m), false, false);
    return fmaxf(__uint_as_float(q[0]), __uint_as_float(q[1])); }
__device__ __forceinline__ float xsum(float m) {
    auto r = __builtin_amdgcn_permlane16_swap(__float_as_uint(m), __float_as_uint(m), false, false);
    m = __uint_as_float(r[0]) + __uint_as_float(r[1]);
    auto q = __builtin_amdgcn_permlane32_swap(__float_as_uint(m), __float_as_uint(m), false, false);
    return __uint_as_float(q[0]) + __uint_as_float(q[1]); }
__device__ __forceinline__ float vmax(float a, float b) { float r; asm("v_max_f32 %0, %1, %2" : "=v"(r) : "v"(a), "v"(b)); return r; }
__device__ __forceinline__ float vmax3(float a, float b, float c) { float r; asm("v_max3_f32 %0, %1, %2, %3" : "=v"(r) : "v"(a), "v"(b), "v"(c)); return r; }
__device__ __forceinline__ u32 ror8(u32 v) { return (u32)__builtin_amdgcn_update_dpp(0, (int)v, 0x128, 0xf, 0xf, true); }
__device__ __forceinline__ float ror8f(float v) { return __uint_as_float(ror8(__float_as_uint(v))); }
__device__ __forceinline__ int wave_max(int d) {
    d = max(d, __builtin_amdgcn_update_dpp(d, d, 0x128, 0xf, 0xf, false));
    d = max(d, __builtin_amdgcn_update_dpp(d, d, 0x124, 0xf, 0xf, false));
    d = max(d, __builtin_amdgcn_update_dpp(d, d, 0x122, 0xf, 0xf, false));
    d = max(d, __builtin_amdgcn_update_dpp(d, d, 0x121, 0xf, 0xf, false));
    return max(max(__builtin_amdgcn_readlane(d, 0), __builtin_amdgcn_readlane(d, 16)), max(__builtin_amdgcn_readlane(d, 32), __builtin_amdgcn_readlane(d, 48)));
}

constexpr int SPIN_MAX = 2000000;
__device__ __forceinline__ int wait_flags(rsrc_t rw, int line0, int stride, int n, int need, int lane, int& budget, bool fast = false) {
    const int vo = (line0 + (lane < n ? lane : 0) * stride) * 128;
    int mn;
    for (;;) {
        const int v = (int)LD4(rw, vo, 0);
        MB();
        mn = -wave_max(-v);
        if (mn >= need) break;
        if (--budget < 0) break;
        if (fast) __builtin_amdgcn_s_sleep(2); else __builtin_amdgcn_s_sleep(12);
    }
    return mn;
}

template <int NRW, int CW>
__device__ __forceinline__ void rows_ld(v4u (&v)[NRW * CW / 512], const u16* g, int gs, int tid) {
#pragma unroll
    for (int j = 0; j < NRW * CW / 512; ++j) { const int i = tid + 512 * j, row = i / CW, c = i % CW; v[j] = *(const v4u*)(g + (size_t)row * gs + c * 8); }
}
template <int NRW, int CW>
__device__ __forceinline__ void rows_st(const v4u (&v)[NRW * CW / 512], lptr lds, int ls, int tid) {
#pragma unroll
    for (int j = 0; j < NRW * CW / 512; ++j) { const int i = tid + 512 * j, row = i / CW, c = i % CW; *(LAS v4u*)(lds + row * ls + c * 16) = v[j]; }
}
__device__ __forceinline__ bf16x8 frag16(lptr base, int ls, int lane) {
    const int i16 = lane & 15, bg = lane >> 4, q = i16 >> 2, p = i16 & 3;
    lptr a = base + (8 * bg + q) * ls + p * 8;
    return as8(trd(a), trd(a + 4 * ls));
}
__device__ __forceinline__ bf16x8 frag32p(lptr base, int ls, int lane) {
    const int i16 = lane & 15, G = lane >> 4, q = i16 >> 2, p = i16 & 3;
    lptr a = base + (8 * (G >> 1) + q) * ls + (16 * (p & 1) + 8 * (G & 1) + 4 * (p >> 1)) * 2;
    return as8(trd(a), trd(a + 4 * ls));
}

constexpr int XS = 1168;
__device__ __forceinline__ void xload(v4u (&st)[9], const u16* x, int tok0, int tid) {
#pragma unroll
    for (int j = 0; j < 9; ++j) st[j] = *(const v4u*)(x + (size_t)tok0 * DM + (tid + 512 * j) * 8);
}
__device__ __forceinline__ void xstore(const v4u (&st)[9], lptr xl, int tid) {
#pragma unroll
    for (int j = 0; j < 9; ++j) { const int i = tid + 512 * j; *(LAS v4u*)(xl + i * 16 + (i / 72) * 16) = st[j]; }
}
__device__ __forceinline__ void load_w2(bf16x8 (&A)[36], const u16* W, int h0, lptr lds, int tid, int lane, int w) {
    v4u v[6][6];
#pragma unroll
    for (int c = 0; c < 6; ++c) rows_ld<96, 32>(v[c], W + (size_t)c * 96 * 4096 + h0 * DH, 4096, tid);
    __builtin_amdgcn_sched_barrier(0);
#pragma unroll
    for (int c = 0; c < 6; ++c) {
        lptr l = lds + (c & 1) * (96 * 544);
        rows_st<96, 32>(v[c], l, 544, tid);
        LBAR();
#pragma unroll
        for (int ks = 0; ks < 6; ++ks) A[c * 6 + ks] = frag32p(l + ks * 16 * 544 + w * 64, 544, lane);
        __builtin_amdgcn_sched_barrier(0);
    }
    LBAR();
}

__device__ __forceinline__ f32x16 proj_tile(const bf16x8 (&A)[36], lptr xb) {
    f32x16 acc = {};
    bf16x8 f[6];
#pragma unroll
    for (int i = 0; i < 6; ++i) f[i] = lds8(xb + i * 32);
    __builtin_amdgcn_sched_barrier(0);
#pragma unroll
    for (int ks = 0; ks < 36; ++ks) {
        acc = MFMA32(A[ks], f[ks % 6], acc);
        if (ks + 6 < 36) f[ks % 6] = lds8(xb + (ks + 6) * 32);
        __builtin_amdgcn_sched_barrier(0);
    }
    return acc;
}
__device__ __forceinline__ void role_k2(const Args& a, lptr lds, rsrc_t rw, int cu) {
    const int tid = threadIdx.x, lane = tid & 63, w = __builtin_amdgcn_readfirstlane(tid >> 6);
    v4u st[9];
    bf16x8 A[36];
    xload(st, a.u, 32, tid);
    load_w2(A, a.Wk, 2 * cu, lds, tid, lane, w);
    xstore(st, lds, tid);
    LAS int* cnt = (LAS int*)(lds + 2 * 64 * XS);
    if (tid == 0) *cnt = 0;
    __syncthreads();
    const int o0 = OFF_KX + (2 * cu + (w >> 2)) * (NSEG * 8192) + ((w & 3) * 4 + (lane >> 5) * 2) * 512 + (lane & 31) * 16;
#pragma unroll 1
    for (int blk0 = 0; blk0 < 32; ++blk0) {
        int blk = blk0;
        asm volatile("" : "+s"(blk));
        lptr xl = lds + (blk & 1) * (64 * XS);
        const int sg0 = min(2 * blk + 1, 62);
        if (blk + 1 < 32) xload(st, a.u, min(2 * blk + 3, 62) * 32, tid);
#pragma unroll
        for (int tt = 0; tt < 2; ++tt) {
            const f32x16 acc = proj_tile(A, xl + (32 * tt + (lane & 31)) * XS + (lane >> 5) * 16);
            v4u c0 = {pack2(acc[0], acc[1]), pack2(acc[2], acc[3]), pack2(acc[4], acc[5]), pack2(acc[6], acc[7])};
            v4u c1 = {pack2(acc[8], acc[9]), pack2(acc[10], acc[11]), pack2(acc[12], acc[13]), pack2(acc[14], acc[15])};
            const int o = o0 + (sg0 + tt) * 8192;
            ST16(c0, rw, o); ST16(c1, rw, o + 512);
            if (blk == 0 && tt == 0) { DRAIN(); LBAR(); if (tid == 0) ST4(1u, rw, (FL_K + cu) * 128); }
            if (tt == 0 && blk >= 3) {
                asm volatile("s_waitcnt vmcnt(2)" ::: "memory");
                if (lane == 0) {
                    const int old = __hip_atomic_fetch_add(cnt, 1, __ATOMIC_RELAXED, __HIP_MEMORY_SCOPE_WORKGROUP);
                    if ((old & 7) == 7) ST4((u32)(2 * blk), rw, (FL_K + cu) * 128);
                }
            }
        }
        if (blk + 1 < 32) xstore(st, lds + ((blk + 1) & 1) * (64 * XS), tid);
        const bool full = blk < 2 || blk == 31;
        if (full) DRAIN();
        LBAR();
        if (tid == 0 && full) ST4((u32)(sg0 + 1), rw, (FL_K + cu) * 128);
    }
}

constexpr int VS = 272;
__device__ __forceinline__ void role_q2p(const Args& a, lptr lds, rsrc_t rw, int cu, int par) {
    const int tid = threadIdx.x, lane = tid & 63, w = __builtin_amdgcn_readfirstlane(tid >> 6);
    v4u st[9];
    bf16x8 A[36];
    load_w2(A, a.Wq, 2 * cu, lds, tid, lane, w);
    lptr xl = lds, ql = lds + 64 * XS;
    LAS int* cnt = (LAS int*)(lds + 64 * XS + 4 * 64 * VS);
    if (tid < 2) cnt[tid] = 0;
    xload(st, a.u, par * 64, tid);
    xstore(st, xl, tid);
    __syncthreads();
    int budget = SPIN_MAX, kdone = -1;
    const int i16 = lane & 15, g = lane >> 4;
    const int sg = w >> 2, hh = (w >> 1) & 1, qt = w & 1, h = 2 * cu + hh;
    const float c2 = 0.08838834764831845f * 1.4426950408889634f;
    for (int it = -1; it < 16; ++it) {
        const int blk = 2 * it + par;
        if (it + 1 < 16) {
            if (it + 2 < 16) xload(st, a.u, (blk + 4) * 64, tid);
            lptr qb = ql + (((it + 1) & 1) * 2 + (w >> 2)) * (64 * VS) + ((w & 3) * 32 + (lane >> 5) * 16) * 2;
#pragma unroll
            for (int tt = 0; tt < 2; ++tt) {
                const f32x16 acc = proj_tile(A, xl + (32 * tt + (lane & 31)) * XS + (lane >> 5) * 16);
                v4u c0 = {pack2(acc[0], acc[1]), pack2(acc[2], acc[3]), pack2(acc[4], acc[5]), pack2(acc[6], acc[7])};
                v4u c1 = {pack2(acc[8], acc[9]), pack2(acc[10], acc[11]), pack2(acc[12], acc[13]), pack2(acc[14], acc[15])};
                lptr qp = qb + (32 * tt + (lane & 31)) * VS;
                *(LAS v4u*)qp = c0; *(LAS v4u*)(qp + 16) = c1;
            }
            __syncthreads();
            if (it + 2 < 16) xstore(st, xl, tid);
        }
        if (it >= 0) {
            const int s = 2 * blk + sg;
            if (s == 0) wait_flags(rw, FL_M + h, 1, 1, 0, lane, budget);
            else if (kdone < s) kdone = wait_flags(rw, FL_K + cu, 1, 1, s, lane, budget);
            bf16x8 k0[4], k1[4], km[4], qf[4];
            const int ko = OFF_KX + (h * NSEG + s) * 8192 + g * 512 + i16 * 16;
#pragma unroll
            for (int ks = 0; ks < 4; ++ks) { k0[ks] = as8(LD16(rw, ko, ks * 2048)); k1[ks] = as8(LD16(rw, ko + 256, ks * 2048)); }
            lptr qp = ql + ((it & 1) * 2 + hh) * (64 * VS) + (32 * sg + 16 * qt + i16) * VS + g * 16;
#pragma unroll
            for (int ks = 0; ks < 4; ++ks) qf[ks] = lds8(qp + ks * 64);
            f32x4 S0 = {}, S1 = {}, Sm = {};
#pragma unroll
            for (int ks = 0; ks < 4; ++ks) { S0 = MFMA16(k0[ks], qf[ks], S0); S1 = MFMA16(k1[ks], qf[ks], S1); }
            wait_flags(rw, FL_M + h, 1, 1, s, lane, budget, s >= 62);
            const int mo = OFF_KM + (s * NH + h) * 2048 + g * 128 + (i16 & 7) * 16;
#pragma unroll
            for (int ks = 0; ks < 4; ++ks) km[ks] = as8(LD16(rw, mo + ks * 512, 0));
#pragma unroll
            for (int ks = 0; ks < 4; ++ks) Sm = MFMA16(km[ks], qf[ks], Sm);
            const int qi = 16 * qt + i16, t0 = 4 * g;
#pragma unroll
            for (int r = 0; r < 4; ++r) {
                if (t0 + r > qi) S0[r] = -INFINITY;
                if (t0 + r + 16 > qi) S1[r] = -INFINITY;
                if (g >= 2) Sm[r] = -INFINITY;
            }
            float m = fmaxf(fmaxf(fmaxf(S0[0], S0[1]), fmaxf(S0[2], S0[3])), fmaxf(fmaxf(S1[0], S1[1]), fmaxf(S1[2], S1[3])));
            m = fmaxf(m, fmaxf(fmaxf(Sm[0], Sm[1]), fmaxf(Sm[2], Sm[3])));
            m = xmax(m);
            const float nm = -m * c2;
            float sum = 0.f;
#pragma unroll
            for (int r = 0; r < 4; ++r) {
                S0[r] = __builtin_amdgcn_exp2f(fmaf(S0[r], c2, nm)); S1[r] = __builtin_amdgcn_exp2f(fmaf(S1[r], c2, nm)); Sm[r] = __builtin_amdgcn_exp2f(fmaf(Sm[r], c2, nm));
                sum += S0[r] + S1[r] + Sm[r];
            }
            sum = xsum(sum);
            const float inv = __builtin_amdgcn_rcpf(sum);
            v4u px = {pack2(S0[0] * inv, S0[1] * inv), pack2(S0[2] * inv, S0[3] * inv), pack2(S1[0] * inv, S1[1] * inv), pack2(S1[2] * inv, S1[3] * inv)};
            const int po = OFF_PX + (s * 2 + qt) * 40960;
            ST16(px, rw, po + h * 1024 + lane * 16);
            const u32 ma = pack2(Sm[0] * inv, Sm[1] * inv), mb = pack2(Sm[2] * inv, Sm[3] * inv);
            auto ra = __builtin_amdgcn_permlane16_swap(ma, ma, false, false);
            auto rb = __builtin_amdgcn_permlane16_swap(mb, mb, false, false);
            v4u pm = {ra[0], rb[0], ra[1], rb[1]};
            if (lane < 16) ST16(pm, rw, po + 32768 + h * 256 + lane * 16);
            DRAIN();
            if (lane == 0) {
                const int old = __hip_atomic_fetch_add(cnt + sg, 1, __ATOMIC_RELAXED, __HIP_MEMORY_SCOPE_WORKGROUP);
                if ((old & 3) == 3) ST4((u32)s, rw, (FL_P + 4 * cu + 2 * par + sg) * 128);
            }
        }
        __syncthreads();
    }
    if (budget < 0 && lane == 0) ST4(5u, rw, FL_ERR * 128);
}

__device__ __forceinline__ void role_vvo(const Args& a, lptr lds, rsrc_t rw, int h, int par) {
    const int tid = threadIdx.x, lane = tid & 63, w = __builtin_amdgcn_readfirstlane(tid >> 6);
    v4u st[9];
#pragma unroll
    for (int j = 0; j < 9; ++j) { const int i = tid + 512 * j; st[j] = *(const v4u*)(a.u + (size_t)(par * 32 + (i >= 2304 ? 32 : 0)) * DM + i * 8); }
    bf16x8 Av[18], Bo[5][4];
    {
        v4u v[3][6], o[2][9];
#pragma unroll
        for (int c = 0; c < 3; ++c) rows_ld<192, 16>(v[c], a.Wv + (size_t)c * 192 * 4096 + h * DH, 4096, tid);
#pragma unroll
        for (int c = 0; c < 2; ++c) rows_ld<64, 72>(o[c], a.Wo + ((size_t)h * DH + c * 64) * DM, DM, tid);
        __builtin_amdgcn_sched_barrier(0);
#pragma unroll
        for (int c = 0; c < 3; ++c) {
            lptr l = lds + (c & 1) * 77824;
            rows_st<192, 16>(v[c], l, 288, tid);
            LBAR();
#pragma unroll
            for (int ks = 0; ks < 6; ++ks) Av[c * 6 + ks] = frag16(l + ks * 32 * 288 + w * 32, 288, lane);
            __builtin_amdgcn_sched_barrier(0);
        }
#pragma unroll
        for (int c = 0; c < 2; ++c) {
            lptr l = lds + ((c + 1) & 1) * 77824;
            rows_st<64, 72>(o[c], l, 1184, tid);
            LBAR();
#pragma unroll
            for (int j = 0; j < 5; ++j) {
                const int r = (w + 8 * j < NR) ? w + 8 * j : w;
#pragma unroll
                for (int ks = 0; ks < 2; ++ks) Bo[j][c * 2 + ks] = frag16(l + ks * 32 * 1184 + r * 32, 1184, lane);
            }
            __builtin_amdgcn_sched_barrier(0);
        }
        LBAR();
    }
    lptr xl = lds, vl = lds + 64 * XS;
    xstore(st, xl, tid);
    __syncthreads();
    const int rho = lane & 15, bg = lane >> 4;
    const int vo0 = OFF_VOX + ((2 * w + ((rho >> 1) & 1)) * 16 + (h >> 1)) * 1024 + ((2 * (h & 1) + (bg & 1)) * 16 + (bg >> 1) * 8 + 2 * (rho >> 2) + (rho & 1)) * 16;
    const int fo = (FL_V + 2 * h + par) * 128;
    auto stage1 = [&](int tt) {
        f32x4 acc = {};
        lptr xb = xl + (16 * tt + rho) * XS + bg * 16;
        bf16x8 f[6];
#pragma unroll
        for (int i = 0; i < 6; ++i) f[i] = lds8(xb + i * 64);
        __builtin_amdgcn_sched_barrier(0);
#pragma unroll
        for (int ks = 0; ks < 18; ++ks) {
            acc = MFMA16(Av[ks], f[ks % 6], acc);
            if (ks + 6 < 18) f[ks % 6] = lds8(xb + (ks + 6) * 64);
            __builtin_amdgcn_sched_barrier(0);
        }
        v2u o = {pack2(acc[0], acc[1]), pack2(acc[2], acc[3])};
        *(LAS v2u*)(vl + (16 * tt + rho) * VS + (16 * w + 4 * bg) * 2) = o;
    };
    auto stage2 = [&](int sg, int s) {
        bf16x8 Af[2][4];
#pragma unroll
        for (int half = 0; half < 2; ++half)
#pragma unroll
            for (int ks = 0; ks < 4; ++ks) Af[half][ks] = lds8(vl + (32 * sg + rho + 16 * half) * VS + ks * 64 + bg * 16);
#pragma unroll
        for (int j = 0; j < 5; ++j) {
            if (j < 4 || w < 4) {
                f32x4 d0 = {}, d1 = {};
#pragma unroll
                for (int ks = 0; ks < 4; ++ks) { d0 = MFMA16(Af[0][ks], Bo[j][ks], d0); d1 = MFMA16(Af[1][ks], Bo[j][ks], d1); }
                v4u o = {pack2(d0[0], d0[1]), pack2(d0[2], d0[3]), pack2(d1[0], d1[1]), pack2(d1[2], d1[3])};
                ST16(o, rw, vo0 + (s * NR + 8 * j) * (NH * 1024));
            }
        }
    };
    int prev = -1;
#pragma unroll 1
    for (int un = 0; un < 32; ++un) {
        const int sg = un & 1, blk = (un >> 1) * 2 + par;
        const int s = un < 2 ? par + 2 * un : 2 * blk + sg;
        if (sg == 0 && blk + 2 < 32) xload(st, a.u, (blk + 2) * 64, tid);
        stage1(2 * sg); stage1(2 * sg + 1);
        if (sg == 1) DRAIN(); else if (blk + 2 < 32) asm volatile("s_waitcnt vmcnt(9)" ::: "memory"); else DRAIN();
        LBAR();
        if (tid == 0 && prev >= 0) ST4((u32)prev, rw, fo);
        if (sg == 1 && blk + 2 < 32) xstore(st, xl, tid);
        stage2(sg, s);
        prev = s;
        if (un == 0 || un == 31) { DRAIN(); LBAR(); if (tid == 0) ST4((u32)s, rw, fo); }
        else if (sg == 1) LBAR();
    }
}
#define VOWN(s) ((s) < 4 ? ((s) & 1) : (((s) >> 1) & 1))

__device__ __forceinline__ u32 rt32() { return (u32)__builtin_amdgcn_s_memrealtime(); }
__device__ __forceinline__ void wait_until(u32 t) {
    if ((int)(t - rt32()) > 1000) return;
    while ((int)(rt32() - t) < -24) __builtin_amdgcn_s_sleep(4);
    while ((int)(rt32() - t) < 0) {}
}
struct TK { u32 XA, XR, EA, ER; int dA0, dA1, dA2, dR0, dR1, dR2; int okA, okR, gap; };
__device__ __forceinline__ int tk_est(int d0, int d1, int d2) { return max(min(d0, d1), min(max(d0, d1), d2)); }
template <int N>
__device__ __forceinline__ u32 beacon_max(rsrc_t rw, int fb, int slot0, int lane, int& budget) {
    const int vo = BCO(slot0 + (lane < N ? lane : 0)), v2 = BCO(64 + (lane & 7));
    u32 v, u = 0;
    for (;;) {
        v = LD4(rw, vo, fb);
        if (N > 64) u = LD4(rw, v2, fb);
        MB();
        if (__all(max(v, u) != 0xFFFFFFFFu)) break;
        if (--budget < 0) break;
        __builtin_amdgcn_s_sleep(1);
    }
    const u32 base = __builtin_amdgcn_readfirstlane(v);
    int d = (int)(v - base);
    if (N > 64) d = max(d, (int)(u - base));
    return base + (u32)wave_max(d);
}
struct BC2 { u32 v, u; };
__device__ __forceinline__ BC2 beacon_issue(rsrc_t rw, int fb, int lane) {
    BC2 b;
    b.v = LD4(rw, BCO(lane), fb);
    b.u = LD4(rw, BCO(64 + (lane & 7)), fb);
    MB();
    return b;
}
__device__ __forceinline__ u32 beacon_finish(BC2 b, rsrc_t rw, int fb, int lane, int& budget) {
    if (!__all(max(b.v, b.u) != 0xFFFFFFFFu)) return beacon_max<NR8>(rw, fb, 0, lane, budget);
    const u32 base = __builtin_amdgcn_readfirstlane(b.v);
    return base + (u32)wave_max(max((int)(b.v - base), (int)(b.u - base)));
}
__device__ __forceinline__ void tk_T0(TK& k, u32 T0, const Args& a) {
    if (k.okR) { k.dR2 = k.dR1; k.dR1 = k.dR0; k.dR0 = (int)(T0 - k.ER); }
    k.okA = (int)(k.XA - T0) >= TK_LA - TK_TOL;
    if (!k.okR) k.gap = k.okA ? max(TK_GAP, k.gap - 6) : min(k.gap + 16, 160);
    k.EA = k.XA;
    k.XR = k.EA + tk_est(k.dA0, k.dA1, k.dA2) + TK_LR;
}
__device__ __forceinline__ void tk_Tp(TK& k, u32 Tp, const Args& a) {
    const int okn = (int)(k.XR - Tp) >= TK_LR - TK_TOL;
    if (k.okA && (k.okR || !okn)) { k.dA2 = k.dA1; k.dA1 = k.dA0; k.dA0 = (int)(Tp - k.EA); }
    k.okR = okn;
    k.ER = k.okR ? k.XR : Tp + k.gap;
    k.XA = k.ER + tk_est(k.dR0, k.dR1, k.dR2) + TK_LA;
}
__device__ __forceinline__ u32 tk_specA(const TK& k, const Args& a) { return k.XR + tk_est(k.dR0, k.dR1, k.dR2) + TK_LA; }
__device__ __forceinline__ u32 tk_specR(const TK& k, const Args& a) { return k.XA + tk_est(k.dA0, k.dA1, k.dA2) + TK_LR; }
__device__ __forceinline__ void tk_init(TK& k) { const u32 n = rt32(); k.XA = k.XR = k.EA = k.ER = n - 100000u; k.dA0 = k.dA1 = k.dA2 = 125; k.dR0 = k.dR1 = k.dR2 = 65; k.okA = k.okR = 0; k.gap = 50; }

__device__ __forceinline__ void tk_post(lptr p, int tag, u32 t, int lane) { if (lane == 0) { v2u v = {(u32)tag, t}; *(volatile LAS v2u*)p = v; } }
__device__ __forceinline__ u32 tk_get(lptr p, int tag, int& budget) {
    for (;;) {
        const v2u v = *(volatile LAS v2u*)p;
        if ((int)__builtin_amdgcn_readfirstlane(v.x) >= tag || --budget < 0) return __builtin_amdgcn_readfirstlane(v.y);
        __builtin_amdgcn_s_sleep(2);
    }
}
__device__ __forceinline__ void spin_to(u32 X) {
    int rem;
    asm volatile("1:\n\ts_memrealtime vcc\n\ts_waitcnt lgkmcnt(0)\n\ts_sub_i32 %0, %1, vcc_lo\n\ts_cmp_gt_i32 %0, 3\n\ts_cbranch_scc1 1b\n\t"
                 "s_cmp_lt_i32 %0, 1\n\ts_cbranch_scc1 2f\n\ts_nop 3\n\ts_cmp_lt_i32 %0, 2\n\ts_cbranch_scc1 2f\n\ts_nop 3\n\t"
                 "s_cmp_lt_i32 %0, 3\n\ts_cbranch_scc1 2f\n\ts_nop 7\n2:"
                 : "=&s"(rem) : "s"(X) : "memory", "scc", "vcc");
}
__device__ __forceinline__ void tk_wait(lptr p, int tag, int& budget) {
    u32 X;
    for (;;) {
        const v2u v = *(volatile LAS v2u*)p;
        const u32 now = rt32();
        X = __builtin_amdgcn_readfirstlane(v.y);
        const int tg = (int)__builtin_amdgcn_readfirstlane(v.x), dt = (int)(X - now);
        if (tg >= tag) {
            if (dt <= 0 || dt > 1000 || tg > tag) return;
            if (dt <= 28) break;
        }
        if (--budget < 0) return;
        __builtin_amdgcn_s_sleep(1);
    }
    spin_to(X);
}
constexpr int OOB = 0x70000000;
__device__ __forceinline__ void gather_mem(rsrc_t rw, int nb, lptr ml, int lane, int w, lptr tkp, int tag, int& budget) {
    const int gi = w == 7 ? 5 : w;
    const int l0 = gi * 1536 + lane * 16, l1 = l0 + 1024;
    const int o0 = l0 < 8192 ? l0 : l0 + (N_MEMB - 8192), o1 = lane < 32 ? (l1 < 8192 ? l1 : l1 + (N_MEMB - 8192)) : OOB;
    lptr p1 = ml + (lane < 32 ? l1 : 14336 + lane * 16);
    asm volatile("" : "+s"(nb));
    tk_wait(tkp, tag, budget);
    v4u v0 = LD16(rw, o0, nb), v1 = LD16(rw, o1, nb);
    MB();
    *(LAS v4u*)(ml + l0) = v0; *(LAS v4u*)p1 = v1;
    MB();
    if (__builtin_expect(!__all(max(bad4(v0), bad4(v1)) != 0xFFFFFFFFu), 0)) {
        do {
            v0 = LD16(rw, o0, nb); v1 = LD16(rw, o1, nb);
            MB();
        } while (!__all(max(bad4(v0), bad4(v1)) != 0xFFFFFFFFu) && --budget >= 0);
        *(LAS v4u*)(ml + l0) = v0; *(LAS v4u*)p1 = v1;
    }
}
__device__ __forceinline__ void gather_mem4(rsrc_t rw, int nb, lptr ml, int lane, int gi, lptr tkp, int tag, int& budget) {
    const int l0 = gi * 2048 + lane * 16;
    const int o2 = lane < 16 ? N_MEMB + gi * 256 + lane * 16 : OOB;
    lptr p2 = ml + (lane < 16 ? 8192 + gi * 256 + lane * 16 : 14336 + lane * 16);
    asm volatile("" : "+s"(nb));
    tk_wait(tkp, tag, budget);
    v4u v0 = LD16(rw, l0, nb), v1 = LD16(rw, l0 + 1024, nb), v2 = LD16(rw, o2, nb);
    MB();
    *(LAS v4u*)(ml + l0) = v0; *(LAS v4u*)(ml + l0 + 1024) = v1; *(LAS v4u*)p2 = v2;
    MB();
    if (__builtin_expect(!__all(max(max(bad4(v0), bad4(v1)), bad4(v2)) != 0xFFFFFFFFu), 0)) {
        do {
            v0 = LD16(rw, l0, nb); v1 = LD16(rw, l0 + 1024, nb); v2 = LD16(rw, o2, nb);
            MB();
        } while (!__all(max(max(bad4(v0), bad4(v1)), bad4(v2)) != 0xFFFFFFFFu) && --budget >= 0);
        *(LAS v4u*)(ml + l0) = v0; *(LAS v4u*)(ml + l0 + 1024) = v1; *(LAS v4u*)p2 = v2;
    }
}
__device__ __forceinline__ int decode_parity(u32 v, int lane) {
    u32 e = v & 0x00FF00FFu, o = (v >> 8) & 0x00FF00FFu;
    { auto r = __builtin_amdgcn_permlane16_swap(e, e, false, false); e = r[0] + r[1]; }
    { auto r = __builtin_amdgcn_permlane16_swap(o, o, false, false); o = r[0] + r[1]; }
    const int l0 = e & 0xFFFF, l2 = e >> 16, l1 = o & 0xFFFF, l3 = o >> 16;
    const bool par = l1 + l3 < l0 + l2;
    const bool hi = par ? l3 < l1 : l2 < l0;
    return (int)((__ballot(par) & 0xFFFFu) | ((__ballot(hi) & 0xFFFFu) << 16));
}
__device__ __forceinline__ int get_parity(rsrc_t rw, int lane, int& budget) {
    const int vo = (FL_PAR + (lane & 31)) * 128;
    u32 v;
    for (;;) {
        v = LD4(rw, vo, 0);
        MB();
        if (__all(v != 0xFFFFFFFFu) || --budget < 0) break;
        __builtin_amdgcn_s_sleep(8);
    }
    return decode_parity(v, lane);
}
__device__ __forceinline__ void role_probe(rsrc_t rw, int rep) {
    const int lane = threadIdx.x & 63, w = __builtin_amdgcn_readfirstlane(threadIdx.x >> 6);
    const int G = (w & 3) * 4 + rep;
    const int o = (G << 17) + N_PROBE + (w >> 2) * 512;
    ST4(0xFFFFFFFFu, rw, lane < 12 ? o + (lane & 3) * 8192 + (lane >> 2) * 128 : OOB);
    DRAIN();
    int best[4] = {254, 254, 254, 254};
#pragma unroll
    for (int k = 0; k < 12; ++k) {
        const unsigned long long t0 = __builtin_amdgcn_s_memtime();
        asm volatile("s_waitcnt lgkmcnt(0)" ::: "memory");
        u32 v = LD4(rw, o + (k & 3) * 8192 + (k >> 2) * 128, 0);
        asm volatile("s_waitcnt vmcnt(0)" : "+v"(v) :: "memory");
        const unsigned long long t1 = __builtin_amdgcn_s_memtime();
        best[k & 3] = min(best[k & 3], (int)(t1 - t0) >> 2);
    }
    if (lane == 0) ST4((u32)(best[0] | (best[1] << 8) | (best[2] << 16) | (best[3] << 24)), rw, (FL_PAR + (w >> 2) * 16 + G) * 128);
}
template <typename F>
__device__ __forceinline__ void qk_project_h(const bf16x16 (&Bq)[9], const bf16x16 (&Bk)[9], lptr ma, lptr xw, int idx, F hook) {
    f32x4 aq = {}, ak = {};
    bf16x8 A[9];
#pragma unroll
    for (int t = 0; t < 9; ++t) A[t] = lds8(ma + t * 1024);
    __builtin_amdgcn_sched_barrier(0);
#pragma unroll
    for (int t = 0; t < 9; ++t) {
        aq = SMF(A[t], Bq[t], aq, idx); ak = SMF(A[t], Bk[t], ak, idx);
        hook(t);
        __builtin_amdgcn_sched_barrier(0);
    }
    const float s0 = swap32_sum(aq[0], ak[0]), s1 = swap32_sum(aq[1], ak[1]), s2 = swap32_sum(aq[2], ak[2]), s3 = swap32_sum(aq[3], ak[3]);
    v2u o = {pack2(s0, s1), pack2(s2, s3)};
    *(LAS v2u*)xw = o;
}
__device__ __forceinline__ void qk_project(const bf16x16 (&Bq)[9], const bf16x16 (&Bk)[9], lptr ma, lptr xw, int idx) {
    qk_project_h(Bq, Bk, ma, xw, idx, [](int) {});
}

template <int GI>
__device__ __forceinline__ void gather_project(rsrc_t rw, int nb, lptr ml, int lane, lptr tkp, int tag, int& budget,
                                               const bf16x16 (&Bq)[9], const bf16x16 (&Bk)[9], lptr xw, int idx) {
    const int l0 = GI * 2048 + lane * 16;
    const int o2 = lane < 16 ? N_MEMB + GI * 256 + lane * 16 : OOB;
    lptr p2 = ml + (lane < 16 ? 8192 + GI * 256 + lane * 16 : 14336 + lane * 16);
    asm volatile("" : "+s"(nb));
    tk_wait(tkp, tag, budget);
    v4u v0 = LD16(rw, l0, nb), v1 = LD16(rw, l0 + 1024, nb), v2 = LD16(rw, o2, nb);
    MB();
    *(LAS v4u*)(ml + l0) = v0; *(LAS v4u*)(ml + l0 + 1024) = v1; *(LAS v4u*)p2 = v2;
    MB();
    if (__builtin_expect(!__all(max(max(bad4(v0), bad4(v1)), bad4(v2)) != 0xFFFFFFFFu), 0)) {
        do {
            v0 = LD16(rw, l0, nb); v1 = LD16(rw, l0 + 1024, nb); v2 = LD16(rw, o2, nb);
            MB();
        } while (!__all(max(max(bad4(v0), bad4(v1)), bad4(v2)) != 0xFFFFFFFFu) && --budget >= 0);
        *(LAS v4u*)(ml + l0) = v0; *(LAS v4u*)(ml + l0 + 1024) = v1; *(LAS v4u*)p2 = v2;
    }
    LBAR();
    bf16x8 A[9];
#pragma unroll
    for (int t = 0; t < 9; ++t) if (t != 2 * GI && t != 2 * GI + 1) A[t] = lds8(ml + lane * 16 + t * 1024);
    __builtin_amdgcn_sched_barrier(0);
    f32x4 aq = {}, ak = {};
    aq = SMF(as8(v0), Bq[2 * GI], aq, idx); ak = SMF(as8(v0), Bk[2 * GI], ak, idx);
    aq = SMF(as8(v1), Bq[2 * GI + 1], aq, idx); ak = SMF(as8(v1), Bk[2 * GI + 1], ak, idx);
    __builtin_amdgcn_sched_barrier(0);
#pragma unroll
    for (int t = 0; t < 9; ++t) if (t != 2 * GI && t != 2 * GI + 1) { aq = SMF(A[t], Bq[t], aq, idx); ak = SMF(A[t], Bk[t], ak, idx); __builtin_amdgcn_sched_barrier(0); }
    const float s0 = swap32_sum(aq[0], ak[0]), s1 = swap32_sum(aq[1], ak[1]), s2 = swap32_sum(aq[2], ak[2]), s3 = swap32_sum(aq[3], ak[3]);
    v2u o = {pack2(s0, s1), pack2(s2, s3)};
    *(LAS v2u*)xw = o;
}
__device__ __forceinline__ void role_qk(const Args& a, lptr lds, rsrc_t rw, int h, int rep) {
    const int tid = threadIdx.x, lane = tid & 63, w = __builtin_amdgcn_readfirstlane(tid >> 6);
    bf16x16 Bq[9], Bk[9];
    {
        v4u xr[6];
#pragma unroll
        for (int j = 0; j < 6; ++j) {
            const int i = min(tid + 512 * j, 2879), row = i / 72, c = i - row * 72;
            xr[j] = *(const v4u*)((row < 8 ? a.mem0 + row * DM : a.u + (row - 8) * DM) + c * 8);
        }
        v4u v[6][6];
#pragma unroll
        for (int c = 0; c < 6; ++c) rows_ld<192, 16>(v[c], (c >= 3 ? a.Wk : a.Wq) + (size_t)(c % 3) * 192 * 4096 + h * DH, 4096, tid);
        __builtin_amdgcn_sched_barrier(0);
#pragma unroll
        for (int j = 0; j < 6; ++j) {
            const int i = min(tid + 512 * j, 2879), row = i / 72, c = i - row * 72, G = row >> 3;
            lptr d = lds + (G < 2 ? 57344 + G * MEMB : 135168 + (G - 2) * MEMB) + (c >> 1) * 256 + (row & 7) * 16 + (c & 1) * 8;
            *(LAS v2u*)d = v2u{xr[j].x, xr[j].z}; *(LAS v2u*)(d + 128) = v2u{xr[j].y, xr[j].w};
        }
#pragma unroll
        for (int c = 0; c < 6; ++c) {
            lptr l = lds + (c & 1) * 77824;
            rows_st<192, 16>(v[c], l, 288, tid);
            LBAR();
#pragma unroll
            for (int t = 0; t < 3; ++t) {
                bf16x16 f = cat16(frag16(l + (64 * t) * 288 + w * 32, 288, lane), frag16(l + (64 * t + 32) * 288 + w * 32, 288, lane));
                if (c >= 3) Bk[(c % 3) * 3 + t] = f; else Bq[c * 3 + t] = f;
            }
            __builtin_amdgcn_sched_barrier(0);
        }
        LBAR();
    }
    lptr ml = lds, xl = lds + MEMB;
    const int idx = (lane & 8) ? 0xEEEE : 0x4444;
    const int i16 = lane & 15, g = lane >> 4;
    const bool hi8 = (lane & 8) != 0;
    int budget = SPIN_MAX;
    lptr pq = xl + (8 * g + (i16 >> 2)) * 32 + (i16 & 1) * 8;
    lptr kl = lds + 16384 + lane * 16;
    lptr ma = ml + lane * 16, xw = xl + (16 * w + i16) * 32 + g * 8;
    const int ox = N_PX + (h >> 4) * 32768 + (h & 15) * 512 + ((g & 1) * 16 + (g >> 1) * 8 + (i16 & 7)) * 16;
    const int om = hi8 ? OOB : N_PMM + h * 128 + (i16 & 7) * 16;
    const int ob = lane < NREP ? BCO(BC_QK + h) : OOB;
    lptr tkp = lds + MEMB + 4096, kfp = tkp + 64;
    if (tid == 0) { tk_post(tkp, 0, rt32() - 100000u, 0); tk_post(kfp, 0, 0u, 0); }
    for (int c = tid; c < 576; c += 512) *(LAS v4u*)(ml + c * 16) = *(LAS v4u*)(lds + 57344 + c * 16);
    __syncthreads();
#pragma unroll 1
    for (int G = 0; G < 4; ++G) {
        qk_project(Bq, Bk, lds + (G ? 135168 + (G - 1) * MEMB : 57344 + MEMB) + lane * 16, xw, idx);
        LBAR();
        if (w < 4) {
            const bf16x8 F = as8(trd(pq + w * 1024 + 16), trd(pq + w * 1024 + 16 + 128));
            if ((i16 >> 3) == (G & 1)) *(LAS bf16x8*)(kl + ((G >> 1) * 4 + w) * 1024) = F;
        }
        LBAR();
    }
    const int pm = get_parity(rw, lane, budget);
    LBAR();
    if (w == 0) {
        const float c2 = 0.08838834764831845f * 1.4426950408889634f;
        for (int s = 0; s < NSEG - 1; ++s) {
            tk_get(kfp, s, budget);
            bf16x8 kx[2][4];
#pragma unroll
            for (int tl = 0; tl < 2; ++tl)
#pragma unroll
                for (int ks = 0; ks < 4; ++ks) kx[tl][ks] = lds8(kl + (s & 1) * 8192 + (tl * 4 + ks) * 1024);
            int oxs = ox + NEARB(s, i16 >> 3, pm), oxt = ox + NEARB(s, 2 + (i16 >> 3), pm), oms = om + NEARB(s, g, pm), obs = ob + FARB(s, lane & 3, pm);
            const int nb = NEARB(s, rep, pm);
            asm volatile("s_waitcnt lgkmcnt(0)" : "+v"(oxs), "+v"(oxt), "+v"(oms), "+v"(obs) :: "memory");
#pragma unroll
            for (int tl = 0; tl < 2; ++tl)
#pragma unroll
                for (int ks = 0; ks < 4; ++ks) PIN4(kx[tl][ks]);
            if (s > 0) gather_project<0>(rw, nb, ml, lane, tkp, s, budget, Bq, Bk, xw, idx);
            else { LBAR(); qk_project(Bq, Bk, ma, xw, idx); }
            __builtin_amdgcn_s_setprio(3);
            LBAR();
            bf16x8 Fq[4], Fk[4];
#pragma unroll
            for (int ks = 0; ks < 4; ++ks) Fq[ks] = as8(trd(pq + ks * 1024), trd(pq + ks * 1024 + 128));
            __builtin_amdgcn_sched_barrier(0);
#pragma unroll
            for (int ks = 0; ks < 4; ++ks) Fk[ks] = as8(trd(pq + ks * 1024 + 16), trd(pq + ks * 1024 + 16 + 128));
            __builtin_amdgcn_sched_barrier(0);
            f32x4 S0 = {}, S1 = {}, Sm = {};
            S0 = MFMA16(kx[0][0], Fq[0], S0); S1 = MFMA16(kx[1][0], Fq[0], S1);
            S0 = MFMA16(kx[0][1], Fq[1], S0); S1 = MFMA16(kx[1][1], Fq[1], S1);
            __builtin_amdgcn_sched_barrier(0);
            Sm = MFMA16(Fk[0], Fq[0], Sm); S0 = MFMA16(kx[0][2], Fq[2], S0);
            Sm = MFMA16(Fk[1], Fq[1], Sm); S1 = MFMA16(kx[1][2], Fq[2], S1);
            Sm = MFMA16(Fk[2], Fq[2], Sm); S0 = MFMA16(kx[0][3], Fq[3], S0);
            Sm = MFMA16(Fk[3], Fq[3], Sm); S1 = MFMA16(kx[1][3], Fq[3], S1);
            __builtin_amdgcn_sched_barrier(0);
            float y0 = hi8 ? Sm[2] : Sm[0], y1 = hi8 ? Sm[3] : Sm[1];
            float x0 = hi8 ? S1[0] : S0[0], x1 = hi8 ? S1[1] : S0[1], x2 = hi8 ? S1[2] : S0[2], x3 = hi8 ? S1[3] : S0[3];
            const float ml_ = vmax3(vmax3(y0, y1, x0), x1, vmax(x2, x3));
            const float nm = -ml_ * c2;
            float m = vmax(ml_, ror8f(ml_));
            x0 = __builtin_amdgcn_exp2f(fmaf(x0, c2, nm)); x1 = __builtin_amdgcn_exp2f(fmaf(x1, c2, nm));
            { auto r = __builtin_amdgcn_permlane16_swap(__float_as_uint(m), __float_as_uint(m), false, false); m = vmax(__uint_as_float(r[0]), __uint_as_float(r[1])); }
            x2 = __builtin_amdgcn_exp2f(fmaf(x2, c2, nm)); x3 = __builtin_amdgcn_exp2f(fmaf(x3, c2, nm));
            { auto r = __builtin_amdgcn_permlane32_swap(__float_as_uint(m), __float_as_uint(m), false, false); m = vmax(__uint_as_float(r[0]), __uint_as_float(r[1])); }
            y0 = __builtin_amdgcn_exp2f(fmaf(y0, c2, nm)); y1 = __builtin_amdgcn_exp2f(fmaf(y1, c2, nm));
            const float f = __builtin_amdgcn_exp2f((ml_ - m) * c2);
            float sum = f * fmaf(0.5f, y0 + y1, (x0 + x1) + (x2 + x3));
            sum += ror8f(sum);
            sum = xsum(sum);
            const float inv = f * __builtin_amdgcn_rcpf(sum);
            const u32 pa = pack2(x0 * inv, x1 * inv) & 0x7fff7fffu, pb = pack2(x2 * inv, x3 * inv) & 0x7fff7fffu, pc = pack2(y0 * inv, y1 * inv) & 0x7fff7fffu;
            const u32 qc = ror8(pc);
            v4u px = {(u32)__builtin_amdgcn_update_dpp((int)pa, (int)pa, 0x128, 0xf, 0xc, false), (u32)__builtin_amdgcn_update_dpp((int)pb, (int)pb, 0x128, 0xf, 0xc, false),
                      (u32)__builtin_amdgcn_update_dpp((int)pa, (int)pa, 0x128, 0xf, 0x3, false), (u32)__builtin_amdgcn_update_dpp((int)pb, (int)pb, 0x128, 0xf, 0x3, false)};
            ST16(px, rw, oxs);
            ST16(px, rw, oxt);
            auto ra = __builtin_amdgcn_permlane16_swap(pc, pc, false, false);
            auto rc = __builtin_amdgcn_permlane16_swap(qc, qc, false, false);
            v4u pmv = {ra[0], rc[0], ra[1], rc[1]};
            ST16(pmv, rw, oms);
            __builtin_amdgcn_sched_barrier(0);
            ST4(nos(rt32()), rw, obs);
            __builtin_amdgcn_s_setprio(0);
        }
        {
            gather_project<0>(rw, NEARB(NSEG - 1, rep, pm), ml, lane, tkp, NSEG - 1, budget, Bq, Bk, xw, idx);
            LBAR();
        }
    } else if (w == 5) {
        int kdone = -1;
        const int kxo = OFF_KX + h * (NSEG * 8192) + g * 512 + i16 * 16;
        for (int s = 0; s < NSEG; ++s) {
            const bool need = s + 1 < NSEG - 1;
            if (need && kdone < s + 1) kdone = wait_flags(rw, FL_K + (h >> 1), 1, 1, -1, lane, budget);
            const bool go = need && kdone >= s + 1;
            int ko = (go ? kxo : OOB) + (s + 1) * 8192;
            int a0 = ox + NEARB(s + 2, i16 >> 3, pm), a1 = ox + NEARB(s + 2, 2 + (i16 >> 3), pm), a2 = om + NEARB(s + 2, g, pm), a3 = ob + FARB(s + 4, lane & 3, pm);
            v4u ff = {0xFFFFFFFFu, 0xFFFFFFFFu, 0xFFFFFFFFu, 0xFFFFFFFFu};
            asm volatile("" : "+v"(ko), "+v"(a0), "+v"(a1), "+v"(a2), "+v"(a3), "+v"(ff));
            LBAR();
            v4u kx[8];
            qk_project_h(Bq, Bk, ma, xw, idx, [&](int t) {
                if (t < 4) { kx[t] = LD16(rw, ko + (t & 1) * 2048, (t >> 1) * 4096); kx[4 + t] = LD16(rw, ko + 256 + (t & 1) * 2048, (t >> 1) * 4096); }
                if (t == 4) { ST16(ff, rw, a0); ST16(ff, rw, a1); }
                if (t == 5) { ST16(ff, rw, a2); ST4(0xFFFFFFFFu, rw, a3); }
            });
            LBAR();
            if (need) {
                if (!go) {
                    kdone = wait_flags(rw, FL_K + (h >> 1), 1, 1, s + 1, lane, budget, true);
#pragma unroll
                    for (int k = 0; k < 8; ++k) kx[k] = LD16(rw, kxo + (k >> 2) * 256 + (k & 3) * 2048, (s + 1) * 8192);
                }
#pragma unroll
                for (int k = 0; k < 8; ++k) *(LAS v4u*)(kl + ((s + 1) & 1) * 8192 + k * 1024) = kx[k];
                asm volatile("s_waitcnt lgkmcnt(0)" ::: "memory");
                tk_post(kfp, s + 1, 0u, lane);
            }
            DRAIN();
        }
    } else if (w == 1 || w == 2 || w == 7) {
        LBAR();
        qk_project(Bq, Bk, ma, xw, idx);
        LBAR();
        for (int s = 1; s < NSEG; ++s) {
            __builtin_amdgcn_s_sleep(14);
            const int nb = NEARB(s, rep, pm);
            if (w == 1) gather_project<1>(rw, nb, ml, lane, tkp, s, budget, Bq, Bk, xw, idx);
            else if (w == 2) gather_project<2>(rw, nb, ml, lane, tkp, s, budget, Bq, Bk, xw, idx);
            else gather_project<3>(rw, nb, ml, lane, tkp, s, budget, Bq, Bk, xw, idx);
            LBAR();
        }
    } else {
        TK tk; tk_init(tk);
        const int kmo = i16 < 8 ? OFF_KM + h * 2048 + g * 128 + i16 * 16 : OOB;
        for (int s = 0; s < NSEG; ++s) {
            if (s > 0 && w == 3) tk_wait(tkp, s, budget);
            BC2 bc = {0u, 0u};
            if (w == 3) bc = beacon_issue(rw, FARB(s, rep, pm), lane);
            LBAR();
            qk_project(Bq, Bk, ma, xw, idx);
            LBAR();
            if (w == 6) {
                bf16x8 F[4];
#pragma unroll
                for (int ks = 0; ks < 4; ++ks) F[ks] = as8(trd(pq + ks * 1024 + 16), trd(pq + ks * 1024 + 16 + 128));
#pragma unroll
                for (int ks = 0; ks < 4; ++ks) ST16(asu(F[ks]), rw, kmo + s * (NH * 2048) + ks * 512);
                if (s == 0) {
#pragma unroll
                    for (int k = 0; k < 8; ++k) ST16(*(LAS v4u*)(kl + k * 1024), rw, OFF_KX + h * (NSEG * 8192) + g * 512 + i16 * 16 + (k >> 2) * 256 + (k & 3) * 2048);
                }
                DRAIN();
                if (lane == 0) ST4((u32)s, rw, (FL_M + h) * 128);
            } else if (w == 3) {
                if (s < NSEG - 1) {
                    if (s > 0) { tk_T0(tk, beacon_finish(bc, rw, FARB(s, rep, pm), lane, budget), a); if (tk.okA) tk_post(tkp, s + 1, tk_specA(tk, a), lane); }
                    wait_until(tk.XR + 8);
                    tk_Tp(tk, beacon_max<NH>(rw, FARB(s, rep, pm), BC_QK, lane, budget), a);
                    tk_post(tkp, s + 1, tk.XA, lane);
                }
            } else __builtin_amdgcn_s_sleep(14);
        }
    }
    if (budget < 0 && lane == 0) ST4(1u, rw, FL_ERR * 128);
}

__device__ __forceinline__ void role_wvo(const Args& a, lptr lds, rsrc_t rw, int idx) {
    const int tid = threadIdx.x, lane = tid & 63, w = __builtin_amdgcn_readfirstlane(tid >> 6);
    const int h = idx / 3, part = idx - 3 * h;
    bf16x8 Bo[5][4];
    v4u v[6], o[2][9];
#pragma unroll
    for (int c = 0; c < 2; ++c) rows_ld<64, 72>(o[c], a.Wo + ((size_t)h * DH + c * 64) * DM, DM, tid);
    rows_ld<192, 16>(v, a.Wv + (size_t)part * 192 * 4096 + h * DH, 4096, tid);
    __builtin_amdgcn_sched_barrier(0);
#pragma unroll
    for (int c = 0; c < 2; ++c) {
        lptr l = lds + c * 77824;
        rows_st<64, 72>(o[c], l, 1184, tid);
        LBAR();
#pragma unroll
        for (int j = 0; j < 5; ++j) {
            const int r = (w + 8 * j < NR) ? w + 8 * j : w;
#pragma unroll
            for (int ks = 0; ks < 2; ++ks) Bo[j][c * 2 + ks] = frag16(l + ks * 32 * 1184 + r * 32, 1184, lane);
        }
        __builtin_amdgcn_sched_barrier(0);
    }
    rows_st<192, 16>(v, lds, VS, tid);
    LBAR();
    const int rho = lane & 15, bg = lane >> 4;
    const int wo0 = OFF_WVO + (2 * w + ((rho >> 1) & 1)) * 294912 + ((h >> 1) * 18 + part * 6) * 1024 + ((h & 1) * 32 + bg * 8 + 2 * (rho >> 2) + (rho & 1)) * 16;
#pragma unroll 1
    for (int kb = 0; kb < 6; ++kb) {
        bf16x8 Af[2][4];
#pragma unroll
        for (int mt = 0; mt < 2; ++mt)
#pragma unroll
            for (int ks = 0; ks < 4; ++ks) Af[mt][ks] = lds8(lds + (32 * kb + 8 * (rho >> 2) + 4 * mt + (rho & 3)) * VS + ks * 64 + bg * 16);
#pragma unroll
        for (int j = 0; j < 5; ++j) {
            if (j < 4 || w < 4) {
                f32x4 d0 = {}, d1 = {};
#pragma unroll
                for (int ks = 0; ks < 4; ++ks) { d0 = MFMA16(Af[0][ks], Bo[j][ks], d0); d1 = MFMA16(Af[1][ks], Bo[j][ks], d1); }
                v4u q = {pack2(d0[0], d0[1]), pack2(d0[2], d0[3]), pack2(d1[0], d1[1]), pack2(d1[2], d1[3])};
                ST16(q, rw, wo0 + j * (16 * 294912) + kb * 1024);
            }
        }
        if (kb == 2 || kb == 4) {
            asm volatile("s_waitcnt vmcnt(4)" ::: "memory");
            LBAR();
            if (tid == 0) ST4((u32)(kb >> 1), rw, (FL_W + idx) * 128);
        }
    }
    DRAIN();
    LBAR();
    if (tid == 0) ST4(3u, rw, (FL_W + idx) * 128);
}

__device__ __forceinline__ void role_rz(const Args& a, lptr lds, rsrc_t rw, int r, int rep) {
    const int tid = threadIdx.x, lane = tid & 63, w = __builtin_amdgcn_readfirstlane(tid >> 6);
    const int idx = (lane & 8) ? 0xEEEE : 0x4444;
    const int i16 = lane & 15, g = lane >> 4;
    int budget = SPIN_MAX;
    bf16x16 Ba[9], Bb[9];
    v4u t0[4], m0[2];
    int pm;
    {
        const int wo = OFF_WVO + r * 294912 + 2 * w * 18432 + ((i16 >> 3) * 32 + g * 8 + (i16 & 7)) * 16;
        const int vq = OFF_VOX + r * 16384 + w * 2048 + lane * 16;
#pragma unroll
        for (int j = 0; j < 2; ++j) { const int i = min(tid + 512 * j, 575), row = i / 72, c = i - row * 72; m0[j] = *(const v4u*)(a.mem0 + row * DM + c * 8); }
        const int vf = (int)LD4(rw, (FL_V + lane) * 128, 0);
        const u32 pw = LD4(rw, (FL_PAR + (lane & 31)) * 128, 0);
        int have = 0;
        bool vgot = false;
#pragma unroll
        for (int p = 0; p < 3; ++p) {
            if (have < p + 1) have = wait_flags(rw, FL_W + 12 * w, 1, 12, p + 1, lane, budget, true);
            if (p == 0 || p == 2) {
                const bool now = p == 0 ? (bool)__all(vf >= 0) : !vgot;
                if (now) {
                    if (p == 2) wait_flags(rw, FL_V, 1, 64, 0, lane, budget, true);
#pragma unroll
                    for (int k = 0; k < 4; ++k) t0[k] = LD16(rw, vq + (k & 1) * 1024, (k >> 1) * (NR * NH * 1024));
                    vgot = true;
                }
            }
            if (p == 0) pm = __all(pw != 0xFFFFFFFFu) ? decode_parity(pw, lane) : get_parity(rw, lane, budget);
#pragma unroll
            for (int q = 0; q < 3; ++q) {
                const int t = 3 * q + p;
                Ba[t] = cat16(as8(LD16(rw, wo, t * 2048)), as8(LD16(rw, wo + 1024, t * 2048)));
                Bb[t] = cat16(as8(LD16(rw, wo, 18432 + t * 2048)), as8(LD16(rw, wo + 1024, 18432 + t * 2048)));
            }
            MB();
        }
#pragma unroll
        for (int j = 0; j < 2; ++j) {
            const int i = min(tid + 512 * j, 575), row = i / 72, c = i - row * 72;
            lptr d = lds + (c >> 1) * 256 + row * 16 + (c & 1) * 8;
            *(LAS v2u*)d = v2u{m0[j].x, m0[j].z}; *(LAS v2u*)(d + 128) = v2u{m0[j].y, m0[j].w};
        }
    }
    lptr ml = lds, zl = lds + MEMB;
    lptr tka = lds + 13312, tkr = tka + 16, cntp = tka + 64;
    volatile LAS int* prod = (volatile LAS int*)(lds + 13312 + 512);
    lptr ring = lds + 16384;
    lptr ma = ml + lane * 16;
    lptr zw = zl + ((2 * w + (lane >> 5)) >> 2) * 1024 + ((((2 * w + (lane >> 5)) & 3) * 16 + (g & 1) * 8 + (i16 & 7)) * 16) + (i16 >> 3) * 8;
    const bool hi8r = (lane & 8) != 0;
    const int mso = i16 < 8 ? (r < 64 ? N_MEMA + r * 128 : N_MEMB + (r - 64) * 128) + i16 * 16 : OOB;
    const int ob = lane < NREP ? BCO(r) : OOB;
    if (tid == 0) { tk_post(tka, 0, rt32() - 100000u, 0); tk_post(tkr, -1, 0u, 0); *(volatile LAS int*)cntp = 0; }
#pragma unroll
    for (int k = 0; k < 4; ++k) *(LAS v4u*)(ring + (k >> 1) * 16384 + w * 2048 + (k & 1) * 1024 + lane * 16) = t0[k];
    if (tid < 4) prod[tid] = 2;
    LBAR();
    if (w < 4) {
        const int po = N_PX + (w >> 1) * 32768 + (w & 1) * 4096 + lane * 16;
        const int mo = N_PMM + w * 1024 + 2 * g * 128 + (i16 & 7) * 16 + (i16 >> 3) * 8;
        lptr cna = lane == 0 ? cntp : cntp + 4 + lane * 4;
        const u32 one = lane == 0 ? 1u : 0u;
        const bool dv = (g >> 1) == (i16 >> 3);
        const int rwo = dv ? (4 * (g & 1) + 2 * (g >> 1)) * 128 + ((i16 & 7) >> 1) * 32 + w * 8 + (i16 & 1) * 4 : 2048 + lane * 4;
        const int msq = lane < 32 ? (r < 64 ? N_MEMA + r * 128 : N_MEMB + (r - 64) * 128) + (lane >> 2) * 16 : OOB;
        lptr zrd = zl + w * 1024 + lane * 16;
        for (int s = 0; s < NSEG; ++s) {
            lptr Rw = lds + 81920 + (s & 1) * 1024 + rwo, Rr = lds + 81920 + (s & 1) * 1024 + (lane & 31) * 32;
            int mss = msq + NEARB(s + 1, lane & 3, pm), obs = ob + FARB(s + 1, lane & 3, pm);
            int sb = NEARB(s, rep, pm);
            asm volatile("" : "+s"(sb));
            lptr rg = ring + (s & 3) * 16384 + w * 4096 + lane * 16;
            bf16x8 b[5];
            const bool early = __builtin_amdgcn_readfirstlane(prod[w >> 1]) > s;
            MB();
#pragma unroll
            for (int k = 0; k < 4; ++k) b[k] = lds8(rg + k * 1024);
            const u32 X = s > 0 ? tk_get(tkr, min(s, NSEG - 2), budget) : 0u;
            asm volatile("s_waitcnt lgkmcnt(0)" : "+v"(mss), "+v"(obs), "+v"(Rw), "+v"(Rr) :: "memory");
#pragma unroll
            for (int k = 0; k < 4; ++k) PIN4(b[k]);
            LBAR();
            qk_project(Ba, Bb, ma, zw, idx);
            LBAR();
            if (s == NSEG - 1) break;
            __builtin_amdgcn_s_setprio(3);
            b[4] = lds8(zrd);
            if (!early) {
                while (__builtin_amdgcn_readfirstlane(prod[w >> 1]) <= s && --budget >= 0) __builtin_amdgcn_s_sleep(1);
                MB();
#pragma unroll
                for (int k = 0; k < 4; ++k) b[k] = lds8(rg + k * 1024);
            }
            if (s > 0 && (int)(X - rt32()) < 1000) spin_to(X);
            f32x4 d;
            u32 bad;
            auto shot = [&]() {
                v4u p[4]; v2u m[2];
                m[0] = LD8(rw, mo, sb); m[1] = LD8(rw, mo + 128, sb);
                __builtin_amdgcn_sched_barrier(0);
#pragma unroll
                for (int k = 0; k < 4; ++k) { p[k] = LD16(rw, po + k * 1024, sb); __builtin_amdgcn_sched_barrier(0); }
                MB();
                d = f32x4{};
                d = MFMA16(as8(m[0], m[1]), b[4], d);
                bad = (m[0].x | m[0].y) | (m[1].x | m[1].y);
                __builtin_amdgcn_sched_barrier(0);
#pragma unroll
                for (int k = 0; k < 4; ++k) {
                    d = MFMA16(as8(p[k]), b[k], d);
                    bad |= (p[k].x | p[k].y) | (p[k].z | p[k].w);
                    __builtin_amdgcn_sched_barrier(0);
                }
            };
            shot();
            if (__builtin_expect(!__all((int)bad >= 0), 0)) {
                do shot(); while (!__all((int)bad >= 0) && --budget >= 0);
            }
            float e0, e1;
            {
                auto r0 = __builtin_amdgcn_permlane32_swap(__float_as_uint(d[0]), __float_as_uint(d[2]), false, false);
                auto r1 = __builtin_amdgcn_permlane32_swap(__float_as_uint(d[1]), __float_as_uint(d[3]), false, false);
                const float t0 = __uint_as_float(hi8r ? r0[1] : r0[0]), t1 = __uint_as_float(hi8r ? r1[1] : r1[0]);
                e0 = t0 + ror8f(t0); e1 = t1 + ror8f(t1);
            }
            f32x4 x0, x1; u32 old;
            asm volatile("ds_write2_b32 %4, %5, %6 offset1:32\n\tds_add_rtn_u32 %0, %7, %8\n\tds_read_b128 %1, %3\n\tds_read_b128 %2, %3 offset:16\n\ts_waitcnt lgkmcnt(0)"
                         : "=&v"(old), "=&v"(x0), "=&v"(x1) : "v"(Rr), "v"(Rw), "v"(e0), "v"(e1), "v"(cna), "v"(one) : "memory");
            {
                const u32 c = nos(pack2((x0[0] + x0[2]) + (x1[0] + x1[2]), (x0[1] + x0[3]) + (x1[1] + x1[3])));
                v4u o = {(u32)__builtin_amdgcn_update_dpp(0, (int)c, 0x00, 0xf, 0xf, true), (u32)__builtin_amdgcn_update_dpp(0, (int)c, 0x55, 0xf, 0xf, true),
                         (u32)__builtin_amdgcn_update_dpp(0, (int)c, 0xAA, 0xf, 0xf, true), (u32)__builtin_amdgcn_update_dpp(0, (int)c, 0xFF, 0xf, 0xf, true)};
                const bool last = (__builtin_amdgcn_readfirstlane(old) & 3) == 3;
                ST16(o, rw, last ? mss : OOB);
                __builtin_amdgcn_sched_barrier(0);
                if (last) ST4(nos(rt32()), rw, obs);
            }
            __builtin_amdgcn_s_setprio(0);
        }
    } else if (w == 5 || w == 6) {
        int vdone0 = -1, vdone1 = -1, staged = 2;
        const int hw = w - 5;
        const int vo = OFF_VOX + r * 16384 + hw * 8192 + lane * 16;
        const int zho = OFF_ZH + r * 4096 + hw * 2048 + lane * 16;
        lptr zhr = zl + hw * 2048 + (((lane >> 4) & 3) * 16 + (lane & 7)) * 16 + ((lane >> 3) & 1) * 8;
        const int aro = w == 5 ? mso : OOB, arb = w == 5 ? ob : OOB;
        v4u zv[2] = {};
        for (int s = 0; s < NSEG; ++s) {
            bool go = false;
            if (staged <= min(s + 3, NSEG - 2)) {
                const int ow = VOWN(staged);
                int& vd = ow ? vdone1 : vdone0;
                if (vd < staged) vd = wait_flags(rw, FL_V + ow, 2, NH, -1, lane, budget);
                go = vd >= staged;
            }
            const int vos = go ? vo : OOB;
            int va = vos + staged * (NR * NH * 1024), zs = s > 0 ? zho + (s - 1) * (NR * NH * 256) : OOB;
            int a0 = aro + NEARB(s + 3, g, pm), a1 = arb + FARB(s + 4, lane & 3, pm);
            v4u ff = {0xFFFFFFFFu, 0xFFFFFFFFu, 0xFFFFFFFFu, 0xFFFFFFFFu};
            asm volatile("" : "+v"(va), "+v"(zs), "+v"(a0), "+v"(a1), "+v"(ff));
            if (s > 0) gather_mem4(rw, NEARB(s, rep, pm), ml, lane, w - 4, tka, s, budget);
            LBAR();
            v4u v[8];
            qk_project_h(Ba, Bb, ma, zw, idx, [&](int t) {
                if (t < 4) { v[2 * t] = LD16(rw, va + (t & 1) * 2048, (t >> 1) * 4096); v[2 * t + 1] = LD16(rw, va + 1024 + (t & 1) * 2048, (t >> 1) * 4096); }
                if (t == 4) { ST16(zv[0], rw, zs); ST16(zv[1], rw, zs + 1024); }
                if (t == 5) { ST16(ff, rw, a0); ST4(0xFFFFFFFFu, rw, a1); }
            });
            LBAR();
#pragma unroll
            for (int k = 0; k < 2; ++k) {
                const v2u lo = *(LAS v2u*)(zhr + k * 1024), hi = *(LAS v2u*)(zhr + k * 1024 + 128);
                zv[k] = v4u{lo.x, lo.y, hi.x, hi.y};
            }
            for (;;) {
                if (go) {
#pragma unroll
                    for (int k = 0; k < 8; ++k) *(LAS v4u*)(ring + (staged & 3) * 16384 + hw * 8192 + k * 1024 + lane * 16) = v[k];
                    asm volatile("s_waitcnt lgkmcnt(0)" ::: "memory");
                    ++staged;
                    prod[hw] = staged;
                }
                if (staged > s + 1 || staged > NSEG - 2) break;
                const int ow = VOWN(staged);
                int& vd = ow ? vdone1 : vdone0;
                if (vd < staged) vd = wait_flags(rw, FL_V + ow, 2, NH, staged <= s ? staged : -1, lane, budget, true);
                if (vd < staged) break;
#pragma unroll
                for (int k = 0; k < 8; ++k) v[k] = LD16(rw, vo + (k & 3) * 1024, staged * (NR * NH * 1024) + (k >> 2) * 4096);
                go = true;
            }
            DRAIN();
        }
        {
            int l2 = (int)__builtin_amdgcn_mbcnt_hi(~0u, __builtin_amdgcn_mbcnt_lo(~0u, 0u));
            asm volatile("" : "+v"(l2));
            const int zf = OFF_ZH + r * 4096 + hw * 2048 + (NSEG - 1) * (NR * NH * 256) + l2 * 16;
            ST16(zv[0], rw, zf); ST16(zv[1], rw, zf + 1024);
        }
    } else {
        TK tk; tk_init(tk);
        for (int s = 0; s < NSEG; ++s) {
            if (s > 0) {
                gather_mem4(rw, NEARB(s, rep, pm), ml, lane, w - 4, tka, s, budget);
            }
            BC2 bc = {0u, 0u};
            if (w == 7) bc = beacon_issue(rw, FARB(s, rep, pm), lane);
            LBAR();
            qk_project(Ba, Bb, ma, zw, idx);
            LBAR();
            if (w == 7) {
                if (s < NSEG - 1) {
                    if (s > 0) { tk_T0(tk, beacon_finish(bc, rw, FARB(s, rep, pm), lane, budget), a); if (tk.okA) tk_post(tka, s + 1, tk_specA(tk, a), lane); }
                    tk_post(tkr, s, tk.XR, lane);
                    wait_until(tk.XR + 8);
                    tk_Tp(tk, beacon_max<NH>(rw, FARB(s, rep, pm), BC_QK, lane, budget), a);
                    tk_post(tka, s + 1, tk.XA, lane);
                    tk_post(tkr, s + 1, tk_specR(tk, a), lane);
                }
            } else {
                if (w == 4 && s >= 2 && lane == 0) ST4((u32)(s - 2), rw, (FL_Z + r) * 128);
                __builtin_amdgcn_s_sleep(14);
            }
        }
    }
    DRAIN();
    LBAR();
    if (tid == 0) ST4((u32)(NSEG - 1), rw, (FL_Z + r) * 128);
    if (budget < 0 && lane == 0) ST4(3u, rw, FL_ERR * 128);
}

__device__ __forceinline__ void cexp_l(float lr, float li, float l, float& er, float& ei) {
    const float mag = __builtin_amdgcn_exp2f(lr * l);
    const float ph = li * l;
    const float pe = fmaf(li, l, -ph);
    const float fr = (ph - floorf(ph)) + pe;
    er = mag * __builtin_amdgcn_cosf(fr);
    ei = mag * __builtin_amdgcn_sinf(fr);
}
constexpr int CV_W = 8 * 260 * 16, CV_U = 96 * 80, CV_CH = CV_W + CV_U;
__device__ __forceinline__ void s4_unit(const Args& a, lptr lds, rsrc_t rw, int unit) {
    const int tid = threadIdx.x, lane = tid & 63, w = __builtin_amdgcn_readfirstlane(tid >> 6);
    const int d0 = unit * 4;
    v2u uv[4];
    {
        const int c = tid >> 7, nn = tid & 127, n = (d0 + c) * 128 + nn;
        const float dt = rbf(expf(bf2f(a.logdt[d0 + c])));
        const float lr = dt * bf2f(a.Are[n]) * 1.4426950408889634f, li = dt * bf2f(a.Aim[n]) * 0.15915494309189535f;
        const float br = dt * bf2f(a.Bre[n]), bi = dt * bf2f(a.Bim[n]);
        const float cr = bf2f(a.Cre[n]), ci = bf2f(a.Cim[n]);
        MB();
#pragma unroll
        for (int k = 0; k < 4; ++k) uv[k] = *(const v2u*)(a.u + (size_t)(2 * (tid + 512 * (k >> 1)) + (k & 1)) * DM + d0);
        MB();
        LAS f32x2* T = (LAS f32x2*)(lds + c * CV_CH) + nn;
        float er, ei, pr, pi;
        cexp_l(lr, li, 1.0f, er, ei); pr = er; pi = ei;
#pragma unroll
        for (int o = 0; o < 7; ++o) { T[o * 130] = f32x2{pr, pi}; const float t = pr * er - pi * ei; pi = pr * ei + pi * er; pr = t; }
#pragma unroll
        for (int q4 = 0; q4 < 4; ++q4) {
            if (q4 == 0) cexp_l(lr, li, 128.0f, er, ei);
            cexp_l(lr, li, (float)(128 + 512 * q4), pr, pi);
#pragma unroll
            for (int o = 0; o < 4; ++o) { T[(7 + 4 * q4 + o) * 130] = f32x2{pr, pi}; const float t = pr * er - pi * ei; pi = pr * ei + pi * er; pr = t; }
        }
        const float cbr = cr * br - ci * bi, cbi = cr * bi + ci * br;
        cexp_l(lr, li, 8.0f, er, ei);
#pragma unroll
        for (int q4 = 0; q4 < 4; ++q4) {
            float sr, si;
            cexp_l(lr, li, (float)(32 * q4), sr, si);
            pr = cbr * sr - cbi * si; pi = cbr * si + cbi * sr;
#pragma unroll
            for (int o = 0; o < 4; ++o) { T[(23 + 4 * q4 + o) * 130] = f32x2{pr, pi}; const float t = pr * er - pi * ei; pi = pr * ei + pi * er; pr = t; }
        }
    }
    LBAR();
    f32x4 kr[3];
    {
        const int x = lane & 15, g = lane >> 4, c = w & 3, nh = w >> 2;
        const int ah0 = x >> 3, o0 = x & 7;
        const int ah1 = x < 8 ? 2 + (x >> 2) : 4 + ((x - 8) >> 1), o1 = x < 8 ? 2 * (x & 3) : 4 * (x & 1);
        const int ah2 = x < 8 ? 8 + x : 16;
        LAS f32x2* T = (LAS f32x2*)(lds + c * CV_CH) + nh * 64 + g;
        LAS f32x2* Ta0 = T + 7 * 130; LAS f32x2* To0 = T + (o0 ? o0 - 1 : 0) * 130;
        LAS f32x2* Ta1 = T + (6 + ah1) * 130; LAS f32x2* To1 = T + (o1 ? o1 - 1 : 0) * 130;
        LAS f32x2* Ta2 = T + (6 + ah2) * 130; LAS f32x2* Tv = T + (23 + x) * 130;
        const f32x2 one = {1.0f, 0.0f};
        f32x4 ar[3] = {}, ai[3] = {};
#pragma unroll
        for (int jj = 0; jj < 16; ++jj) {
            const f32x2 v = Tv[4 * jj], u2 = Ta2[4 * jj], a1 = Ta1[4 * jj];
            f32x2 a0v = Ta0[4 * jj], e0 = To0[4 * jj], e1 = To1[4 * jj];
            a0v = ah0 ? a0v : one; e0 = o0 ? e0 : one; e1 = o1 ? e1 : one;
            const float u0r = a0v[0] * e0[0] - a0v[1] * e0[1], u0i = a0v[0] * e0[1] + a0v[1] * e0[0];
            const float u1r = a1[0] * e1[0] - a1[1] * e1[1], u1i = a1[0] * e1[1] + a1[1] * e1[0];
            ar[2] = __builtin_amdgcn_mfma_f32_16x16x4f32(u2[0], v[0], ar[2], 0, 0, 0);
            ai[2] = __builtin_amdgcn_mfma_f32_16x16x4f32(u2[1], v[1], ai[2], 0, 0, 0);
            ar[0] = __builtin_amdgcn_mfma_f32_16x16x4f32(u0r, v[0], ar[0], 0, 0, 0);
            ai[0] = __builtin_amdgcn_mfma_f32_16x16x4f32(u0i, v[1], ai[0], 0, 0, 0);
            ar[1] = __builtin_amdgcn_mfma_f32_16x16x4f32(u1r, v[0], ar[1], 0, 0, 0);
            ai[1] = __builtin_amdgcn_mfma_f32_16x16x4f32(u1i, v[1], ai[1], 0, 0, 0);
        }
#pragma unroll
        for (int t = 0; t < 3; ++t) kr[t] = ar[t] - ai[t];
    }
    LBAR();
    {
        LAS float* T = (LAS float*)(lds + (w & 3) * CV_CH + 4096 + (w >> 2) * 3072);
#pragma unroll
        for (int t = 0; t < 3; ++t)
#pragma unroll
            for (int r = 0; r < 4; ++r) T[(16 * t + 4 * (lane >> 4) + r) * 16 + (lane & 15)] = kr[t][r];
    }
    LBAR();
#pragma unroll
    for (int k = 0; k < 8; ++k) {
        const int i = tid + 512 * k, c = i >> 10, l2 = i & 1023;
        LAS float* T = (LAS float*)(lds + c * CV_CH + 4096);
        float v[2];
#pragma unroll
        for (int e = 0; e < 2; ++e) {
            const int lb = (int)rbf((float)(2 * l2 + e));
            const int hi = lb >> 7, o = lb & 7;
            const int row = lb < 256 ? 8 * hi + o : lb < 512 ? 16 + 4 * (hi - 2) + (o >> 1) : lb < 1024 ? 24 + 2 * (hi - 4) + (o >> 2) : 32 + (hi - 8);
            const int ix = row * 16 + ((lb >> 3) & 15);
            v[e] = T[ix] + T[768 + ix];
        }
        *(LAS u32*)(lds + c * CV_CH + CV_W + 2704 + l2 * 4) = pack2(v[0], v[1]);
    }
    LBAR();
    for (int i = tid; i < 4 * 259; i += 512) {
        const int c = i / 259, zz = i - c * 259, z = zz - 3;
        lptr ch = lds + c * CV_CH;
        v4u lo = {0, 0, 0, 0}, hi = {0, 0, 0, 0};
        if (z >= 1) lo = *(LAS v4u*)(ch + CV_W + 2704 + (z - 1) * 16);
        if (z >= 0) hi = *(LAS v4u*)(ch + CV_W + 2704 + z * 16);
        const u32 e[8] = {lo.x, lo.y, lo.z, lo.w, hi.x, hi.y, hi.z, hi.w};
#pragma unroll
        for (int cc = 0; cc < 8; ++cc) {
            v4u o;
#pragma unroll
            for (int k = 0; k < 4; ++k) {
                const int t = 8 + cc - 2 * k;
                if (t & 1) o[k] = (e[t >> 1] >> 16) | (e[t >> 1] << 16);
                else o[k] = (e[t >> 1] & 0xffffu) | (e[(t >> 1) - 1] & 0xffff0000u);
            }
            *(LAS v4u*)(ch + (cc * 260 + zz) * 16) = o;
        }
    }
    LBAR();
    for (int i = tid; i < 4 * 160; i += 512) *(LAS v4u*)(lds + (i / 160) * CV_CH + CV_W + (i % 160) * 16) = v4u{0, 0, 0, 0};
#pragma unroll
    for (int k2 = 0; k2 < 2; ++k2) {
        const int l = 2 * (tid + 512 * k2);
        const int o = CV_W + (32 + (l >> 5)) * 80 + (l & 31) * 2;
        const v2u p = uv[2 * k2], q = uv[2 * k2 + 1];
        *(LAS u32*)(lds + 0 * CV_CH + o) = (p.x & 0xffffu) | (q.x << 16);
        *(LAS u32*)(lds + 1 * CV_CH + o) = (p.x >> 16) | (q.x & 0xffff0000u);
        *(LAS u32*)(lds + 2 * CV_CH + o) = (p.y & 0xffffu) | (q.y << 16);
        *(LAS u32*)(lds + 3 * CV_CH + o) = (p.y >> 16) | (q.y & 0xffff0000u);
    }
    LBAR();
    const int c = w & 3, par = w >> 2, b = lane & 31, hh = lane >> 5;
    lptr ch = lds + c * CV_CH;
    lptr wa = ch + ((b & 7) * 260 + (b >> 3) - hh + 3) * 16;
    lptr ub = ch + CV_W + (32 + b) * 80 + hh * 16;
    f32x16 y0 = {}, y1 = {};
    wa += par * 64; ub -= par * 80;
    {
        bf16x8 fa[2][2], fb[2][2], fc[2][2];
#pragma unroll
        for (int ks = 0; ks < 2; ++ks) { fa[0][ks] = lds8(wa - ks * 32); fb[0][ks] = lds8(ub + ks * 32); fc[0][ks] = lds8(ub + 2560 + ks * 32); }
#pragma unroll
        for (int i = 0; i < 32; ++i) {
            const int cur = i & 1, nx = cur ^ 1;
            if (i + 1 < 32) {
#pragma unroll
                for (int ks = 0; ks < 2; ++ks) {
                    fa[nx][ks] = lds8(wa + (i + 1) * 128 - ks * 32);
                    if (i + 1 < 16) fb[nx][ks] = lds8(ub - (i + 1) * 160 + ks * 32);
                    fc[nx][ks] = lds8(ub + 2560 - (i + 1) * 160 + ks * 32);
                }
            }
            __builtin_amdgcn_sched_barrier(0);
#pragma unroll
            for (int ks = 0; ks < 2; ++ks) {
                if (i < 16) y0 = MFMA32(fa[cur][ks], fb[cur][ks], y0);
                y1 = MFMA32(fa[cur][ks], fc[cur][ks], y1);
            }
            __builtin_amdgcn_sched_barrier(0);
        }
    }
    LBAR();
    {
        LAS f32x4* X = (LAS f32x4*)(ch + par * 4096);
        const f32x16 src = par ? y0 : y1;
#pragma unroll
        for (int k = 0; k < 4; ++k) X[k * 64 + lane] = f32x4{src[4 * k], src[4 * k + 1], src[4 * k + 2], src[4 * k + 3]};
    }
    LBAR();
    {
        LAS f32x4* X = (LAS f32x4*)(ch + (1 - par) * 4096);
        lptr Y = ch + 8192 + (32 * par + b) * 72 + hh * 8;
        const f32x16 own = par ? y1 : y0;
#pragma unroll
        for (int k = 0; k < 4; ++k) {
            const f32x4 o = X[k * 64 + lane];
            v2u yv = {pack2(own[4 * k] + o[0], own[4 * k + 1] + o[1]), pack2(own[4 * k + 2] + o[2], own[4 * k + 3] + o[3])};
            *(LAS v2u*)(Y + k * 16) = yv;
        }
    }
    LBAR();
    const v2u dk = *(const v2u*)(a.dskip + d0);
#pragma unroll
    for (int k2 = 0; k2 < 2; ++k2) {
        const int i = tid + 512 * k2;
        lptr yp = lds + 8192 + (i >> 4) * 72 + (i & 15) * 4;
        const u32 ya = *(LAS u32*)(yp), yb = *(LAS u32*)(yp + CV_CH), yc = *(LAS u32*)(yp + 2 * CV_CH), yd = *(LAS u32*)(yp + 3 * CV_CH);
        const v2u p = uv[2 * k2], q = uv[2 * k2 + 1];
        v4u o;
        o.x = pack2(bflo(ya) + rbf(bflo(p.x) * bflo(dk.x)), bflo(yb) + rbf(bfhi(p.x) * bfhi(dk.x)));
        o.y = pack2(bflo(yc) + rbf(bflo(p.y) * bflo(dk.y)), bflo(yd) + rbf(bfhi(p.y) * bfhi(dk.y)));
        o.z = pack2(bfhi(ya) + rbf(bflo(q.x) * bflo(dk.x)), bfhi(yb) + rbf(bfhi(q.x) * bfhi(dk.x)));
        o.w = pack2(bfhi(yc) + rbf(bflo(q.y) * bflo(dk.y)), bfhi(yd) + rbf(bfhi(q.y) * bfhi(dk.y)));
        ST16(o, rw, OFF_A4 + unit * 16384 + i * 16);
    }
    LBAR();
}
__device__ __forceinline__ void role_s4(const Args& a, lptr lds, rsrc_t rw, int idx) {
    if (idx < 32) {
        const int tid = threadIdx.x, t = idx * 64 + (tid >> 3), j = tid & 7;
        float acc = 0.f;
#pragma unroll
        for (int k = 0; k < 9; ++k) {
            const v4u xv = *(const v4u*)(a.u + (size_t)t * DM + (k * 8 + j) * 8), gv = *(const v4u*)(a.gatew + (k * 8 + j) * 8);
#pragma unroll
            for (int e = 0; e < 4; ++e) acc += rbf(bflo(xv[e]) * bflo(gv[e])) + rbf(bfhi(xv[e]) * bfhi(gv[e]));
        }
        acc += __shfl_xor(acc, 1); acc += __shfl_xor(acc, 2); acc += __shfl_xor(acc, 4);
        acc = rbf(acc);
        if (j == 0) ST4(__float_as_uint(rbf(1.0f / (1.0f + __expf(-acc)))), rw, OFF_G + t * 4);
    }
    const bool qwg = idx >= 16 && idx < 48;
    const int u0 = qwg ? 96 + idx : (idx < 16 ? 24 + idx : idx - 48), uend = qwg ? DM / 4 : 112;
#pragma unroll 1
    for (int unit = u0; unit < uend; unit += 40) s4_unit(a, lds, rw, unit);
    DRAIN();
    __syncthreads();
    if (threadIdx.x == 0) ST4(1u, rw, (FL_S + idx) * 128);
}

__device__ __forceinline__ void role_post(const Args& a, lptr lds, rsrc_t rw) {
    const int tid = threadIdx.x, lane = tid & 63, w = __builtin_amdgcn_readfirstlane(tid >> 6);
    const int i16 = lane & 15, g = lane >> 4;
    int budget = SPIN_MAX;
    volatile LAS int* U = (volatile LAS int*)(lds + 81920);
    constexpr int NFULL = 62 * 6, NUNIT = NFULL + 24;
    if (w >= 6) {
#pragma unroll 1
        for (;;) {
            if (tid == 384) *U = 1 + (int)__hip_atomic_fetch_add((u32*)(a.ws + FL_Q * 128), 1u, __ATOMIC_RELAXED, __HIP_MEMORY_SCOPE_AGENT);
            LBAR();
            const int unit = __builtin_amdgcn_readfirstlane(*U);
            if (unit >= NUNIT) break;
            const bool half = unit >= NFULL;
            const int uu = half ? NFULL + ((unit - NFULL) >> 1) : unit, q0 = half ? (unit - NFULL) & 1 : 0;
            const int s = uu / 6;
            wait_flags(rw, FL_P + (s & 3), 4, 16, s, lane, budget, half);
            const int nb = half ? 1 : 2, pb = half ? (w - 6) * 20480 : (w - 6) * 40960;
            const int pq = OFF_PX + s * 81920 + q0 * 40960 + pb + lane * 16;
            v4u pv[2][20];
#pragma unroll
            for (int hb = 0; hb < 2; ++hb)
#pragma unroll
                for (int k = 0; k < 20; ++k) pv[hb][k] = LD16(rw, (hb < nb ? pq : OOB) + (k & 3) * 1024, hb * 20480 + (k >> 2) * 4096);
#pragma unroll
            for (int k = 0; k < 20; ++k) *(LAS v4u*)(lds + pb + k * 1024 + lane * 16) = pv[0][k];
            if (!half) {
#pragma unroll
                for (int k = 0; k < 20; ++k) *(LAS v4u*)(lds + pb + 20480 + k * 1024 + lane * 16) = pv[1][k];
            }
            LBAR();
            if (half) break;
        }
    } else {
        int vdone0 = -1, vdone1 = -1;
        wait_flags(rw, FL_S, 1, 64, 1, lane, budget);
        wait_flags(rw, FL_S + 64, 1, NS4 - 64, 1, lane, budget);
#pragma unroll 1
        for (;;) {
            LBAR();
            const int unit = __builtin_amdgcn_readfirstlane(*U);
            if (unit >= NUNIT) break;
            const bool half = unit >= NFULL;
            const int uu = half ? NFULL + ((unit - NFULL) >> 1) : unit, q0 = half ? (unit - NFULL) & 1 : 0;
            const int s = uu / 6, r = (uu - s * 6) * 6 + w;
            bf16x8 b[32];
            v2u av[2]; float gt[2];
            const bool zearly = wait_flags(rw, FL_Z + 2 * r, 1, 2, -1, lane, budget) >= s;
            if (VOWN(s)) { if (vdone1 < s) vdone1 = wait_flags(rw, FL_V + 1, 2, NH, s, lane, budget); }
            else { if (vdone0 < s) vdone0 = wait_flags(rw, FL_V, 2, NH, s, lane, budget); }
            const int vo = OFF_VOX + (s * NR + r) * (NH * 1024) + ((i16 >> 1) & 1) * 16384 + ((g & 1) * 16 + (g >> 1) * 8 + 2 * (i16 >> 2) + (i16 & 1)) * 16;
#pragma unroll
            for (int k = 0; k < 32; ++k) b[k] = as8(LD16(rw, vo + (k & 7) * 512, (k >> 3) * 4096));
#pragma unroll
            for (int qt = 0; qt < 2; ++qt) {
                av[qt] = LD8(rw, OFF_A4 + (4 * r + g) * 16384 + (32 * s + 16 * (qt ^ q0) + i16) * 8, 0);
                gt[qt] = __uint_as_float(LD4(rw, OFF_G + (32 * s + 16 * (qt ^ q0) + i16) * 4, 0));
            }
            if (!zearly) wait_flags(rw, FL_Z + 2 * r, 1, 2, s, lane, budget, half);
            v4u z[8];
            const int zo = OFF_ZH + (s * NR + r) * (NH * 256) + ((i16 >> 1) & 1) * 4096 + g * 128 + (2 * (i16 >> 2) + (i16 & 1)) * 16;
#pragma unroll
            for (int k = 0; k < 8; ++k) z[k] = LD16(rw, zo + k * 512, 0);
            LBAR();
#pragma unroll 1
            for (int qt = 0; qt < (half ? 1 : 2); ++qt) {
                f32x4 d = {}, e = {};
                lptr pl = lds + qt * 40960 + lane * 16;
                bf16x8 f[6];
#pragma unroll
                for (int i = 0; i < 6; ++i) f[i] = lds8(pl + i * 1024);
                __builtin_amdgcn_sched_barrier(0);
#pragma unroll
                for (int k = 0; k < 40; ++k) {
                    const bf16x8 v = k < 32 ? b[k < 32 ? k : 0] : as8(z[k < 32 ? 0 : k - 32]);
                    if (k & 1) e = MFMA16(v, f[k % 6], e); else d = MFMA16(v, f[k % 6], d);
                    if (k + 6 < 40) f[k % 6] = lds8(pl + (k + 6) * 1024);
                    __builtin_amdgcn_sched_barrier(0);
                }
                d += e;
                const float gq = qt ? gt[1] : gt[0], og = rbf(1.0f - gq);
                const v2u aq = qt ? av[1] : av[0];
                const float r0 = rbf(gq * bflo(aq.x)) + rbf(og * rbf(d[0])), r1 = rbf(gq * bfhi(aq.x)) + rbf(og * rbf(d[1]));
                const float r2 = rbf(gq * bflo(aq.y)) + rbf(og * rbf(d[2])), r3 = rbf(gq * bfhi(aq.y)) + rbf(og * rbf(d[3]));
                v2u ov = {pack2(r0, r1), pack2(r2, r3)};
                *(v2u*)(a.out + (size_t)(32 * s + 16 * (qt ^ q0) + i16) * DM + 16 * r + 4 * g) = ov;
            }
            if (half) break;
        }
    }
    if (budget < 0 && lane == 0) ST4(6u, rw, FL_ERR * 128);
}

__global__ void __launch_bounds__(512, 2) k_all(Args a) {
    extern __shared__ __attribute__((aligned(16))) char smem[];
    lptr lds = (lptr)smem;
    a.ws += (0u - (u32)(uintptr_t)a.ws) & 0x7FFFu;
    const rsrc_t rw = __builtin_amdgcn_make_buffer_rsrc(a.ws, 0, WS_BYTES, 0x00020000);
    if (threadIdx.x == 0) {
        const int x = (int)(__builtin_amdgcn_s_getreg(20 | (3 << 11)) & 7u);
        int role = 0;
        for (int k = 0; k < 8; ++k) {
            const int gq = (x + k) & 7;
            const int t = 1 + (int)__hip_atomic_fetch_add((u32*)(a.ws + (FL_T + gq) * 128), 1u, __ATOMIC_RELAXED, __HIP_MEMORY_SCOPE_AGENT);
            if (t < 32) { role = gq + 8 * (31 - t); break; }
        }
        *(volatile LAS int*)lds = role;
    }
    __syncthreads();
    const int b = __builtin_amdgcn_readfirstlane(*(volatile LAS int*)lds), xg = b & 7, slot = b >> 3;
    __syncthreads();

    if (slot == 8 && xg < 4) role_probe(rw, xg);
    if (slot >= 20) { role_wvo(a, lds, rw, (xg & 3) * 24 + (xg < 4 ? 12 : 0) + slot - 20); __syncthreads(); }
    if ((xg < 4 && slot >= 26) || (xg >= 4 && slot >= 20)) {
        const int idx = xg < 4 ? 48 + (slot - 26) * 4 + xg : (31 - slot) * 4 + (xg - 4);
        role_s4(a, lds, rw, idx);
        __syncthreads();
    }
    bool post = false;
    if (xg < 4) {
        if (slot < 8) role_qk(a, lds, rw, xg * 8 + slot, xg);
        else if (slot < 26) role_rz(a, lds, rw, xg * 18 + slot - 8, xg);
    } else {
        const int j = xg - 4;
        post = true;
        if (slot < 16) { role_vvo(a, lds, rw, j * 8 + (slot >> 1), slot & 1); post = slot < NJOIN; }
        else if (slot < 20) role_k2(a, lds, rw, j * 4 + slot - 16);
        else if (slot < 28) { role_q2p(a, lds, rw, j * 4 + ((slot - 20) >> 1), slot & 1); post = false; }
    }
    if (post) { __syncthreads(); role_post(a, lds, rw); }
}

extern "C" void kernel_launch(void* const* d_in, const int* in_sizes, int n_in, void* d_out, int out_size, void* d_ws, size_t ws_size, hipStream_t stream) {
    Args a;
    a.u = (const u16*)d_in[0]; a.Are = (const u16*)d_in[1]; a.Aim = (const u16*)d_in[2]; a.Bre = (const u16*)d_in[3]; a.Bim = (const u16*)d_in[4];
    a.Cre = (const u16*)d_in[5]; a.Cim = (const u16*)d_in[6]; a.logdt = (const u16*)d_in[7]; a.dskip = (const u16*)d_in[8];
    a.mem0 = (const u16*)d_in[10]; a.Wq = (const u16*)d_in[11]; a.Wk = (const u16*)d_in[12]; a.Wv = (const u16*)d_in[13]; a.Wo = (const u16*)d_in[14];
    a.gatew = (const u16*)d_in[16]; a.out = (u16*)d_out; a.ws = (char*)d_ws;
    const int LDS_ALL = 160 * 1024;
    (void)hipFuncSetAttribute((const void*)k_all, hipFuncAttributeMaxDynamicSharedMemorySize, LDS_ALL);
    (void)hipMemsetAsync(d_ws, 0xFF, INIT_BYTES + 32768, stream);
    hipLaunchKernelGGL(k_all, dim3(256), dim3(512), LDS_ALL, stream, a);
}
```

```cpp
#include <hip/hip_runtime.h>
#include <hip/hip_bf16.h>
#include <stdint.h>

typedef unsigned short u16;
typedef unsigned int u32;
typedef u32 v4u __attribute__((ext_vector_type(4)));
typedef u32 v2u __attribute__((ext_vector_type(2)));
typedef float f32x4 __attribute__((ext_vector_type(4)));
typedef float f32x2 __attribute__((ext_vector_type(2)));
typedef float f32x16 __attribute__((ext_vector_type(16)));
typedef __bf16 bf16x2 __attribute__((ext_vector_type(2)));
typedef __bf16 bf16x8 __attribute__((ext_vector_type(8)));
typedef __bf16 bf16x16 __attribute__((ext_vector_type(16)));
typedef short s16x4 __attribute__((ext_vector_type(4)));
#define LAS __attribute__((address_space(3)))
typedef LAS char* lptr;
typedef __amdgpu_buffer_rsrc_t rsrc_t;

constexpr int SEQ = 2048, DM = 576, NH = 32, DH = 128, NSEG = 64, NR = 36, NR8 = 72, NREP = 4;
constexpr int MEMB = 9216, BC_QK = 80;
constexpr int FL0 = 16384;
constexpr int FL_K = FL0, FL_V = FL0 + 16, FL_M = FL0 + 80, FL_P = FL0 + 112, FL_S = FL0 + 176, FL_Q = FL0 + 254, FL_ERR = FL0 + 255, FL_Z = FL0 + 256, FL_W = FL0 + 328, FL_T = FL0 + 424, FL_PAR = FL0 + 432, NS4 = 72;
constexpr int N_MEMA = 0, N_PX = 32768, N_PMM = 98304, N_MEMB = 98304 + 4096, N_PROBE = 98304 + 6144;
constexpr int INIT_BYTES = 16 * 131072 + 65536;
constexpr int OFF_ZH = INIT_BYTES;
constexpr int OFF_KX = OFF_ZH + NSEG * NR * NH * 256;
constexpr int OFF_VOX = OFF_KX + NH * SEQ * DH * 2;
constexpr int OFF_KM = OFF_VOX + NSEG * NR * NH * 1024;
constexpr int OFF_PX = OFF_KM + NSEG * NH * 2048;
constexpr int OFF_A4 = OFF_PX + NSEG * 81920;
constexpr int OFF_G = OFF_A4 + SEQ * DM * 2;
constexpr int OFF_WVO = OFF_G + SEQ * 4;
constexpr int WS_BYTES = OFF_WVO + NR8 * 294912;
constexpr int TK_LA = 8, TK_LR = 8, TK_TOL = 8, TK_GAP = 30;
constexpr int NJOIN = 6;

struct Args {
    const u16* u; const u16* Are; const u16* Aim; const u16* Bre; const u16* Bim; const u16* Cre; const u16* Cim;
    const u16* logdt; const u16* dskip; const u16* mem0; const u16* Wq; const u16* Wk; const u16* Wv; const u16* Wo;
    const u16* gatew; u16* out; char* ws;
};

__device__ __forceinline__ float bf2f(u16 v) { return __uint_as_float((u32)v << 16); }
__device__ __forceinline__ float bflo(u32 v) { return __uint_as_float(v << 16); }
__device__ __forceinline__ float bfhi(u32 v) { return __uint_as_float(v & 0xffff0000u); }
__device__ __forceinline__ u32 pack2(float lo, float hi) { f32x2 f = {lo, hi}; bf16x2 b = __builtin_convertvector(f, bf16x2); return __builtin_bit_cast(u32, b); }
__device__ __forceinline__ float rbf(float x) { return bflo(pack2(x, x)); }
__device__ __forceinline__ u32 nos(u32 v) { return min(v, 0xFFFFFFFEu); }
__device__ __forceinline__ v2u trd(lptr p) { return __builtin_bit_cast(v2u, __builtin_amdgcn_ds_read_tr16_b64_v4i16((LAS s16x4*)p)); }
__device__ __forceinline__ bf16x8 as8(v4u v) { return __builtin_bit_cast(bf16x8, v); }
__device__ __forceinline__ bf16x8 as8(v2u a, v2u b) { v4u v = {a.x, a.y, b.x, b.y}; return __builtin_bit_cast(bf16x8, v); }
__device__ __forceinline__ v4u asu(bf16x8 v) { return __builtin_bit_cast(v4u, v); }
__device__ __forceinline__ bf16x16 cat16(bf16x8 a, bf16x8 b) { return __builtin_shufflevector(a, b, 0, 1, 2, 3, 4, 5, 6, 7, 8, 9, 10, 11, 12, 13, 14, 15); }
__device__ __forceinline__ bf16x8 lds8(lptr p) { return *(LAS bf16x8*)p; }
__device__ __forceinline__ u32 bad4(v4u v) { return max(max(v.x, v.y), max(v.z, v.w)); }
#define NEARB(s, rep, pm) (((((s) & 3) * 4 + (rep)) << 17) + (int)(((((u32)(pm) >> (((s) & 3) * 4 + (rep))) & 0x10001u) * 0x20004000u >> 16) & 0x6000u))
#define FARB(s, rep, pm) ((NEARB(s, rep, pm) ^ 16384) + (((s) & 4) << 14))
#define BCO(slot) (((slot) & 63) * 128 + ((slot) >> 6) * 32768)
#define LBAR() do { asm volatile("s_waitcnt lgkmcnt(0)" ::: "memory"); __builtin_amdgcn_s_barrier(); asm volatile("" ::: "memory"); } while (0)
#define MB() asm volatile("" ::: "memory")
#define DRAIN() asm volatile("s_waitcnt vmcnt(0)" ::: "memory")
#define PIN4(x) asm volatile("" : "+v"(x))
#define MFMA16(a, b, c) __builtin_amdgcn_mfma_f32_16x16x32_bf16((a), (b), (c), 0, 0, 0)
#define MFMA32(a, b, c) __builtin_amdgcn_mfma_f32_32x32x16_bf16((a), (b), (c), 0, 0, 0)
#define SMF(a, b, c, idx) __builtin_amdgcn_smfmac_f32_16x16x64_bf16((a), (b), (c), (idx), 0, 0)
#define LD16(r, vo, so) __builtin_amdgcn_raw_buffer_load_b128((r), (vo), (so), 16)
#define LD8(r, vo, so) __builtin_amdgcn_raw_buffer_load_b64((r), (vo), (so), 16)
#define LD4(r, vo, so) __builtin_amdgcn_raw_buffer_load_b32((r), (vo), (so), 16)
#define ST16(v, r, vo) __builtin_amdgcn_raw_buffer_store_b128((v), (r), (vo), 0, 16)
#define ST8(v, r, vo) __builtin_amdgcn_raw_buffer_store_b64((v), (r), (vo), 0, 16)
#define ST4(v, r, vo) __builtin_amdgcn_raw_buffer_store_b32((v), (r), (vo), 0, 16)

__device__ __forceinline__ float swap32_sum(float a, float b) {
    auto r = __builtin_amdgcn_permlane32_swap(__float_as_uint(a), __float_as_uint(b), false, false);
    return __uint_as_float(r[0]) + __uint_as_float(r[1]); }
__device__ __forceinline__ float xmax(float m) {
    auto r = __builtin_amdgcn_permlane16_swap(__float_as_uint(m), __float_as_uint(m), false, false);
    m = fmaxf(__uint_as_float(r[0]), __uint_as_float(r[1]));
    auto q = __builtin_amdgcn_permlane32_swap(__float_as_uint(m), __float_as_uint(m), false, false);
    return fmaxf(__uint_as_float(q[0]), __uint_as_float(q[1])); }
__device__ __forceinline__ float xsum(float m) {
    auto r = __builtin_amdgcn_permlane16_swap(__float_as_uint(m), __float_as_uint(m), false, false);
    m = __uint_as_float(r[0]) + __uint_as_float(r[1]);
    auto q = __builtin_amdgcn_permlane32_swap(__float_as_uint(m), __float_as_uint(m), false, false);
    return __uint_as_float(q[0]) + __uint_as_float(q[1]); }
__device__ __forceinline__ float vmax(float a, float b) { float r; asm("v_max_f32 %0, %1, %2" : "=v"(r) : "v"(a), "v"(b)); return r; }
__device__ __forceinline__ float vmax3(float a, float b, float c) { float r; asm("v_max3_f32 %0, %1, %2, %3" : "=v"(r) : "v"(a), "v"(b), "v"(c)); return r; }
__device__ __forceinline__ u32 ror8(u32 v) { return (u32)__builtin_amdgcn_update_dpp(0, (int)v, 0x128, 0xf, 0xf, true); }
__device__ __forceinline__ float ror8f(float v) { return __uint_as_float(ror8(__float_as_uint(v))); }
__device__ __forceinline__ int wave_max(int d) {
    d = max(d, __builtin_amdgcn_update_dpp(d, d, 0x128, 0xf, 0xf, false));
    d = max(d, __builtin_amdgcn_update_dpp(d, d, 0x124, 0xf, 0xf, false));
    d = max(d, __builtin_amdgcn_update_dpp(d, d, 0x122, 0xf, 0xf, false));
    d = max(d, __builtin_amdgcn_update_dpp(d, d, 0x121, 0xf, 0xf, false));
    return max(max(__builtin_amdgcn_readlane(d, 0), __builtin_amdgcn_readlane(d, 16)), max(__builtin_amdgcn_readlane(d, 32), __builtin_amdgcn_readlane(d, 48)));
}

constexpr int SPIN_MAX = 2000000;
__device__ __forceinline__ int wait_flags(rsrc_t rw, int line0, int stride, int n, int need, int lane, int& budget, bool fast = false) {
    const int vo = (line0 + (lane < n ? lane : 0) * stride) * 128;
    int mn;
    for (;;) {
        const int v = (int)LD4(rw, vo, 0);
        MB();
        mn = -wave_max(-v);
        if (mn >= need) break;
        if (--budget < 0) break;
        if (fast) __builtin_amdgcn_s_sleep(2); else __builtin_amdgcn_s_sleep(12);
    }
    return mn;
}

template <int NRW, int CW>
__device__ __forceinline__ void rows_ld(v4u (&v)[NRW * CW / 512], const u16* g, int gs, int tid) {
#pragma unroll
    for (int j = 0; j < NRW * CW / 512; ++j) { const int i = tid + 512 * j, row = i / CW, c = i % CW; v[j] = *(const v4u*)(g + (size_t)row * gs + c * 8); }
}
template <int NRW, int CW>
__device__ __forceinline__ void rows_st(const v4u (&v)[NRW * CW / 512], lptr lds, int ls, int tid) {
#pragma unroll
    for (int j = 0; j < NRW * CW / 512; ++j) { const int i = tid + 512 * j, row = i / CW, c = i % CW; *(LAS v4u*)(lds + row * ls + c * 16) = v[j]; }
}
__device__ __forceinline__ bf16x8 frag16(lptr base, int ls, int lane) {
    const int i16 = lane & 15, bg = lane >> 4, q = i16 >> 2, p = i16 & 3;
    lptr a = base + (8 * bg + q) * ls + p * 8;
    return as8(trd(a), trd(a + 4 * ls));
}
__device__ __forceinline__ bf16x8 frag32p(lptr base, int ls, int lane) {
    const int i16 = lane & 15, G = lane >> 4, q = i16 >> 2, p = i16 & 3;
    lptr a = base + (8 * (G >> 1) + q) * ls + (16 * (p & 1) + 8 * (G & 1) + 4 * (p >> 1)) * 2;
    return as8(trd(a), trd(a + 4 * ls));
}

constexpr int XS = 1168;
__device__ __forceinline__ void xload(v4u (&st)[9], const u16* x, int tok0, int tid) {
#pragma unroll
    for (int j = 0; j < 9; ++j) st[j] = *(const v4u*)(x + (size_t)tok0 * DM + (tid + 512 * j) * 8);
}
__device__ __forceinline__ void xstore(const v4u (&st)[9], lptr xl, int tid) {
#pragma unroll
    for (int j = 0; j < 9; ++j) { const int i = tid + 512 * j; *(LAS v4u*)(xl + i * 16 + (i / 72) * 16) = st[j]; }
}
__device__ __forceinline__ void load_w2(bf16x8 (&A)[36], const u16* W, int h0, lptr lds, int tid, int lane, int w) {
    v4u v[6][6];
#pragma unroll
    for (int c = 0; c < 6; ++c) rows_ld<96, 32>(v[c], W + (size_t)c * 96 * 4096 + h0 * DH, 4096, tid);
    __builtin_amdgcn_sched_barrier(0);
#pragma unroll
    for (int c = 0; c < 6; ++c) {
        lptr l = lds + (c & 1) * (96 * 544);
        rows_st<96, 32>(v[c], l, 544, tid);
        LBAR();
#pragma unroll
        for (int ks = 0; ks < 6; ++ks) A[c * 6 + ks] = frag32p(l + ks * 16 * 544 + w * 64, 544, lane);
        __builtin_amdgcn_sched_barrier(0);
    }
    LBAR();
}

__device__ __forceinline__ f32x16 proj_tile(const bf16x8 (&A)[36], lptr xb) {
    f32x16 acc = {};
    bf16x8 f[6];
#pragma unroll
    for (int i = 0; i < 6; ++i) f[i] = lds8(xb + i * 32);
    __builtin_amdgcn_sched_barrier(0);
#pragma unroll
    for (int ks = 0; ks < 36; ++ks) {
        acc = MFMA32(A[ks], f[ks % 6], acc);
        if (ks + 6 < 36) f[ks % 6] = lds8(xb + (ks + 6) * 32);
        __builtin_amdgcn_sched_barrier(0);
    }
    return acc;
}
__device__ __forceinline__ void role_k2(const Args& a, lptr lds, rsrc_t rw, int cu) {
    const int tid = threadIdx.x, lane = tid & 63, w = __builtin_amdgcn_readfirstlane(tid >> 6);
    v4u st[9];
    bf16x8 A[36];
    xload(st, a.u, 32, tid);
    load_w2(A, a.Wk, 2 * cu, lds, tid, lane, w);
    xstore(st, lds, tid);
    LAS int* cnt = (LAS int*)(lds + 2 * 64 * XS);
    if (tid == 0) *cnt = 0;
    __syncthreads();
    const int o0 = OFF_KX + (2 * cu + (w >> 2)) * (NSEG * 8192) + ((w & 3) * 4 + (lane >> 5) * 2) * 512 + (lane & 31) * 16;
#pragma unroll 1
    for (int blk0 = 0; blk0 < 32; ++blk0) {
        int blk = blk0;
        asm volatile("" : "+s"(blk));
        lptr xl = lds + (blk & 1) * (64 * XS);
        const int sg0 = min(2 * blk + 1, 62);
        if (blk + 1 < 32) xload(st, a.u, min(2 * blk + 3, 62) * 32, tid);
#pragma unroll
        for (int tt = 0; tt < 2; ++tt) {
            const f32x16 acc = proj_tile(A, xl + (32 * tt + (lane & 31)) * XS + (lane >> 5) * 16);
            v4u c0 = {pack2(acc[0], acc[1]), pack2(acc[2], acc[3]), pack2(acc[4], acc[5]), pack2(acc[6], acc[7])};
            v4u c1 = {pack2(acc[8], acc[9]), pack2(acc[10], acc[11]), pack2(acc[12], acc[13]), pack2(acc[14], acc[15])};
            const int o = o0 + (sg0 + tt) * 8192;
            ST16(c0, rw, o); ST16(c1, rw, o + 512);
            if (blk == 0 && tt == 0) { DRAIN(); LBAR(); if (tid == 0) ST4(1u, rw, (FL_K + cu) * 128); }
            if (tt == 0 && blk >= 3) {
                asm volatile("s_waitcnt vmcnt(2)" ::: "memory");
                if (lane == 0) {
                    const int old = __hip_atomic_fetch_add(cnt, 1, __ATOMIC_RELAXED, __HIP_MEMORY_SCOPE_WORKGROUP);
                    if ((old & 7) == 7) ST4((u32)(2 * blk), rw, (FL_K + cu) * 128);
                }
            }
        }
        if (blk + 1 < 32) xstore(st, lds + ((blk + 1) & 1) * (64 * XS), tid);
        const bool full = blk < 2 || blk == 31;
        if (full) DRAIN();
        LBAR();
        if (tid == 0 && full) ST4((u32)(sg0 + 1), rw, (FL_K + cu) * 128);
    }
}

constexpr int VS = 272;
__device__ __forceinline__ void role_q2p(const Args& a, lptr lds, rsrc_t rw, int cu, int par) {
    const int tid = threadIdx.x, lane = tid & 63, w = __builtin_amdgcn_readfirstlane(tid >> 6);
    v4u st[9];
    bf16x8 A[36];
    load_w2(A, a.Wq, 2 * cu, lds, tid, lane, w);
    lptr xl = lds, ql = lds + 64 * XS;
    LAS int* cnt = (LAS int*)(lds + 64 * XS + 4 * 64 * VS);
    if (tid < 2) cnt[tid] = 0;
    xload(st, a.u, par * 64, tid);
    xstore(st, xl, tid);
    __syncthreads();
    int budget = SPIN_MAX, kdone = -1;
    const int i16 = lane & 15, g = lane >> 4;
    const int sg = w >> 2, hh = (w >> 1) & 1, qt = w & 1, h = 2 * cu + hh;
    const float c2 = 0.08838834764831845f * 1.4426950408889634f;
    for (int it = -1; it < 16; ++it) {
        const int blk = 2 * it + par;
        if (it + 1 < 16) {
            if (it + 2 < 16) xload(st, a.u, (blk + 4) * 64, tid);
            lptr qb = ql + (((it + 1) & 1) * 2 + (w >> 2)) * (64 * VS) + ((w & 3) * 32 + (lane >> 5) * 16) * 2;
#pragma unroll
            for (int tt = 0; tt < 2; ++tt) {
                const f32x16 acc = proj_tile(A, xl + (32 * tt + (lane & 31)) * XS + (lane >> 5) * 16);
                v4u c0 = {pack2(acc[0], acc[1]), pack2(acc[2], acc[3]), pack2(acc[4], acc[5]), pack2(acc[6], acc[7])};
                v4u c1 = {pack2(acc[8], acc[9]), pack2(acc[10], acc[11]), pack2(acc[12], acc[13]), pack2(acc[14], acc[15])};
                lptr qp = qb + (32 * tt + (lane & 31)) * VS;
                *(LAS v4u*)qp = c0; *(LAS v4u*)(qp + 16) = c1;
            }
            __syncthreads();
            if (it + 2 < 16) xstore(st, xl, tid);
        }
        if (it >= 0) {
            const int s = 2 * blk + sg;
            if (s == 0) wait_flags(rw, FL_M + h, 1, 1, 0, lane, budget);
            else if (kdone < s) kdone = wait_flags(rw, FL_K + cu, 1, 1, s, lane, budget);
            bf16x8 k0[4], k1[4], km[4], qf[4];
            const int ko = OFF_KX + (h * NSEG + s) * 8192 + g * 512 + i16 * 16;
#pragma unroll
            for (int ks = 0; ks < 4; ++ks) { k0[ks] = as8(LD16(rw, ko, ks * 2048)); k1[ks] = as8(LD16(rw, ko + 256, ks * 2048)); }
            lptr qp = ql + ((it & 1) * 2 + hh) * (64 * VS) + (32 * sg + 16 * qt + i16) * VS + g * 16;
#pragma unroll
            for (int ks = 0; ks < 4; ++ks) qf[ks] = lds8(qp + ks * 64);
            f32x4 S0 = {}, S1 = {}, Sm = {};
#pragma unroll
            for (int ks = 0; ks < 4; ++ks) { S0 = MFMA16(k0[ks], qf[ks], S0); S1 = MFMA16(k1[ks], qf[ks], S1); }
            wait_flags(rw, FL_M + h, 1, 1, s, lane, budget, s >= 62);
            const int mo = OFF_KM + (s * NH + h) * 2048 + g * 128 + (i16 & 7) * 16;
#pragma unroll
            for (int ks = 0; ks < 4; ++ks) km[ks] = as8(LD16(rw, mo + ks * 512, 0));
#pragma unroll
            for (int ks = 0; ks < 4; ++ks) Sm = MFMA16(km[ks], qf[ks], Sm);
            const int qi = 16 * qt + i16, t0 = 4 * g;
#pragma unroll
            for (int r = 0; r < 4; ++r) {
                if (t0 + r > qi) S0[r] = -INFINITY;
                if (t0 + r + 16 > qi) S1[r] = -INFINITY;
                if (g >= 2) Sm[r] = -INFINITY;
            }
            float m = fmaxf(fmaxf(fmaxf(S0[0], S0[1]), fmaxf(S0[2], S0[3])), fmaxf(fmaxf(S1[0], S1[1]), fmaxf(S1[2], S1[3])));
            m = fmaxf(m, fmaxf(fmaxf(Sm[0], Sm[1]), fmaxf(Sm[2], Sm[3])));
            m = xmax(m);
            const float nm = -m * c2;
            float sum = 0.f;
#pragma unroll
            for (int r = 0; r < 4; ++r) {
                S0[r] = __builtin_amdgcn_exp2f(fmaf(S0[r], c2, nm)); S1[r] = __builtin_amdgcn_exp2f(fmaf(S1[r], c2, nm)); Sm[r] = __builtin_amdgcn_exp2f(fmaf(Sm[r], c2, nm));
                sum += S0[r] + S1[r] + Sm[r];
            }
            sum = xsum(sum);
            const float inv = __builtin_amdgcn_rcpf(sum);
            v4u px = {pack2(S0[0] * inv, S0[1] * inv), pack2(S0[2] * inv, S0[3] * inv), pack2(S1[0] * inv, S1[1] * inv), pack2(S1[2] * inv, S1[3] * inv)};
            const int po = OFF_PX + (s * 2 + qt) * 40960;
            ST16(px, rw, po + h * 1024 + lane * 16);
            const u32 ma = pack2(Sm[0] * inv, Sm[1] * inv), mb = pack2(Sm[2] * inv, Sm[3] * inv);
            auto ra = __builtin_amdgcn_permlane16_swap(ma, ma, false, false);
            auto rb = __builtin_amdgcn_permlane16_swap(mb, mb, false, false);
            v4u pm = {ra[0], rb[0], ra[1], rb[1]};
            if (lane < 16) ST16(pm, rw, po + 32768 + h * 256 + lane * 16);
            DRAIN();
            if (lane == 0) {
                const int old = __hip_atomic_fetch_add(cnt + sg, 1, __ATOMIC_RELAXED, __HIP_MEMORY_SCOPE_WORKGROUP);
                if ((old & 3) == 3) ST4((u32)s, rw, (FL_P + 4 * cu + 2 * par + sg) * 128);
            }
        }
        __syncthreads();
    }
    if (budget < 0 && lane == 0) ST4(5u, rw, FL_ERR * 128);
}

__device__ __forceinline__ void role_vvo(const Args& a, lptr lds, rsrc_t rw, int h, int par) {
    const int tid = threadIdx.x, lane = tid & 63, w = __builtin_amdgcn_readfirstlane(tid >> 6);
    v4u st[9];
#pragma unroll
    for (int j = 0; j < 9; ++j) { const int i = tid + 512 * j; st[j] = *(const v4u*)(a.u + (size_t)(par * 32 + (i >= 2304 ? 32 : 0)) * DM + i * 8); }
    bf16x8 Av[18], Bo[5][4];
    {
        v4u v[3][6], o[2][9];
#pragma unroll
        for (int c = 0; c < 3; ++c) rows_ld<192, 16>(v[c], a.Wv + (size_t)c * 192 * 4096 + h * DH, 4096, tid);
#pragma unroll
        for (int c = 0; c < 2; ++c) rows_ld<64, 72>(o[c], a.Wo + ((size_t)h * DH + c * 64) * DM, DM, tid);
        __builtin_amdgcn_sched_barrier(0);
#pragma unroll
        for (int c = 0; c < 3; ++c) {
            lptr l = lds + (c & 1) * 77824;
            rows_st<192, 16>(v[c], l, 288, tid);
            LBAR();
#pragma unroll
            for (int ks = 0; ks < 6; ++ks) Av[c * 6 + ks] = frag16(l + ks * 32 * 288 + w * 32, 288, lane);
            __builtin_amdgcn_sched_barrier(0);
        }
#pragma unroll
        for (int c = 0; c < 2; ++c) {
            lptr l = lds + ((c + 1) & 1) * 77824;
            rows_st<64, 72>(o[c], l, 1184, tid);
            LBAR();
#pragma unroll
            for (int j = 0; j < 5; ++j) {
                const int r = (w + 8 * j < NR) ? w + 8 * j : w;
#pragma unroll
                for (int ks = 0; ks < 2; ++ks) Bo[j][c * 2 + ks] = frag16(l + ks * 32 * 1184 + r * 32, 1184, lane);
            }
            __builtin_amdgcn_sched_barrier(0);
        }
        LBAR();
    }
    lptr xl = lds, vl = lds + 64 * XS;
    xstore(st, xl, tid);
    __syncthreads();
    const int rho = lane & 15, bg = lane >> 4;
    const int vo0 = OFF_VOX + ((2 * w + ((rho >> 1) & 1)) * 16 + (h >> 1)) * 1024 + ((2 * (h & 1) + (bg & 1)) * 16 + (bg >> 1) * 8 + 2 * (rho >> 2) + (rho & 1)) * 16;
    const int fo = (FL_V + 2 * h + par) * 128;
    auto stage1 = [&](int tt) {
        f32x4 acc = {};
        lptr xb = xl + (16 * tt + rho) * XS + bg * 16;
        bf16x8 f[6];
#pragma unroll
        for (int i = 0; i < 6; ++i) f[i] = lds8(xb + i * 64);
        __builtin_amdgcn_sched_barrier(0);
#pragma unroll
        for (int ks = 0; ks < 18; ++ks) {
            acc = MFMA16(Av[ks], f[ks % 6], acc);
            if (ks + 6 < 18) f[ks % 6] = lds8(xb + (ks + 6) * 64);
            __builtin_amdgcn_sched_barrier(0);
        }
        v2u o = {pack2(acc[0], acc[1]), pack2(acc[2], acc[3])};
        *(LAS v2u*)(vl + (16 * tt + rho) * VS + (16 * w + 4 * bg) * 2) = o;
    };
    auto stage2 = [&](int sg, int s) {
        bf16x8 Af[2][4];
#pragma unroll
        for (int half = 0; half < 2; ++half)
#pragma unroll
            for (int ks = 0; ks < 4; ++ks) Af[half][ks] = lds8(vl + (32 * sg + rho + 16 * half) * VS + ks * 64 + bg * 16);
#pragma unroll
        for (int j = 0; j < 5; ++j) {
            if (j < 4 || w < 4) {
                f32x4 d0 = {}, d1 = {};
#pragma unroll
                for (int ks = 0; ks < 4; ++ks) { d0 = MFMA16(Af[0][ks], Bo[j][ks], d0); d1 = MFMA16(Af[1][ks], Bo[j][ks], d1); }
                v4u o = {pack2(d0[0], d0[1]), pack2(d0[2], d0[3]), pack2(d1[0], d1[1]), pack2(d1[2], d1[3])};
                ST16(o, rw, vo0 + (s * NR + 8 * j) * (NH * 1024));
            }
        }
    };
    int prev = -1;
#pragma unroll 1
    for (int un = 0; un < 32; ++un) {
        const int sg = un & 1, blk = (un >> 1) * 2 + par;
        const int s = un < 2 ? par + 2 * un : 2 * blk + sg;
        if (sg == 0 && blk + 2 < 32) xload(st, a.u, (blk + 2) * 64, tid);
        stage1(2 * sg); stage1(2 * sg + 1);
        if (sg == 1) DRAIN(); else if (blk + 2 < 32) asm volatile("s_waitcnt vmcnt(9)" ::: "memory"); else DRAIN();
        LBAR();
        if (tid == 0 && prev >= 0) ST4((u32)prev, rw, fo);
        if (sg == 1 && blk + 2 < 32) xstore(st, xl, tid);
        stage2(sg, s);
        prev = s;
        if (un == 0 || un == 31) { DRAIN(); LBAR(); if (tid == 0) ST4((u32)s, rw, fo); }
        else if (sg == 1) LBAR();
    }
}
#define VOWN(s) ((s) < 4 ? ((s) & 1) : (((s) >> 1) & 1))

__device__ __forceinline__ u32 rt32() { return (u32)__builtin_amdgcn_s_memrealtime(); }
__device__ __forceinline__ void wait_until(u32 t) {
    if ((int)(t - rt32()) > 1000) return;
    while ((int)(rt32() - t) < -24) __builtin_amdgcn_s_sleep(4);
    while ((int)(rt32() - t) < 0) {}
}
struct TK { u32 XA, XR, EA, ER; int dA0, dA1, dA2, dR0, dR1, dR2; int okA, okR, gap; };
__device__ __forceinline__ int tk_est(int d0, int d1, int d2) { return max(min(d0, d1), min(max(d0, d1), d2)); }
template <int N>
__device__ __forceinline__ u32 beacon_max(rsrc_t rw, int fb, int slot0, int lane, int& budget) {
    const int vo = BCO(slot0 + (lane < N ? lane : 0)), v2 = BCO(64 + (lane & 7));
    u32 v, u = 0;
    for (;;) {
        v = LD4(rw, vo, fb);
        if (N > 64) u = LD4(rw, v2, fb);
        MB();
        if (__all(max(v, u) != 0xFFFFFFFFu)) break;
        if (--budget < 0) break;
        __builtin_amdgcn_s_sleep(1);
    }
    const u32 base = __builtin_amdgcn_readfirstlane(v);
    int d = (int)(v - base);
    if (N > 64) d = max(d, (int)(u - base));
    return base + (u32)wave_max(d);
}
struct BC2 { u32 v, u; };
__device__ __forceinline__ BC2 beacon_issue(rsrc_t rw, int fb, int lane) {
    BC2 b;
    b.v = LD4(rw, BCO(lane), fb);
    b.u = LD4(rw, BCO(64 + (lane & 7)), fb);
    MB();
    return b;
}
__device__ __forceinline__ u32 beacon_finish(BC2 b, rsrc_t rw, int fb, int lane, int& budget) {
    if (!__all(max(b.v, b.u) != 0xFFFFFFFFu)) return beacon_max<NR8>(rw, fb, 0, lane, budget);
    const u32 base = __builtin_amdgcn_readfirstlane(b.v);
    return base + (u32)wave_max(max((int)(b.v - base), (int)(b.u - base)));
}
__device__ __forceinline__ void tk_T0(TK& k, u32 T0, const Args& a) {
    if (k.okR) { k.dR2 = k.dR1; k.dR1 = k.dR0; k.dR0 = (int)(T0 - k.ER); }
    k.okA = (int)(k.XA - T0) >= TK_LA - TK_TOL;
    if (!k.okR) k.gap = k.okA ? max(TK_GAP, k.gap - 6) : min(k.gap + 16, 160);
    k.EA = k.XA;
    k.XR = k.EA + tk_est(k.dA0, k.dA1, k.dA2) + TK_LR;
}
__device__ __forceinline__ void tk_Tp(TK& k, u32 Tp, const Args& a) {
    const int okn = (int)(k.XR - Tp) >= TK_LR - TK_TOL;
    if (k.okA && (k.okR || !okn)) { k.dA2 = k.dA1; k.dA1 = k.dA0; k.dA0 = (int)(Tp - k.EA); }
    k.okR = okn;
    k.ER = k.okR ? k.XR : Tp + k.gap;
    k.XA = k.ER + tk_est(k.dR0, k.dR1, k.dR2) + TK_LA;
}
__device__ __forceinline__ u32 tk_specA(const TK& k, const Args& a) { return k.XR + tk_est(k.dR0, k.dR1, k.dR2) + TK_LA; }
__device__ __forceinline__ u32 tk_specR(const TK& k, const Args& a) { return k.XA + tk_est(k.dA0, k.dA1, k.dA2) + TK_LR; }
__device__ __forceinline__ void tk_init(TK& k) { const u32 n = rt32(); k.XA = k.XR = k.EA = k.ER = n - 100000u; k.dA0 = k.dA1 = k.dA2 = 123; k.dR0 = k.dR1 = k.dR2 = 65; k.okA = k.okR = 0; k.gap = 50; }

__device__ __forceinline__ void tk_post(lptr p, int tag, u32 t, int lane) { if (lane == 0) { v2u v = {(u32)tag, t}; *(volatile LAS v2u*)p = v; } }
__device__ __forceinline__ u32 tk_get(lptr p, int tag, int& budget) {
    for (;;) {
        const v2u v = *(volatile LAS v2u*)p;
        if ((int)__builtin_amdgcn_readfirstlane(v.x) >= tag || --budget < 0) return __builtin_amdgcn_readfirstlane(v.y);
        __builtin_amdgcn_s_sleep(2);
    }
}
__device__ __forceinline__ void spin_to(u32 X) {
    int rem;
    asm volatile("1:\n\ts_memrealtime vcc\n\ts_waitcnt lgkmcnt(0)\n\ts_sub_i32 %0, %1, vcc_lo\n\ts_cmp_gt_i32 %0, 3\n\ts_cbranch_scc1 1b\n\t"
                 "s_cmp_lt_i32 %0, 1\n\ts_cbranch_scc1 2f\n\ts_nop 3\n\ts_cmp_lt_i32 %0, 2\n\ts_cbranch_scc1 2f\n\ts_nop 3\n\t"
                 "s_cmp_lt_i32 %0, 3\n\ts_cbranch_scc1 2f\n\ts_nop 7\n2:"
                 : "=&s"(rem) : "s"(X) : "memory", "scc", "vcc");
}
__device__ __forceinline__ void tk_wait(lptr p, int tag, int& budget) {
    u32 X;
    for (;;) {
        const v2u v = *(volatile LAS v2u*)p;
        const u32 now = rt32();
        X = __builtin_amdgcn_readfirstlane(v.y);
        const int tg = (int)__builtin_amdgcn_readfirstlane(v.x), dt = (int)(X - now);
        if (tg >= tag) {
            if (dt <= 0 || dt > 1000 || tg > tag) return;
            if (dt <= 28) break;
        }
        if (--budget < 0) return;
        __builtin_amdgcn_s_sleep(1);
    }
    spin_to(X);
}
constexpr int OOB = 0x70000000;
__device__ __forceinline__ void gather_mem4(rsrc_t rw, int nb, lptr ml, int lane, int gi, lptr tkp, int tag, int& budget) {
    const int l0 = gi * 2048 + lane * 16;
    const int o2 = lane < 16 ? N_MEMB + gi * 256 + lane * 16 : OOB;
    lptr p2 = ml + (lane < 16 ? 8192 + gi * 256 + lane * 16 : 14336 + lane * 16);
    asm volatile("" : "+s"(nb));
    tk_wait(tkp, tag, budget);
    v4u v0 = LD16(rw, l0, nb), v1 = LD16(rw, l0 + 1024, nb), v2 = LD16(rw, o2, nb);
    MB();
    *(LAS v4u*)(ml + l0) = v0; *(LAS v4u*)(ml + l0 + 1024) = v1; *(LAS v4u*)p2 = v2;
    MB();
    if (__builtin_expect(!__all(max(max(bad4(v0), bad4(v1)), bad4(v2)) != 0xFFFFFFFFu), 0)) {
        do {
            v0 = LD16(rw, l0, nb); v1 = LD16(rw, l0 + 1024, nb); v2 = LD16(rw, o2, nb);
            MB();
        } while (!__all(max(max(bad4(v0), bad4(v1)), bad4(v2)) != 0xFFFFFFFFu) && --budget >= 0);
        *(LAS v4u*)(ml + l0) = v0; *(LAS v4u*)(ml + l0 + 1024) = v1; *(LAS v4u*)p2 = v2;
    }
}
__device__ __forceinline__ int decode_parity(u32 v, int lane) {
    u32 e = v & 0x00FF00FFu, o = (v >> 8) & 0x00FF00FFu;
    { auto r = __builtin_amdgcn_permlane16_swap(e, e, false, false); e = r[0] + r[1]; }
    { auto r = __builtin_amdgcn_permlane16_swap(o, o, false, false); o = r[0] + r[1]; }
    const int l0 = e & 0xFFFF, l2 = e >> 16, l1 = o & 0xFFFF, l3 = o >> 16;
    const bool par = l1 + l3 < l0 + l2;
    const bool hi = par ? l3 < l1 : l2 < l0;
    return (int)((__ballot(par) & 0xFFFFu) | ((__ballot(hi) & 0xFFFFu) << 16));
}
__device__ __forceinline__ int get_parity(rsrc_t rw, int lane, int& budget) {
    const int vo = (FL_PAR + (lane & 31)) * 128;
    u32 v;
    for (;;) {
        v = LD4(rw, vo, 0);
        MB();
        if (__all(v != 0xFFFFFFFFu) || --budget < 0) break;
        __builtin_amdgcn_s_sleep(8);
    }
    return decode_parity(v, lane);
}
__device__ __forceinline__ void role_probe(rsrc_t rw, int rep) {
    const int lane = threadIdx.x & 63, w = __builtin_amdgcn_readfirstlane(threadIdx.x >> 6);
    const int G = (w & 3) * 4 + rep;
    const int o = (G << 17) + N_PROBE + (w >> 2) * 512;
    ST4(0xFFFFFFFFu, rw, lane < 12 ? o + (lane & 3) * 8192 + (lane >> 2) * 128 : OOB);
    DRAIN();
    int best[4] = {254, 254, 254, 254};
#pragma unroll
    for (int k = 0; k < 12; ++k) {
        const unsigned long long t0 = __builtin_amdgcn_s_memtime();
        asm volatile("s_waitcnt lgkmcnt(0)" ::: "memory");
        u32 v = LD4(rw, o + (k & 3) * 8192 + (k >> 2) * 128, 0);
        asm volatile("s_waitcnt vmcnt(0)" : "+v"(v) :: "memory");
        const unsigned long long t1 = __builtin_amdgcn_s_memtime();
        best[k & 3] = min(best[k & 3], (int)(t1 - t0) >> 2);
    }
    if (lane == 0) ST4((u32)(best[0] | (best[1] << 8) | (best[2] << 16) | (best[3] << 24)), rw, (FL_PAR + (w >> 2) * 16 + G) * 128);
}
template <typename F>
__device__ __forceinline__ void qk_project_h(const bf16x16 (&Bq)[9], const bf16x16 (&Bk)[9], lptr ma, lptr xw, int idx, F hook) {
    f32x4 aq = {}, ak = {};
    bf16x8 A[9];
#pragma unroll
    for (int t = 0; t < 9; ++t) A[t] = lds8(ma + t * 1024);
    __builtin_amdgcn_sched_barrier(0);
#pragma unroll
    for (int t = 0; t < 9; ++t) {
        aq = SMF(A[t], Bq[t], aq, idx); ak = SMF(A[t], Bk[t], ak, idx);
        hook(t);
        __builtin_amdgcn_sched_barrier(0);
    }
    const float s0 = swap32_sum(aq[0], ak[0]), s1 = swap32_sum(aq[1], ak[1]), s2 = swap32_sum(aq[2], ak[2]), s3 = swap32_sum(aq[3], ak[3]);
    v2u o = {pack2(s0, s1), pack2(s2, s3)};
    *(LAS v2u*)xw = o;
}
__device__ __forceinline__ void qk_project(const bf16x16 (&Bq)[9], const bf16x16 (&Bk)[9], lptr ma, lptr xw, int idx) {
    qk_project_h(Bq, Bk, ma, xw, idx, [](int) {});
}

template <int GI>
__device__ __forceinline__ void gather_project(rsrc_t rw, int nb, lptr ml, int lane, lptr tkp, int tag, int& budget,
                                               const bf16x16 (&Bq)[9], const bf16x16 (&Bk)[9], lptr xw, int idx) {
    const int l0 = GI * 2048 + lane * 16;
    const int o2 = lane < 16 ? N_MEMB + GI * 256 + lane * 16 : OOB;
    lptr p2 = ml + (lane < 16 ? 8192 + GI * 256 + lane * 16 : 14336 + lane * 16);
    asm volatile("" : "+s"(nb));
    tk_wait(tkp, tag, budget);
    v4u v0 = LD16(rw, l0, nb), v1 = LD16(rw, l0 + 1024, nb), v2 = LD16(rw, o2, nb);
    MB();
    *(LAS v4u*)(ml + l0) = v0; *(LAS v4u*)(ml + l0 + 1024) = v1; *(LAS v4u*)p2 = v2;
    MB();
    if (__builtin_expect(!__all(max(max(bad4(v0), bad4(v1)), bad4(v2)) != 0xFFFFFFFFu), 0)) {
        do {
            v0 = LD16(rw, l0, nb); v1 = LD16(rw, l0 + 1024, nb); v2 = LD16(rw, o2, nb);
            MB();
        } while (!__all(max(max(bad4(v0), bad4(v1)), bad4(v2)) != 0xFFFFFFFFu) && --budget >= 0);
        *(LAS v4u*)(ml + l0) = v0; *(LAS v4u*)(ml + l0 + 1024) = v1; *(LAS v4u*)p2 = v2;
    }
    LBAR();
    bf16x8 A[9];
#pragma unroll
    for (int t = 0; t < 9; ++t) if (t != 2 * GI && t != 2 * GI + 1) A[t] = lds8(ml + lane * 16 + t * 1024);
    __builtin_amdgcn_sched_barrier(0);
    f32x4 aq = {}, ak = {};
    aq = SMF(as8(v0), Bq[2 * GI], aq, idx); ak = SMF(as8(v0), Bk[2 * GI], ak, idx);
    aq = SMF(as8(v1), Bq[2 * GI + 1], aq, idx); ak = SMF(as8(v1), Bk[2 * GI + 1], ak, idx);
    __builtin_amdgcn_sched_barrier(0);
#pragma unroll
    for (int t = 0; t < 9; ++t) if (t != 2 * GI && t != 2 * GI + 1) { aq = SMF(A[t], Bq[t], aq, idx); ak = SMF(A[t], Bk[t], ak, idx); __builtin_amdgcn_sched_barrier(0); }
    const float s0 = swap32_sum(aq[0], ak[0]), s1 = swap32_sum(aq[1], ak[1]), s2 = swap32_sum(aq[2], ak[2]), s3 = swap32_sum(aq[3], ak[3]);
    v2u o = {pack2(s0, s1), pack2(s2, s3)};
    *(LAS v2u*)xw = o;
}
__device__ __forceinline__ void role_qk(const Args& a, lptr lds, rsrc_t rw, int h, int rep) {
    const int tid = threadIdx.x, lane = tid & 63, w = __builtin_amdgcn_readfirstlane(tid >> 6);
    bf16x16 Bq[9], Bk[9];
    {
        v4u xr[6];
#pragma unroll
        for (int j = 0; j < 6; ++j) {
            const int i = min(tid + 512 * j, 2879), row = i / 72, c = i - row * 72;
            xr[j] = *(const v4u*)((row < 8 ? a.mem0 + row * DM : a.u + (row - 8) * DM) + c * 8);
        }
        v4u v[6][6];
#pragma unroll
        for (int c = 0; c < 6; ++c) rows_ld<192, 16>(v[c], (c >= 3 ? a.Wk : a.Wq) + (size_t)(c % 3) * 192 * 4096 + h * DH, 4096, tid);
        __builtin_amdgcn_sched_barrier(0);
#pragma unroll
        for (int j = 0; j < 6; ++j) {
            const int i = min(tid + 512 * j, 2879), row = i / 72, c = i - row * 72, G = row >> 3;
            lptr d = lds + (G < 2 ? 57344 + G * MEMB : 135168 + (G - 2) * MEMB) + (c >> 1) * 256 + (row & 7) * 16 + (c & 1) * 8;
            *(LAS v2u*)d = v2u{xr[j].x, xr[j].z}; *(LAS v2u*)(d + 128) = v2u{xr[j].y, xr[j].w};
        }
#pragma unroll
        for (int c = 0; c < 6; ++c) {
            lptr l = lds + (c & 1) * 77824;
            rows_st<192, 16>(v[c], l, 288, tid);
            LBAR();
#pragma unroll
            for (int t = 0; t < 3; ++t) {
                bf16x16 f = cat16(frag16(l + (64 * t) * 288 + w * 32, 288, lane), frag16(l + (64 * t + 32) * 288 + w * 32, 288, lane));
                if (c >= 3) Bk[(c % 3) * 3 + t] = f; else Bq[c * 3 + t] = f;
            }
            __builtin_amdgcn_sched_barrier(0);
        }
        LBAR();
    }
    lptr ml = lds, xl = lds + MEMB;
    const int idx = (lane & 8) ? 0xEEEE : 0x4444;
    const int i16 = lane & 15, g = lane >> 4;
    const bool hi8 = (lane & 8) != 0;
    int budget = SPIN_MAX;
    lptr pq = xl + (8 * g + (i16 >> 2)) * 32 + (i16 & 1) * 8;
    lptr kl = lds + 16384 + lane * 16;
    lptr ma = ml + lane * 16, xw = xl + (16 * w + i16) * 32 + g * 8;
    const int ox = N_PX + (h >> 4) * 32768 + (h & 15) * 512 + ((g & 1) * 16 + (g >> 1) * 8 + (i16 & 7)) * 16;
    const int om = hi8 ? OOB : N_PMM + h * 128 + (i16 & 7) * 16;
    const int ob = lane < NREP ? BCO(BC_QK + h) : OOB;
    lptr tkp = lds + MEMB + 4096, kfp = tkp + 64;
    if (tid == 0) { tk_post(tkp, 0, rt32() - 100000u, 0); tk_post(kfp, 0, 0u, 0); }
    for (int c = tid; c < 576; c += 512) *(LAS v4u*)(ml + c * 16) = *(LAS v4u*)(lds + 57344 + c * 16);
    __syncthreads();
#pragma unroll 1
    for (int G = 0; G < 4; ++G) {
        qk_project(Bq, Bk, lds + (G ? 135168 + (G - 1) * MEMB : 57344 + MEMB) + lane * 16, xw, idx);
        LBAR();
        if (w < 4) {
            const bf16x8 F = as8(trd(pq + w * 1024 + 16), trd(pq + w * 1024 + 16 + 128));
            if ((i16 >> 3) == (G & 1)) *(LAS bf16x8*)(kl + ((G >> 1) * 4 + w) * 1024) = F;
        }
        LBAR();
    }
    const int pm = get_parity(rw, lane, budget);
    LBAR();
    if (w == 0) {
        const float c2 = 0.08838834764831845f * 1.4426950408889634f;
        for (int s = 0; s < NSEG - 1; ++s) {
            tk_get(kfp, s, budget);
            bf16x8 kx[2][4];
#pragma unroll
            for (int tl = 0; tl < 2; ++tl)
#pragma unroll
                for (int ks = 0; ks < 4; ++ks) kx[tl][ks] = lds8(kl + (s & 1) * 8192 + (tl * 4 + ks) * 1024);
            int oxs = ox + NEARB(s, i16 >> 3, pm), oxt = ox + NEARB(s, 2 + (i16 >> 3), pm), oms = om + NEARB(s, g, pm), obs = ob + FARB(s, lane & 3, pm);
            const int nb = NEARB(s, rep, pm);
            asm volatile("s_waitcnt lgkmcnt(0)" : "+v"(oxs), "+v"(oxt), "+v"(oms), "+v"(obs) :: "memory");
#pragma unroll
            for (int tl = 0; tl < 2; ++tl)
#pragma unroll
                for (int ks = 0; ks < 4; ++ks) PIN4(kx[tl][ks]);
            if (s > 0) gather_project<0>(rw, nb, ml, lane, tkp, s, budget, Bq, Bk, xw, idx);
            else { LBAR(); qk_project(Bq, Bk, ma, xw, idx); }
            __builtin_amdgcn_s_setprio(3);
            LBAR();
            bf16x8 Fq[4], Fk[4];
#pragma unroll
            for (int ks = 0; ks < 4; ++ks) Fq[ks] = as8(trd(pq + ks * 1024), trd(pq + ks * 1024 + 128));
            __builtin_amdgcn_sched_barrier(0);
#pragma unroll
            for (int ks = 0; ks < 4; ++ks) Fk[ks] = as8(trd(pq + ks * 1024 + 16), trd(pq + ks * 1024 + 16 + 128));
            __builtin_amdgcn_sched_barrier(0);
            f32x4 S0 = {}, S1 = {}, Sm = {};
            S0 = MFMA16(kx[0][0], Fq[0], S0); S1 = MFMA16(kx[1][0], Fq[0], S1);
            S0 = MFMA16(kx[0][1], Fq[1], S0); S1 = MFMA16(kx[1][1], Fq[1], S1);
            __builtin_amdgcn_sched_barrier(0);
            Sm = MFMA16(Fk[0], Fq[0], Sm); S0 = MFMA16(kx[0][2], Fq[2], S0);
            Sm = MFMA16(Fk[1], Fq[1], Sm); S1 = MFMA16(kx[1][2], Fq[2], S1);
            Sm = MFMA16(Fk[2], Fq[2], Sm); S0 = MFMA16(kx[0][3], Fq[3], S0);
            Sm = MFMA16(Fk[3], Fq[3], Sm); S1 = MFMA16(kx[1][3], Fq[3], S1);
            __builtin_amdgcn_sched_barrier(0);
            float y0 = hi8 ? Sm[2] : Sm[0], y1 = hi8 ? Sm[3] : Sm[1];
            float x0 = hi8 ? S1[0] : S0[0], x1 = hi8 ? S1[1] : S0[1], x2 = hi8 ? S1[2] : S0[2], x3 = hi8 ? S1[3] : S0[3];
            const float ml_ = vmax3(vmax3(y0, y1, x0), x1, vmax(x2, x3));
            const float nm = -ml_ * c2;
            float m = vmax(ml_, ror8f(ml_));
            x0 = __builtin_amdgcn_exp2f(fmaf(x0, c2, nm)); x1 = __builtin_amdgcn_exp2f(fmaf(x1, c2, nm));
            { auto r = __builtin_amdgcn_permlane16_swap(__float_as_uint(m), __float_as_uint(m), false, false); m = vmax(__uint_as_float(r[0]), __uint_as_float(r[1])); }
            x2 = __builtin_amdgcn_exp2f(fmaf(x2, c2, nm)); x3 = __builtin_amdgcn_exp2f(fmaf(x3, c2, nm));
            { auto r = __builtin_amdgcn_permlane32_swap(__float_as_uint(m), __float_as_uint(m), false, false); m = vmax(__uint_as_float(r[0]), __uint_as_float(r[1])); }
            y0 = __builtin_amdgcn_exp2f(fmaf(y0, c2, nm)); y1 = __builtin_amdgcn_exp2f(fmaf(y1, c2, nm));
            const float f = __builtin_amdgcn_exp2f((ml_ - m) * c2);
            float sum = f * fmaf(0.5f, y0 + y1, (x0 + x1) + (x2 + x3));
            sum += ror8f(sum);
            sum = xsum(sum);
            const float inv = f * __builtin_amdgcn_rcpf(sum);
            const u32 pa = pack2(x0 * inv, x1 * inv) & 0x7fff7fffu, pb = pack2(x2 * inv, x3 * inv) & 0x7fff7fffu, pc = pack2(y0 * inv, y1 * inv) & 0x7fff7fffu;
            const u32 qc = ror8(pc);
            v4u px = {(u32)__builtin_amdgcn_update_dpp((int)pa, (int)pa, 0x128, 0xf, 0xc, false), (u32)__builtin_amdgcn_update_dpp((int)pb, (int)pb, 0x128, 0xf, 0xc, false),
                      (u32)__builtin_amdgcn_update_dpp((int)pa, (int)pa, 0x128, 0xf, 0x3, false), (u32)__builtin_amdgcn_update_dpp((int)pb, (int)pb, 0x128, 0xf, 0x3, false)};
            ST16(px, rw, oxs);
            ST16(px, rw, oxt);
            auto ra = __builtin_amdgcn_permlane16_swap(pc, pc, false, false);
            auto rc = __builtin_amdgcn_permlane16_swap(qc, qc, false, false);
            v4u pmv = {ra[0], rc[0], ra[1], rc[1]};
            ST16(pmv, rw, oms);
            __builtin_amdgcn_sched_barrier(0);
            ST4(nos(rt32()), rw, obs);
            __builtin_amdgcn_s_setprio(0);
        }
        {
            gather_project<0>(rw, NEARB(NSEG - 1, rep, pm), ml, lane, tkp, NSEG - 1, budget, Bq, Bk, xw, idx);
            LBAR();
        }
    } else if (w == 5) {
        int kdone = -1;
        const int kxo = OFF_KX + h * (NSEG * 8192) + g * 512 + i16 * 16;
        for (int s = 0; s < NSEG; ++s) {
            const bool need = s + 1 < NSEG - 1;
            if (need && kdone < s + 1) kdone = wait_flags(rw, FL_K + (h >> 1), 1, 1, -1, lane, budget);
            const bool go = need && kdone >= s + 1;
            int ko = (go ? kxo : OOB) + (s + 1) * 8192;
            int a0 = ox + NEARB(s + 2, i16 >> 3, pm), a1 = ox + NEARB(s + 2, 2 + (i16 >> 3), pm), a2 = om + NEARB(s + 2, g, pm), a3 = ob + FARB(s + 4, lane & 3, pm);
            v4u ff = {0xFFFFFFFFu, 0xFFFFFFFFu, 0xFFFFFFFFu, 0xFFFFFFFFu};
            asm volatile("" : "+v"(ko), "+v"(a0), "+v"(a1), "+v"(a2), "+v"(a3), "+v"(ff));
            LBAR();
            v4u kx[8];
            qk_project_h(Bq, Bk, ma, xw, idx, [&](int t) {
                if (t < 4) { kx[t] = LD16(rw, ko + (t & 1) * 2048, (t >> 1) * 4096); kx[4 + t] = LD16(rw, ko + 256 + (t & 1) * 2048, (t >> 1) * 4096); }
                if (t == 4) { ST16(ff, rw, a0); ST16(ff, rw, a1); }
                if (t == 5) { ST16(ff, rw, a2); ST4(0xFFFFFFFFu, rw, a3); }
            });
            LBAR();
            if (need) {
                if (!go) {
                    kdone = wait_flags(rw, FL_K + (h >> 1), 1, 1, s + 1, lane, budget, true);
#pragma unroll
                    for (int k = 0; k < 8; ++k) kx[k] = LD16(rw, kxo + (k >> 2) * 256 + (k & 3) * 2048, (s + 1) * 8192);
                }
#pragma unroll
                for (int k = 0; k < 8; ++k) *(LAS v4u*)(kl + ((s + 1) & 1) * 8192 + k * 1024) = kx[k];
                asm volatile("s_waitcnt lgkmcnt(0)" ::: "memory");
                tk_post(kfp, s + 1, 0u, lane);
            }
            DRAIN();
        }
    } else if (w == 1 || w == 2 || w == 7) {
        LBAR();
        qk_project(Bq, Bk, ma, xw, idx);
        LBAR();
        for (int s = 1; s < NSEG; ++s) {
            __builtin_amdgcn_s_sleep(14);
            const int nb = NEARB(s, rep, pm);
            if (w == 1) gather_project<1>(rw, nb, ml, lane, tkp, s, budget, Bq, Bk, xw, idx);
            else if (w == 2) gather_project<2>(rw, nb, ml, lane, tkp, s, budget, Bq, Bk, xw, idx);
            else gather_project<3>(rw, nb, ml, lane, tkp, s, budget, Bq, Bk, xw, idx);
            LBAR();
        }
    } else {
        TK tk; tk_init(tk);
        const int kmo = i16 < 8 ? OFF_KM + h * 2048 + g * 128 + i16 * 16 : OOB;
        for (int s = 0; s < NSEG; ++s) {
            if (s > 0 && w == 3) tk_wait(tkp, s, budget);
            BC2 bc = {0u, 0u};
            if (w == 3) bc = beacon_issue(rw, FARB(s, rep, pm), lane);
            LBAR();
            qk_project(Bq, Bk, ma, xw, idx);
            LBAR();
            if (w == 6) {
                bf16x8 F[4];
#pragma unroll
                for (int ks = 0; ks < 4; ++ks) F[ks] = as8(trd(pq + ks * 1024 + 16), trd(pq + ks * 1024 + 16 + 128));
#pragma unroll
                for (int ks = 0; ks < 4; ++ks) ST16(asu(F[ks]), rw, kmo + s * (NH * 2048) + ks * 512);
                if (s == 0) {
#pragma unroll
                    for (int k = 0; k < 8; ++k) ST16(*(LAS v4u*)(kl + k * 1024), rw, OFF_KX + h * (NSEG * 8192) + g * 512 + i16 * 16 + (k >> 2) * 256 + (k & 3) * 2048);
                }
                DRAIN();
                if (lane == 0) ST4((u32)s, rw, (FL_M + h) * 128);
            } else if (w == 3) {
                if (s < NSEG - 1) {
                    if (s > 0) { tk_T0(tk, beacon_finish(bc, rw, FARB(s, rep, pm), lane, budget), a); if (tk.okA) tk_post(tkp, s + 1, tk_specA(tk, a), lane); }
                    wait_until(tk.XR + 8);
                    tk_Tp(tk, beacon_max<NH>(rw, FARB(s, rep, pm), BC_QK, lane, budget), a);
                    tk_post(tkp, s + 1, tk.XA, lane);
                }
            } else __builtin_amdgcn_s_sleep(14);
        }
    }
    if (budget < 0 && lane == 0) ST4(1u, rw, FL_ERR * 128);
}

__device__ __forceinline__ void role_wvo(const Args& a, lptr lds, rsrc_t rw, int idx) {
    const int tid = threadIdx.x, lane = tid & 63, w = __builtin_amdgcn_readfirstlane(tid >> 6);
    const int h = idx / 3, part = idx - 3 * h;
    bf16x8 Bo[5][4];
    v4u v[6], o[2][9];
#pragma unroll
    for (int c = 0; c < 2; ++c) rows_ld<64, 72>(o[c], a.Wo + ((size_t)h * DH + c * 64) * DM, DM, tid);
    rows_ld<192, 16>(v, a.Wv + (size_t)part * 192 * 4096 + h * DH, 4096, tid);
    __builtin_amdgcn_sched_barrier(0);
#pragma unroll
    for (int c = 0; c < 2; ++c) {
        lptr l = lds + c * 77824;
        rows_st<64, 72>(o[c], l, 1184, tid);
        LBAR();
#pragma unroll
        for (int j = 0; j < 5; ++j) {
            const int r = (w + 8 * j < NR) ? w + 8 * j : w;
#pragma unroll
            for (int ks = 0; ks < 2; ++ks) Bo[j][c * 2 + ks] = frag16(l + ks * 32 * 1184 + r * 32, 1184, lane);
        }
        __builtin_amdgcn_sched_barrier(0);
    }
    rows_st<192, 16>(v, lds, VS, tid);
    LBAR();
    const int rho = lane & 15, bg = lane >> 4;
    const int wo0 = OFF_WVO + (2 * w + ((rho >> 1) & 1)) * 294912 + ((h >> 1) * 18 + part * 6) * 1024 + ((h & 1) * 32 + bg * 8 + 2 * (rho >> 2) + (rho & 1)) * 16;
#pragma unroll 1
    for (int kb = 0; kb < 6; ++kb) {
        bf16x8 Af[2][4];
#pragma unroll
        for (int mt = 0; mt < 2; ++mt)
#pragma unroll
            for (int ks = 0; ks < 4; ++ks) Af[mt][ks] = lds8(lds + (32 * kb + 8 * (rho >> 2) + 4 * mt + (rho & 3)) * VS + ks * 64 + bg * 16);
#pragma unroll
        for (int j = 0; j < 5; ++j) {
            if (j < 4 || w < 4) {
                f32x4 d0 = {}, d1 = {};
#pragma unroll
                for (int ks = 0; ks < 4; ++ks) { d0 = MFMA16(Af[0][ks], Bo[j][ks], d0); d1 = MFMA16(Af[1][ks], Bo[j][ks], d1); }
                v4u q = {pack2(d0[0], d0[1]), pack2(d0[2], d0[3]), pack2(d1[0], d1[1]), pack2(d1[2], d1[3])};
                ST16(q, rw, wo0 + j * (16 * 294912) + kb * 1024);
            }
        }
        if (kb == 2 || kb == 4) {
            asm volatile("s_waitcnt vmcnt(4)" ::: "memory");
            LBAR();
            if (tid == 0) ST4((u32)(kb >> 1), rw, (FL_W + idx) * 128);
        }
    }
    DRAIN();
    LBAR();
    if (tid == 0) ST4(3u, rw, (FL_W + idx) * 128);
}

__device__ __forceinline__ void role_rz(const Args& a, lptr lds, rsrc_t rw, int r, int rep) {
    const int tid = threadIdx.x, lane = tid & 63, w = __builtin_amdgcn_readfirstlane(tid >> 6);
    const int idx = (lane & 8) ? 0xEEEE : 0x4444;
    const int i16 = lane & 15, g = lane >> 4;
    int budget = SPIN_MAX;
    bf16x16 Ba[9], Bb[9];
    v4u t0[4], m0[2];
    int pm;
    {
        const int wo = OFF_WVO + r * 294912 + 2 * w * 18432 + ((i16 >> 3) * 32 + g * 8 + (i16 & 7)) * 16;
        const int vq = OFF_VOX + r * 16384 + w * 2048 + lane * 16;
#pragma unroll
        for (int j = 0; j < 2; ++j) { const int i = min(tid + 512 * j, 575), row = i / 72, c = i - row * 72; m0[j] = *(const v4u*)(a.mem0 + row * DM + c * 8); }
        const int vf = (int)LD4(rw, (FL_V + lane) * 128, 0);
        const u32 pw = LD4(rw, (FL_PAR + (lane & 31)) * 128, 0);
        int have = 0;
        bool vgot = false;
#pragma unroll
        for (int p = 0; p < 3; ++p) {
            if (have < p + 1) have = wait_flags(rw, FL_W + 12 * w, 1, 12, p + 1, lane, budget, true);
            if (p == 0 || p == 2) {
                const bool now = p == 0 ? (bool)__all(vf >= 0) : !vgot;
                if (now) {
                    if (p == 2) wait_flags(rw, FL_V, 1, 64, 0, lane, budget, true);
#pragma unroll
                    for (int k = 0; k < 4; ++k) t0[k] = LD16(rw, vq + (k & 1) * 1024, (k >> 1) * (NR * NH * 1024));
                    vgot = true;
                }
            }
            if (p == 0) pm = __all(pw != 0xFFFFFFFFu) ? decode_parity(pw, lane) : get_parity(rw, lane, budget);
#pragma unroll
            for (int q = 0; q < 3; ++q) {
                const int t = 3 * q + p;
                Ba[t] = cat16(as8(LD16(rw, wo, t * 2048)), as8(LD16(rw, wo + 1024, t * 2048)));
                Bb[t] = cat16(as8(LD16(rw, wo, 18432 + t * 2048)), as8(LD16(rw, wo + 1024, 18432 + t * 2048)));
            }
            MB();
        }
#pragma unroll
        for (int j = 0; j < 2; ++j) {
            const int i = min(tid + 512 * j, 575), row = i / 72, c = i - row * 72;
            lptr d = lds + (c >> 1) * 256 + row * 16 + (c & 1) * 8;
            *(LAS v2u*)d = v2u{m0[j].x, m0[j].z}; *(LAS v2u*)(d + 128) = v2u{m0[j].y, m0[j].w};
        }
    }
    lptr ml = lds, zl = lds + MEMB;
    lptr tka = lds + 13312, tkr = tka + 16, cntp = tka + 64;
    volatile LAS int* prod = (volatile LAS int*)(lds + 13312 + 512);
    lptr ring = lds + 16384;
    lptr ma = ml + lane * 16;
    lptr zw = zl + ((2 * w + (lane >> 5)) >> 2) * 1024 + ((((2 * w + (lane >> 5)) & 3) * 16 + (g & 1) * 8 + (i16 & 7)) * 16) + (i16 >> 3) * 8;
    const bool hi8r = (lane & 8) != 0;
    const int mso = i16 < 8 ? (r < 64 ? N_MEMA + r * 128 : N_MEMB + (r - 64) * 128) + i16 * 16 : OOB;
    const int ob = lane < NREP ? BCO(r) : OOB;
    if (tid == 0) { tk_post(tka, 0, rt32() - 100000u, 0); tk_post(tkr, -1, 0u, 0); *(volatile LAS int*)cntp = 0; }
#pragma unroll
    for (int k = 0; k < 4; ++k) *(LAS v4u*)(ring + (k >> 1) * 16384 + w * 2048 + (k & 1) * 1024 + lane * 16) = t0[k];
    if (tid < 4) prod[tid] = 2;
    LBAR();
    if (w < 4) {
        const int po = N_PX + (w >> 1) * 32768 + (w & 1) * 4096 + lane * 16;
        const int mo = N_PMM + w * 1024 + 2 * g * 128 + (i16 & 7) * 16 + (i16 >> 3) * 8;
        lptr cna = lane == 0 ? cntp : cntp + 4 + lane * 4;
        const u32 one = lane == 0 ? 1u : 0u;
        const bool dv = (g >> 1) == (i16 >> 3);
        const int rwo = dv ? (4 * (g & 1) + 2 * (g >> 1)) * 128 + ((i16 & 7) >> 1) * 32 + w * 8 + (i16 & 1) * 4 : 2048 + lane * 4;
        const int msq = lane < 32 ? (r < 64 ? N_MEMA + r * 128 : N_MEMB + (r - 64) * 128) + (lane >> 2) * 16 : OOB;
        lptr zrd = zl + w * 1024 + lane * 16;
        for (int s = 0; s < NSEG; ++s) {
            lptr Rw = lds + 81920 + (s & 1) * 1024 + rwo, Rr = lds + 81920 + (s & 1) * 1024 + (lane & 31) * 32;
            int mss = msq + NEARB(s + 1, lane & 3, pm), obs = ob + FARB(s + 1, lane & 3, pm);
            int sb = NEARB(s, rep, pm);
            asm volatile("" : "+s"(sb));
            lptr rg = ring + (s & 3) * 16384 + w * 4096 + lane * 16;
            bf16x8 b[5];
            const bool early = __builtin_amdgcn_readfirstlane(prod[w >> 1]) > s;
            MB();
#pragma unroll
            for (int k = 0; k < 4; ++k) b[k] = lds8(rg + k * 1024);
            const u32 X = s > 0 ? tk_get(tkr, min(s, NSEG - 2), budget) : 0u;
            asm volatile("s_waitcnt lgkmcnt(0)" : "+v"(mss), "+v"(obs), "+v"(Rw), "+v"(Rr) :: "memory");
#pragma unroll
            for (int k = 0; k < 4; ++k) PIN4(b[k]);
            LBAR();
            qk_project(Ba, Bb, ma, zw, idx);
            LBAR();
            if (s == NSEG - 1) break;
            __builtin_amdgcn_s_setprio(3);
            b[4] = lds8(zrd);
            if (!early) {
                while (__builtin_amdgcn_readfirstlane(prod[w >> 1]) <= s && --budget >= 0) __builtin_amdgcn_s_sleep(1);
                MB();
#pragma unroll
                for (int k = 0; k < 4; ++k) b[k] = lds8(rg + k * 1024);
            }
            if (s > 0 && (int)(X - rt32()) < 1000) spin_to(X);
            f32x4 d;
            u32 bad;
            auto shot = [&]() {
                v4u p[4]; v2u m[2];
                m[0] = LD8(rw, mo, sb); m[1] = LD8(rw, mo + 128, sb);
                __builtin_amdgcn_sched_barrier(0);
#pragma unroll
                for (int k = 0; k < 4; ++k) { p[k] = LD16(rw, po + k * 1024, sb); __builtin_amdgcn_sched_barrier(0); }
                MB();
                d = f32x4{};
                d = MFMA16(as8(m[0], m[1]), b[4], d);
                bad = (m[0].x | m[0].y) | (m[1].x | m[1].y);
                __builtin_amdgcn_sched_barrier(0);
#pragma unroll
                for (int k = 0; k < 4; ++k) {
                    d = MFMA16(as8(p[k]), b[k], d);
                    bad |= (p[k].x | p[k].y) | (p[k].z | p[k].w);
                    __builtin_amdgcn_sched_barrier(0);
                }
            };
            shot();
            if (__builtin_expect(!__all((int)bad >= 0), 0)) {
                do shot(); while (!__all((int)bad >= 0) && --budget >= 0);
            }
            float e0, e1;
            {
                auto r0 = __builtin_amdgcn_permlane32_swap(__float_as_uint(d[0]), __float_as_uint(d[2]), false, false);
                auto r1 = __builtin_amdgcn_permlane32_swap(__float_as_uint(d[1]), __float_as_uint(d[3]), false, false);
                const float t0 = __uint_as_float(hi8r ? r0[1] : r0[0]), t1 = __uint_as_float(hi8r ? r1[1] : r1[0]);
                e0 = t0 + ror8f(t0); e1 = t1 + ror8f(t1);
            }
            f32x4 x0, x1; u32 old;
            asm volatile("ds_write2_b32 %4, %5, %6 offset1:32\n\tds_add_rtn_u32 %0, %7, %8\n\tds_read_b128 %1, %3\n\tds_read_b128 %2, %3 offset:16\n\ts_waitcnt lgkmcnt(0)"
                         : "=&v"(old), "=&v"(x0), "=&v"(x1) : "v"(Rr), "v"(Rw), "v"(e0), "v"(e1), "v"(cna), "v"(one) : "memory");
            {
                const u32 c = nos(pack2((x0[0] + x0[2]) + (x1[0] + x1[2]), (x0[1] + x0[3]) + (x1[1] + x1[3])));
                v4u o = {(u32)__builtin_amdgcn_update_dpp(0, (int)c, 0x00, 0xf, 0xf, true), (u32)__builtin_amdgcn_update_dpp(0, (int)c, 0x55, 0xf, 0xf, true),
                         (u32)__builtin_amdgcn_update_dpp(0, (int)c, 0xAA, 0xf, 0xf, true), (u32)__builtin_amdgcn_update_dpp(0, (int)c, 0xFF, 0xf, 0xf, true)};
                const bool last = (__builtin_amdgcn_readfirstlane(old) & 3) == 3;
                ST16(o, rw, last ? mss : OOB);
                __builtin_amdgcn_sched_barrier(0);
                if (last) ST4(nos(rt32()), rw, obs);
            }
            __builtin_amdgcn_s_setprio(0);
        }
    } else if (w == 5 || w == 6) {
        int vdone0 = -1, vdone1 = -1, staged = 2;
        const int hw = w - 5;
        const int vo = OFF_VOX + r * 16384 + hw * 8192 + lane * 16;
        const int zho = OFF_ZH + r * 4096 + hw * 2048 + lane * 16;
        lptr zhr = zl + hw * 2048 + (((lane >> 4) & 3) * 16 + (lane & 7)) * 16 + ((lane >> 3) & 1) * 8;
        const int aro = w == 5 ? mso : OOB, arb = w == 5 ? ob : OOB;
        v4u zv[2] = {};
        for (int s = 0; s < NSEG; ++s) {
            bool go = false;
            if (staged <= min(s + 3, NSEG - 2)) {
                const int ow = VOWN(staged);
                int& vd = ow ? vdone1 : vdone0;
                if (vd < staged) vd = wait_flags(rw, FL_V + ow, 2, NH, -1, lane, budget);
                go = vd >= staged;
            }
            const int vos = go ? vo : OOB;
            int va = vos + staged * (NR * NH * 1024), zs = s > 0 ? zho + (s - 1) * (NR * NH * 256) : OOB;
            int a0 = aro + NEARB(s + 3, g, pm), a1 = arb + FARB(s + 4, lane & 3, pm);
            v4u ff = {0xFFFFFFFFu, 0xFFFFFFFFu, 0xFFFFFFFFu, 0xFFFFFFFFu};
            asm volatile("" : "+v"(va), "+v"(zs), "+v"(a0), "+v"(a1), "+v"(ff));
            if (s > 0) gather_mem4(rw, NEARB(s, rep, pm), ml, lane, w - 4, tka, s, budget);
            LBAR();
            v4u v[8];
            qk_project_h(Ba, Bb, ma, zw, idx, [&](int t) {
                if (t < 4) { v[2 * t] = LD16(rw, va + (t & 1) * 2048, (t >> 1) * 4096); v[2 * t + 1] = LD16(rw, va + 1024 + (t & 1) * 2048, (t >> 1) * 4096); }
                if (t == 4) { ST16(zv[0], rw, zs); ST16(zv[1], rw, zs + 1024); }
                if (t == 5) { ST16(ff, rw, a0); ST4(0xFFFFFFFFu, rw, a1); }
            });
            LBAR();
#pragma unroll
            for (int k = 0; k < 2; ++k) {
                const v2u lo = *(LAS v2u*)(zhr + k * 1024), hi = *(LAS v2u*)(zhr + k * 1024 + 128);
                zv[k] = v4u{lo.x, lo.y, hi.x, hi.y};
            }
            for (;;) {
                if (go) {
#pragma unroll
                    for (int k = 0; k < 8; ++k) *(LAS v4u*)(ring + (staged & 3) * 16384 + hw * 8192 + k * 1024 + lane * 16) = v[k];
                    asm volatile("s_waitcnt lgkmcnt(0)" ::: "memory");
                    ++staged;
                    prod[hw] = staged;
                }
                if (staged > s + 1 || staged > NSEG - 2) break;
                const int ow = VOWN(staged);
                int& vd = ow ? vdone1 : vdone0;
                if (vd < staged) vd = wait_flags(rw, FL_V + ow, 2, NH, staged <= s ? staged : -1, lane, budget, true);
                if (vd < staged) break;
#pragma unroll
                for (int k = 0; k < 8; ++k) v[k] = LD16(rw, vo + (k & 3) * 1024, staged * (NR * NH * 1024) + (k >> 2) * 4096);
                go = true;
            }
            DRAIN();
        }
        {
            int l2 = (int)__builtin_amdgcn_mbcnt_hi(~0u, __builtin_amdgcn_mbcnt_lo(~0u, 0u));
            asm volatile("" : "+v"(l2));
            const int zf = OFF_ZH + r * 4096 + hw * 2048 + (NSEG - 1) * (NR * NH * 256) + l2 * 16;
            ST16(zv[0], rw, zf); ST16(zv[1], rw, zf + 1024);
        }
    } else {
        TK tk; tk_init(tk);
        for (int s = 0; s < NSEG; ++s) {
            if (s > 0) {
                gather_mem4(rw, NEARB(s, rep, pm), ml, lane, w - 4, tka, s, budget);
            }
            BC2 bc = {0u, 0u};
            if (w == 7) bc = beacon_issue(rw, FARB(s, rep, pm), lane);
            LBAR();
            qk_project(Ba, Bb, ma, zw, idx);
            LBAR();
            if (w == 7) {
                if (s < NSEG - 1) {
                    if (s > 0) { tk_T0(tk, beacon_finish(bc, rw, FARB(s, rep, pm), lane, budget), a); if (tk.okA) tk_post(tka, s + 1, tk_specA(tk, a), lane); }
                    tk_post(tkr, s, tk.XR, lane);
                    wait_until(tk.XR + 8);
                    tk_Tp(tk, beacon_max<NH>(rw, FARB(s, rep, pm), BC_QK, lane, budget), a);
                    tk_post(tka, s + 1, tk.XA, lane);
                    tk_post(tkr, s + 1, tk_specR(tk, a), lane);
                }
            } else {
                if (w == 4 && s >= 2 && lane == 0) ST4((u32)(s - 2), rw, (FL_Z + r) * 128);
                __builtin_amdgcn_s_sleep(14);
            }
        }
    }
    DRAIN();
    LBAR();
    if (tid == 0) ST4((u32)(NSEG - 1), rw, (FL_Z + r) * 128);
    if (budget < 0 && lane == 0) ST4(3u, rw, FL_ERR * 128);
}

__device__ __forceinline__ void cexp_l(float lr, float li, float l, float& er, float& ei) {
    const float mag = __builtin_amdgcn_exp2f(lr * l);
    const float ph = li * l;
    const float pe = fmaf(li, l, -ph);
    const float fr = (ph - floorf(ph)) + pe;
    er = mag * __builtin_amdgcn_cosf(fr);
    ei = mag * __builtin_amdgcn_sinf(fr);
}
constexpr int CV_W = 8 * 260 * 16, CV_U = 96 * 80, CV_CH = CV_W + CV_U;
__device__ __forceinline__ void s4_unit(const Args& a, lptr lds, rsrc_t rw, int unit) {
    const int tid = threadIdx.x, lane = tid & 63, w = __builtin_amdgcn_readfirstlane(tid >> 6);
    const int d0 = unit * 4;
    v2u uv[4];
    {
        const int c = tid >> 7, nn = tid & 127, n = (d0 + c) * 128 + nn;
        const float dt = rbf(expf(bf2f(a.logdt[d0 + c])));
        const float lr = dt * bf2f(a.Are[n]) * 1.4426950408889634f, li = dt * bf2f(a.Aim[n]) * 0.15915494309189535f;
        const float br = dt * bf2f(a.Bre[n]), bi = dt * bf2f(a.Bim[n]);
        const float cr = bf2f(a.Cre[n]), ci = bf2f(a.Cim[n]);
        MB();
#pragma unroll
        for (int k = 0; k < 4; ++k) uv[k] = *(const v2u*)(a.u + (size_t)(2 * (tid + 512 * (k >> 1)) + (k & 1)) * DM + d0);
        MB();
        LAS f32x2* T = (LAS f32x2*)(lds + c * CV_CH) + nn;
        float er, ei, pr, pi;
        cexp_l(lr, li, 1.0f, er, ei); pr = er; pi = ei;
#pragma unroll
        for (int o = 0; o < 7; ++o) { T[o * 130] = f32x2{pr, pi}; const float t = pr * er - pi * ei; pi = pr * ei + pi * er; pr = t; }
#pragma unroll
        for (int q4 = 0; q4 < 4; ++q4) {
            if (q4 == 0) cexp_l(lr, li, 128.0f, er, ei);
            cexp_l(lr, li, (float)(128 + 512 * q4), pr, pi);
#pragma unroll
            for (int o = 0; o < 4; ++o) { T[(7 + 4 * q4 + o) * 130] = f32x2{pr, pi}; const float t = pr * er - pi * ei; pi = pr * ei + pi * er; pr = t; }
        }
        const float cbr = cr * br - ci * bi, cbi = cr * bi + ci * br;
        cexp_l(lr, li, 8.0f, er, ei);
#pragma unroll
        for (int q4 = 0; q4 < 4; ++q4) {
            float sr, si;
            cexp_l(lr, li, (float)(32 * q4), sr, si);
            pr = cbr * sr - cbi * si; pi = cbr * si + cbi * sr;
#pragma unroll
            for (int o = 0; o < 4; ++o) { T[(23 + 4 * q4 + o) * 130] = f32x2{pr, pi}; const float t = pr * er - pi * ei; pi = pr * ei + pi * er; pr = t; }
        }
    }
    LBAR();
    f32x4 kr[3];
    {
        const int x = lane & 15, g = lane >> 4, c = w & 3, nh = w >> 2;
        const int ah0 = x >> 3, o0 = x & 7;
        const int ah1 = x < 8 ? 2 + (x >> 2) : 4 + ((x - 8) >> 1), o1 = x < 8 ? 2 * (x & 3) : 4 * (x & 1);
        const int ah2 = x < 8 ? 8 + x : 16;
        LAS f32x2* T = (LAS f32x2*)(lds + c * CV_CH) + nh * 64 + g;
        LAS f32x2* Ta0 = T + 7 * 130; LAS f32x2* To0 = T + (o0 ? o0 - 1 : 0) * 130;
        LAS f32x2* Ta1 = T + (6 + ah1) * 130; LAS f32x2* To1 = T + (o1 ? o1 - 1 : 0) * 130;
        LAS f32x2* Ta2 = T + (6 + ah2) * 130; LAS f32x2* Tv = T + (23 + x) * 130;
        const f32x2 one = {1.0f, 0.0f};
        f32x4 ar[3] = {}, ai[3] = {};
#pragma unroll
        for (int jj = 0; jj < 16; ++jj) {
            const f32x2 v = Tv[4 * jj], u2 = Ta2[4 * jj], a1 = Ta1[4 * jj];
            f32x2 a0v = Ta0[4 * jj], e0 = To0[4 * jj], e1 = To1[4 * jj];
            a0v = ah0 ? a0v : one; e0 = o0 ? e0 : one; e1 = o1 ? e1 : one;
            const float u0r = a0v[0] * e0[0] - a0v[1] * e0[1], u0i = a0v[0] * e0[1] + a0v[1] * e0[0];
            const float u1r = a1[0] * e1[0] - a1[1] * e1[1], u1i = a1[0] * e1[1] + a1[1] * e1[0];
            ar[2] = __builtin_amdgcn_mfma_f32_16x16x4f32(u2[0], v[0], ar[2], 0, 0, 0);
            ai[2] = __builtin_amdgcn_mfma_f32_16x16x4f32(u2[1], v[1], ai[2], 0, 0, 0);
            ar[0] = __builtin_amdgcn_mfma_f32_16x16x4f32(u0r, v[0], ar[0], 0, 0, 0);
            ai[0] = __builtin_amdgcn_mfma_f32_16x16x4f32(u0i, v[1], ai[0], 0, 0, 0);
            ar[1] = __builtin_amdgcn_mfma_f32_16x16x4f32(u1r, v[0], ar[1], 0, 0, 0);
            ai[1] = __builtin_amdgcn_mfma_f32_16x16x4f32(u1i, v[1], ai[1], 0, 0, 0);
        }
#pragma unroll
        for (int t = 0; t < 3; ++t) kr[t] = ar[t] - ai[t];
    }
    LBAR();
    {
        LAS float* T = (LAS float*)(lds + (w & 3) * CV_CH + 4096 + (w >> 2) * 3072);
#pragma unroll
        for (int t = 0; t < 3; ++t)
#pragma unroll
            for (int r = 0; r < 4; ++r) T[(16 * t + 4 * (lane >> 4) + r) * 16 + (lane & 15)] = kr[t][r];
    }
    LBAR();
#pragma unroll
    for (int k = 0; k < 8; ++k) {
        const int i = tid + 512 * k, c = i >> 10, l2 = i & 1023;
        LAS float* T = (LAS float*)(lds + c * CV_CH + 4096);
        float v[2];
#pragma unroll
        for (int e = 0; e < 2; ++e) {
            const int lb = (int)rbf((float)(2 * l2 + e));
            const int hi = lb >> 7, o = lb & 7;
            const int row = lb < 256 ? 8 * hi + o : lb < 512 ? 16 + 4 * (hi - 2) + (o >> 1) : lb < 1024 ? 24 + 2 * (hi - 4) + (o >> 2) : 32 + (hi - 8);
            const int ix = row * 16 + ((lb >> 3) & 15);
            v[e] = T[ix] + T[768 + ix];
        }
        *(LAS u32*)(lds + c * CV_CH + CV_W + 2704 + l2 * 4) = pack2(v[0], v[1]);
    }
    LBAR();
    for (int i = tid; i < 4 * 259; i += 512) {
        const int c = i / 259, zz = i - c * 259, z = zz - 3;
        lptr ch = lds + c * CV_CH;
        v4u lo = {0, 0, 0, 0}, hi = {0, 0, 0, 0};
        if (z >= 1) lo = *(LAS v4u*)(ch + CV_W + 2704 + (z - 1) * 16);
        if (z >= 0) hi = *(LAS v4u*)(ch + CV_W + 2704 + z * 16);
        const u32 e[8] = {lo.x, lo.y, lo.z, lo.w, hi.x, hi.y, hi.z, hi.w};
#pragma unroll
        for (int cc = 0; cc < 8; ++cc) {
            v4u o;
#pragma unroll
            for (int k = 0; k < 4; ++k) {
                const int t = 8 + cc - 2 * k;
                if (t & 1) o[k] = (e[t >> 1] >> 16) | (e[t >> 1] << 16);
                else o[k] = (e[t >> 1] & 0xffffu) | (e[(t >> 1) - 1] & 0xffff0000u);
            }
            *(LAS v4u*)(ch + (cc * 260 + zz) * 16) = o;
        }
    }
    LBAR();
    for (int i = tid; i < 4 * 160; i += 512) *(LAS v4u*)(lds + (i / 160) * CV_CH + CV_W + (i % 160) * 16) = v4u{0, 0, 0, 0};
#pragma unroll
    for (int k2 = 0; k2 < 2; ++k2) {
        const int l = 2 * (tid + 512 * k2);
        const int o = CV_W + (32 + (l >> 5)) * 80 + (l & 31) * 2;
        const v2u p = uv[2 * k2], q = uv[2 * k2 + 1];
        *(LAS u32*)(lds + 0 * CV_CH + o) = (p.x & 0xffffu) | (q.x << 16);
        *(LAS u32*)(lds + 1 * CV_CH + o) = (p.x >> 16) | (q.x & 0xffff0000u);
        *(LAS u32*)(lds + 2 * CV_CH + o) = (p.y & 0xffffu) | (q.y << 16);
        *(LAS u32*)(lds + 3 * CV_CH + o) = (p.y >> 16) | (q.y & 0xffff0000u);
    }
    LBAR();
    const int c = w & 3, par = w >> 2, b = lane & 31, hh = lane >> 5;
    lptr ch = lds + c * CV_CH;
    lptr wa = ch + ((b & 7) * 260 + (b >> 3) - hh + 3) * 16;
    lptr ub = ch + CV_W + (32 + b) * 80 + hh * 16;
    f32x16 y0 = {}, y1 = {};
    wa += par * 64; ub -= par * 80;
    {
        bf16x8 fa[2][2], fb[2][2], fc[2][2];
#pragma unroll
        for (int ks = 0; ks < 2; ++ks) { fa[0][ks] = lds8(wa - ks * 32); fb[0][ks] = lds8(ub + ks * 32); fc[0][ks] = lds8(ub + 2560 + ks * 32); }
#pragma unroll
        for (int i = 0; i < 32; ++i) {
            const int cur = i & 1, nx = cur ^ 1;
            if (i + 1 < 32) {
#pragma unroll
                for (int ks = 0; ks < 2; ++ks) {
                    fa[nx][ks] = lds8(wa + (i + 1) * 128 - ks * 32);
                    if (i + 1 < 16) fb[nx][ks] = lds8(ub - (i + 1) * 160 + ks * 32);
                    fc[nx][ks] = lds8(ub + 2560 - (i + 1) * 160 + ks * 32);
                }
            }
            __builtin_amdgcn_sched_barrier(0);
#pragma unroll
            for (int ks = 0; ks < 2; ++ks) {
                if (i < 16) y0 = MFMA32(fa[cur][ks], fb[cur][ks], y0);
                y1 = MFMA32(fa[cur][ks], fc[cur][ks], y1);
            }
            __builtin_amdgcn_sched_barrier(0);
        }
    }
    LBAR();
    {
        LAS f32x4* X = (LAS f32x4*)(ch + par * 4096);
        const f32x16 src = par ? y0 : y1;
#pragma unroll
        for (int k = 0; k < 4; ++k) X[k * 64 + lane] = f32x4{src[4 * k], src[4 * k + 1], src[4 * k + 2], src[4 * k + 3]};
    }
    LBAR();
    {
        LAS f32x4* X = (LAS f32x4*)(ch + (1 - par) * 4096);
        lptr Y = ch + 8192 + (32 * par + b) * 72 + hh * 8;
        const f32x16 own = par ? y1 : y0;
#pragma unroll
        for (int k = 0; k < 4; ++k) {
            const f32x4 o = X[k * 64 + lane];
            v2u yv = {pack2(own[4 * k] + o[0], own[4 * k + 1] + o[1]), pack2(own[4 * k + 2] + o[2], own[4 * k + 3] + o[3])};
            *(LAS v2u*)(Y + k * 16) = yv;
        }
    }
    LBAR();
    const v2u dk = *(const v2u*)(a.dskip + d0);
#pragma unroll
    for (int k2 = 0; k2 < 2; ++k2) {
        const int i = tid + 512 * k2;
        lptr yp = lds + 8192 + (i >> 4) * 72 + (i & 15) * 4;
        const u32 ya = *(LAS u32*)(yp), yb = *(LAS u32*)(yp + CV_CH), yc = *(LAS u32*)(yp + 2 * CV_CH), yd = *(LAS u32*)(yp + 3 * CV_CH);
        const v2u p = uv[2 * k2], q = uv[2 * k2 + 1];
        v4u o;
        o.x = pack2(bflo(ya) + rbf(bflo(p.x) * bflo(dk.x)), bflo(yb) + rbf(bfhi(p.x) * bfhi(dk.x)));
        o.y = pack2(bflo(yc) + rbf(bflo(p.y) * bflo(dk.y)), bflo(yd) + rbf(bfhi(p.y) * bfhi(dk.y)));
        o.z = pack2(bfhi(ya) + rbf(bflo(q.x) * bflo(dk.x)), bfhi(yb) + rbf(bfhi(q.x) * bfhi(dk.x)));
        o.w = pack2(bfhi(yc) + rbf(bflo(q.y) * bflo(dk.y)), bfhi(yd) + rbf(bfhi(q.y) * bfhi(dk.y)));
        ST16(o, rw, OFF_A4 + unit * 16384 + i * 16);
    }
    LBAR();
}
__device__ __forceinline__ void role_s4(const Args& a, lptr lds, rsrc_t rw, int idx) {
    if (idx < 32) {
        const int tid = threadIdx.x, t = idx * 64 + (tid >> 3), j = tid & 7;
        float acc = 0.f;
#pragma unroll
        for (int k = 0; k < 9; ++k) {
            const v4u xv = *(const v4u*)(a.u + (size_t)t * DM + (k * 8 + j) * 8), gv = *(const v4u*)(a.gatew + (k * 8 + j) * 8);
#pragma unroll
            for (int e = 0; e < 4; ++e) acc += rbf(bflo(xv[e]) * bflo(gv[e])) + rbf(bfhi(xv[e]) * bfhi(gv[e]));
        }
        acc += __shfl_xor(acc, 1); acc += __shfl_xor(acc, 2); acc += __shfl_xor(acc, 4);
        acc = rbf(acc);
        if (j == 0) ST4(__float_as_uint(rbf(1.0f / (1.0f + __expf(-acc)))), rw, OFF_G + t * 4);
    }
    const bool qwg = idx >= 16 && idx < 48;
    const int u0 = qwg ? 96 + idx : (idx < 16 ? 24 + idx : idx - 48), uend = qwg ? DM / 4 : 112;
#pragma unroll 1
    for (int unit = u0; unit < uend; unit += 40) s4_unit(a, lds, rw, unit);
    DRAIN();
    __syncthreads();
    if (threadIdx.x == 0) ST4(1u, rw, (FL_S + idx) * 128);
}

__device__ __forceinline__ void role_post(const Args& a, lptr lds, rsrc_t rw) {
    const int tid = threadIdx.x, lane = tid & 63, w = __builtin_amdgcn_readfirstlane(tid >> 6);
    const int i16 = lane & 15, g = lane >> 4;
    int budget = SPIN_MAX, vdone0 = -1, vdone1 = -1;
    volatile LAS int* U = (volatile LAS int*)(lds + 81920);
    wait_flags(rw, FL_S, 1, 64, 1, lane, budget);
    wait_flags(rw, FL_S + 64, 1, NS4 - 64, 1, lane, budget);
    constexpr int NFULL = 62 * 6, NUNIT = NFULL + 24;
#pragma unroll 1
    for (;;) {
        if (tid == 0) *U = 1 + (int)__hip_atomic_fetch_add((u32*)(a.ws + FL_Q * 128), 1u, __ATOMIC_RELAXED, __HIP_MEMORY_SCOPE_AGENT);
        __syncthreads();
        const int unit = __builtin_amdgcn_readfirstlane(*U);
        if (unit >= NUNIT || budget < 0) break;
        const bool half = unit >= NFULL;
        const int uu = half ? NFULL + ((unit - NFULL) >> 1) : unit, q0 = half ? (unit - NFULL) & 1 : 0;
        const int s = uu / 6, r = (uu - s * 6) * 6 + (w < 6 ? w : 0);
        bf16x8 b[32];
        v2u av[2]; float gt[2];
        if (w < 6) {
            if (VOWN(s)) { if (vdone1 < s) vdone1 = wait_flags(rw, FL_V + 1, 2, NH, s, lane, budget); }
            else { if (vdone0 < s) vdone0 = wait_flags(rw, FL_V, 2, NH, s, lane, budget); }
            const int vo = OFF_VOX + (s * NR + r) * (NH * 1024) + ((i16 >> 1) & 1) * 16384 + ((g & 1) * 16 + (g >> 1) * 8 + 2 * (i16 >> 2) + (i16 & 1)) * 16;
#pragma unroll
            for (int k = 0; k < 32; ++k) b[k] = as8(LD16(rw, vo + (k & 7) * 512, (k >> 3) * 4096));
#pragma unroll
            for (int qt = 0; qt < 2; ++qt) {
                av[qt] = LD8(rw, OFF_A4 + (4 * r + g) * 16384 + (32 * s + 16 * (qt ^ q0) + i16) * 8, 0);
                gt[qt] = __uint_as_float(LD4(rw, OFF_G + (32 * s + 16 * (qt ^ q0) + i16) * 4, 0));
            }
        }
        v4u z[8];
        const int zo = OFF_ZH + (s * NR + r) * (NH * 256) + ((i16 >> 1) & 1) * 4096 + g * 128 + (2 * (i16 >> 2) + (i16 & 1)) * 16;
        if (half) {
            wait_flags(rw, FL_Z + 2 * r, 1, 2, s, lane, budget, true);
#pragma unroll
            for (int k = 0; k < 8; ++k) z[k] = LD16(rw, zo + k * 512, 0);
            wait_flags(rw, FL_P + (s & 3), 4, 16, s, lane, budget, true);
            v4u pv[5];
#pragma unroll
            for (int k = 0; k < 5; ++k) pv[k] = LD16(rw, OFF_PX + s * 81920 + q0 * 40960 + tid * 16, k * 8192);
#pragma unroll
            for (int k = 0; k < 5; ++k) *(LAS v4u*)(lds + k * 8192 + tid * 16) = pv[k];
        } else {
            wait_flags(rw, FL_P + (s & 3), 4, 16, s, lane, budget);
            v4u pv[10];
#pragma unroll
            for (int k = 0; k < 10; ++k) pv[k] = LD16(rw, OFF_PX + s * 81920 + tid * 16, k * 8192);
            wait_flags(rw, FL_Z + 2 * r, 1, 2, s, lane, budget);
#pragma unroll
            for (int k = 0; k < 8; ++k) z[k] = LD16(rw, zo + k * 512, 0);
            MB();
#pragma unroll
            for (int k = 0; k < 10; ++k) *(LAS v4u*)(lds + k * 8192 + tid * 16) = pv[k];
        }
        __syncthreads();
        if (w < 6) {
#pragma unroll 1
            for (int qt = 0; qt < (half ? 1 : 2); ++qt) {
                f32x4 d = {}, e = {};
                lptr pl = lds + qt * 40960 + lane * 16;
                bf16x8 f[6];
#pragma unroll
                for (int i = 0; i < 6; ++i) f[i] = lds8(pl + i * 1024);
                __builtin_amdgcn_sched_barrier(0);
#pragma unroll
                for (int k = 0; k < 40; ++k) {
                    const bf16x8 v = k < 32 ? b[k < 32 ? k : 0] : as8(z[k < 32 ? 0 : k - 32]);
                    if (k & 1) e = MFMA16(v, f[k % 6], e); else d = MFMA16(v, f[k % 6], d);
                    if (k + 6 < 40) f[k % 6] = lds8(pl + (k + 6) * 1024);
                    __builtin_amdgcn_sched_barrier(0);
                }
                d += e;
                const float gq = qt ? gt[1] : gt[0], og = rbf(1.0f - gq);
                const v2u aq = qt ? av[1] : av[0];
                const float r0 = rbf(gq * bflo(aq.x)) + rbf(og * rbf(d[0])), r1 = rbf(gq * bfhi(aq.x)) + rbf(og * rbf(d[1]));
                const float r2 = rbf(gq * bflo(aq.y)) + rbf(og * rbf(d[2])), r3 = rbf(gq * bfhi(aq.y)) + rbf(og * rbf(d[3]));
                v2u ov = {pack2(r0, r1), pack2(r2, r3)};
                *(v2u*)(a.out + (size_t)(32 * s + 16 * (qt ^ q0) + i16) * DM + 16 * r + 4 * g) = ov;
            }
        }
        if (half) break;
        LBAR();
    }
    if (budget < 0 && lane == 0) ST4(6u, rw, FL_ERR * 128);
}

__global__ void __launch_bounds__(512, 2) k_all(Args a) {
    extern __shared__ __attribute__((aligned(16))) char smem[];
    lptr lds = (lptr)smem;
    a.ws += (0u - (u32)(uintptr_t)a.ws) & 0x7FFFu;
    const rsrc_t rw = __builtin_amdgcn_make_buffer_rsrc(a.ws, 0, WS_BYTES, 0x00020000);
    if (threadIdx.x == 0) {
        const int x = (int)(__builtin_amdgcn_s_getreg(20 | (3 << 11)) & 7u);
        int role = 0;
        for (int k = 0; k < 8; ++k) {
            const int gq = (x + k) & 7;
            const int t = 1 + (int)__hip_atomic_fetch_add((u32*)(a.ws + (FL_T + gq) * 128), 1u, __ATOMIC_RELAXED, __HIP_MEMORY_SCOPE_AGENT);
            if (t < 32) { role = gq + 8 * (31 - t); break; }
        }
        *(volatile LAS int*)lds = role;
    }
    __syncthreads();
    const int b = __builtin_amdgcn_readfirstlane(*(volatile LAS int*)lds), xg = b & 7, slot = b >> 3;
    __syncthreads();

    if (slot == 8 && xg < 4) role_probe(rw, xg);
    if (slot >= 20) { role_wvo(a, lds, rw, (xg & 3) * 24 + (xg < 4 ? 12 : 0) + slot - 20); __syncthreads(); }
    if ((xg < 4 && slot >= 26) || (xg >= 4 && slot >= 20)) {
        const int idx = xg < 4 ? 48 + (slot - 26) * 4 + xg : (31 - slot) * 4 + (xg - 4);
        role_s4(a, lds, rw, idx);
        __syncthreads();
    }
    bool post = false;
    if (xg < 4) {
        if (slot < 8) role_qk(a, lds, rw, xg * 8 + slot, xg);
        else if (slot < 26) role_rz(a, lds, rw, xg * 18 + slot - 8, xg);
    } else {
        const int j = xg - 4;
        post = true;
        if (slot < 16) { role_vvo(a, lds, rw, j * 8 + (slot >> 1), slot & 1); post = slot < NJOIN; }
        else if (slot < 20) role_k2(a, lds, rw, j * 4 + slot - 16);
        else if (slot < 28) { role_q2p(a, lds, rw, j * 4 + ((slot - 20) >> 1), slot & 1); post = false; }
    }
    if (post) { __syncthreads(); role_post(a, lds, rw); }
}

extern "C" void kernel_launch(void* const* d_in, const int* in_sizes, int n_in, void* d_out, int out_size, void* d_ws, size_t ws_size, hipStream_t stream) {
    Args a;
    a.u = (const u16*)d_in[0]; a.Are = (const u16*)d_in[1]; a.Aim = (const u16*)d_in[2]; a.Bre = (const u16*)d_in[3]; a.Bim = (const u16*)d_in[4];
    a.Cre = (const u16*)d_in[5]; a.Cim = (const u16*)d_in[6]; a.logdt = (const u16*)d_in[7]; a.dskip = (const u16*)d_in[8];
    a.mem0 = (const u16*)d_in[10]; a.Wq = (const u16*)d_in[11]; a.Wk = (const u16*)d_in[12]; a.Wv = (const u16*)d_in[13]; a.Wo = (const u16*)d_in[14];
    a.gatew = (const u16*)d_in[16]; a.out = (u16*)d_out; a.ws = (char*)d_ws;
    const int LDS_ALL = 160 * 1024;
    (void)hipFuncSetAttribute((const void*)k_all, hipFuncAttributeMaxDynamicSharedMemorySize, LDS_ALL);
    (void)hipMemsetAsync(d_ws, 0xFF, INIT_BYTES + 32768, stream);
    hipLaunchKernelGGL(k_all, dim3(256), dim3(512), LDS_ALL, stream, a);
}
```
